```python
import torch

def _softmax(x, axis=-1):
    return torch.softmax(x, dim=axis)

def _gelu(x):
    return torch.nn.functional.gelu(x, approximate='tanh')

def _silu(x):
    return torch.nn.functional.silu(x)

def _sigmoid(x):
    return torch.sigmoid(x)

def _layernorm(x, axis=-1, eps=1e-05):
    m = x.mean(dim=axis, keepdim=True)
    v = x.var(dim=axis, keepdim=True, unbiased=False)
    return (x - m) / torch.sqrt(v + eps)

def _scatter_add(tgt, idx, src, axis=0):
    return tgt.clone().index_add_(axis, idx, src)

def _segment_sum(src, seg, n):
    out = torch.zeros((n,) + src.shape[1:], dtype=src.dtype)
    return out.index_add_(0, seg, src)

def _pad(x, pw):
    flat = tuple((v for pair in reversed(pw) for v in pair))
    return torch.nn.functional.pad(x, flat)
kernel_name = 'adaLN_cond__sliding_local_dilated__mixer_channel_mlp__prompt'

def setup_inputs():

    _w = lambda a: (lambda t: t.to(torch.bfloat16) if t.is_floating_point() else t)(torch.as_tensor(a))

    def _A_setup():
        B, S, D = (1, 2048, 640)
        return {'x': _w((lambda *s: torch.randn(*s, dtype=torch.bfloat16) * 0.05)(B, S, D)), 'wqkv': _w((lambda *s: torch.randn(*s, dtype=torch.bfloat16) * 0.05)(D, 3 * D)), 'wo': _w((lambda *s: torch.randn(*s, dtype=torch.bfloat16) * 0.05)(D, D))}
    _A_in = _A_setup()

    def _B_setup():
        B, S, D, F = (1, 2048, 640, 1728)
        return {'x': _w((lambda *s: torch.randn(*s, dtype=torch.bfloat16) * 0.05)(B, S, D)), 'w1': _w((lambda *s: torch.randn(*s, dtype=torch.bfloat16) * 3.2)(D, F) * D ** (-0.5)), 'b1': _w((lambda *s: torch.randn(*s, dtype=torch.bfloat16) * 1.6)(F)), 'w2': _w((lambda *s: torch.randn(*s, dtype=torch.bfloat16) * 0.4)(F, D) * F ** (-0.5)), 'b2': _w((lambda *s: torch.randn(*s, dtype=torch.bfloat16) * 0.8)(D))}
    _B_in = _B_setup()
    return {'A_x': _A_in['x'], 'A_wqkv': _A_in['wqkv'], 'A_wo': _A_in['wo'], 'B_x': _B_in['x'], 'B_w1': _B_in['w1'], 'B_b1': _B_in['b1'], 'B_w2': _B_in['w2'], 'B_b2': _B_in['b2'], 'c': _w((lambda *s: torch.randn(*s, dtype=torch.bfloat16) * 0.1)(1, 640) * 8.0), 'w_mod': _w((lambda *s: torch.randn(*s, dtype=torch.bfloat16) * 0.1)(640, 6 * 640) * 640 ** (-0.5) * 8.0)}

def reference(A_x, A_wqkv, A_wo, B_x, B_w1, B_b1, B_w2, B_b2, c, w_mod):

    def _A_ref(x, wqkv, wo):
        B, S, D = x.shape
        H = 32
        Dh = D // H
        W, dil = (8, 2)
        qkv = (lambda x, *d: x.permute(*d))((x @ wqkv).reshape(B, S, 3, H, Dh), 2, 0, 3, 1, 4)
        q, k, v = (qkv[0], qkv[1], qkv[2])
        scale = Dh ** (-0.5)
        pos = torch.arange(S)
        off = torch.arange(W) * dil
        idx = pos[:, None] - off[None, :]
        mask = idx >= 0
        idx_c = torch.where(mask, idx, 0).reshape(-1)
        kg = k[:, :, idx_c, :].reshape(B, H, S, W, Dh)
        vg = v[:, :, idx_c, :].reshape(B, H, S, W, Dh)
        logits = torch.einsum('bhsd,bhswd->bhsw', q, kg) * scale
        logits = torch.where(mask[None, None, :, :], logits, -1000000000.0)
        attn = _softmax(logits, axis=-1)
        o = torch.einsum('bhsw,bhswd->bhsd', attn, vg)
        return (lambda x, *d: x.permute(*d))(o, 0, 2, 1, 3).reshape(B, S, D) @ wo

    def _B_ref(x, w1, b1, w2, b2):
        y = _layernorm(x, axis=-1)
        h = _gelu(torch.einsum('bsd,df->bsf', y, w1) + b1)
        return torch.einsum('bsf,fd->bsd', h, w2) + b2
    x = A_x
    mod = (_silu(c) @ w_mod).reshape(c.shape[0], 6, 1, -1)
    sa, ca, ga, sb, cb, gb = (mod[:, 0].clone(), mod[:, 1].clone(), mod[:, 2].clone(), mod[:, 3].clone(), mod[:, 4].clone(), mod[:, 5].clone())
    h = _layernorm(x, axis=-1) * (1 + ca) + sa
    x = x + ga * _A_ref(h, A_wqkv, A_wo)
    h = _layernorm(x, axis=-1) * (1 + cb) + sb
    x = x + gb * _B_ref(h, B_w1, B_b1, B_w2, B_b2)
    return x
```

```cpp
#include <hip/hip_runtime.h>
#include <hip/hip_bf16.h>
#include <stdint.h>

typedef unsigned short u16;
static constexpr int S = 2048, D = 640, F = 1728, H = 32, DH = 20;

__device__ __forceinline__ float bf2f(u16 v) { return __uint_as_float(((unsigned)v) << 16); }
__device__ __forceinline__ u16 f2bf(float f) { unsigned u = __float_as_uint(f); return (u16)((u + 0x7FFF + ((u >> 16) & 1)) >> 16); }

__global__ void k_mod(const u16* c, const u16* wm, float* mod) {
    int j = blockIdx.x * blockDim.x + threadIdx.x;
    if (j >= 6 * D) return;
    float acc = 0.f;
    for (int k = 0; k < D; ++k) { float v = bf2f(c[k]); float s = v / (1.f + __expf(-v)); acc += s * bf2f(wm[(size_t)k * 6 * D + j]); }
    mod[j] = acc;
}
__device__ float blk_sum(float v, float* red) {
    for (int m = 32; m >= 1; m >>= 1) v += __shfl_xor(v, m);
    __syncthreads();
    if ((threadIdx.x & 63) == 0) red[threadIdx.x >> 6] = v;
    __syncthreads();
    float s = 0; for (int i = 0; i < (int)(blockDim.x >> 6); ++i) s += red[i];
    return s;
}
__global__ void k_ln(const u16* xb, const float* xf, const float* sh, const float* sc, float* out, int mode) {
    __shared__ float red[16];
    int r = blockIdx.x, t = threadIdx.x;
    float v = mode == 0 ? bf2f(xb[(size_t)r * D + t]) : xf[(size_t)r * D + t];
    float m = blk_sum(v, red) / D; float d = v - m;
    float var = blk_sum(d * d, red) / D;
    float h = d * rsqrtf(var + 1e-5f) * (1.f + sc[t]) + sh[t];
    if (mode == 1) { float m2 = blk_sum(h, red) / D; float d2 = h - m2; float v2 = blk_sum(d2 * d2, red) / D; h = d2 * rsqrtf(v2 + 1e-5f); }
    out[(size_t)r * D + t] = h;
}
__global__ void k_gemm(const float* A, const u16* W, float* C, int M, int N, int K) {
    int n = blockIdx.x * blockDim.x + threadIdx.x, m = blockIdx.y;
    if (n >= N) return;
    float acc = 0.f;
    for (int k = 0; k < K; ++k) acc += A[(size_t)m * K + k] * bf2f(W[(size_t)k * N + n]);
    C[(size_t)m * N + n] = acc;
}
__global__ void k_attn(const float* qkv, float* o) {
    int s = blockIdx.x, h = threadIdx.x; if (h >= H) return;
    const float* q = qkv + (size_t)s * 3 * D + h * DH;
    float lg[8]; float mx = -1e30f;
    for (int w = 0; w < 8; ++w) { int p = s - 2 * w; if (p < 0) { lg[w] = -1e9f; } else { const float* k = qkv + (size_t)p * 3 * D + D + h * DH; float a = 0; for (int d = 0; d < DH; ++d) a += q[d] * k[d]; lg[w] = a * rsqrtf((float)DH); } mx = fmaxf(mx, lg[w]); }
    float sum = 0; for (int w = 0; w < 8; ++w) { lg[w] = __expf(lg[w] - mx); sum += lg[w]; }
    for (int d = 0; d < DH; ++d) { float a = 0; for (int w = 0; w < 8; ++w) { int p = s - 2 * w; if (p < 0) p = 0; a += lg[w] * qkv[(size_t)p * 3 * D + 2 * D + h * DH + d]; } o[(size_t)s * D + h * DH + d] = a / sum; }
}
__global__ void k_res(const u16* x, const float* a, const float* g, float* x1) {
    int i = blockIdx.x * blockDim.x + threadIdx.x; x1[i] = bf2f(x[i]) + g[i % D] * a[i];
}
__global__ void k_gelu(float* h, const u16* b1) {
    int i = blockIdx.x * blockDim.x + threadIdx.x; float v = h[i] + bf2f(b1[i % F]);
    float u = 0.7978845608f * (v + 0.044715f * v * v * v); h[i] = 0.5f * v * (1.f + tanhf(u));
}
__global__ void k_out(const float* x1, const float* a, const u16* b2, const float* g, u16* out) {
    int i = blockIdx.x * blockDim.x + threadIdx.x; out[i] = f2bf(x1[i] + g[i % D] * (a[i] + bf2f(b2[i % D])));
}

extern "C" void kernel_launch(void* const* d_in, const int* in_sizes, int n_in, void* d_out, int out_size, void* d_ws, size_t ws_size, hipStream_t stream) {
    const u16* x = (const u16*)d_in[0]; const u16* wqkv = (const u16*)d_in[1]; const u16* wo = (const u16*)d_in[2];
    const u16* w1 = (const u16*)d_in[4]; const u16* b1 = (const u16*)d_in[5]; const u16* w2 = (const u16*)d_in[6]; const u16* b2 = (const u16*)d_in[7];
    const u16* c = (const u16*)d_in[8]; const u16* wm = (const u16*)d_in[9];
    float* ws = (float*)d_ws;
    float* mod = ws; float* h = mod + 4096; float* qkv = h + S * D; float* o = qkv + S * 3 * D; float* a = o + S * D; float* x1 = a + S * D; float* hid = x1 + S * D;
    k_mod<<<15, 256, 0, stream>>>(c, wm, mod);
    k_ln<<<S, D, 0, stream>>>(x, nullptr, mod, mod + D, h, 0);
    k_gemm<<<dim3((3 * D + 255) / 256, S), 256, 0, stream>>>(h, wqkv, qkv, S, 3 * D, D);
    k_attn<<<S, 64, 0, stream>>>(qkv, o);
    k_gemm<<<dim3((D + 255) / 256, S), 256, 0, stream>>>(o, wo, a, S, D, D);
    k_res<<<S * D / 256, 256, 0, stream>>>(x, a, mod + 2 * D, x1);
    k_ln<<<S, D, 0, stream>>>(nullptr, x1, mod + 3 * D, mod + 4 * D, h, 1);
    k_gemm<<<dim3((F + 255) / 256, S), 256, 0, stream>>>(h, w1, hid, S, F, D);
    k_gelu<<<S * F / 256, 256, 0, stream>>>(hid, b1);
    k_gemm<<<dim3((D + 255) / 256, S), 256, 0, stream>>>(hid, w2, a, S, D, F);
    k_out<<<S * D / 256, 256, 0, stream>>>(x1, a, b2, mod + 5 * D, (u16*)d_out);
}
```

```cpp
#include <hip/hip_runtime.h>
#include <stdint.h>

typedef unsigned short u16;
typedef float f32x4 __attribute__((ext_vector_type(4)));
typedef float f32x2 __attribute__((ext_vector_type(2)));
typedef __bf16 bf16x8 __attribute__((ext_vector_type(8)));
typedef __bf16 bf16x2 __attribute__((ext_vector_type(2)));
typedef unsigned u32x4 __attribute__((ext_vector_type(4)));
typedef unsigned u32x2 __attribute__((ext_vector_type(2)));
typedef short s16x4 __attribute__((ext_vector_type(4)));
#define LAS __attribute__((address_space(3)))
#define GAS __attribute__((address_space(1)))
#define DI __device__ __forceinline__

static constexpr int S_ = 2048, D_ = 640, F_ = 1728;

static constexpr unsigned WS_MODV = 0;
static constexpr unsigned WS_GSC  = 16384;
static constexpr unsigned WS_XNF  = 65536;
static constexpr unsigned WS_WQF  = WS_XNF + 128 * 20 * 1024;
static constexpr unsigned WS_WOF  = WS_WQF + 16 * 20 * 8 * 1024;
static constexpr unsigned WS_W1F  = WS_WOF + 8 * 20 * 5 * 1024;
static constexpr unsigned WS_W2F  = WS_W1F + 108 * 20 * 1024;
static constexpr unsigned WS_OF   = WS_W2F + 8 * 54 * 5 * 1024;
static constexpr unsigned WS_UF   = WS_OF + 128 * 20 * 1024;
static constexpr unsigned WS_X1   = WS_UF + 128 * 20 * 1024;
static constexpr unsigned WS_ST   = WS_X1 + 128 * 40 * 512;
static constexpr unsigned WS_HF   = WS_ST + 2048 * 8 * 8 * 4;
static constexpr unsigned WS_END  = WS_HF + 128 * 54 * 1024;

struct Params {
    const u16* x; const u16* wqkv; const u16* wo; const u16* w1; const u16* b1; const u16* w2; const u16* b2; const u16* c; const u16* wm;
    u16* out; char* ws;
};

DI float bf2f(unsigned v16) { return __uint_as_float(v16 << 16); }
DI float lo_f(unsigned d) { return __uint_as_float(d << 16); }
DI float hi_f(unsigned d) { return __uint_as_float(d & 0xffff0000u); }
DI unsigned pack2(float a, float b) { f32x2 v = {a, b}; return __builtin_bit_cast(unsigned, __builtin_convertvector(v, bf16x2)); }
DI float rbf(float a) { return lo_f(pack2(a, 0.f) & 0xffffu); }
DI f32x4 mfma(u32x4 a, u32x4 b, f32x4 c) { return __builtin_amdgcn_mfma_f32_16x16x32_bf16(__builtin_bit_cast(bf16x8, a), __builtin_bit_cast(bf16x8, b), c, 0, 0, 0); }
DI float red_add_q(float v) {
    auto a = __builtin_amdgcn_permlane16_swap(__float_as_uint(v), __float_as_uint(v), false, false);
    v = __uint_as_float(a[0]) + __uint_as_float(a[1]);
    auto b = __builtin_amdgcn_permlane32_swap(__float_as_uint(v), __float_as_uint(v), false, false);
    return __uint_as_float(b[0]) + __uint_as_float(b[1]);
}
DI float red_max_q(float v) {
    auto a = __builtin_amdgcn_permlane16_swap(__float_as_uint(v), __float_as_uint(v), false, false);
    v = fmaxf(__uint_as_float(a[0]), __uint_as_float(a[1]));
    auto b = __builtin_amdgcn_permlane32_swap(__float_as_uint(v), __float_as_uint(v), false, false);
    return fmaxf(__uint_as_float(b[0]), __uint_as_float(b[1]));
}
DI __amdgpu_buffer_rsrc_t mk_rsrc(const void* p, unsigned bytes) { return __builtin_amdgcn_make_buffer_rsrc((void*)p, 0, bytes, 0x00020000); }
template <int AUX> DI u32x4 bload16(__amdgpu_buffer_rsrc_t r, unsigned voff, unsigned soff) { return __builtin_bit_cast(u32x4, __builtin_amdgcn_raw_buffer_load_b128(r, voff, soff, AUX)); }
template <int AUX> DI void bstore16(u32x4 v, __amdgpu_buffer_rsrc_t r, unsigned voff, unsigned soff) { __builtin_amdgcn_raw_buffer_store_b128(v, r, voff, soff, AUX); }
template <int AUX> DI void bstore8(u32x2 v, __amdgpu_buffer_rsrc_t r, unsigned voff, unsigned soff) { __builtin_amdgcn_raw_buffer_store_b64(v, r, voff, soff, AUX); }
#define BAR() asm volatile("s_waitcnt lgkmcnt(0)\n\ts_barrier" ::: "memory")
DI u32x4 lds_rd16(const char* lds, unsigned off) { return *(const LAS u32x4*)((const LAS char*)lds + off); }

DI void rows_wo(int s, int qd, int& ra, int& rb) { if (s < 16) { ra = 40 * s + 4 * qd; rb = ra + 20; } else { ra = 40 * (4 * (s - 16) + qd) + 16; rb = ra + 20; } }
DI void tiles_p3(int s, int& ta, int& tb) { if (s < 16) { ta = 5 * (s >> 1) + 2 * (s & 1); tb = ta + 1; } else { ta = 10 * (s - 16) + 4; tb = ta + 5; } }
DI void tiles_p4(int s, int& ta, int& tb) { if (s < 48) { int j = s / 3, pr = s % 3; int st = j < 12 ? 7 * j : 84 + 6 * (j - 12); ta = st + 2 * pr; tb = ta + 1; } else { int m = s - 48; ta = 14 * m + 6; tb = ta + 7; } }

__global__ __launch_bounds__(256) void k0_prep(Params p) {
    __shared__ __attribute__((aligned(16))) char lds[4 * 8192 + 1024];
    const int b = blockIdx.x, tid = threadIdx.x, w = __builtin_amdgcn_readfirstlane(tid >> 6), l = tid & 63;
    char* ws = p.ws;
    {
        const int L = (b & 7) + 8 * (b >> 5), sub = (b >> 3) & 3;
        if (L < 60) {
            const int col0 = 64 * L + 16 * sub, half = tid & 1, rg = tid >> 1;
            u32x4 wv[5]; float cs[5];
#pragma unroll
            for (int i = 0; i < 5; ++i) wv[i] = *(const GAS u32x4*)(p.wm + (size_t)(rg + 128 * i) * 3840 + col0 + 8 * half);
#pragma unroll
            for (int i = 0; i < 5; ++i) { float v = bf2f(p.c[rg + 128 * i]); cs[i] = rbf(v / (1.f + __expf(-v))); }
            float a[8] = {0, 0, 0, 0, 0, 0, 0, 0};
#pragma unroll
            for (int i = 0; i < 5; ++i) {
#pragma unroll
                for (int d = 0; d < 4; ++d) { a[2 * d] += cs[i] * lo_f(wv[i][d]); a[2 * d + 1] += cs[i] * hi_f(wv[i][d]); }
            }
#pragma unroll
            for (int e = 0; e < 8; ++e) {
#pragma unroll
                for (int m = 2; m <= 32; m <<= 1) a[e] += __shfl_xor(a[e], m);
            }
            float* red = (float*)(lds + 4 * 8192);
            if (l < 2) {
#pragma unroll
                for (int e = 0; e < 8; ++e) red[w * 16 + l * 8 + e] = a[e];
            }
            __syncthreads();
            if (tid < 16) {
                float v = rbf(red[tid] + red[16 + tid] + red[32 + tid] + red[48 + tid]);
                const int col = col0 + tid, sec = col / 640;
                if (sec == 1 || sec == 4) v = rbf(1.f + v);
                ((float*)(ws + WS_MODV))[col] = v;
            }
        }
    }
    if (b >= 128) {
        const int T = b - 128, t = l & 15, qd = l >> 4;
        u32x4 xv[5];
#pragma unroll
        for (int i = 0; i < 5; ++i) xv[i] = *(const GAS u32x4*)(p.x + (size_t)(16 * T + t) * 640 + 32 * (w + 4 * i) + 8 * qd);
        float sum = 0.f;
#pragma unroll
        for (int i = 0; i < 5; ++i)
#pragma unroll
            for (int d = 0; d < 4; ++d) sum += lo_f(xv[i][d]) + hi_f(xv[i][d]);
        sum = red_add_q(sum);
        float* red = (float*)(lds + 4 * 8192 + 512);
        __syncthreads();
        if (qd == 0) red[w * 16 + t] = sum;
        __syncthreads();
        const float mean = (red[t] + red[16 + t] + red[32 + t] + red[48 + t]) * (1.f / 640.f);
        float vs = 0.f;
#pragma unroll
        for (int i = 0; i < 5; ++i)
#pragma unroll
            for (int d = 0; d < 4; ++d) { float a = lo_f(xv[i][d]) - mean, c2 = hi_f(xv[i][d]) - mean; vs += a * a + c2 * c2; }
        vs = red_add_q(vs);
        __syncthreads();
        if (qd == 0) red[w * 16 + t] = vs;
        __syncthreads();
        const float rstd = rsqrtf((red[t] + red[16 + t] + red[32 + t] + red[48 + t]) * (1.f / 640.f) + 1e-5f);
#pragma unroll
        for (int i = 0; i < 5; ++i) {
            u32x4 o;
#pragma unroll
            for (int d = 0; d < 4; ++d) o[d] = pack2((lo_f(xv[i][d]) - mean) * rstd, (hi_f(xv[i][d]) - mean) * rstd);
            *(GAS u32x4*)(ws + WS_XNF + (size_t)(T * 20 + w + 4 * i) * 1024 + l * 16) = o;
        }
    }
    {
        const int gw = b * 4 + w;
        if (gw < 960) {
            const int qd = l >> 4, q = (l >> 2) & 3, pp = l & 3;
            u32x2 va[8], vb[8]; unsigned dst[8];
#pragma unroll
            for (int i = 0; i < 8; ++i) {
                const int f = gw * 8 + i;
                const u16* W; int pitch, ra, rb, col; bool zero = false;
                if (f < 2560) {
                    const int j = f / 160, s = (f % 160) >> 3, nt = f & 7;
                    W = p.wqkv; pitch = 1920; ra = 32 * s + 8 * qd; rb = ra + 4;
                    const int cq = 40 * j;
                    if (nt < 4) col = (nt >> 1) * 640 + cq + (nt & 1) * 20 + 4 * pp;
                    else if (nt == 4) col = (pp >> 1) * 640 + cq + (pp & 1) * 20 + 16;
                    else if (nt < 7) col = 1280 + cq + (nt - 5) * 20 + 4 * pp;
                    else { col = 1280 + cq + (pp & 1) * 20 + 16; zero = pp >= 2; }
                    dst[i] = WS_WQF + (unsigned)f * 1024;
                } else if (f < 3360) {
                    const int f2 = f - 2560, s = f2 / 40, ng = f2 % 40;
                    W = p.wo; pitch = 640; rows_wo(s, qd, ra, rb); col = 16 * ng + 4 * pp;
                    dst[i] = WS_WOF + (unsigned)(((ng / 5) * 20 + s) * 5 + ng % 5) * 1024;
                } else if (f < 5520) {
                    const int f2 = f - 3360, s = f2 / 108, tl = f2 % 108; int ta, tb; tiles_p3(s, ta, tb);
                    W = p.w1; pitch = 1728; ra = 16 * ta + 4 * qd; rb = 16 * tb + 4 * qd; col = 16 * tl + 4 * pp;
                    dst[i] = WS_W1F + (unsigned)(tl * 20 + s) * 1024;
                } else {
                    const int f2 = f - 5520, s = f2 / 40, ng = f2 % 40; int ta, tb; tiles_p4(s, ta, tb);
                    W = p.w2; pitch = 640; ra = 16 * ta + 4 * qd; rb = 16 * tb + 4 * qd; col = 16 * ng + 4 * pp;
                    dst[i] = WS_W2F + (unsigned)(((ng / 5) * 54 + s) * 5 + ng % 5) * 1024;
                }
                va[i] = *(const GAS u32x2*)(W + (size_t)(ra + q) * pitch + col);
                vb[i] = *(const GAS u32x2*)(W + (size_t)(rb + q) * pitch + col);
                if (zero) { va[i] = (u32x2){0u, 0u}; vb[i] = (u32x2){0u, 0u}; }
            }
            LAS char* sc = (LAS char*)lds + w * 8192;
#pragma unroll
            for (int i = 0; i < 8; ++i) { *(LAS u32x2*)(sc + i * 1024 + l * 8) = va[i]; *(LAS u32x2*)(sc + i * 1024 + 512 + l * 8) = vb[i]; }
#pragma unroll
            for (int i = 0; i < 8; ++i) {
                s16x4 a = __builtin_amdgcn_ds_read_tr16_b64_v4i16((LAS s16x4*)(sc + i * 1024 + l * 8));
                s16x4 c2 = __builtin_amdgcn_ds_read_tr16_b64_v4i16((LAS s16x4*)(sc + i * 1024 + 512 + l * 8));
                u32x2 ua = __builtin_bit_cast(u32x2, a), ub = __builtin_bit_cast(u32x2, c2);
                *(GAS u32x4*)(ws + dst[i] + l * 16) = (u32x4){ua[0], ua[1], ub[0], ub[1]};
            }
        }
    }
}

static constexpr int P1_RING = 8192;
static constexpr int P1_LDS = 8192 + 3 * 16384;

DI u32x4 modulate(u32x4 x, const f32x4& g0, const f32x4& g1, const f32x4& s0, const f32x4& s1) {
    u32x4 o;
    o[0] = pack2(fmaf(lo_f(x[0]), g0[0], s0[0]), fmaf(hi_f(x[0]), g0[1], s0[1]));
    o[1] = pack2(fmaf(lo_f(x[1]), g0[2], s0[2]), fmaf(hi_f(x[1]), g0[3], s0[3]));
    o[2] = pack2(fmaf(lo_f(x[2]), g1[0], s1[0]), fmaf(hi_f(x[2]), g1[1], s1[1]));
    o[3] = pack2(fmaf(lo_f(x[3]), g1[2], s1[2]), fmaf(hi_f(x[3]), g1[3], s1[3]));
    return o;
}
DI u32x2 pack4(const f32x4& v) { return (u32x2){pack2(v[0], v[1]), pack2(v[2], v[3])}; }

struct TileKV { u32x2 ka0, ka1, k2, va0, va1, z; };

DI void attn_unit(const u32x2 (&qa)[2], const u32x2& xq, const TileKV& cur, const TileKV& prev, bool prev_ok, int t, int qd,
                  u32x4& omain, u32x2& oleft) {
    const float cs = 0.22360679774997896f * 1.4426950408889634f;
    f32x4 oa[2], oz[2];
#pragma unroll
    for (int h = 0; h < 2; ++h) {
        const u32x4 qf = {qa[h][0], qa[h][1], qd == h ? xq[0] : 0u, qd == h ? xq[1] : 0u};
        const u32x2 kcp = h == 0 ? cur.ka0 : cur.ka1, kpp = h == 0 ? prev.ka0 : prev.ka1;
        const f32x4 z4 = {0.f, 0.f, 0.f, 0.f};
        f32x4 sp = mfma((u32x4){kpp[0], kpp[1], prev.k2[0], prev.k2[1]}, qf, z4);
        f32x4 sc = mfma((u32x4){kcp[0], kcp[1], cur.k2[0], cur.k2[1]}, qf, z4);
        float e[8]; float mx = -3.0e38f;
#pragma unroll
        for (int r = 0; r < 4; ++r) {
            const int dp = t + 16 - 4 * qd - r, dc = t - 4 * qd - r;
            const bool vp = prev_ok && dp <= 14 && !(dp & 1), vc = dc >= 0 && !(dc & 1);
            e[r] = vp ? sp[r] * cs : -3.0e38f; e[4 + r] = vc ? sc[r] * cs : -3.0e38f;
            mx = fmaxf(mx, fmaxf(e[r], e[4 + r]));
        }
        mx = red_max_q(mx);
        float sum = 0.f;
#pragma unroll
        for (int r = 0; r < 8; ++r) { e[r] = __builtin_amdgcn_exp2f(e[r] - mx); sum += e[r]; }
        sum = red_add_q(sum);
        const float inv = __builtin_amdgcn_rcpf(sum);
        const u32x4 pf = {pack2(e[0] * inv, e[1] * inv), pack2(e[2] * inv, e[3] * inv), pack2(e[4] * inv, e[5] * inv), pack2(e[6] * inv, e[7] * inv)};
        const u32x2 vc2 = h == 0 ? cur.va0 : cur.va1, vp2 = h == 0 ? prev.va0 : prev.va1;
        oa[h] = mfma((u32x4){vp2[0], vp2[1], vc2[0], vc2[1]}, pf, z4);
        oz[h] = mfma((u32x4){prev.z[0], prev.z[1], cur.z[0], cur.z[1]}, pf, z4);
    }
    const u32x2 a0 = pack4(oa[0]), a1 = pack4(oa[1]);
    omain = (u32x4){a0[0], a0[1], a1[0], a1[1]};
    const u32x2 l0 = pack4(oz[0]), l1 = pack4(oz[1]);
    oleft = qd == 0 ? l0 : l1;
}

__global__ __launch_bounds__(256, 1) void k1_qkv_attn(Params p) {
    extern __shared__ __attribute__((aligned(16))) char lds[];
    const int b = blockIdx.x, tid = threadIdx.x, w = __builtin_amdgcn_readfirstlane(tid >> 6), l = tid & 63, t = l & 15, qd = l >> 4;
    const int g = 2 * (b & 7) + (b >> 7), j = (b >> 3) & 15;
    const __amdgpu_buffer_rsrc_t rs = mk_rsrc(p.ws, WS_END);
    const unsigned lo16 = l * 16;
    const int T0 = 8 * g + 2 * w, TH = g == 0 ? 0 : 8 * g - 1;
    const unsigned xb0 = WS_XNF + T0 * 20480, xbh = WS_XNF + TH * 20480 + w * 1024, wb = WS_WQF + j * 163840 + w * 4096;
    constexpr int PF = 4;
    u32x4 xr[10][4], wr[10][4], xh[5];
#pragma unroll
    for (int G = 0; G < PF; ++G) {
#pragma unroll
        for (int i = 0; i < 4; ++i) wr[G][i] = bload16<0>(rs, lo16, wb + G * 16384 + i * 1024);
#pragma unroll
        for (int i = 0; i < 4; ++i) xr[G][i] = bload16<0>(rs, lo16, xb0 + (i & 1) * 20480 + (2 * G + (i >> 1)) * 1024);
    }
#pragma unroll
    for (int i = 0; i < 5; ++i) xh[i] = bload16<0>(rs, lo16, xbh + i * 4096);
    {
        const float* mv = (const float*)(p.ws + WS_MODV);
        for (int i = tid; i < 320; i += 256) { *(LAS f32x4*)((LAS char*)lds + i * 16) = *(const GAS f32x4*)(mv + (i < 160 ? 640 + 4 * i : 4 * (i - 160))); }
    }
    LAS char* ring = (LAS char*)lds + P1_RING;
#pragma unroll
    for (int G = 0; G < 2; ++G)
#pragma unroll
        for (int i = 0; i < 4; ++i) *(LAS u32x4*)(ring + G * 16384 + w * 4096 + i * 1024 + lo16) = wr[G][i];
    BAR();
    f32x4 acc[2][8], hacc[6];
#pragma unroll
    for (int i = 0; i < 8; ++i) { acc[0][i] = (f32x4){0, 0, 0, 0}; acc[1][i] = (f32x4){0, 0, 0, 0}; }
#pragma unroll
    for (int i = 0; i < 6; ++i) hacc[i] = (f32x4){0, 0, 0, 0};
    const LAS char* gs = (const LAS char*)lds + qd * 32;
#pragma unroll
    for (int G = 0; G < 10; ++G) {
        if (G + PF < 10) {
#pragma unroll
            for (int i = 0; i < 4; ++i) wr[G + PF][i] = bload16<0>(rs, lo16, wb + (G + PF) * 16384 + i * 1024);
#pragma unroll
            for (int i = 0; i < 4; ++i) xr[G + PF][i] = bload16<0>(rs, lo16, xb0 + (i & 1) * 20480 + (2 * (G + PF) + (i >> 1)) * 1024);
        }
        if (G + 2 < 10) {
#pragma unroll
            for (int i = 0; i < 4; ++i) *(LAS u32x4*)(ring + ((G + 2) % 3) * 16384 + w * 4096 + i * 1024 + lo16) = wr[G + 2][i];
        }
#pragma unroll
        for (int st = 0; st < 2; ++st) {
            const int s = 2 * G + st;
            const LAS char* slot = ring + (G % 3) * 16384 + st * 8192 + lo16;
            u32x4 wf[8];
#pragma unroll
            for (int n = 0; n < 8; ++n) wf[n] = *(const LAS u32x4*)(slot + n * 1024);
            const f32x4 g0 = *(const LAS f32x4*)(gs + s * 128), g1 = *(const LAS f32x4*)(gs + s * 128 + 16);
            const f32x4 s0 = *(const LAS f32x4*)(gs + 2560 + s * 128), s1 = *(const LAS f32x4*)(gs + 2560 + s * 128 + 16);
#pragma unroll
            for (int tt = 0; tt < 2; ++tt) {
                const u32x4 h = modulate(xr[G][st * 2 + tt], g0, g1, s0, s1);
#pragma unroll
                for (int n = 0; n < 5; ++n) acc[tt][n] = mfma(wf[n], h, acc[tt][n]);
#pragma unroll
                for (int n = 5; n < 8; ++n) acc[tt][n] = mfma(h, wf[n], acc[tt][n]);
            }
            if ((s & 3) == w) {
                const u32x4 h = modulate(xh[s >> 2], g0, g1, s0, s1);
                hacc[0] = mfma(wf[2], h, hacc[0]); hacc[1] = mfma(wf[3], h, hacc[1]); hacc[2] = mfma(wf[4], h, hacc[2]);
                hacc[3] = mfma(h, wf[5], hacc[3]); hacc[4] = mfma(h, wf[6], hacc[4]); hacc[5] = mfma(h, wf[7], hacc[5]);
            }
        }
        BAR();
    }
    LAS char* HP = ring; LAS char* EX = ring + 24576;
#pragma unroll
    for (int i = 0; i < 6; ++i) *(LAS f32x4*)(HP + (w * 6 + i) * 1024 + lo16) = hacc[i];
    TileKV kv[2]; u32x2 qa[2][2], xq[2];
#pragma unroll
    for (int tt = 0; tt < 2; ++tt) {
        qa[tt][0] = pack4(acc[tt][0]); qa[tt][1] = pack4(acc[tt][1]);
        kv[tt].ka0 = pack4(acc[tt][2]); kv[tt].ka1 = pack4(acc[tt][3]);
        xq[tt] = pack4(acc[tt][4]);
        kv[tt].k2[0] = __shfl_xor(xq[tt][0], 32); kv[tt].k2[1] = __shfl_xor(xq[tt][1], 32);
        kv[tt].va0 = pack4(acc[tt][5]); kv[tt].va1 = pack4(acc[tt][6]); kv[tt].z = pack4(acc[tt][7]);
    }
    if (w < 3) {
        LAS char* e = EX + (w + 1) * 3072 + lo16;
        *(LAS u32x4*)(e) = (u32x4){kv[1].ka0[0], kv[1].ka0[1], kv[1].ka1[0], kv[1].ka1[1]};
        *(LAS u32x4*)(e + 1024) = (u32x4){kv[1].k2[0], kv[1].k2[1], kv[1].va0[0], kv[1].va0[1]};
        *(LAS u32x4*)(e + 2048) = (u32x4){kv[1].va1[0], kv[1].va1[1], kv[1].z[0], kv[1].z[1]};
    }
    BAR();
    {
        const int n_t = w < 2 ? 2 : 1;
        for (int k = 0; k < n_t; ++k) {
            const int i = w + 4 * k;
            f32x4 a = *(const LAS f32x4*)(HP + i * 1024 + lo16);
#pragma unroll
            for (int u = 1; u < 4; ++u) a += *(const LAS f32x4*)(HP + (u * 6 + i) * 1024 + lo16);
            u32x2 pk = pack4(a);
            if (i == 2) { pk[0] = __shfl_xor(pk[0], 32); pk[1] = __shfl_xor(pk[1], 32); }
            *(LAS u32x2*)(EX + (i >> 1) * 1024 + lo16 + (i & 1) * 8) = pk;
        }
    }
    const unsigned ob = WS_OF + T0 * 20480;
    u32x4 om; u32x2 ol;
    attn_unit(qa[1], xq[1], kv[1], kv[0], true, t, qd, om, ol);
    bstore16<0>(om, rs, lo16, ob + 20480 + j * 1024);
    if (qd < 2) bstore8<0>(ol, rs, ((j & 3) * 16 + t) * 16 + 8 * qd, ob + 20480 + (16 + (j >> 2)) * 1024);
    BAR();
    TileKV pv;
    {
        const LAS char* e = EX + w * 3072 + lo16;
        const u32x4 a = *(const LAS u32x4*)e, bb = *(const LAS u32x4*)(e + 1024), cc = *(const LAS u32x4*)(e + 2048);
        pv.ka0 = (u32x2){a[0], a[1]}; pv.ka1 = (u32x2){a[2], a[3]}; pv.k2 = (u32x2){bb[0], bb[1]}; pv.va0 = (u32x2){bb[2], bb[3]};
        pv.va1 = (u32x2){cc[0], cc[1]}; pv.z = (u32x2){cc[2], cc[3]};
    }
    attn_unit(qa[0], xq[0], kv[0], pv, !(g == 0 && w == 0), t, qd, om, ol);
    bstore16<0>(om, rs, lo16, ob + j * 1024);
    if (qd < 2) bstore8<0>(ol, rs, ((j & 3) * 16 + t) * 16 + 8 * qd, ob + (16 + (j >> 2)) * 1024);
}

template <int NS, int PD> DI void gemm_ksplit(__amdgpu_buffer_rsrc_t rs, unsigned tokb, unsigned wb, int w, unsigned lo16, char* lds, f32x4 (&res)[5]) {
    constexpr int PER = (NS + 3) / 4;
    const int s0 = NS == 20 ? 5 * w : (w < 2 ? 14 * w : 28 + 13 * (w - 2));
    const int cnt = NS == 20 ? 5 : (w < 2 ? 14 : 13);
    u32x4 tk[PER][4], wf[PER][5];
    f32x4 acc[4][5];
#pragma unroll
    for (int a = 0; a < 4; ++a)
#pragma unroll
        for (int n = 0; n < 5; ++n) acc[a][n] = (f32x4){0, 0, 0, 0};
    auto issue = [&](int i) {
        const int s = s0 + (i < cnt ? i : cnt - 1);
#pragma unroll
        for (int n = 0; n < 5; ++n) wf[i][n] = bload16<0>(rs, lo16, wb + (s * 5 + n) * 1024);
#pragma unroll
        for (int a = 0; a < 4; ++a) tk[i][a] = bload16<0>(rs, lo16, tokb + a * NS * 1024 + s * 1024);
    };
#pragma unroll
    for (int i = 0; i < PD && i < PER; ++i) issue(i);
#pragma unroll
    for (int i = 0; i < PER; ++i) {
        if (i + PD < PER) issue(i + PD);
        if (i < PER - 1 || NS == 20 || i < cnt) {
#pragma unroll
            for (int a = 0; a < 4; ++a)
#pragma unroll
                for (int n = 0; n < 5; ++n) acc[a][n] = mfma(wf[i][n], tk[i][a], acc[a][n]);
        }
    }
    LAS char* R = (LAS char*)lds;
#pragma unroll
    for (int a = 0; a < 4; ++a) {
        if (a != w) {
            const int src = w < a ? w : w - 1;
#pragma unroll
            for (int n = 0; n < 5; ++n) *(LAS f32x4*)(R + ((a * 3 + src) * 5 + n) * 1024 + lo16) = acc[a][n];
        }
    }
    BAR();
#pragma unroll
    for (int n = 0; n < 5; ++n) {
        f32x4 v = w == 0 ? acc[0][n] : w == 1 ? acc[1][n] : w == 2 ? acc[2][n] : acc[3][n];
#pragma unroll
        for (int u = 0; u < 3; ++u) v += *(const LAS f32x4*)(R + ((w * 3 + u) * 5 + n) * 1024 + lo16);
        res[n] = v;
    }
}

__global__ __launch_bounds__(256, 1) void k2_wo(Params p) {
    extern __shared__ __attribute__((aligned(16))) char lds[];
    const int b = blockIdx.x, tid = threadIdx.x, w = __builtin_amdgcn_readfirstlane(tid >> 6), l = tid & 63, t = l & 15, qd = l >> 4;
    const int i32 = b >> 3, P = 4 * (b & 7) + (i32 >> 3), c = i32 & 7;
    const __amdgpu_buffer_rsrc_t rs = mk_rsrc(p.ws, WS_END);
    const unsigned lo16 = l * 16;
    const float* mv = (const float*)(p.ws + WS_MODV);
    const int T = 4 * P + w, tok = 16 * T + t;
    u32x2 xv[5]; f32x4 ga[5], g2[5], s2[5];
#pragma unroll
    for (int n = 0; n < 5; ++n) {
        const int f = 80 * c + 16 * n + 4 * qd;
        xv[n] = *(const GAS u32x2*)(p.x + (size_t)tok * 640 + f);
        ga[n] = *(const GAS f32x4*)(mv + 1280 + f); s2[n] = *(const GAS f32x4*)(mv + 1920 + f); g2[n] = *(const GAS f32x4*)(mv + 2560 + f);
    }
    f32x4 res[5];
    gemm_ksplit<20, 5>(rs, WS_OF + 4 * P * 20480, WS_WOF + c * 102400, w, lo16, lds, res);
    float st[6] = {0, 0, 0, 0, 0, 0};
    u32x2 xb[5], ub[5];
#pragma unroll
    for (int n = 0; n < 5; ++n) {
        float x1[4] = {lo_f(xv[n][0]), hi_f(xv[n][0]), lo_f(xv[n][1]), hi_f(xv[n][1])};
#pragma unroll
        for (int r = 0; r < 4; ++r) x1[r] = fmaf(ga[n][r], res[n][r], x1[r]);
        xb[n] = (u32x2){pack2(x1[0], x1[1]), pack2(x1[2], x1[3])};
        const float xr[4] = {lo_f(xb[n][0]), hi_f(xb[n][0]), lo_f(xb[n][1]), hi_f(xb[n][1])};
        ub[n] = (u32x2){pack2(xr[0] * g2[n][0], xr[1] * g2[n][1]), pack2(xr[2] * g2[n][2], xr[3] * g2[n][3])};
        const float ur[4] = {lo_f(ub[n][0]), hi_f(ub[n][0]), lo_f(ub[n][1]), hi_f(ub[n][1])};
#pragma unroll
        for (int r = 0; r < 4; ++r) {
            st[0] += xr[r]; st[1] = fmaf(xr[r], xr[r], st[1]); st[2] += ur[r]; st[3] = fmaf(ur[r], g2[n][r], st[3]);
            st[4] = fmaf(ur[r], ur[r], st[4]); st[5] = fmaf(ur[r], s2[n][r], st[5]);
        }
    }
#pragma unroll
    for (int i = 0; i < 6; ++i) st[i] = red_add_q(st[i]);
    if (qd == 0) {
        float* sp = (float*)(p.ws + WS_ST) + ((size_t)tok * 8 + c) * 8;
        *(GAS f32x4*)sp = (f32x4){st[0], st[1], st[2], st[3]}; *(GAS f32x2*)(sp + 4) = (f32x2){st[4], st[5]};
    }
    const unsigned ufb = WS_UF + T * 20480;
    bstore16<0>((u32x4){ub[0][0], ub[0][1], ub[1][0], ub[1][1]}, rs, lo16, ufb + (2 * c) * 1024);
    bstore16<0>((u32x4){ub[2][0], ub[2][1], ub[3][0], ub[3][1]}, rs, lo16, ufb + (2 * c + 1) * 1024);
    bstore8<0>(ub[4], rs, lo16 + 8 * (c & 1), ufb + (16 + (c >> 1)) * 1024);
#pragma unroll
    for (int n = 0; n < 5; ++n) bstore8<0>(xb[n], rs, l * 8, WS_X1 + (T * 40 + 5 * c + n) * 512);
    if (b < 108 && w == 0) {
        f32x4 a = {0, 0, 0, 0};
#pragma unroll
        for (int s = 0; s < 20; ++s) {
            int ta, tb; tiles_p3(s, ta, tb);
            const u32x4 wfr = bload16<0>(rs, lo16, WS_W1F + (b * 20 + s) * 1024);
            const float* src = mv + (t == 0 ? 2560 : 1920);
            f32x4 va = *(const GAS f32x4*)(src + 16 * ta + 4 * qd), vb = *(const GAS f32x4*)(src + 16 * tb + 4 * qd);
            if (t == 2) { va = (f32x4){1, 1, 1, 1}; vb = va; } else if (t > 2) { va = (f32x4){0, 0, 0, 0}; vb = va; }
            a = mfma(wfr, (u32x4){pack2(va[0], va[1]), pack2(va[2], va[3]), pack2(vb[0], vb[1]), pack2(vb[2], vb[3])}, a);
        }
        if (t < 3) *(GAS f32x4*)((float*)(p.ws + WS_GSC) + t * 1728 + 16 * b + 4 * qd) = a;
    } else if (b == 108 && w == 0) {
        float v[5] = {0, 0, 0, 0, 0};
        for (int k = l; k < 640; k += 64) { const float gg = mv[2560 + k], ss = mv[1920 + k]; v[0] += gg; v[1] += ss; v[2] += gg * gg; v[3] += ss * ss; v[4] += gg * ss; }
#pragma unroll
        for (int i = 0; i < 5; ++i) { for (int m = 1; m < 64; m <<= 1) v[i] += __shfl_xor(v[i], m); }
        if (l == 0) { float* d = (float*)(p.ws + WS_GSC) + 3 * 1728; for (int i = 0; i < 5; ++i) d[i] = v[i]; }
    }
}

__global__ __launch_bounds__(256, 1) void k3_ffn1(Params p) {
    extern __shared__ __attribute__((aligned(16))) char lds[];
    const int b = blockIdx.x, tid = threadIdx.x, w = __builtin_amdgcn_readfirstlane(tid >> 6), l = tid & 63, t = l & 15, qd = l >> 4;
    const int g = 2 * (b & 7) + (b >> 7), j = (b >> 3) & 15;
    const int t0 = j < 12 ? 7 * j : 84 + 6 * (j - 12), cnt = j < 12 ? 7 : 6;
    const __amdgpu_buffer_rsrc_t rs = mk_rsrc(p.ws, WS_END);
    const unsigned lo16 = l * 16;
    const char* wsrc = p.ws + WS_W1F + (size_t)t0 * 20480;
    for (int i = w; i < cnt * 20; i += 4)
        __builtin_amdgcn_global_load_lds((const GAS unsigned*)(wsrc + i * 1024 + lo16), (LAS unsigned*)((LAS char*)lds + i * 1024), 16, 0, 0);
    const int T0 = 8 * g + 2 * w;
    u32x4 tk[20][2];
#pragma unroll
    for (int s = 0; s < 20; ++s) { tk[s][0] = bload16<0>(rs, lo16, WS_UF + T0 * 20480 + s * 1024); tk[s][1] = bload16<0>(rs, lo16, WS_UF + (T0 + 1) * 20480 + s * 1024); }
    const float* gsc = (const float*)(p.ws + WS_GSC);
    float al[2], be[2], gm[2], de[2];
    {
        const float Gs = gsc[3 * 1728], Ss = gsc[3 * 1728 + 1], Gg = gsc[3 * 1728 + 2], Sss = gsc[3 * 1728 + 3], Ggs = gsc[3 * 1728 + 4];
#pragma unroll
        for (int tt = 0; tt < 2; ++tt) {
            const float* sp = (const float*)(p.ws + WS_ST) + ((size_t)(16 * (T0 + tt) + t) * 8 + 2 * qd) * 8;
            const f32x4 a0 = *(const GAS f32x4*)sp, a1 = *(const GAS f32x4*)(sp + 8); const f32x2 b0 = *(const GAS f32x2*)(sp + 4), b1 = *(const GAS f32x2*)(sp + 12);
            float s0 = red_add_q(a0[0] + a1[0]), s1 = red_add_q(a0[1] + a1[1]), s2 = red_add_q(a0[2] + a1[2]), s3 = red_add_q(a0[3] + a1[3]);
            float s4 = red_add_q(b0[0] + b1[0]), s5 = red_add_q(b0[1] + b1[1]);
            const float iD = 1.f / 640.f;
            const float m1 = s0 * iD, v1 = fmaxf(s1 * iD - m1 * m1, 0.f), r1 = rsqrtf(v1 + 1e-5f);
            const float m2 = (r1 * (s2 - m1 * Gs) + Ss) * iD;
            const float e2 = (r1 * r1 * (s4 - 2.f * m1 * s3 + m1 * m1 * Gg) + 2.f * r1 * (s5 - m1 * Ggs) + Sss) * iD;
            const float r2 = rsqrtf(fmaxf(e2 - m2 * m2, 0.f) + 1e-5f);
            al[tt] = r1 * r2; be[tt] = -r1 * r2 * m1; gm[tt] = r2; de[tt] = -r2 * m2;
        }
    }
    asm volatile("s_waitcnt vmcnt(0)" ::: "memory");
    BAR();
    f32x4 acc[2][7];
#pragma unroll
    for (int n = 0; n < 7; ++n) { acc[0][n] = (f32x4){0, 0, 0, 0}; acc[1][n] = (f32x4){0, 0, 0, 0}; }
    const LAS char* wl = (const LAS char*)lds + lo16;
#pragma unroll
    for (int s = 0; s < 20; ++s) {
#pragma unroll
        for (int n = 0; n < 7; ++n) {
            const u32x4 wf = *(const LAS u32x4*)(wl + (n * 20 + s) * 1024);
            acc[0][n] = mfma(wf, tk[s][0], acc[0][n]); acc[1][n] = mfma(wf, tk[s][1], acc[1][n]);
        }
    }
    u32x2 hb[2][7];
#pragma unroll
    for (int n = 0; n < 7; ++n) {
        const int f = 16 * (t0 + (n < cnt ? n : 0)) + 4 * qd;
        const f32x4 G = *(const GAS f32x4*)(gsc + f), Sv = *(const GAS f32x4*)(gsc + 1728 + f), C = *(const GAS f32x4*)(gsc + 3456 + f);
        const u32x2 bb = *(const GAS u32x2*)(p.b1 + f);
        const float b1v[4] = {lo_f(bb[0]), hi_f(bb[0]), lo_f(bb[1]), hi_f(bb[1])};
#pragma unroll
        for (int tt = 0; tt < 2; ++tt) {
            float hv[4];
#pragma unroll
            for (int r = 0; r < 4; ++r) {
                float z = fmaf(gm[tt], Sv[r], b1v[r]); z = fmaf(be[tt], G[r], z); z = fmaf(de[tt], C[r], z); z = fmaf(al[tt], acc[tt][n][r], z);
                const float u = z * fmaf(z * z, -0.044715f * 1.5957691216057308f * 1.4426950408889634f, -1.5957691216057308f * 1.4426950408889634f);
                hv[r] = z * __builtin_amdgcn_rcpf(1.f + __builtin_amdgcn_exp2f(u));
            }
            hb[tt][n] = (u32x2){pack2(hv[0], hv[1]), pack2(hv[2], hv[3])};
        }
    }
#pragma unroll
    for (int tt = 0; tt < 2; ++tt) {
        const unsigned hbase = WS_HF + (T0 + tt) * 54 * 1024;
#pragma unroll
        for (int pr = 0; pr < 3; ++pr)
            bstore16<0>((u32x4){hb[tt][2 * pr][0], hb[tt][2 * pr][1], hb[tt][2 * pr + 1][0], hb[tt][2 * pr + 1][1]}, rs, lo16, hbase + (3 * j + pr) * 1024);
        if (j < 12) bstore8<0>(hb[tt][6], rs, lo16 + 8 * (j & 1), hbase + (48 + (j >> 1)) * 1024);
    }
}

__global__ __launch_bounds__(256, 1) void k4_ffn2(Params p) {
    extern __shared__ __attribute__((aligned(16))) char lds[];
    const int b = blockIdx.x, tid = threadIdx.x, w = __builtin_amdgcn_readfirstlane(tid >> 6), l = tid & 63, t = l & 15, qd = l >> 4;
    const int i32 = b >> 3, P = 4 * (b & 7) + (i32 >> 3), c = i32 & 7;
    const __amdgpu_buffer_rsrc_t rs = mk_rsrc(p.ws, WS_END);
    const unsigned lo16 = l * 16;
    const float* mv = (const float*)(p.ws + WS_MODV);
    const int T = 4 * P + w, tok = 16 * T + t;
    u32x2 xv[5], b2v[5]; f32x4 gb[5];
#pragma unroll
    for (int n = 0; n < 5; ++n) {
        const int f = 80 * c + 16 * n + 4 * qd;
        xv[n] = *(const GAS u32x2*)(p.ws + WS_X1 + (size_t)(T * 40 + 5 * c + n) * 512 + l * 8);
        gb[n] = *(const GAS f32x4*)(mv + 3200 + f); b2v[n] = *(const GAS u32x2*)(p.b2 + f);
    }
    f32x4 res[5];
    gemm_ksplit<54, 4>(rs, WS_HF + 4 * P * 54 * 1024, WS_W2F + c * 54 * 5 * 1024, w, lo16, lds, res);
#pragma unroll
    for (int n = 0; n < 5; ++n) {
        const float x1[4] = {lo_f(xv[n][0]), hi_f(xv[n][0]), lo_f(xv[n][1]), hi_f(xv[n][1])};
        const float bv[4] = {lo_f(b2v[n][0]), hi_f(b2v[n][0]), lo_f(b2v[n][1]), hi_f(b2v[n][1])};
        float o[4];
#pragma unroll
        for (int r = 0; r < 4; ++r) o[r] = fmaf(gb[n][r], res[n][r] + bv[r], x1[r]);
        *(GAS u32x2*)(p.out + (size_t)tok * 640 + 80 * c + 16 * n + 4 * qd) = (u32x2){pack2(o[0], o[1]), pack2(o[2], o[3])};
    }
}

extern "C" void kernel_launch(void* const* d_in, const int* in_sizes, int n_in, void* d_out, int out_size, void* d_ws, size_t ws_size, hipStream_t stream) {
    Params p;
    p.x = (const u16*)d_in[0]; p.wqkv = (const u16*)d_in[1]; p.wo = (const u16*)d_in[2]; p.w1 = (const u16*)d_in[4]; p.b1 = (const u16*)d_in[5];
    p.w2 = (const u16*)d_in[6]; p.b2 = (const u16*)d_in[7]; p.c = (const u16*)d_in[8]; p.wm = (const u16*)d_in[9];
    p.out = (u16*)d_out; p.ws = (char*)d_ws;
    (void)hipFuncSetAttribute((const void*)k1_qkv_attn, hipFuncAttributeMaxDynamicSharedMemorySize, P1_LDS);
    (void)hipFuncSetAttribute((const void*)k2_wo, hipFuncAttributeMaxDynamicSharedMemorySize, 61440);
    (void)hipFuncSetAttribute((const void*)k3_ffn1, hipFuncAttributeMaxDynamicSharedMemorySize, 143360);
    (void)hipFuncSetAttribute((const void*)k4_ffn2, hipFuncAttributeMaxDynamicSharedMemorySize, 61440);
    k0_prep<<<256, 256, 0, stream>>>(p);
    k1_qkv_attn<<<256, 256, P1_LDS, stream>>>(p);
    k2_wo<<<256, 256, 61440, stream>>>(p);
    k3_ffn1<<<256, 256, 143360, stream>>>(p);
    k4_ffn2<<<256, 256, 61440, stream>>>(p);
}
```

```cpp
#include <hip/hip_runtime.h>
#include <stdint.h>

typedef unsigned short u16;
typedef float f32x4 __attribute__((ext_vector_type(4)));
typedef float f32x2 __attribute__((ext_vector_type(2)));
typedef __bf16 bf16x8 __attribute__((ext_vector_type(8)));
typedef __bf16 bf16x2 __attribute__((ext_vector_type(2)));
typedef unsigned u32x4 __attribute__((ext_vector_type(4)));
typedef unsigned u32x2 __attribute__((ext_vector_type(2)));
typedef short s16x4 __attribute__((ext_vector_type(4)));
typedef __amdgpu_buffer_rsrc_t rsrc_t;
#define LAS __attribute__((address_space(3)))
#define GAS __attribute__((address_space(1)))
#define DI __device__ __forceinline__

static constexpr unsigned FS = 64;
static constexpr unsigned WS_EPOCH = 0;
static constexpr unsigned WS_TMO   = 256;
static constexpr unsigned WS_FA    = 1024;
static constexpr unsigned WS_FC    = WS_FA + 160 * FS;
static constexpr unsigned WS_FD    = WS_FC + 128 * FS;
static constexpr unsigned WS_F1    = WS_FD + 256 * FS;
static constexpr unsigned WS_F2    = WS_F1 + 256 * FS;
static constexpr unsigned WS_F3    = WS_F2 + 256 * FS;
static constexpr unsigned WS_FEND  = WS_F3 + 256 * FS;
static constexpr unsigned WS_MODV = 131072;
static constexpr unsigned WS_BP   = WS_MODV + 16384;
static constexpr unsigned WS_XNF  = WS_BP + 20 * 16 * 128 * 4;
static constexpr unsigned WS_X0F  = WS_XNF + 128 * 20 * 1024;
static constexpr unsigned WS_WQF  = WS_X0F + 128 * 40 * 512;
static constexpr unsigned WS_WOF  = WS_WQF + 16 * 20 * 8 * 1024;
static constexpr unsigned WS_W1F  = WS_WOF + 8 * 20 * 5 * 1024;
static constexpr unsigned WS_W2F  = WS_W1F + 108 * 20 * 1024;
static constexpr unsigned WS_OF   = WS_W2F + 8 * 54 * 5 * 1024;
static constexpr unsigned WS_UF   = WS_OF + 128 * 20 * 1024;
static constexpr unsigned WS_ST   = WS_UF + 128 * 20 * 1024;
static constexpr unsigned WS_HF   = WS_ST + 128 * 8 * 512;
static constexpr unsigned WS_END  = WS_HF + 128 * 54 * 1024;
static_assert(WS_FEND <= WS_MODV, "flags overflow");

static constexpr int LDS_BYTES = 163840;
static constexpr int SC1 = 16;
static constexpr unsigned SPIN_CAP = 40000;

#define STAMP(i)

struct Params {
    const u16* x; const u16* wqkv; const u16* wo; const u16* w1; const u16* b1; const u16* w2; const u16* b2; const u16* c; const u16* wm;
    u16* out; char* ws;
};

DI float lo_f(unsigned d) { return __uint_as_float(d << 16); }
DI float hi_f(unsigned d) { return __uint_as_float(d & 0xffff0000u); }
DI unsigned pack2(float a, float b) { f32x2 v = {a, b}; return __builtin_bit_cast(unsigned, __builtin_convertvector(v, bf16x2)); }
DI float rbf(float a) { return lo_f(pack2(a, 0.f)); }
DI u32x2 pack4(const f32x4& v) { return (u32x2){pack2(v[0], v[1]), pack2(v[2], v[3])}; }
DI f32x4 mfma(u32x4 a, u32x4 b, f32x4 c) { return __builtin_amdgcn_mfma_f32_16x16x32_bf16(__builtin_bit_cast(bf16x8, a), __builtin_bit_cast(bf16x8, b), c, 0, 0, 0); }
DI float red_add_q(float v) {
    auto a = __builtin_amdgcn_permlane16_swap(__float_as_uint(v), __float_as_uint(v), false, false);
    v = __uint_as_float(a[0]) + __uint_as_float(a[1]);
    auto b = __builtin_amdgcn_permlane32_swap(__float_as_uint(v), __float_as_uint(v), false, false);
    return __uint_as_float(b[0]) + __uint_as_float(b[1]);
}
DI float red_max_q(float v) {
    auto a = __builtin_amdgcn_permlane16_swap(__float_as_uint(v), __float_as_uint(v), false, false);
    v = fmaxf(__uint_as_float(a[0]), __uint_as_float(a[1]));
    auto b = __builtin_amdgcn_permlane32_swap(__float_as_uint(v), __float_as_uint(v), false, false);
    return fmaxf(__uint_as_float(b[0]), __uint_as_float(b[1]));
}
DI unsigned swap32(unsigned v) { auto a = __builtin_amdgcn_permlane32_swap(v, v, false, false); return (threadIdx.x & 32) ? a[0] : a[1]; }
DI rsrc_t mk_rsrc(const void* p, unsigned bytes) { return __builtin_amdgcn_make_buffer_rsrc((void*)p, 0, bytes, 0x00020000); }
template <int AUX> DI u32x4 bload16(rsrc_t r, unsigned voff, unsigned soff) { return __builtin_bit_cast(u32x4, __builtin_amdgcn_raw_buffer_load_b128(r, voff, soff, AUX)); }
template <int AUX> DI u32x2 bload8(rsrc_t r, unsigned voff, unsigned soff) { return __builtin_bit_cast(u32x2, __builtin_amdgcn_raw_buffer_load_b64(r, voff, soff, AUX)); }
template <int AUX> DI unsigned bload4(rsrc_t r, unsigned voff, unsigned soff) { return __builtin_amdgcn_raw_buffer_load_b32(r, voff, soff, AUX); }
template <int AUX> DI void bstore16(u32x4 v, rsrc_t r, unsigned voff, unsigned soff) { __builtin_amdgcn_raw_buffer_store_b128(v, r, voff, soff, AUX); }
template <int AUX> DI void bstore8(u32x2 v, rsrc_t r, unsigned voff, unsigned soff) { __builtin_amdgcn_raw_buffer_store_b64(v, r, voff, soff, AUX); }
template <int AUX> DI void bstore4(unsigned v, rsrc_t r, unsigned voff, unsigned soff) { __builtin_amdgcn_raw_buffer_store_b32(v, r, voff, soff, AUX); }
DI f32x4 asf4(u32x4 v) { return __builtin_bit_cast(f32x4, v); }
DI u32x4 asu4(f32x4 v) { return __builtin_bit_cast(u32x4, v); }
#define LANE_IDS() int tz_ = threadIdx.x; asm volatile("" : "+v"(tz_)); const int tid = tz_, l = tid & 63, t = l & 15, qd = l >> 4; const unsigned lo16 = l * 16; (void)t; (void)qd; (void)lo16
#define SB() __builtin_amdgcn_sched_barrier(0)
#define BAR() asm volatile("s_waitcnt lgkmcnt(0)\n\ts_barrier" ::: "memory")
#define DRAIN() asm volatile("s_waitcnt vmcnt(0)" ::: "memory")
#define SGB(mask, n) __builtin_amdgcn_sched_group_barrier(mask, n, 0)
#define LD16(off) (*(const LAS u32x4*)((const LAS char*)lds + (off)))
#define LD8(off) (*(const LAS u32x2*)((const LAS char*)lds + (off)))
#define LDF4(off) (*(const LAS f32x4*)((const LAS char*)lds + (off)))
#define LDF(off) (*(const LAS float*)((const LAS char*)lds + (off)))
#define ST16(off, v) (*(LAS u32x4*)((LAS char*)lds + (off)) = (v))
#define ST8(off, v) (*(LAS u32x2*)((LAS char*)lds + (off)) = (v))
#define STF4(off, v) (*(LAS f32x4*)((LAS char*)lds + (off)) = (v))
#define STF(off, v) (*(LAS float*)((LAS char*)lds + (off)) = (v))
DI u32x2 tr_read(const char* lds, unsigned off) { return __builtin_bit_cast(u32x2, __builtin_amdgcn_ds_read_tr16_b64_v4i16((LAS s16x4*)((LAS char*)lds + off))); }

DI void flag_store(char* ws, unsigned off, unsigned tag) { __hip_atomic_store((GAS unsigned*)(ws + off), tag, __ATOMIC_RELAXED, __HIP_MEMORY_SCOPE_AGENT); }
DI unsigned flag_load(const char* ws, unsigned off) { return __hip_atomic_load((const GAS unsigned*)(ws + off), __ATOMIC_RELAXED, __HIP_MEMORY_SCOPE_AGENT); }
DI void publish(char* ws, unsigned off, unsigned tag) { DRAIN(); BAR(); if (threadIdx.x == 0) flag_store(ws, off, tag); }
template <int N> DI void poll_words(char* ws, const unsigned (&off)[N], bool active, unsigned tag) {
    for (unsigned spins = 0;; ++spins) {
        bool ok = true;
#pragma unroll
        for (int k = 0; k < N; ++k) ok &= flag_load(ws, off[k]) == tag;
        if (__all(ok || !active)) break;
        if (spins > SPIN_CAP) { if ((threadIdx.x & 63) == 0) flag_store(ws, WS_TMO, tag); break; }
        __builtin_amdgcn_s_sleep(1);
    }
}

DI void rows_wo(int s, int qd, int& ra, int& rb) { if (s < 16) { ra = 40 * s + 4 * qd; rb = ra + 20; } else { ra = 40 * (4 * (s - 16) + qd) + 16; rb = ra + 20; } }
DI void tiles_p3(int s, int& ta, int& tb) { if (s < 16) { ta = 5 * (s >> 1) + 2 * (s & 1); tb = ta + 1; } else { ta = 10 * (s - 16) + 4; tb = ta + 5; } }
DI void tiles_p4(int s, int& ta, int& tb) { if (s < 48) { int j = s / 3, pr = s % 3; int st = j < 12 ? 7 * j : 84 + 6 * (j - 12); ta = st + 2 * pr; tb = ta + 1; } else { int m = s - 48; ta = 14 * m + 6; tb = ta + 7; } }

static constexpr int L0_SC = 81920, L0_GS = 83200, L0_CRAW = 83968, L0_RW = 86016, L0_D = 101376, L0_XI = 134144, L0_XS = 156160;

DI void dma16(char* lds, unsigned lds_off, rsrc_t r, unsigned voff, unsigned soff) {
    const unsigned dst = (unsigned)(size_t)(LAS char*)lds + lds_off;
    asm volatile("s_mov_b32 m0, %0\n\ts_nop 0\n\tbuffer_load_dwordx4 %1, %2, %3 offen lds" :: "s"(dst), "v"(voff), "s"(r), "s"(soff) : "memory");
}
#define WAITV(n) asm volatile("s_waitcnt vmcnt(%0)" :: "n"(n) : "memory")

struct DTask { unsigned dst; int kind, s, cg; };
DI DTask dtask_issue(const Params& p, char* lds, int id, int w, int l) {
    DTask d; int ra[4], rb[4]; unsigned pitch; rsrc_t r;
    if (id < 100) {
        d.kind = 0; d.s = id / 5; d.cg = id % 5; pitch = 1280; r = mk_rsrc(p.wo, 640 * 1280);
#pragma unroll
        for (int k = 0; k < 4; ++k) rows_wo(d.s, k, ra[k], rb[k]);
    } else if (id < 370) {
        d.kind = 1; d.s = (id - 100) / 5; d.cg = (id - 100) % 5; pitch = 1280; r = mk_rsrc(p.w2, 1728 * 1280);
        int ta, tb; tiles_p4(d.s, ta, tb);
#pragma unroll
        for (int k = 0; k < 4; ++k) { ra[k] = 16 * ta + 4 * k; rb[k] = 16 * tb + 4 * k; }
    } else {
        const int i2 = id < 650 ? id - 370 : 0;
        d.kind = id < 650 ? 2 : 3; d.s = i2 / 14; d.cg = i2 % 14; pitch = 3456; r = mk_rsrc(p.w1, 640 * 3456);
        int ta, tb; tiles_p3(d.s, ta, tb);
#pragma unroll
        for (int k = 0; k < 4; ++k) { ra[k] = 16 * ta + 4 * k; rb[k] = 16 * tb + 4 * k; }
    }
    const int q = l >> 4, cp = l & 15;
#pragma unroll
    for (int i = 0; i < 8; ++i) {
        const int base = i < 4 ? ra[i] : rb[i - 4];
        const unsigned c = cp ^ ((4 * (i & 1) + q) << 1);
        dma16(lds, L0_D + w * 8192 + i * 1024, r, q * pitch + c * 16, base * pitch + d.cg * 256);
    }
    return d;
}
DI void dtask_finish(const DTask& d, char* lds, rsrc_t rs, int w, int l) {
    if (d.kind == 3) return;
    const int qd = l >> 4, q = (l >> 2) & 3, pp = l & 3, r7 = 4 * (qd & 1) + q;
    const unsigned a0 = L0_D + w * 8192 + (4 * qd + q) * 256 + pp * 8;
#pragma unroll
    for (int nt = 0; nt < 8; ++nt) {
        const u32x2 ua = tr_read(lds, a0 + ((nt ^ r7) * 32)), ub = tr_read(lds, a0 + 4096 + ((nt ^ r7) * 32));
        const int ng = 8 * d.cg + nt;
        unsigned dst;
        if (d.kind == 2) dst = WS_W1F + (ng * 20 + d.s) * 1024;
        else dst = (d.kind == 0 ? WS_WOF + ((ng / 5) * 20 + d.s) * 5120 : WS_W2F + ((ng / 5) * 54 + d.s) * 5120) + (ng % 5) * 1024;
        if (d.kind != 2 || ng < 108) bstore16<SC1>((u32x4){ua[0], ua[1], ub[0], ub[1]}, rs, l * 16, dst);
    }
}

DI void wm_dma(const Params& p, char* lds, int w, int l, unsigned colA, unsigned colB, int rot) {
    const rsrc_t rw = mk_rsrc(p.wm, 640 * 3840 * 2);
    const int rl = l >> 3, cp = l & 7;
    const unsigned sg = ((rl >> 1) & 1);
    const unsigned c0 = cp ^ (sg << 1), c1 = cp ^ ((sg | 2) << 1);
    const unsigned v0 = rl * 7680 + (c0 < 4 ? colA + 8 * c0 : colB + 8 * (c0 - 4)) * 2, v1 = rl * 7680 + (c1 < 4 ? colA + 8 * c1 : colB + 8 * (c1 - 4)) * 2;
#pragma unroll
    for (int i = 0; i < 20; ++i) {
        int piece = 4 * i + w + rot; piece = piece >= 80 ? piece - 80 : piece;
        dma16(lds, piece * 1024, rw, (piece & 1) ? v1 : v0, piece * 8 * 7680);
    }
}
DI void c_dma(const Params& p, char* lds, int w, int l) {
    dma16(lds, L0_CRAW + (w & 1) * 1024, mk_rsrc(p.c, 1280), ((w & 1) * 64 + l) * 16, 0);
}
template <int NC, int NW> DI float mod_gemv(char* lds, int w, int l, int tid) {
    WAITV(NC);
    if (tid < 80) {
        const u32x4 creg = LD16(L0_CRAW + tid * 16);
        u32x4 o;
#pragma unroll
        for (int d = 0; d < 4; ++d) { const float a = lo_f(creg[d]), b = hi_f(creg[d]); o[d] = pack2(a * __builtin_amdgcn_rcpf(1.f + __expf(-a)), b * __builtin_amdgcn_rcpf(1.f + __expf(-b))); }
        ST16(L0_SC + tid * 16, o);
    }
    WAITV(NW);
    BAR();
    const int qd = l >> 4, q = (l >> 2) & 3, pp = l & 3;
    f32x4 acc = {0, 0, 0, 0}, acc2 = {0, 0, 0, 0};
    const int r0 = 8 * qd + q;
    const unsigned sg = ((r0 >> 1) & 1) | (((r0 >> 3) & 1) << 1);
    const unsigned a0 = r0 * 128 + ((w ^ sg) * 32) + pp * 8;
#pragma unroll
    for (int s = 0; s < 20; s += 2) {
        const u32x2 ba = tr_read(lds, a0 + s * 4096), bb = tr_read(lds, a0 + s * 4096 + 512);
        const u32x2 bc = tr_read(lds, a0 + s * 4096 + 4096), bd = tr_read(lds, a0 + s * 4096 + 4608);
        acc = mfma(LD16(L0_SC + s * 64 + qd * 16), (u32x4){ba[0], ba[1], bb[0], bb[1]}, acc);
        acc2 = mfma(LD16(L0_SC + s * 64 + 64 + qd * 16), (u32x4){bc[0], bc[1], bd[0], bd[1]}, acc2);
    }
    return rbf(acc[0] + acc2[0]);
}
DI void x_dma(const Params& p, char* lds, unsigned img, int T, int w, int l) {
    const rsrc_t rx = mk_rsrc(p.x, 2048 * 1280);
    const int r4 = l >> 4, row = 4 * w + r4;
    const unsigned voff = row * 1280 + (((l & 15) ^ row) * 16);
#pragma unroll
    for (int cb = 0; cb < 5; ++cb) dma16(lds, img + (w * 5 + cb) * 1024, rx, voff, T * 16 * 1280 + cb * 256);
}
DI void x_stats(char* lds, unsigned img, unsigned stat, int w, int l) {
    u32x4 v[5];
#pragma unroll
    for (int cb = 0; cb < 5; ++cb) v[cb] = LD16(img + (w * 5 + cb) * 1024 + l * 16);
    float sum = 0.f;
#pragma unroll
    for (int i = 0; i < 5; ++i)
#pragma unroll
        for (int d = 0; d < 4; ++d) sum += lo_f(v[i][d]) + hi_f(v[i][d]);
#pragma unroll
    for (int m = 1; m < 16; m <<= 1) sum += __shfl_xor(sum, m);
    const float mean = sum * (1.f / 640.f);
    float vs = 0.f;
#pragma unroll
    for (int i = 0; i < 5; ++i)
#pragma unroll
        for (int d = 0; d < 4; ++d) { const float a = lo_f(v[i][d]) - mean, c2 = hi_f(v[i][d]) - mean; vs += a * a + c2 * c2; }
#pragma unroll
    for (int m = 1; m < 16; m <<= 1) vs += __shfl_xor(vs, m);
    const float rstd = rsqrtf(vs * (1.f / 640.f) + 1e-5f);
    if ((l & 15) == 0) { STF(stat + (4 * w + (l >> 4)) * 8, mean); STF(stat + (4 * w + (l >> 4)) * 8 + 4, rstd); }
}
DI unsigned ximg_off(int t, int byte_in_row) { return ((t >> 2) * 5 + (byte_in_row >> 8)) * 1024 + (t & 3) * 256 + ((((byte_in_row >> 4) & 15) ^ t) * 16) + (byte_in_row & 15); }
DI void x_frags(char* lds, unsigned img, unsigned stat, rsrc_t rs, int T, int w, int l) {
    const int t = l & 15, qd = l >> 4;
    const float mean = LDF(stat + t * 8), rstd = LDF(stat + t * 8 + 4);
#pragma unroll
    for (int i = 0; i < 5; ++i) {
        const int s = w + 4 * i;
        const u32x4 v = LD16(img + ximg_off(t, s * 64 + qd * 16));
        u32x4 o;
#pragma unroll
        for (int d = 0; d < 4; ++d) o[d] = pack2((lo_f(v[d]) - mean) * rstd, (hi_f(v[d]) - mean) * rstd);
        bstore16<SC1>(o, rs, l * 16, WS_XNF + (T * 20 + s) * 1024);
    }
}
DI void x_raw_tiles(char* lds, unsigned img, rsrc_t rs, int T, int w, int l) {
    const int t = l & 15, qd = l >> 4;
#pragma unroll
    for (int i = 0; i < 10; ++i) {
        const int ng = w * 10 + i;
        bstore8<SC1>(LD8(img + ximg_off(t, ng * 32 + qd * 8)), rs, l * 8, WS_X0F + (T * 40 + ng) * 512);
    }
}

struct TileKV { u32x2 ka0, ka1, k2, va0, va1, z; };
DI void attn_unit(const u32x2 (&qa)[2], const u32x2& xq, const TileKV& cur, const TileKV& prev, bool prev_ok, int t, int qd, u32x4& omain, u32x2& oleft) {
    const float cs = 0.22360679774997896f * 1.4426950408889634f;
    f32x4 oa[2], oz[2];
#pragma unroll
    for (int h = 0; h < 2; ++h) {
        const u32x4 qf = {qa[h][0], qa[h][1], qd == h ? xq[0] : 0u, qd == h ? xq[1] : 0u};
        const u32x2 kcp = h == 0 ? cur.ka0 : cur.ka1, kpp = h == 0 ? prev.ka0 : prev.ka1;
        const f32x4 z4 = {0.f, 0.f, 0.f, 0.f};
        f32x4 sp = mfma((u32x4){kpp[0], kpp[1], prev.k2[0], prev.k2[1]}, qf, z4);
        f32x4 sc = mfma((u32x4){kcp[0], kcp[1], cur.k2[0], cur.k2[1]}, qf, z4);
        float e[8]; float mx = -3.0e38f;
#pragma unroll
        for (int r = 0; r < 4; ++r) {
            const int dp = t + 16 - 4 * qd - r, dc = t - 4 * qd - r;
            const bool vp = prev_ok && dp <= 14 && !(dp & 1), vc = dc >= 0 && !(dc & 1);
            e[r] = vp ? sp[r] * cs : -3.0e38f; e[4 + r] = vc ? sc[r] * cs : -3.0e38f;
            mx = fmaxf(mx, fmaxf(e[r], e[4 + r]));
        }
        mx = red_max_q(mx);
        float sum = 0.f;
#pragma unroll
        for (int r = 0; r < 8; ++r) { e[r] = __builtin_amdgcn_exp2f(e[r] - mx); sum += e[r]; }
        sum = red_add_q(sum);
        const float inv = __builtin_amdgcn_rcpf(sum);
        const u32x4 pf = {pack2(e[0] * inv, e[1] * inv), pack2(e[2] * inv, e[3] * inv), pack2(e[4] * inv, e[5] * inv), pack2(e[6] * inv, e[7] * inv)};
        const u32x2 vc2 = h == 0 ? cur.va0 : cur.va1, vp2 = h == 0 ? prev.va0 : prev.va1;
        oa[h] = mfma((u32x4){vp2[0], vp2[1], vc2[0], vc2[1]}, pf, z4);
        oz[h] = mfma((u32x4){prev.z[0], prev.z[1], cur.z[0], cur.z[1]}, pf, z4);
    }
    const u32x2 a0 = pack4(oa[0]), a1 = pack4(oa[1]);
    omain = (u32x4){a0[0], a0[1], a1[0], a1[1]};
    const u32x2 l0 = pack4(oz[0]), l1 = pack4(oz[1]);
    oleft = qd == 0 ? l0 : l1;
}

__global__ __launch_bounds__(256, 1) void adaLN_block_kernel(Params p) {
    extern __shared__ __attribute__((aligned(16))) char lds[];
    const int b = blockIdx.x, w = __builtin_amdgcn_readfirstlane(threadIdx.x >> 6);
    const int xcd = b & 7, bi = b >> 3;
    char* ws = p.ws;
    const rsrc_t rs = mk_rsrc(ws, WS_END);
    STAMP(0);
    const unsigned epoch = *(const GAS unsigned*)(ws + WS_EPOCH);
    const unsigned tag = epoch + 0x9E3779B9u;

    {
        LANE_IDS();
        const int nA = xcd < 4 ? 24 : 16;
        const int did = (255 - b) * 4 + w;
        DTask dt;
        if (bi < nA) {
            const int s = xcd + 8 * (bi >> 3), jg = bi & 7;
            c_dma(p, lds, w, l);
            {
                const rsrc_t rq = mk_rsrc(p.wqkv, 640 * 3840);
#pragma unroll
                for (int m = 0; m < 4; ++m) {
                    const int n = 64 * (4 * m + w) + l, rho = n / 30, c = n % 30;
                    const int k = (rho & 19) | ((rho & 4) << 1) | ((rho & 8) >> 1);
                    if (4 * m + w < 15) dma16(lds, L0_RW + (4 * m + w) * 1024, rq, k * 3840 + (c / 10) * 1280 + jg * 160 + (c % 10) * 16, s * 32 * 3840);
                }
            }
            wm_dma(p, lds, w, l, 640 + 32 * s, 32 * s, 10 * jg);
            dt = dtask_issue(p, lds, did, w, l);
            STAMP(18);
            const float v = mod_gemv<28, 8>(lds, w, l, tid);
            STAMP(19);
            const float gv = w < 2 ? rbf(1.f + v) : v;
            if (l < 16) { STF(L0_GS + (w * 16 + l) * 4, gv); *(LAS u16*)((LAS char*)lds + L0_GS + 256 + (w * 16 + l) * 2) = (u16)(__float_as_uint(gv) >> 16); }
            BAR();
            const f32x4 g0 = LDF4(L0_GS + qd * 32), g1 = LDF4(L0_GS + qd * 32 + 16);
            const u32x4 sfr = LD16(L0_GS + 256 + 64 + qd * 16);
            const int q = (l >> 2) & 3, pp = l & 3, jj = w >> 1, j = 2 * jg + jj;
            const unsigned rbase = L0_RW + (16 * (qd >> 1) + 4 * (qd & 1) + q) * 480 + 80 * jj;
#pragma unroll
            for (int i = 0; i < 4; ++i) {
                const int nt = 4 * (w & 1) + i;
                unsigned po;
                if (nt < 4) po = (nt >> 1) * 160 + (nt & 1) * 40 + 8 * pp;
                else if (nt == 4) po = (pp >> 1) * 160 + (pp & 1) * 40 + 32;
                else if (nt < 7) po = 320 + (nt - 5) * 40 + 8 * pp;
                else po = 320 + (pp & 1) * 40 + 32;
                u32x2 ua = tr_read(lds, rbase + po), ub = tr_read(lds, rbase + po + 8 * 480);
                if (nt == 7 && t >= 8) { ua = (u32x2){0u, 0u}; ub = ua; }
                const f32x4 z4 = {0, 0, 0, 0};
                const f32x4 bias = mfma((u32x4){ua[0], ua[1], ub[0], ub[1]}, sfr, z4);
                const u32x4 o = {pack2(lo_f(ua[0]) * g0[0], hi_f(ua[0]) * g0[1]), pack2(lo_f(ua[1]) * g0[2], hi_f(ua[1]) * g0[3]),
                                 pack2(lo_f(ub[0]) * g1[0], hi_f(ub[0]) * g1[1]), pack2(lo_f(ub[1]) * g1[2], hi_f(ub[1]) * g1[3])};
                bstore16<SC1>(o, rs, lo16, WS_WQF + ((j * 20 + s) * 8 + nt) * 1024);
                if (t == 0) bstore16<SC1>(asu4(bias), rs, qd * 16, WS_BP + ((s * 16 + j) * 128 + nt * 16) * 4);
            }
            STAMP(20);
            publish(ws, WS_FA + (jg * 20 + s) * FS, tag);
        } else if (bi < nA + 5) {
            const int idxB = xcd * 5 + (bi - nA);
            c_dma(p, lds, w, l);
            wm_dma(p, lds, w, l, 1280 + 64 * idxB, 1280 + 64 * idxB + 32, 0);
            dt = dtask_issue(p, lds, did, w, l);
            STAMP(18);
            float v = mod_gemv<28, 8>(lds, w, l, tid);
            STAMP(19);
            const int col = 1280 + 64 * idxB + 16 * w + t;
            if (col >= 2560 && col < 3200) v = rbf(1.f + v);
            if (l < 16) bstore4<SC1>(__float_as_uint(v), rs, l * 4, WS_MODV + (1280 + 64 * idxB + 16 * w) * 4);
        } else {
            const int idx = xcd < 4 ? xcd * 3 + (bi - 29) : 12 + (xcd - 4) * 11 + (bi - 21);
            const int Ts = idx < 16 ? 3 * idx : 48 + 2 * (idx - 16), nT = idx < 16 ? 3 : 2;
#pragma unroll
            for (int k = 0; k < 3; ++k) x_dma(p, lds, k * 20480, Ts + (k < nT ? k : 0), w, l);
            STAMP(18);
            WAITV(10); x_stats(lds, 0, L0_XS, w, l);
            WAITV(5); x_stats(lds, 20480, L0_XS + 128, w, l);
            WAITV(0); x_stats(lds, 40960, L0_XS + 256, w, l);
            BAR();
            STAMP(19);
#pragma unroll
            for (int k = 0; k < 3; ++k) if (k < nT) x_frags(lds, k * 20480, L0_XS + k * 128, rs, Ts + k, w, l);
            dt = dtask_issue(p, lds, did, w, l);
            WAITV(8); BAR();
            if (tid < nT) flag_store(ws, WS_FC + (Ts + tid) * FS, tag);
            STAMP(20);
#pragma unroll
            for (int k = 0; k < 3; ++k) if (k < nT) x_raw_tiles(lds, k * 20480, rs, Ts + k, w, l);
        }
        STAMP(1);
        WAITV(0);
        dtask_finish(dt, lds, rs, w, l);
    }

    const int g1 = 4 * (xcd >> 1) + (bi >> 3), j1 = 8 * (xcd & 1) + (bi & 7);
    const int P2 = 4 * xcd + (bi >> 3), c2 = bi & 7;
    {
        LANE_IDS();
        if (w == 0) {
            unsigned off[1];
            const int TH = g1 == 0 ? 0 : 8 * g1 - 1;
            off[0] = l < 20 ? WS_FA + ((j1 >> 1) * 20 + l) * FS : (l == 20 ? WS_FC + TH * FS : WS_FC + (8 * g1 + (l < 29 ? l - 21 : 0)) * FS);
            poll_words<1>(ws, off, l < 29, tag);
        }
        DRAIN(); BAR();
        if (tid == 0) flag_store(ws, WS_FD + b * FS, tag);
    }
    STAMP(2);
    u32x4 wo_st[25];
    {
        LANE_IDS();
        const int T0 = 8 * g1 + 2 * w, TH = g1 == 0 ? 0 : 8 * g1 - 1;
        const unsigned xb0 = WS_XNF + T0 * 20480, xbh = WS_XNF + TH * 20480 + (w >> 1) * 1024, wb = WS_WQF + j1 * 163840 + w * 4096;
        constexpr int PF = 4;
        u32x4 xr[10][4], wr[10][4], xh[10];
#pragma unroll
        for (int G = 0; G < PF; ++G) {
#pragma unroll
            for (int i = 0; i < 4; ++i) wr[G][i] = bload16<SC1>(rs, lo16, wb + G * 16384 + i * 1024);
#pragma unroll
            for (int i = 0; i < 4; ++i) xr[G][i] = bload16<SC1>(rs, lo16, xb0 + (i & 1) * 20480 + (2 * G + (i >> 1)) * 1024);
            xh[G] = bload16<SC1>(rs, lo16, xbh + G * 2048);
        }
        SB();
#pragma unroll
        for (int G = 0; G < 2; ++G)
#pragma unroll
            for (int i = 0; i < 4; ++i) ST16(G * 16384 + w * 4096 + i * 1024 + lo16, wr[G][i]);
        BAR();
        STAMP(3);
        f32x4 acc[2][8], hacc[3];
#pragma unroll
        for (int i = 0; i < 8; ++i) { acc[0][i] = (f32x4){0, 0, 0, 0}; acc[1][i] = (f32x4){0, 0, 0, 0}; }
#pragma unroll
        for (int i = 0; i < 3; ++i) hacc[i] = (f32x4){0, 0, 0, 0};
        u32x4 wf[2][8];
#pragma unroll
        for (int n = 0; n < 8; ++n) wf[0][n] = LD16(n * 1024 + lo16);
        const unsigned hoff = (w >> 1) * 8192 + ((w & 1) ? 5 : 2) * 1024 + lo16;
        unsigned bpv[20];
        SB();
#pragma unroll
        for (int G = 0; G < 10; ++G) {
            if (G + PF < 10) {
#pragma unroll
                for (int i = 0; i < 4; ++i) wr[G + PF][i] = bload16<SC1>(rs, lo16, wb + (G + PF) * 16384 + i * 1024);
#pragma unroll
                for (int i = 0; i < 4; ++i) xr[G + PF][i] = bload16<SC1>(rs, lo16, xb0 + (i & 1) * 20480 + (2 * (G + PF) + (i >> 1)) * 1024);
                xh[G + PF] = bload16<SC1>(rs, lo16, xbh + (G + PF) * 2048);
            }
            if (G == 6) {
#pragma unroll
                for (int i = 0; i < 20; ++i) bpv[i] = bload4<SC1>(rs, (tid & 127) * 4, WS_BP + (i * 16 + j1) * 512);
            }
            SB();
            u32x4 hw[3];
#pragma unroll
            for (int st = 0; st < 2; ++st) {
                const int s = 2 * G + st;
                if (st == 0 && G + 2 < 10) {
#pragma unroll
                    for (int i = 0; i < 4; ++i) ST16(((G + 2) % 3) * 16384 + w * 4096 + i * 1024 + lo16, wr[G + 2][i]);
                }
                if (st == 0) {
#pragma unroll
                    for (int i = 0; i < 3; ++i) hw[i] = LD16((G % 3) * 16384 + hoff + i * 1024);
                }
                if (s + 1 < 20) {
                    const unsigned nb = (((s + 1) >> 1) % 3) * 16384 + ((s + 1) & 1) * 8192 + lo16;
#pragma unroll
                    for (int n = 0; n < 8; ++n) wf[(s + 1) & 1][n] = LD16(nb + n * 1024);
                }
#pragma unroll
                for (int n = 0; n < 5; ++n) { acc[0][n] = mfma(wf[s & 1][n], xr[G][st * 2], acc[0][n]); acc[1][n] = mfma(wf[s & 1][n], xr[G][st * 2 + 1], acc[1][n]); }
#pragma unroll
                for (int n = 5; n < 8; ++n) { acc[0][n] = mfma(xr[G][st * 2], wf[s & 1][n], acc[0][n]); acc[1][n] = mfma(xr[G][st * 2 + 1], wf[s & 1][n], acc[1][n]); }
                if (s + 1 < 20) {
#pragma unroll
                    for (int n = 0; n < 8; ++n) { SGB(0x100, 1); SGB(0x8, 2); }
                }
                SB();
            }
            if (w & 1) {
#pragma unroll
                for (int i = 0; i < 3; ++i) hacc[i] = mfma(xh[G], hw[i], hacc[i]);
            } else {
#pragma unroll
                for (int i = 0; i < 3; ++i) hacc[i] = mfma(hw[i], xh[G], hacc[i]);
            }
            BAR();
        }
        STAMP(4);
        if (w == 0) {
            unsigned off[4];
#pragma unroll
            for (int k = 0; k < 4; ++k) off[k] = WS_FD + (l + 64 * k) * FS;
            poll_words<4>(ws, off, true, tag);
            if (b == 0 && l == 0) flag_store(ws, WS_EPOCH, tag);
        }
        constexpr int L1_BIAS = 49152, L1_HP = 0, L1_EX = 8192;
        if (tid < 128) {
            float bs = 0.f;
#pragma unroll
            for (int i = 0; i < 20; ++i) bs += __uint_as_float(bpv[i]);
            STF(L1_BIAS + tid * 4, bs);
        }
        if (w >= 2) {
#pragma unroll
            for (int i = 0; i < 3; ++i) STF4(L1_HP + ((w & 1) * 3 + i) * 1024 + lo16, hacc[i]);
        }
        BAR();
#pragma unroll
        for (int i = 0; i < 25; ++i) wo_st[i] = bload16<SC1>(rs, lo16, WS_WOF + c2 * 102400 + (w * 25 + i) * 1024);
        SB();
        f32x4 bq[5]; float bv[3];
#pragma unroll
        for (int n = 0; n < 5; ++n) bq[n] = LDF4(L1_BIAS + (n * 16 + 4 * qd) * 4);
#pragma unroll
        for (int n = 0; n < 3; ++n) bv[n] = LDF(L1_BIAS + ((5 + n) * 16 + t) * 4);
        TileKV kv[2]; u32x2 qa[2][2], xq[2];
#pragma unroll
        for (int tt = 0; tt < 2; ++tt) {
            qa[tt][0] = pack4(acc[tt][0] + bq[0]); qa[tt][1] = pack4(acc[tt][1] + bq[1]);
            kv[tt].ka0 = pack4(acc[tt][2] + bq[2]); kv[tt].ka1 = pack4(acc[tt][3] + bq[3]);
            xq[tt] = pack4(acc[tt][4] + bq[4]);
            kv[tt].k2[0] = swap32(xq[tt][0]); kv[tt].k2[1] = swap32(xq[tt][1]);
            kv[tt].va0 = pack4(acc[tt][5] + bv[0]); kv[tt].va1 = pack4(acc[tt][6] + bv[1]); kv[tt].z = pack4(acc[tt][7] + bv[2]);
        }
        if (w < 3) {
            const unsigned e = L1_EX + (w + 1) * 3072 + lo16;
            ST16(e, ((u32x4){kv[1].ka0[0], kv[1].ka0[1], kv[1].ka1[0], kv[1].ka1[1]}));
            ST16(e + 1024, ((u32x4){kv[1].k2[0], kv[1].k2[1], kv[1].va0[0], kv[1].va0[1]}));
            ST16(e + 2048, ((u32x4){kv[1].va1[0], kv[1].va1[1], kv[1].z[0], kv[1].z[1]}));
        }
        if (w == 0) {
            const u32x2 k0 = pack4(hacc[0] + LDF4(L1_HP + lo16) + bq[2]), k1 = pack4(hacc[1] + LDF4(L1_HP + 1024 + lo16) + bq[3]);
            const u32x2 xx = pack4(hacc[2] + LDF4(L1_HP + 2048 + lo16) + bq[4]);
            ST16(L1_EX + lo16, ((u32x4){k0[0], k0[1], k1[0], k1[1]}));
            ST8(L1_EX + 1024 + lo16, ((u32x2){swap32(xx[0]), swap32(xx[1])}));
        } else if (w == 1) {
            const u32x2 v0 = pack4(hacc[0] + LDF4(L1_HP + 3072 + lo16) + bv[0]), v1 = pack4(hacc[1] + LDF4(L1_HP + 4096 + lo16) + bv[1]);
            const u32x2 zz = pack4(hacc[2] + LDF4(L1_HP + 5120 + lo16) + bv[2]);
            ST8(L1_EX + 1024 + lo16 + 8, v0);
            ST16(L1_EX + 2048 + lo16, ((u32x4){v1[0], v1[1], zz[0], zz[1]}));
        }
        const unsigned ob = WS_OF + T0 * 20480;
        u32x4 om; u32x2 ol;
        attn_unit(qa[1], xq[1], kv[1], kv[0], true, t, qd, om, ol);
        bstore16<SC1>(om, rs, lo16, ob + 20480 + j1 * 1024);
        if (qd < 2) bstore8<SC1>(ol, rs, ((j1 & 3) * 16 + t) * 16 + 8 * qd, ob + 20480 + (16 + (j1 >> 2)) * 1024);
        BAR();
        TileKV pv;
        {
            const unsigned e = L1_EX + w * 3072 + lo16;
            const u32x4 a = LD16(e), bb = LD16(e + 1024), cc = LD16(e + 2048);
            pv.ka0 = (u32x2){a[0], a[1]}; pv.ka1 = (u32x2){a[2], a[3]}; pv.k2 = (u32x2){bb[0], bb[1]}; pv.va0 = (u32x2){bb[2], bb[3]};
            pv.va1 = (u32x2){cc[0], cc[1]}; pv.z = (u32x2){cc[2], cc[3]};
        }
        attn_unit(qa[0], xq[0], kv[0], pv, !(g1 == 0 && w == 0), t, qd, om, ol);
        bstore16<SC1>(om, rs, lo16, ob + j1 * 1024);
        if (qd < 2) bstore8<SC1>(ol, rs, ((j1 & 3) * 16 + t) * 16 + 8 * qd, ob + (16 + (j1 >> 2)) * 1024);
        STAMP(5);
        publish(ws, WS_F1 + (g1 * 16 + j1) * FS, tag);
    }
    STAMP(6);

    constexpr int L2_WO = 61440;
    const int g3 = 2 * xcd + (bi >> 4), j3 = bi & 15;
    const int t3 = j3 < 12 ? 7 * j3 : 84 + 6 * (j3 - 12), cnt3 = j3 < 12 ? 7 : 6;
    u32x2 x1b[5];
    u32x4 w2f[14][5];
    const int s4 = __builtin_amdgcn_readfirstlane(w < 2 ? 14 * w : 28 + 13 * (w - 2)), cnt4 = __builtin_amdgcn_readfirstlane(w < 2 ? 14 : 13);
    const unsigned w2b = __builtin_amdgcn_readfirstlane(WS_W2F + (c2 * 54 + s4) * 5 * 1024);
    {
        LANE_IDS();
        const int T = 4 * P2 + w;
#pragma unroll
        for (int i = 0; i < 25; ++i) ST16(L2_WO + (w * 25 + i) * 1024 + lo16, wo_st[i]);
        constexpr int L2_MV = 57344;
        u32x2 xv[5];
#pragma unroll
        for (int n = 0; n < 5; ++n) xv[n] = bload8<SC1>(rs, l * 8, WS_X0F + (T * 40 + 5 * c2 + n) * 512);
        if (tid < 60) ST16(L2_MV + tid * 16, bload16<SC1>(rs, (1280 + (tid / 20) * 640 + 80 * c2 + (tid % 20) * 4) * 4, WS_MODV));
        SB();
        if (w == 0) {
            unsigned off[1] = {WS_F1 + ((P2 >> 1) * 16 + (l & 15)) * FS};
            poll_words<1>(ws, off, l < 16, tag);
        }
        BAR();
        STAMP(7);
        u32x4 tk[20];
#pragma unroll
        for (int s = 0; s < 20; ++s) tk[s] = bload16<SC1>(rs, lo16, WS_OF + (T * 20 + s) * 1024);
        u32x4 w1a[14];
#pragma unroll
        for (int i = 0; i < 14; ++i) { const int f = w * 14 + i, s = f / 7, n = f % 7; w1a[i] = bload16<SC1>(rs, lo16, WS_W1F + ((t3 + (n < cnt3 ? n : 0)) * 20 + s) * 1024); }
        SB();
        f32x4 acc[5];
#pragma unroll
        for (int n = 0; n < 5; ++n) acc[n] = (f32x4){0, 0, 0, 0};
        u32x4 wf[2][5];
#pragma unroll
        for (int n = 0; n < 5; ++n) wf[0][n] = LD16(L2_WO + n * 1024 + lo16);
        SB();
#pragma unroll
        for (int s = 0; s < 20; ++s) {
            if (s + 1 < 20) {
#pragma unroll
                for (int n = 0; n < 5; ++n) wf[(s + 1) & 1][n] = LD16(L2_WO + ((s + 1) * 5 + n) * 1024 + lo16);
            }
#pragma unroll
            for (int n = 0; n < 5; ++n) acc[n] = mfma(wf[s & 1][n], tk[s], acc[n]);
            if (s + 1 < 20) {
#pragma unroll
                for (int n = 0; n < 5; ++n) { SGB(0x100, 1); SGB(0x8, 1); }
            }
            SB();
            if (s == 0) STAMP(28);
            if (s == 9) STAMP(29);
        }
        STAMP(8);
#pragma unroll
        for (int i = 0; i < 14; ++i) ST16((w * 14 + i) * 1024 + lo16, w1a[i]);
        BAR();
        u32x4 w1b[21];
#pragma unroll
        for (int i = 0; i < 21; ++i) { const int f = 56 + w * 21 + i, s = f / 7, n = f % 7; w1b[i] = bload16<SC1>(rs, lo16, WS_W1F + ((t3 + (n < cnt3 ? n : 0)) * 20 + s) * 1024); }
        SB();
        float st[6] = {0, 0, 0, 0, 0, 0};
        u32x2 ub[5];
#pragma unroll
        for (int n = 0; n < 5; ++n) {
            const f32x4 ga_n = LDF4(L2_MV + (16 * n + 4 * qd) * 4), s2_n = LDF4(L2_MV + 320 + (16 * n + 4 * qd) * 4), g2_n = LDF4(L2_MV + 640 + (16 * n + 4 * qd) * 4);
            float x1[4] = {lo_f(xv[n][0]), hi_f(xv[n][0]), lo_f(xv[n][1]), hi_f(xv[n][1])};
#pragma unroll
            for (int r = 0; r < 4; ++r) x1[r] = fmaf(ga_n[r], acc[n][r], x1[r]);
            x1b[n] = (u32x2){pack2(x1[0], x1[1]), pack2(x1[2], x1[3])};
            const float xr[4] = {lo_f(x1b[n][0]), hi_f(x1b[n][0]), lo_f(x1b[n][1]), hi_f(x1b[n][1])};
            ub[n] = (u32x2){pack2(xr[0] * g2_n[0], xr[1] * g2_n[1]), pack2(xr[2] * g2_n[2], xr[3] * g2_n[3])};
            const float ur[4] = {lo_f(ub[n][0]), hi_f(ub[n][0]), lo_f(ub[n][1]), hi_f(ub[n][1])};
#pragma unroll
            for (int r = 0; r < 4; ++r) {
                st[0] += xr[r]; st[1] = fmaf(xr[r], xr[r], st[1]); st[2] += ur[r]; st[3] = fmaf(ur[r], g2_n[r], st[3]);
                st[4] = fmaf(ur[r], ur[r], st[4]); st[5] = fmaf(ur[r], s2_n[r], st[5]);
            }
        }
#pragma unroll
        for (int i = 0; i < 6; ++i) st[i] = red_add_q(st[i]);
        const unsigned ufb = WS_UF + T * 20480;
        bstore16<SC1>((u32x4){ub[0][0], ub[0][1], ub[1][0], ub[1][1]}, rs, lo16, ufb + (2 * c2) * 1024);
        bstore16<SC1>((u32x4){ub[2][0], ub[2][1], ub[3][0], ub[3][1]}, rs, lo16, ufb + (2 * c2 + 1) * 1024);
        bstore8<SC1>(ub[4], rs, lo16 + 8 * (c2 & 1), ufb + (16 + (c2 >> 1)) * 1024);
        if (qd < 2) bstore16<SC1>(asu4(qd == 0 ? (f32x4){st[0], st[1], st[2], st[3]} : (f32x4){st[4], st[5], 0.f, 0.f}), rs, qd * 256 + t * 16, WS_ST + (T * 8 + c2) * 512);
#pragma unroll
        for (int n = 0; n < 5; ++n) asm volatile("" : "+v"(x1b[n]));
        STAMP(9);
        publish(ws, WS_F2 + (P2 * 8 + c2) * FS, tag);
#pragma unroll
        for (int i = 0; i < 21; ++i) ST16((56 + w * 21 + i) * 1024 + lo16, w1b[i]);
    }
    STAMP(10);

    {
        LANE_IDS();
        constexpr int L3_GSC = 143360;
        u32x4 vf[5];
#pragma unroll
        for (int i = 0; i < 5; ++i) {
            int ta, tb; tiles_p3(5 * w + i, ta, tb);
            const unsigned sec = WS_MODV + (t == 0 ? 2560 : 1920) * 4 + qd * 16;
            f32x4 va = asf4(bload16<SC1>(rs, sec + ta * 64, 0)), vb = asf4(bload16<SC1>(rs, sec + tb * 64, 0));
            if (t == 2) { va = (f32x4){1, 1, 1, 1}; vb = va; } else if (t > 2) { va = (f32x4){0, 0, 0, 0}; vb = va; }
            vf[i] = (u32x4){pack2(va[0], va[1]), pack2(va[2], va[3]), pack2(vb[0], vb[1]), pack2(vb[2], vb[3])};
        }
        u32x2 b1v[7];
#pragma unroll
        for (int i = 0; i < 20; ++i) { if (i < 5) {
#pragma unroll
            for (int n = 0; n < 5; ++n) w2f[i][n] = bload16<SC1>(rs, lo16, w2b + (i * 5 + n) * 1024); } }
        SB();
        BAR();
        {
            f32x4 va[7], gram = {0, 0, 0, 0};
#pragma unroll
            for (int n = 0; n < 7; ++n) va[n] = (f32x4){0, 0, 0, 0};
#pragma unroll
            for (int i = 0; i < 5; ++i) {
#pragma unroll
                for (int n = 0; n < 7; ++n) va[n] = mfma(LD16(((5 * w + i) * 7 + n) * 1024 + lo16), vf[i], va[n]);
                gram = mfma(vf[i], vf[i], gram);
            }
            if (t < 3) {
#pragma unroll
                for (int n = 0; n < 7; ++n) STF4(L3_GSC + ((w * 8 + n) * 3 + t) * 64 + qd * 16, va[n]);
                if (qd == 0) STF4(L3_GSC + ((w * 8 + 7) * 3 + t) * 64, gram);
            }
        }
        BAR();
        if (tid < 96) {
            const int n8 = tid / 12, r12 = tid % 12;
            f32x4 a = LDF4(L3_GSC + (n8 * 12 + r12) * 16);
#pragma unroll
            for (int u = 1; u < 4; ++u) a += LDF4(L3_GSC + (u * 96 + n8 * 12 + r12) * 16);
            STF4(L3_GSC + 6144 + (n8 * 12 + r12) * 16, a);
        }
        const int T0 = 8 * g3 + 2 * w;
        if (w == 0) {
            unsigned off[1] = {WS_F2 + ((2 * g3 + ((l >> 3) & 1)) * 8 + (l & 7)) * FS};
            poll_words<1>(ws, off, l < 16, tag);
        }
        BAR();
        STAMP(11);
        u32x4 tk[20][2];
        u32x4 sp[2][4];
#pragma unroll
        for (int s = 0; s < 20; ++s) { tk[s][0] = bload16<SC1>(rs, lo16, WS_UF + (T0 * 20 + s) * 1024); tk[s][1] = bload16<SC1>(rs, lo16, WS_UF + ((T0 + 1) * 20 + s) * 1024); }
        SB();
        f32x4 acc[2][7];
#pragma unroll
        for (int n = 0; n < 7; ++n) { acc[0][n] = (f32x4){0, 0, 0, 0}; acc[1][n] = (f32x4){0, 0, 0, 0}; }
        u32x4 wf[2][7];
#pragma unroll
        for (int n = 0; n < 7; ++n) wf[0][n] = LD16(n * 1024 + lo16);
        SB();
#pragma unroll
        for (int s = 0; s < 20; ++s) {
            if (s == 10) {
#pragma unroll
                for (int n = 0; n < 7; ++n) b1v[n] = *(const GAS u32x2*)(p.b1 + 16 * (t3 + (n < cnt3 ? n : 0)) + 4 * qd);
            }
            if (s == 11) {
#pragma unroll
                for (int tt = 0; tt < 2; ++tt)
#pragma unroll
                    for (int e = 0; e < 4; ++e) sp[tt][e] = bload16<SC1>(rs, (2 * qd + (e >> 1)) * 512 + (e & 1) * 256 + t * 16, WS_ST + (T0 + tt) * 4096);
            }
            if (s >= 12 && s < 18) {
#pragma unroll
                for (int n = 0; n < 5; ++n) { const int i = s - 7; w2f[i][n] = bload16<SC1>(rs, lo16, w2b + (i * 5 + n) * 1024); }
            }
            if (s + 1 < 20) {
#pragma unroll
                for (int n = 0; n < 7; ++n) wf[(s + 1) & 1][n] = LD16(((s + 1) * 7 + n) * 1024 + lo16);
            }
#pragma unroll
            for (int n = 0; n < 7; ++n) { acc[0][n] = mfma(wf[s & 1][n], tk[s][0], acc[0][n]); acc[1][n] = mfma(wf[s & 1][n], tk[s][1], acc[1][n]); }
            if (s + 1 < 20) {
#pragma unroll
                for (int n = 0; n < 7; ++n) { SGB(0x100, 1); SGB(0x8, 2); }
            }
            SB();
            if (s == 0) STAMP(21);
            if (s == 4) STAMP(22);
            if (s == 9) STAMP(23);
            if (s == 14) STAMP(24);
        }
        STAMP(12);
        float al[2], be[2], gm[2], de[2];
        {
            const f32x4 r0 = LDF4(L3_GSC + 6144 + 84 * 16), r1 = LDF4(L3_GSC + 6144 + 88 * 16), r2 = LDF4(L3_GSC + 6144 + 92 * 16);
            const float Gg = r0[0], Ggs = r0[1], Gs = r0[2], Sss = r1[1], Ss = r1[2];
            (void)r2;
#pragma unroll
            for (int tt = 0; tt < 2; ++tt) {
                const f32x4 a0 = asf4(sp[tt][0]) + asf4(sp[tt][2]), b0 = asf4(sp[tt][1]) + asf4(sp[tt][3]);
                const float s0 = red_add_q(a0[0]), s1 = red_add_q(a0[1]), s2 = red_add_q(a0[2]), s3 = red_add_q(a0[3]), s4v = red_add_q(b0[0]), s5 = red_add_q(b0[1]);
                const float iD = 1.f / 640.f;
                const float m1 = s0 * iD, v1 = fmaxf(s1 * iD - m1 * m1, 0.f), r1v = rsqrtf(v1 + 1e-5f);
                const float m2 = (r1v * (s2 - m1 * Gs) + Ss) * iD;
                const float e2 = (r1v * r1v * (s4v - 2.f * m1 * s3 + m1 * m1 * Gg) + 2.f * r1v * (s5 - m1 * Ggs) + Sss) * iD;
                const float r2v = rsqrtf(fmaxf(e2 - m2 * m2, 0.f) + 1e-5f);
                al[tt] = r1v * r2v; be[tt] = -r1v * r2v * m1; gm[tt] = r2v; de[tt] = -r2v * m2;
            }
        }
        u32x2 hb[2][7];
#pragma unroll
        for (int n = 0; n < 7; ++n) {
            const f32x4 G = LDF4(L3_GSC + 6144 + (n * 12 + qd) * 16), Sv = LDF4(L3_GSC + 6144 + (n * 12 + 4 + qd) * 16), C = LDF4(L3_GSC + 6144 + (n * 12 + 8 + qd) * 16);
            const float bb[4] = {lo_f(b1v[n][0]), hi_f(b1v[n][0]), lo_f(b1v[n][1]), hi_f(b1v[n][1])};
#pragma unroll
            for (int tt = 0; tt < 2; ++tt) {
                float hv[4];
#pragma unroll
                for (int r = 0; r < 4; ++r) {
                    float z = fmaf(gm[tt], Sv[r], bb[r]); z = fmaf(be[tt], G[r], z); z = fmaf(de[tt], C[r], z); z = fmaf(al[tt], acc[tt][n][r], z);
                    const float u = z * fmaf(z * z, -0.044715f * 1.5957691216057308f * 1.4426950408889634f, -1.5957691216057308f * 1.4426950408889634f);
                    hv[r] = z * __builtin_amdgcn_rcpf(1.f + __builtin_amdgcn_exp2f(u));
                }
                hb[tt][n] = (u32x2){pack2(hv[0], hv[1]), pack2(hv[2], hv[3])};
            }
        }
#pragma unroll
        for (int tt = 0; tt < 2; ++tt) {
            const unsigned hbase = WS_HF + (T0 + tt) * 54 * 1024;
#pragma unroll
            for (int pr = 0; pr < 3; ++pr)
                bstore16<SC1>((u32x4){hb[tt][2 * pr][0], hb[tt][2 * pr][1], hb[tt][2 * pr + 1][0], hb[tt][2 * pr + 1][1]}, rs, lo16, hbase + (3 * j3 + pr) * 1024);
            if (j3 < 12) bstore8<SC1>(hb[tt][6], rs, lo16 + 8 * (j3 & 1), hbase + (48 + (j3 >> 1)) * 1024);
        }
        STAMP(13);
        publish(ws, WS_F3 + (g3 * 16 + j3) * FS, tag);
    }
    STAMP(14);

    {
        LANE_IDS();
        f32x4 gb[5]; u32x2 b2v[5];
#pragma unroll
        for (int i = 11; i < 14; ++i)
#pragma unroll
            for (int n = 0; n < 5; ++n) w2f[i][n] = bload16<SC1>(rs, lo16, w2b + ((i < 13 ? i : cnt4 - 1) * 5 + n) * 1024);
        SB();
        if (w == 0) {
            unsigned off[1] = {WS_F3 + ((P2 >> 1) * 16 + (l & 15)) * FS};
            poll_words<1>(ws, off, l < 16, tag);
        }
        BAR();
        STAMP(15);
        constexpr int PD = 6;
        u32x4 tk[14][4];
        const unsigned tokb = __builtin_amdgcn_readfirstlane(WS_HF + (4 * P2 * 54 + s4) * 1024);
        auto issue = [&](int i) {
            const int s = i < 13 ? i : cnt4 - 1;
#pragma unroll
            for (int a = 0; a < 4; ++a) tk[i][a] = bload16<SC1>(rs, lo16, tokb + (a * 54 + s) * 1024);
        };
#pragma unroll
        for (int i = 0; i < PD; ++i) issue(i);
        f32x4 acc[4][5];
#pragma unroll
        for (int a = 0; a < 4; ++a)
#pragma unroll
            for (int n = 0; n < 5; ++n) acc[a][n] = (f32x4){0, 0, 0, 0};
        SB();
#pragma unroll
        for (int i = 0; i < 14; ++i) {
            if (i + PD < 14) issue(i + PD);
            if (i == 9) {
#pragma unroll
                for (int n = 0; n < 5; ++n) {
                    const int f = 80 * c2 + 16 * n;
                    gb[n] = asf4(bload16<SC1>(rs, qd * 16, WS_MODV + (3200 + f) * 4));
                    b2v[n] = *(const GAS u32x2*)(p.b2 + f + 4 * qd);
                }
            }
            SB();
            if (i < 13 || w < 2) {
#pragma unroll
                for (int a = 0; a < 4; ++a)
#pragma unroll
                    for (int n = 0; n < 5; ++n) acc[a][n] = mfma(w2f[i][n], tk[i][a], acc[a][n]);
            }
            SB();
            if (i == 0) STAMP(25);
            if (i == 4) STAMP(26);
            if (i == 9) STAMP(27);
        }
        STAMP(16);
#pragma unroll
        for (int a = 0; a < 4; ++a) {
            if (a != w) {
                const int src = w < a ? w : w - 1;
#pragma unroll
                for (int n = 0; n < 5; ++n) STF4(((a * 3 + src) * 5 + n) * 1024 + lo16, acc[a][n]);
            }
        }
        BAR();
        constexpr int L4_OUT = 61440;
#pragma unroll
        for (int n = 0; n < 5; ++n) {
            f32x4 v = w == 0 ? acc[0][n] : w == 1 ? acc[1][n] : w == 2 ? acc[2][n] : acc[3][n];
#pragma unroll
            for (int u = 0; u < 3; ++u) v += LDF4(((w * 3 + u) * 5 + n) * 1024 + lo16);
            const float x1[4] = {lo_f(x1b[n][0]), hi_f(x1b[n][0]), lo_f(x1b[n][1]), hi_f(x1b[n][1])};
            const float bv[4] = {lo_f(b2v[n][0]), hi_f(b2v[n][0]), lo_f(b2v[n][1]), hi_f(b2v[n][1])};
            float o[4];
#pragma unroll
            for (int r = 0; r < 4; ++r) o[r] = fmaf(gb[n][r], v[r] + bv[r], x1[r]);
            ST8(L4_OUT + (16 * w + t) * 176 + n * 32 + qd * 8, ((u32x2){pack2(o[0], o[1]), pack2(o[2], o[3])}));
        }
        asm volatile("s_waitcnt lgkmcnt(0)" ::: "memory");
#pragma unroll
        for (int k = 0; k < 3; ++k) {
            const int ch = l + 64 * k;
            if (ch < 160) {
                const int row = ch / 10, cc = ch % 10;
                const u32x4 v = LD16(L4_OUT + (16 * w + row) * 176 + cc * 16);
                *(GAS u32x4*)(p.out + (size_t)(16 * (4 * P2 + w) + row) * 640 + 80 * c2 + cc * 8) = v;
            }
        }
    }
    STAMP(17);
}

extern "C" void kernel_launch(void* const* d_in, const int* in_sizes, int n_in, void* d_out, int out_size, void* d_ws, size_t ws_size, hipStream_t stream) {
    Params p;
    p.x = (const u16*)d_in[0]; p.wqkv = (const u16*)d_in[1]; p.wo = (const u16*)d_in[2]; p.w1 = (const u16*)d_in[4]; p.b1 = (const u16*)d_in[5];
    p.w2 = (const u16*)d_in[6]; p.b2 = (const u16*)d_in[7]; p.c = (const u16*)d_in[8]; p.wm = (const u16*)d_in[9];
    p.out = (u16*)d_out; p.ws = (char*)d_ws;
    (void)hipFuncSetAttribute((const void*)adaLN_block_kernel, hipFuncAttributeMaxDynamicSharedMemorySize, LDS_BYTES);
    adaLN_block_kernel<<<256, 256, LDS_BYTES, stream>>>(p);
}
```

```cpp
#include <hip/hip_runtime.h>
#include <stdint.h>

typedef unsigned short u16;
typedef float f32x4 __attribute__((ext_vector_type(4)));
typedef float f32x2 __attribute__((ext_vector_type(2)));
typedef __bf16 bf16x8 __attribute__((ext_vector_type(8)));
typedef __bf16 bf16x2 __attribute__((ext_vector_type(2)));
typedef unsigned u32x4 __attribute__((ext_vector_type(4)));
typedef unsigned u32x2 __attribute__((ext_vector_type(2)));
typedef short s16x4 __attribute__((ext_vector_type(4)));
typedef __amdgpu_buffer_rsrc_t rsrc_t;
#define LAS __attribute__((address_space(3)))
#define GAS __attribute__((address_space(1)))
#define DI __device__ __forceinline__

static constexpr unsigned FS = 64;
static constexpr unsigned WS_EPOCH = 0;
static constexpr unsigned WS_TMO   = 256;
static constexpr unsigned WS_FA    = 1024;
static constexpr unsigned WS_FC    = WS_FA + 160 * FS;
static constexpr unsigned WS_FD    = WS_FC + 128 * FS;
static constexpr unsigned WS_F1    = WS_FD + 256 * FS;
static constexpr unsigned WS_F2    = WS_F1 + 256 * FS;
static constexpr unsigned WS_F3    = WS_F2 + 256 * FS;
static constexpr unsigned WS_FEND  = WS_F3 + 256 * FS;
static constexpr unsigned WS_MODV = 131072;
static constexpr unsigned WS_BP   = WS_MODV + 16384;
static constexpr unsigned WS_XNF  = WS_BP + 20 * 16 * 128 * 4;
static constexpr unsigned WS_X0F  = WS_XNF + 128 * 20 * 1024;
static constexpr unsigned WS_WQF  = WS_X0F + 128 * 40 * 512;
static constexpr unsigned WS_WOF  = WS_WQF + 16 * 20 * 8 * 1024;
static constexpr unsigned WS_W1F  = WS_WOF + 8 * 20 * 5 * 1024;
static constexpr unsigned WS_W2F  = WS_W1F + 108 * 20 * 1024;
static constexpr unsigned WS_OF   = WS_W2F + 8 * 54 * 5 * 1024;
static constexpr unsigned WS_UF   = WS_OF + 128 * 20 * 1024;
static constexpr unsigned WS_ST   = WS_UF + 128 * 20 * 1024;
static constexpr unsigned WS_HF   = WS_ST + 128 * 8 * 512;
static constexpr unsigned WS_END  = WS_HF + 128 * 54 * 1024;
static_assert(WS_FEND <= WS_MODV, "flags overflow");

static constexpr int LDS_BYTES = 163840;
static constexpr int SC1 = 16;
static constexpr unsigned SPIN_CAP = 40000;

#define STAMP(i)

struct Params {
    const u16* x; const u16* wqkv; const u16* wo; const u16* w1; const u16* b1; const u16* w2; const u16* b2; const u16* c; const u16* wm;
    u16* out; char* ws;
};

DI float lo_f(unsigned d) { return __uint_as_float(d << 16); }
DI float hi_f(unsigned d) { return __uint_as_float(d & 0xffff0000u); }
DI unsigned pack2(float a, float b) { f32x2 v = {a, b}; return __builtin_bit_cast(unsigned, __builtin_convertvector(v, bf16x2)); }
DI float rbf(float a) { return lo_f(pack2(a, 0.f)); }
DI u32x2 pack4(const f32x4& v) { return (u32x2){pack2(v[0], v[1]), pack2(v[2], v[3])}; }
DI f32x4 mfma(u32x4 a, u32x4 b, f32x4 c) { return __builtin_amdgcn_mfma_f32_16x16x32_bf16(__builtin_bit_cast(bf16x8, a), __builtin_bit_cast(bf16x8, b), c, 0, 0, 0); }
DI float red_add_q(float v) {
    auto a = __builtin_amdgcn_permlane16_swap(__float_as_uint(v), __float_as_uint(v), false, false);
    v = __uint_as_float(a[0]) + __uint_as_float(a[1]);
    auto b = __builtin_amdgcn_permlane32_swap(__float_as_uint(v), __float_as_uint(v), false, false);
    return __uint_as_float(b[0]) + __uint_as_float(b[1]);
}
DI float red_max_q(float v) {
    auto a = __builtin_amdgcn_permlane16_swap(__float_as_uint(v), __float_as_uint(v), false, false);
    v = fmaxf(__uint_as_float(a[0]), __uint_as_float(a[1]));
    auto b = __builtin_amdgcn_permlane32_swap(__float_as_uint(v), __float_as_uint(v), false, false);
    return fmaxf(__uint_as_float(b[0]), __uint_as_float(b[1]));
}
template <int C> DI float dpp_add(float v) { return v + __builtin_bit_cast(float, __builtin_amdgcn_update_dpp(0, __builtin_bit_cast(int, v), C, 0xf, 0xf, true)); }
DI float red16(float v) { v = dpp_add<0xB1>(v); v = dpp_add<0x4E>(v); v = dpp_add<0x141>(v); return dpp_add<0x140>(v); }
DI unsigned swap32(unsigned v) { auto a = __builtin_amdgcn_permlane32_swap(v, v, false, false); return (threadIdx.x & 32) ? a[0] : a[1]; }
DI rsrc_t mk_rsrc(const void* p, unsigned bytes) { return __builtin_amdgcn_make_buffer_rsrc((void*)p, 0, bytes, 0x00020000); }
template <int AUX> DI u32x4 bload16(rsrc_t r, unsigned voff, unsigned soff) { return __builtin_bit_cast(u32x4, __builtin_amdgcn_raw_buffer_load_b128(r, voff, soff, AUX)); }
template <int AUX> DI u32x2 bload8(rsrc_t r, unsigned voff, unsigned soff) { return __builtin_bit_cast(u32x2, __builtin_amdgcn_raw_buffer_load_b64(r, voff, soff, AUX)); }
template <int AUX> DI unsigned bload4(rsrc_t r, unsigned voff, unsigned soff) { return __builtin_amdgcn_raw_buffer_load_b32(r, voff, soff, AUX); }
template <int AUX> DI void bstore16(u32x4 v, rsrc_t r, unsigned voff, unsigned soff) { __builtin_amdgcn_raw_buffer_store_b128(v, r, voff, soff, AUX); }
template <int AUX> DI void bstore8(u32x2 v, rsrc_t r, unsigned voff, unsigned soff) { __builtin_amdgcn_raw_buffer_store_b64(v, r, voff, soff, AUX); }
template <int AUX> DI void bstore4(unsigned v, rsrc_t r, unsigned voff, unsigned soff) { __builtin_amdgcn_raw_buffer_store_b32(v, r, voff, soff, AUX); }
DI f32x4 asf4(u32x4 v) { return __builtin_bit_cast(f32x4, v); }
DI u32x4 asu4(f32x4 v) { return __builtin_bit_cast(u32x4, v); }
#define LANE_IDS() int tz_ = threadIdx.x; asm volatile("" : "+v"(tz_)); const int tid = tz_, l = tid & 63, t = l & 15, qd = l >> 4; const unsigned lo16 = l * 16; (void)t; (void)qd; (void)lo16
#define SB() __builtin_amdgcn_sched_barrier(0)
#define BAR() asm volatile("s_waitcnt lgkmcnt(0)\n\ts_barrier" ::: "memory")
#define DRAIN() asm volatile("s_waitcnt vmcnt(0)" ::: "memory")
#define SGB(mask, n) __builtin_amdgcn_sched_group_barrier(mask, n, 0)
#define LD16(off) (*(const LAS u32x4*)((const LAS char*)lds + (off)))
#define LD8(off) (*(const LAS u32x2*)((const LAS char*)lds + (off)))
#define LDF4(off) (*(const LAS f32x4*)((const LAS char*)lds + (off)))
#define LDF(off) (*(const LAS float*)((const LAS char*)lds + (off)))
#define ST16(off, v) (*(LAS u32x4*)((LAS char*)lds + (off)) = (v))
#define ST8(off, v) (*(LAS u32x2*)((LAS char*)lds + (off)) = (v))
#define STF4(off, v) (*(LAS f32x4*)((LAS char*)lds + (off)) = (v))
#define STF(off, v) (*(LAS float*)((LAS char*)lds + (off)) = (v))
DI u32x2 tr_read(const char* lds, unsigned off) { return __builtin_bit_cast(u32x2, __builtin_amdgcn_ds_read_tr16_b64_v4i16((LAS s16x4*)((LAS char*)lds + off))); }

DI void flag_store(char* ws, unsigned off, unsigned tag) { __hip_atomic_store((GAS unsigned*)(ws + off), tag, __ATOMIC_RELAXED, __HIP_MEMORY_SCOPE_AGENT); }
DI unsigned flag_load(const char* ws, unsigned off) { return __hip_atomic_load((const GAS unsigned*)(ws + off), __ATOMIC_RELAXED, __HIP_MEMORY_SCOPE_AGENT); }
DI void publish(char* ws, unsigned off, unsigned tag) { DRAIN(); BAR(); if (threadIdx.x == 0) flag_store(ws, off, tag); }
template <int N> DI void poll_words(char* ws, const unsigned (&off)[N], bool active, unsigned tag) {
    for (unsigned spins = 0;; ++spins) {
        bool ok = true;
#pragma unroll
        for (int k = 0; k < N; ++k) ok &= flag_load(ws, off[k]) == tag;
        if (__all(ok || !active)) break;
        if (spins > SPIN_CAP) { if ((threadIdx.x & 63) == 0) flag_store(ws, WS_TMO, tag); break; }
        __builtin_amdgcn_s_sleep(1);
    }
}

DI void rows_wo(int s, int qd, int& ra, int& rb) { if (s < 16) { ra = 40 * s + 4 * qd; rb = ra + 20; } else { ra = 40 * (4 * (s - 16) + qd) + 16; rb = ra + 20; } }
DI void tiles_p3(int s, int& ta, int& tb) { if (s < 16) { ta = 5 * (s >> 1) + 2 * (s & 1); tb = ta + 1; } else { ta = 10 * (s - 16) + 4; tb = ta + 5; } }
DI void tiles_p4(int s, int& ta, int& tb) { if (s < 48) { int j = s / 3, pr = s % 3; int st = j < 12 ? 7 * j : 84 + 6 * (j - 12); ta = st + 2 * pr; tb = ta + 1; } else { int m = s - 48; ta = 14 * m + 6; tb = ta + 7; } }

static constexpr int L0_SC = 81920, L0_GS = 83200, L0_CRAW = 83968, L0_RW = 86016, L0_D = 101376, L0_XI = 134144, L0_XS = 156160;

DI void dma16(char* lds, unsigned lds_off, rsrc_t r, unsigned voff, unsigned soff) {
    const unsigned dst = (unsigned)(size_t)(LAS char*)lds + lds_off;
    asm volatile("s_mov_b32 m0, %0\n\ts_nop 0\n\tbuffer_load_dwordx4 %1, %2, %3 offen lds" :: "s"(dst), "v"(voff), "s"(r), "s"(soff) : "memory");
}
#define WAITV(n) asm volatile("s_waitcnt vmcnt(%0)" :: "n"(n) : "memory")

struct DTask { unsigned dst; int kind, s, cg; };
DI DTask dtask_issue(const Params& p, char* lds, unsigned scr, int id, int w, int l) {
    DTask d; int ra[4], rb[4]; unsigned pitch; rsrc_t r;
    if (id < 100) {
        d.kind = 0; d.s = id / 5; d.cg = id % 5; pitch = 1280; r = mk_rsrc(p.wo, 640 * 1280);
#pragma unroll
        for (int k = 0; k < 4; ++k) rows_wo(d.s, k, ra[k], rb[k]);
    } else if (id < 370) {
        d.kind = 1; d.s = (id - 100) / 5; d.cg = (id - 100) % 5; pitch = 1280; r = mk_rsrc(p.w2, 1728 * 1280);
        int ta, tb; tiles_p4(d.s, ta, tb);
#pragma unroll
        for (int k = 0; k < 4; ++k) { ra[k] = 16 * ta + 4 * k; rb[k] = 16 * tb + 4 * k; }
    } else {
        const int i2 = id < 650 ? id - 370 : 0;
        d.kind = id < 650 ? 2 : 3; d.s = i2 / 14; d.cg = i2 % 14; pitch = 3456; r = mk_rsrc(p.w1, 640 * 3456);
        int ta, tb; tiles_p3(d.s, ta, tb);
#pragma unroll
        for (int k = 0; k < 4; ++k) { ra[k] = 16 * ta + 4 * k; rb[k] = 16 * tb + 4 * k; }
    }
    const int q = l >> 4, cp = l & 15;
#pragma unroll
    for (int i = 0; i < 8; ++i) {
        const int base = i < 4 ? ra[i] : rb[i - 4];
        const unsigned c = cp ^ ((4 * (i & 1) + q) << 1);
        dma16(lds, scr + w * 8192 + i * 1024, r, q * pitch + c * 16, base * pitch + d.cg * 256);
    }
    return d;
}
DI void dtask_finish(const DTask& d, char* lds, unsigned scr, rsrc_t rs, int w, int l) {
    if (d.kind == 3) return;
    const int qd = l >> 4, q = (l >> 2) & 3, pp = l & 3, r7 = 4 * (qd & 1) + q;
    const unsigned a0 = scr + w * 8192 + (4 * qd + q) * 256 + pp * 8;
#pragma unroll
    for (int nt = 0; nt < 8; ++nt) {
        const u32x2 ua = tr_read(lds, a0 + ((nt ^ r7) * 32)), ub = tr_read(lds, a0 + 4096 + ((nt ^ r7) * 32));
        const int ng = 8 * d.cg + nt;
        unsigned dst;
        if (d.kind == 2) dst = WS_W1F + (ng * 20 + d.s) * 1024;
        else dst = (d.kind == 0 ? WS_WOF + ((ng / 5) * 20 + d.s) * 5120 : WS_W2F + ((ng / 5) * 54 + d.s) * 5120) + (ng % 5) * 1024;
        if (d.kind != 2 || ng < 108) bstore16<SC1>((u32x4){ua[0], ua[1], ub[0], ub[1]}, rs, l * 16, dst);
    }
}

DI void wm_dma(const Params& p, char* lds, int w, int l, unsigned colA, unsigned colB, int rot) {
    const rsrc_t rw = mk_rsrc(p.wm, 640 * 3840 * 2);
    const int rl = l >> 3, cp = l & 7;
    const unsigned sg = ((rl >> 1) & 1);
    const unsigned c0 = cp ^ (sg << 1), c1 = cp ^ ((sg | 2) << 1);
    const unsigned v0 = rl * 7680 + (c0 < 4 ? colA + 8 * c0 : colB + 8 * (c0 - 4)) * 2, v1 = rl * 7680 + (c1 < 4 ? colA + 8 * c1 : colB + 8 * (c1 - 4)) * 2;
#pragma unroll
    for (int i = 0; i < 20; ++i) {
        int piece = 4 * i + w + rot; piece = piece >= 80 ? piece - 80 : piece;
        dma16(lds, piece * 1024, rw, (piece & 1) ? v1 : v0, piece * 8 * 7680);
    }
}
DI void c_dma(const Params& p, char* lds, int w, int l) {
    dma16(lds, L0_CRAW + (w & 1) * 1024, mk_rsrc(p.c, 1280), ((w & 1) * 64 + l) * 16, 0);
}
template <int NC, int NW> DI float mod_gemv(char* lds, int w, int l, int tid) {
    WAITV(NC);
    if (tid < 80) {
        const u32x4 creg = LD16(L0_CRAW + tid * 16);
        u32x4 o;
#pragma unroll
        for (int d = 0; d < 4; ++d) { const float a = lo_f(creg[d]), b = hi_f(creg[d]); o[d] = pack2(a * __builtin_amdgcn_rcpf(1.f + __expf(-a)), b * __builtin_amdgcn_rcpf(1.f + __expf(-b))); }
        ST16(L0_SC + tid * 16, o);
    }
    WAITV(NW);
    BAR();
    const int qd = l >> 4, q = (l >> 2) & 3, pp = l & 3;
    f32x4 acc = {0, 0, 0, 0}, acc2 = {0, 0, 0, 0};
    const int r0 = 8 * qd + q;
    const unsigned sg = ((r0 >> 1) & 1) | (((r0 >> 3) & 1) << 1);
    const unsigned a0 = r0 * 128 + ((w ^ sg) * 32) + pp * 8;
#pragma unroll
    for (int s = 0; s < 20; s += 2) {
        const u32x2 ba = tr_read(lds, a0 + s * 4096), bb = tr_read(lds, a0 + s * 4096 + 512);
        const u32x2 bc = tr_read(lds, a0 + s * 4096 + 4096), bd = tr_read(lds, a0 + s * 4096 + 4608);
        acc = mfma(LD16(L0_SC + s * 64 + qd * 16), (u32x4){ba[0], ba[1], bb[0], bb[1]}, acc);
        acc2 = mfma(LD16(L0_SC + s * 64 + 64 + qd * 16), (u32x4){bc[0], bc[1], bd[0], bd[1]}, acc2);
    }
    return rbf(acc[0] + acc2[0]);
}
DI void x_dma(const Params& p, char* lds, unsigned img, int T, int w, int l) {
    const rsrc_t rx = mk_rsrc(p.x, 2048 * 1280);
    const int r4 = l >> 4, row = 4 * w + r4;
    const unsigned voff = row * 1280 + (((l & 15) ^ row) * 16);
#pragma unroll
    for (int cb = 0; cb < 5; ++cb) dma16(lds, img + (w * 5 + cb) * 1024, rx, voff, T * 16 * 1280 + cb * 256);
}
DI void x_stats(char* lds, unsigned img, unsigned stat, int w, int l) {
    u32x4 v[5];
#pragma unroll
    for (int cb = 0; cb < 5; ++cb) v[cb] = LD16(img + (w * 5 + cb) * 1024 + l * 16);
    float sum = 0.f;
#pragma unroll
    for (int i = 0; i < 5; ++i)
#pragma unroll
        for (int d = 0; d < 4; ++d) sum += lo_f(v[i][d]) + hi_f(v[i][d]);
    sum = red16(sum);
    const float mean = sum * (1.f / 640.f);
    float vs = 0.f;
#pragma unroll
    for (int i = 0; i < 5; ++i)
#pragma unroll
        for (int d = 0; d < 4; ++d) { const float a = lo_f(v[i][d]) - mean, c2 = hi_f(v[i][d]) - mean; vs += a * a + c2 * c2; }
    vs = red16(vs);
    const float rstd = rsqrtf(vs * (1.f / 640.f) + 1e-5f);
    if ((l & 15) == 0) { STF(stat + (4 * w + (l >> 4)) * 8, mean); STF(stat + (4 * w + (l >> 4)) * 8 + 4, rstd); }
}
DI unsigned ximg_off(int t, int byte_in_row) { return ((t >> 2) * 5 + (byte_in_row >> 8)) * 1024 + (t & 3) * 256 + ((((byte_in_row >> 4) & 15) ^ t) * 16) + (byte_in_row & 15); }
DI void x_frags(char* lds, unsigned img, unsigned stat, rsrc_t rs, int T, int w, int l) {
    const int t = l & 15, qd = l >> 4;
    const float mean = LDF(stat + t * 8), rstd = LDF(stat + t * 8 + 4);
#pragma unroll
    for (int i = 0; i < 5; ++i) {
        const int s = w + 4 * i;
        const u32x4 v = LD16(img + ximg_off(t, s * 64 + qd * 16));
        u32x4 o;
#pragma unroll
        for (int d = 0; d < 4; ++d) o[d] = pack2((lo_f(v[d]) - mean) * rstd, (hi_f(v[d]) - mean) * rstd);
        bstore16<SC1>(o, rs, l * 16, WS_XNF + (T * 20 + s) * 1024);
    }
}
DI void x_raw_tiles(char* lds, unsigned img, rsrc_t rs, int T, int w, int l) {
    const int t = l & 15, qd = l >> 4;
#pragma unroll
    for (int i = 0; i < 10; ++i) {
        const int ng = w * 10 + i;
        bstore8<SC1>(LD8(img + ximg_off(t, ng * 32 + qd * 8)), rs, l * 8, WS_X0F + (T * 40 + ng) * 512);
    }
}

struct TileKV { u32x2 ka0, ka1, k2, va0, va1, z; };
DI void attn_unit(const u32x2 (&qa)[2], const u32x2& xq, const TileKV& cur, const TileKV& prev, bool prev_ok, int t, int qd, u32x4& omain, u32x2& oleft) {
    const float cs = 0.22360679774997896f * 1.4426950408889634f;
    f32x4 oa[2], oz[2];
#pragma unroll
    for (int h = 0; h < 2; ++h) {
        const u32x4 qf = {qa[h][0], qa[h][1], qd == h ? xq[0] : 0u, qd == h ? xq[1] : 0u};
        const u32x2 kcp = h == 0 ? cur.ka0 : cur.ka1, kpp = h == 0 ? prev.ka0 : prev.ka1;
        const f32x4 z4 = {0.f, 0.f, 0.f, 0.f};
        f32x4 sp = mfma((u32x4){kpp[0], kpp[1], prev.k2[0], prev.k2[1]}, qf, z4);
        f32x4 sc = mfma((u32x4){kcp[0], kcp[1], cur.k2[0], cur.k2[1]}, qf, z4);
        float e[8]; float mx = -3.0e38f;
#pragma unroll
        for (int r = 0; r < 4; ++r) {
            const int dp = t + 16 - 4 * qd - r, dc = t - 4 * qd - r;
            const bool vp = prev_ok && dp <= 14 && !(dp & 1), vc = dc >= 0 && !(dc & 1);
            e[r] = vp ? sp[r] * cs : -3.0e38f; e[4 + r] = vc ? sc[r] * cs : -3.0e38f;
            mx = fmaxf(mx, fmaxf(e[r], e[4 + r]));
        }
        mx = red_max_q(mx);
        float sum = 0.f;
#pragma unroll
        for (int r = 0; r < 8; ++r) { e[r] = __builtin_amdgcn_exp2f(e[r] - mx); sum += e[r]; }
        sum = red_add_q(sum);
        const float inv = __builtin_amdgcn_rcpf(sum);
        const u32x4 pf = {pack2(e[0] * inv, e[1] * inv), pack2(e[2] * inv, e[3] * inv), pack2(e[4] * inv, e[5] * inv), pack2(e[6] * inv, e[7] * inv)};
        const u32x2 vc2 = h == 0 ? cur.va0 : cur.va1, vp2 = h == 0 ? prev.va0 : prev.va1;
        oa[h] = mfma((u32x4){vp2[0], vp2[1], vc2[0], vc2[1]}, pf, z4);
        oz[h] = mfma((u32x4){prev.z[0], prev.z[1], cur.z[0], cur.z[1]}, pf, z4);
    }
    const u32x2 a0 = pack4(oa[0]), a1 = pack4(oa[1]);
    omain = (u32x4){a0[0], a0[1], a1[0], a1[1]};
    const u32x2 l0 = pack4(oz[0]), l1 = pack4(oz[1]);
    oleft = qd == 0 ? l0 : l1;
}

__global__ __launch_bounds__(256, 1) void adaLN_block_kernel(Params p) {
    extern __shared__ __attribute__((aligned(16))) char lds[];
    const int b = blockIdx.x, w = __builtin_amdgcn_readfirstlane(threadIdx.x >> 6);
    const int xcd = b & 7, bi = b >> 3;
    char* ws = p.ws;
    const rsrc_t rs = mk_rsrc(ws, WS_END);
    STAMP(0);
    {
        unsigned long long pc; unsigned len;
        asm volatile("s_getpc_b64 %0\n.Ladaln_pc:\n\ts_mov_b32 %1, .Ladaln_end-.Ladaln_pc" : "=s"(pc), "=s"(len));
        unsigned off = (bi * 256 + threadIdx.x) * 64u;
        off = off < len ? off : 0u;
        const unsigned long long a = pc + off;
        const unsigned dst = (unsigned)(size_t)(LAS char*)lds + LDS_BYTES - 1024 + w * 256;
        asm volatile("s_mov_b32 m0, %0\n\ts_nop 0\n\tglobal_load_lds_dword %1, off" :: "s"(dst), "v"(a) : "memory");
    }
    const unsigned epoch = *(const __attribute__((address_space(4))) unsigned*)(size_t)(ws + WS_EPOCH);
    unsigned tag;
#define MAKE_TAG() do { unsigned e_ = epoch; asm volatile("" : "+s"(e_)); tag = e_ + 0x9E3779B9u; } while (0)

    {
        LANE_IDS();
        const int nA = xcd < 4 ? 24 : 16;
        if (bi < nA) {
            const int s = xcd + 8 * (bi >> 3), jg = bi & 7;
            c_dma(p, lds, w, l);
            {
                const rsrc_t rq = mk_rsrc(p.wqkv, 640 * 3840);
#pragma unroll
                for (int m = 0; m < 4; ++m) {
                    const int n = 64 * (4 * m + w) + l, rho = n / 30, c = n % 30;
                    const int k = (rho & 19) | ((rho & 4) << 1) | ((rho & 8) >> 1);
                    if (4 * m + w < 15) dma16(lds, L0_RW + (4 * m + w) * 1024, rq, k * 3840 + (c / 10) * 1280 + jg * 160 + (c % 10) * 16, s * 32 * 3840);
                }
            }
            wm_dma(p, lds, w, l, 640 + 32 * s, 32 * s, 10 * jg);
            MAKE_TAG();
            STAMP(18);
            const float v = mod_gemv<20, 0>(lds, w, l, tid);
            STAMP(19);
            const float gv = w < 2 ? rbf(1.f + v) : v;
            if (l < 16) { STF(L0_GS + (w * 16 + l) * 4, gv); *(LAS u16*)((LAS char*)lds + L0_GS + 256 + (w * 16 + l) * 2) = (u16)(__float_as_uint(gv) >> 16); }
            BAR();
            const f32x4 g0 = LDF4(L0_GS + qd * 32), g1 = LDF4(L0_GS + qd * 32 + 16);
            const u32x4 sfr = LD16(L0_GS + 256 + 64 + qd * 16);
            const int q = (l >> 2) & 3, pp = l & 3, jj = w >> 1, j = 2 * jg + jj;
            const unsigned rbase = L0_RW + (16 * (qd >> 1) + 4 * (qd & 1) + q) * 480 + 80 * jj;
#pragma unroll
            for (int i = 0; i < 4; ++i) {
                const int nt = 4 * (w & 1) + i;
                unsigned po;
                if (nt < 4) po = (nt >> 1) * 160 + (nt & 1) * 40 + 8 * pp;
                else if (nt == 4) po = (pp >> 1) * 160 + (pp & 1) * 40 + 32;
                else if (nt < 7) po = 320 + (nt - 5) * 40 + 8 * pp;
                else po = 320 + (pp & 1) * 40 + 32;
                u32x2 ua = tr_read(lds, rbase + po), ub = tr_read(lds, rbase + po + 8 * 480);
                if (nt == 7 && t >= 8) { ua = (u32x2){0u, 0u}; ub = ua; }
                const f32x4 z4 = {0, 0, 0, 0};
                const f32x4 bias = mfma((u32x4){ua[0], ua[1], ub[0], ub[1]}, sfr, z4);
                const u32x4 o = {pack2(lo_f(ua[0]) * g0[0], hi_f(ua[0]) * g0[1]), pack2(lo_f(ua[1]) * g0[2], hi_f(ua[1]) * g0[3]),
                                 pack2(lo_f(ub[0]) * g1[0], hi_f(ub[0]) * g1[1]), pack2(lo_f(ub[1]) * g1[2], hi_f(ub[1]) * g1[3])};
                bstore16<SC1>(o, rs, lo16, WS_WQF + ((j * 20 + s) * 8 + nt) * 1024);
                if (t == 0) bstore16<SC1>(asu4(bias), rs, qd * 16, WS_BP + ((s * 16 + j) * 128 + nt * 16) * 4);
            }
            STAMP(20);
            publish(ws, WS_FA + (jg * 20 + s) * FS, tag);
        } else if (bi < nA + 5) {
            const int idxB = xcd * 5 + (bi - nA);
            c_dma(p, lds, w, l);
            wm_dma(p, lds, w, l, 1280 + 64 * idxB, 1280 + 64 * idxB + 32, 0);
            MAKE_TAG();
            STAMP(18);
            float v = mod_gemv<20, 0>(lds, w, l, tid);
            STAMP(19);
            const int col = 1280 + 64 * idxB + 16 * w + t;
            if (col >= 2560 && col < 3200) v = rbf(1.f + v);
            if (l < 16) bstore4<SC1>(__float_as_uint(v), rs, l * 4, WS_MODV + (1280 + 64 * idxB + 16 * w) * 4);
        } else {
            const int idx = xcd < 4 ? xcd * 3 + (bi - 29) : 12 + (xcd - 4) * 11 + (bi - 21);
            const int Ts = idx < 16 ? 3 * idx : 48 + 2 * (idx - 16), nT = idx < 16 ? 3 : 2;
#pragma unroll
            for (int k = 0; k < 3; ++k) x_dma(p, lds, k * 20480, Ts + (k < nT ? k : 0), w, l);
            MAKE_TAG();
            STAMP(18);
            WAITV(10); x_stats(lds, 0, L0_XS, w, l);
            WAITV(5); x_stats(lds, 20480, L0_XS + 128, w, l);
            WAITV(0); x_stats(lds, 40960, L0_XS + 256, w, l);
            BAR();
            STAMP(19);
#pragma unroll
            for (int k = 0; k < 3; ++k) if (k < nT) x_frags(lds, k * 20480, L0_XS + k * 128, rs, Ts + k, w, l);
            const int did = idx * 4 + w;
            const DTask dt = dtask_issue(p, lds, L0_D, did < 100 ? did : 1000, w, l);
            WAITV(8); BAR();
            if (tid < nT) flag_store(ws, WS_FC + (Ts + tid) * FS, tag);
            STAMP(20);
#pragma unroll
            for (int k = 0; k < 3; ++k) if (k < nT) x_raw_tiles(lds, k * 20480, rs, Ts + k, w, l);
            WAITV(0);
            dtask_finish(dt, lds, L0_D, rs, w, l);
        }
        STAMP(1);
    }

    const int g1 = 4 * (xcd >> 1) + (bi >> 3), j1 = 8 * (xcd & 1) + (bi & 7);
    const int P2 = 4 * xcd + (bi >> 3), c2 = bi & 7;
    const int g3 = g1, j3 = j1;
    const int t3 = j3 < 12 ? 7 * j3 : 84 + 6 * (j3 - 12), cnt3 = j3 < 12 ? 7 : 6;
    u32x4 wo_st[25];
    {
        LANE_IDS();
        const int T0 = 8 * g1 + 2 * w, TH = g1 == 0 ? 0 : 8 * g1 - 1;
        const unsigned xb0 = WS_XNF + T0 * 20480, xbh = WS_XNF + TH * 20480 + (w >> 1) * 1024, wb = WS_WQF + j1 * 163840 + w * 4096;
        u32x4 xr[10][4], wr[10][4], xh[10];
        DRAIN();
        if (w == 0) {
            unsigned off[1] = {l == 0 ? WS_FC + TH * FS : WS_FC + (8 * g1 + ((l - 1) & 7)) * FS};
            poll_words<1>(ws, off, l < 9, tag);
        }
        BAR();
        if (tid == 0) flag_store(ws, WS_FD + b * FS, tag);
#pragma unroll
        for (int G = 0; G < 4; ++G) {
#pragma unroll
            for (int i = 0; i < 4; ++i) xr[G][i] = bload16<SC1>(rs, lo16, xb0 + (i & 1) * 20480 + (2 * G + (i >> 1)) * 1024);
            xh[G] = bload16<SC1>(rs, lo16, xbh + G * 2048);
        }
        SB();
        if (w == 0) {
            unsigned off[1] = {WS_FA + ((j1 >> 1) * 20 + (l < 20 ? l : 0)) * FS};
            poll_words<1>(ws, off, l < 20, tag);
        }
        BAR();
        STAMP(2);
#pragma unroll
        for (int G = 0; G < 3; ++G)
#pragma unroll
            for (int i = 0; i < 4; ++i) wr[G][i] = bload16<SC1>(rs, lo16, wb + G * 16384 + i * 1024);
        SB();
#pragma unroll
        for (int G = 0; G < 2; ++G)
#pragma unroll
            for (int i = 0; i < 4; ++i) ST16(G * 16384 + w * 4096 + i * 1024 + lo16, wr[G][i]);
        BAR();
        STAMP(3);
        f32x4 acc[2][8], hacc[3];
#pragma unroll
        for (int i = 0; i < 8; ++i) { acc[0][i] = (f32x4){0, 0, 0, 0}; acc[1][i] = (f32x4){0, 0, 0, 0}; }
#pragma unroll
        for (int i = 0; i < 3; ++i) hacc[i] = (f32x4){0, 0, 0, 0};
        u32x4 wf[2][8];
#pragma unroll
        for (int n = 0; n < 8; ++n) wf[0][n] = LD16(n * 1024 + lo16);
        const unsigned hoff = (w >> 1) * 8192 + ((w & 1) ? 5 : 2) * 1024 + lo16;
        unsigned bpv[20], fdv[4];
        SB();
#pragma unroll
        for (int G = 0; G < 10; ++G) {
            u32x4 hw[3];
#pragma unroll
            for (int st = 0; st < 2; ++st) {
                const int s = 2 * G + st;
                int nv = 0;
                if (st == 0) {
                    if (G + 3 < 10) {
#pragma unroll
                        for (int i = 0; i < 4; ++i) wr[G + 3][i] = bload16<SC1>(rs, lo16, wb + (G + 3) * 16384 + i * 1024);
                        nv += 4;
                    }
                    if (G + 4 < 10) { xh[G + 4] = bload16<SC1>(rs, lo16, xbh + (G + 4) * 2048); nv += 1; }
                    if (G == 7) {
#pragma unroll
                        for (int i = 0; i < 4; ++i) fdv[i] = flag_load(ws, WS_FD + (l + 64 * i) * FS);
                    }
                } else {
                    if (G + 4 < 10) {
#pragma unroll
                        for (int i = 0; i < 4; ++i) xr[G + 4][i] = bload16<SC1>(rs, lo16, xb0 + (i & 1) * 20480 + (2 * (G + 4) + (i >> 1)) * 1024);
                        nv += 4;
                    }
                }
                if (G == 6 || G == 7) {
#pragma unroll
                    for (int i = 0; i < 5; ++i) { const int k = ((G - 6) * 2 + st) * 5 + i; bpv[k] = bload4<SC1>(rs, (tid & 127) * 4, WS_BP + (k * 16 + j1) * 512); }
                    nv += 5;
                }
                if (st == 0) {
#pragma unroll
                    for (int i = 0; i < 3; ++i) hw[i] = LD16((G & 3) * 16384 + hoff + i * 1024);
                }
                if (s + 1 < 20) {
                    const unsigned nb = (((s + 1) >> 1) & 3) * 16384 + ((s + 1) & 1) * 8192 + lo16;
#pragma unroll
                    for (int n = 0; n < 8; ++n) wf[(s + 1) & 1][n] = LD16(nb + n * 1024);
                }
#pragma unroll
                for (int n = 0; n < 5; ++n) { acc[0][n] = mfma(wf[s & 1][n], xr[G][st * 2], acc[0][n]); acc[1][n] = mfma(wf[s & 1][n], xr[G][st * 2 + 1], acc[1][n]); }
#pragma unroll
                for (int n = 5; n < 8; ++n) { acc[0][n] = mfma(xr[G][st * 2], wf[s & 1][n], acc[0][n]); acc[1][n] = mfma(xr[G][st * 2 + 1], wf[s & 1][n], acc[1][n]); }
                if (st == 1 && G + 2 < 10) {
#pragma unroll
                    for (int i = 0; i < 4; ++i) ST16(((G + 2) & 3) * 16384 + w * 4096 + i * 1024 + lo16, wr[G + 2][i]);
                }
#pragma unroll
                for (int n = 0; n < 8; ++n) {
                    if (s + 1 < 20) SGB(0x100, 1);
                    if (st == 0 && n < 3) SGB(0x100, 1);
                    SGB(0x8, 2);
                    if (n < nv) SGB(0x20, 1);
                    if (st == 1 && G + 2 < 10 && n >= 4) SGB(0x200, 1);
                }
                SB();
            }
            if (w & 1) {
#pragma unroll
                for (int i = 0; i < 3; ++i) hacc[i] = mfma(xh[G], hw[i], hacc[i]);
            } else {
#pragma unroll
                for (int i = 0; i < 3; ++i) hacc[i] = mfma(hw[i], xh[G], hacc[i]);
            }
            BAR();
        }
        STAMP(4);
        {
            const bool ok = fdv[0] == tag && fdv[1] == tag && fdv[2] == tag && fdv[3] == tag;
            if (!__all(ok)) {
                unsigned off[4];
#pragma unroll
                for (int k = 0; k < 4; ++k) off[k] = WS_FD + (l + 64 * k) * FS;
                poll_words<4>(ws, off, true, tag);
            }
            if (b == 0 && tid == 0) flag_store(ws, WS_EPOCH, tag);
        }
        constexpr int L1_BIAS = 49152, L1_HP = 0, L1_EX = 8192;
        {
            float bs = 0.f;
#pragma unroll
            for (int i = 0; i < 20; ++i) bs += __uint_as_float(bpv[i]);
            if (tid < 128) STF(L1_BIAS + tid * 4, bs);
        }
        SB();
        constexpr int L1_D = 65536;
        const int did1 = b * 4 + w;
        const DTask dt1 = dtask_issue(p, lds, L1_D, did1 < 280 ? 370 + did1 : 1000, w, l);
        if (w >= 2) {
#pragma unroll
            for (int i = 0; i < 3; ++i) STF4(L1_HP + ((w & 1) * 3 + i) * 1024 + lo16, hacc[i]);
        }
        BAR();
        f32x4 bq[5]; float bv[3];
#pragma unroll
        for (int n = 0; n < 5; ++n) bq[n] = LDF4(L1_BIAS + (n * 16 + 4 * qd) * 4);
#pragma unroll
        for (int n = 0; n < 3; ++n) bv[n] = LDF(L1_BIAS + ((5 + n) * 16 + t) * 4);
        TileKV kv[2]; u32x2 qa[2][2], xq[2];
#pragma unroll
        for (int tt = 0; tt < 2; ++tt) {
            qa[tt][0] = pack4(acc[tt][0] + bq[0]); qa[tt][1] = pack4(acc[tt][1] + bq[1]);
            kv[tt].ka0 = pack4(acc[tt][2] + bq[2]); kv[tt].ka1 = pack4(acc[tt][3] + bq[3]);
            xq[tt] = pack4(acc[tt][4] + bq[4]);
            kv[tt].k2[0] = swap32(xq[tt][0]); kv[tt].k2[1] = swap32(xq[tt][1]);
            kv[tt].va0 = pack4(acc[tt][5] + bv[0]); kv[tt].va1 = pack4(acc[tt][6] + bv[1]); kv[tt].z = pack4(acc[tt][7] + bv[2]);
        }
        if (w < 3) {
            const unsigned e = L1_EX + (w + 1) * 3072 + lo16;
            ST16(e, ((u32x4){kv[1].ka0[0], kv[1].ka0[1], kv[1].ka1[0], kv[1].ka1[1]}));
            ST16(e + 1024, ((u32x4){kv[1].k2[0], kv[1].k2[1], kv[1].va0[0], kv[1].va0[1]}));
            ST16(e + 2048, ((u32x4){kv[1].va1[0], kv[1].va1[1], kv[1].z[0], kv[1].z[1]}));
        }
        if (w == 0) {
            const u32x2 k0 = pack4(hacc[0] + LDF4(L1_HP + lo16) + bq[2]), k1 = pack4(hacc[1] + LDF4(L1_HP + 1024 + lo16) + bq[3]);
            const u32x2 xx = pack4(hacc[2] + LDF4(L1_HP + 2048 + lo16) + bq[4]);
            ST16(L1_EX + lo16, ((u32x4){k0[0], k0[1], k1[0], k1[1]}));
            ST8(L1_EX + 1024 + lo16, ((u32x2){swap32(xx[0]), swap32(xx[1])}));
        } else if (w == 1) {
            const u32x2 v0 = pack4(hacc[0] + LDF4(L1_HP + 3072 + lo16) + bv[0]), v1 = pack4(hacc[1] + LDF4(L1_HP + 4096 + lo16) + bv[1]);
            const u32x2 zz = pack4(hacc[2] + LDF4(L1_HP + 5120 + lo16) + bv[2]);
            ST8(L1_EX + 1024 + lo16 + 8, v0);
            ST16(L1_EX + 2048 + lo16, ((u32x4){v1[0], v1[1], zz[0], zz[1]}));
        }
        const unsigned ob = WS_OF + T0 * 20480;
        u32x4 om1, om0; u32x2 ol1, ol0;
        attn_unit(qa[1], xq[1], kv[1], kv[0], true, t, qd, om1, ol1);
        BAR();
        TileKV pv;
        {
            const unsigned e = L1_EX + w * 3072 + lo16;
            const u32x4 a = LD16(e), bb = LD16(e + 1024), cc = LD16(e + 2048);
            pv.ka0 = (u32x2){a[0], a[1]}; pv.ka1 = (u32x2){a[2], a[3]}; pv.k2 = (u32x2){bb[0], bb[1]}; pv.va0 = (u32x2){bb[2], bb[3]};
            pv.va1 = (u32x2){cc[0], cc[1]}; pv.z = (u32x2){cc[2], cc[3]};
        }
        attn_unit(qa[0], xq[0], kv[0], pv, !(g1 == 0 && w == 0), t, qd, om0, ol0);
        SB();
        WAITV(0);
        dtask_finish(dt1, lds, L1_D, rs, w, l);
        bstore16<SC1>(om1, rs, lo16, ob + 20480 + j1 * 1024);
        bstore16<SC1>(om0, rs, lo16, ob + j1 * 1024);
        if (qd < 2) {
            bstore8<SC1>(ol1, rs, ((j1 & 3) * 16 + t) * 16 + 8 * qd, ob + 20480 + (16 + (j1 >> 2)) * 1024);
            bstore8<SC1>(ol0, rs, ((j1 & 3) * 16 + t) * 16 + 8 * qd, ob + (16 + (j1 >> 2)) * 1024);
        }
        STAMP(5);
        publish(ws, WS_F1 + (g1 * 16 + j1) * FS, tag);
#pragma unroll
        for (int i = 0; i < 25; ++i) wo_st[i] = bload16<SC1>(rs, lo16, WS_WOF + c2 * 102400 + (w * 25 + i) * 1024);
    }
    STAMP(6);

    constexpr int L2_WO = 61440;
    u32x2 x1b[5];
    u32x4 w2f[14][5];
    const int s4 = __builtin_amdgcn_readfirstlane(w < 2 ? 14 * w : 28 + 13 * (w - 2)), cnt4 = __builtin_amdgcn_readfirstlane(w < 2 ? 14 : 13);
    const unsigned w2b = __builtin_amdgcn_readfirstlane(WS_W2F + (c2 * 54 + s4) * 5 * 1024);
    {
        LANE_IDS();
        const int T = 4 * P2 + w;
        constexpr int L2_MV = 57344;
        u32x2 xv[5];
#pragma unroll
        for (int n = 0; n < 5; ++n) xv[n] = bload8<SC1>(rs, l * 8, WS_X0F + (T * 40 + 5 * c2 + n) * 512);
        const u32x4 mvl = bload16<SC1>(rs, (1280 + ((tid / 20) % 3) * 640 + 80 * c2 + (tid % 20) * 4) * 4, WS_MODV);
        SB();
#pragma unroll
        for (int i = 0; i < 25; ++i) ST16(L2_WO + (w * 25 + i) * 1024 + lo16, wo_st[i]);
        if (tid < 60) ST16(L2_MV + tid * 16, mvl);
        if (w == 0) {
            unsigned off[1] = {WS_F1 + ((P2 >> 1) * 16 + (l & 15)) * FS};
            poll_words<1>(ws, off, l < 16, tag);
        }
        BAR();
        STAMP(7);
        constexpr int PW = 8;
        u32x4 tk[20];
#pragma unroll
        for (int s = 0; s < PW; ++s) tk[s] = bload16<SC1>(rs, lo16, WS_OF + (T * 20 + s) * 1024);
        f32x4 acc[5];
#pragma unroll
        for (int n = 0; n < 5; ++n) acc[n] = (f32x4){0, 0, 0, 0};
        u32x4 wf[2][5];
#pragma unroll
        for (int n = 0; n < 5; ++n) wf[0][n] = LD16(L2_WO + n * 1024 + lo16);
        unsigned f1v[4];
        SB();
#pragma unroll
        for (int s = 0; s < 20; ++s) {
            if (s + PW < 20) tk[s + PW] = bload16<SC1>(rs, lo16, WS_OF + (T * 20 + s + PW) * 1024);
            if (s == 13) {
#pragma unroll
                for (int i = 0; i < 4; ++i) f1v[i] = flag_load(ws, WS_F1 + (l + 64 * i) * FS);
            }
            if (s + 1 < 20) {
#pragma unroll
                for (int n = 0; n < 5; ++n) wf[(s + 1) & 1][n] = LD16(L2_WO + ((s + 1) * 5 + n) * 1024 + lo16);
            }
#pragma unroll
            for (int n = 0; n < 5; ++n) acc[n] = mfma(wf[s & 1][n], tk[s], acc[n]);
#pragma unroll
            for (int n = 0; n < 5; ++n) { if (s + 1 < 20) SGB(0x100, 1); SGB(0x8, 1); if (n == 1 && s + PW < 20) SGB(0x20, 1); }
            SB();
        }
        STAMP(8);
        if (!__all(f1v[0] == tag && f1v[1] == tag && f1v[2] == tag && f1v[3] == tag)) {
            unsigned off[4];
#pragma unroll
            for (int k = 0; k < 4; ++k) off[k] = WS_F1 + (l + 64 * k) * FS;
            poll_words<4>(ws, off, true, tag);
        }
        BAR();
        constexpr int L2_D = 131072;
        const int did2 = b * 4 + w;
        const DTask dt2 = dtask_issue(p, lds, L2_D, did2 < 270 ? 100 + did2 : 1000, w, l);
        float st[6] = {0, 0, 0, 0, 0, 0};
        u32x2 ub[5];
#pragma unroll
        for (int n = 0; n < 5; ++n) {
            const f32x4 ga_n = LDF4(L2_MV + (16 * n + 4 * qd) * 4), s2_n = LDF4(L2_MV + 320 + (16 * n + 4 * qd) * 4), g2_n = LDF4(L2_MV + 640 + (16 * n + 4 * qd) * 4);
            float x1[4] = {lo_f(xv[n][0]), hi_f(xv[n][0]), lo_f(xv[n][1]), hi_f(xv[n][1])};
#pragma unroll
            for (int r = 0; r < 4; ++r) x1[r] = fmaf(ga_n[r], acc[n][r], x1[r]);
            x1b[n] = (u32x2){pack2(x1[0], x1[1]), pack2(x1[2], x1[3])};
            const float xr[4] = {lo_f(x1b[n][0]), hi_f(x1b[n][0]), lo_f(x1b[n][1]), hi_f(x1b[n][1])};
            ub[n] = (u32x2){pack2(xr[0] * g2_n[0], xr[1] * g2_n[1]), pack2(xr[2] * g2_n[2], xr[3] * g2_n[3])};
            const float ur[4] = {lo_f(ub[n][0]), hi_f(ub[n][0]), lo_f(ub[n][1]), hi_f(ub[n][1])};
#pragma unroll
            for (int r = 0; r < 4; ++r) {
                st[0] += xr[r]; st[1] = fmaf(xr[r], xr[r], st[1]); st[2] += ur[r]; st[3] = fmaf(ur[r], g2_n[r], st[3]);
                st[4] = fmaf(ur[r], ur[r], st[4]); st[5] = fmaf(ur[r], s2_n[r], st[5]);
            }
        }
#pragma unroll
        for (int i = 0; i < 6; ++i) st[i] = red_add_q(st[i]);
        SB();
        WAITV(0);
        dtask_finish(dt2, lds, L2_D, rs, w, l);
        const unsigned ufb = WS_UF + T * 20480;
        bstore16<SC1>((u32x4){ub[0][0], ub[0][1], ub[1][0], ub[1][1]}, rs, lo16, ufb + (2 * c2) * 1024);
        bstore16<SC1>((u32x4){ub[2][0], ub[2][1], ub[3][0], ub[3][1]}, rs, lo16, ufb + (2 * c2 + 1) * 1024);
        bstore8<SC1>(ub[4], rs, lo16 + 8 * (c2 & 1), ufb + (16 + (c2 >> 1)) * 1024);
        if (qd < 2) bstore16<SC1>(asu4(qd == 0 ? (f32x4){st[0], st[1], st[2], st[3]} : (f32x4){st[4], st[5], 0.f, 0.f}), rs, qd * 256 + t * 16, WS_ST + (T * 8 + c2) * 512);
#pragma unroll
        for (int n = 0; n < 5; ++n) asm volatile("" : "+v"(x1b[n]));
        STAMP(9);
        publish(ws, WS_F2 + (P2 * 8 + c2) * FS, tag);
        {
            u32x4 w1a[18];
#pragma unroll
            for (int i = 0; i < 18; ++i) { const int f = w * 35 + i, s = f / 7, n = f % 7; w1a[i] = bload16<SC1>(rs, lo16, WS_W1F + ((t3 + (n < cnt3 ? n : 0)) * 20 + s) * 1024); }
            SB();
            u32x4 w1b[17];
#pragma unroll
            for (int i = 0; i < 17; ++i) { const int f = w * 35 + 18 + i, s = f / 7, n = f % 7; w1b[i] = bload16<SC1>(rs, lo16, WS_W1F + ((t3 + (n < cnt3 ? n : 0)) * 20 + s) * 1024); }
            SB();
#pragma unroll
            for (int i = 0; i < 18; ++i) ST16((w * 35 + i) * 1024 + lo16, w1a[i]);
#pragma unroll
            for (int i = 0; i < 17; ++i) ST16((w * 35 + 18 + i) * 1024 + lo16, w1b[i]);
        }
    }
    STAMP(10);

    {
        LANE_IDS();
        constexpr int L3_GSC = 143360;
        u32x4 vf[5];
#pragma unroll
        for (int i = 0; i < 5; ++i) {
            int ta, tb; tiles_p3(5 * w + i, ta, tb);
            const unsigned sec = WS_MODV + (t == 0 ? 2560 : 1920) * 4 + qd * 16;
            f32x4 va = asf4(bload16<SC1>(rs, sec + ta * 64, 0)), vb = asf4(bload16<SC1>(rs, sec + tb * 64, 0));
            if (t == 2) { va = (f32x4){1, 1, 1, 1}; vb = va; } else if (t > 2) { va = (f32x4){0, 0, 0, 0}; vb = va; }
            vf[i] = (u32x4){pack2(va[0], va[1]), pack2(va[2], va[3]), pack2(vb[0], vb[1]), pack2(vb[2], vb[3])};
        }
        u32x2 b1v[7];
#pragma unroll
        for (int n = 0; n < 7; ++n) b1v[n] = *(const GAS u32x2*)(p.b1 + 16 * (t3 + (n < cnt3 ? n : 0)) + 4 * qd);
        SB();
        BAR();
        {
            f32x4 va[7], gram = {0, 0, 0, 0};
#pragma unroll
            for (int n = 0; n < 7; ++n) va[n] = (f32x4){0, 0, 0, 0};
#pragma unroll
            for (int i = 0; i < 5; ++i) {
#pragma unroll
                for (int n = 0; n < 7; ++n) va[n] = mfma(LD16(((5 * w + i) * 7 + n) * 1024 + lo16), vf[i], va[n]);
                gram = mfma(vf[i], vf[i], gram);
            }
            if (t < 3) {
#pragma unroll
                for (int n = 0; n < 7; ++n) STF4(L3_GSC + ((w * 8 + n) * 3 + t) * 64 + qd * 16, va[n]);
                if (qd == 0) STF4(L3_GSC + ((w * 8 + 7) * 3 + t) * 64, gram);
            }
        }
        BAR();
        if (tid < 96) {
            const int n8 = tid / 12, r12 = tid % 12;
            f32x4 a = LDF4(L3_GSC + (n8 * 12 + r12) * 16);
#pragma unroll
            for (int u = 1; u < 4; ++u) a += LDF4(L3_GSC + (u * 96 + n8 * 12 + r12) * 16);
            STF4(L3_GSC + 6144 + (n8 * 12 + r12) * 16, a);
        }
        const int T0 = 8 * g3 + 2 * w;
        if (w == 0) {
            unsigned off[1] = {WS_F2 + ((2 * g3 + ((l >> 3) & 1)) * 8 + (l & 7)) * FS};
            poll_words<1>(ws, off, l < 16, tag);
        }
        BAR();
        STAMP(11);
        constexpr int PW = 4;
        u32x4 tk[20][2];
        u32x4 sp[2][4];
        unsigned f2v[4];
#pragma unroll
        for (int s = 0; s < PW; ++s) { tk[s][0] = bload16<SC1>(rs, lo16, WS_UF + (T0 * 20 + s) * 1024); tk[s][1] = bload16<SC1>(rs, lo16, WS_UF + ((T0 + 1) * 20 + s) * 1024); }
        f32x4 acc[2][7];
#pragma unroll
        for (int n = 0; n < 7; ++n) { acc[0][n] = (f32x4){0, 0, 0, 0}; acc[1][n] = (f32x4){0, 0, 0, 0}; }
        u32x4 wf[2][7];
#pragma unroll
        for (int n = 0; n < 7; ++n) wf[0][n] = LD16(n * 1024 + lo16);
        SB();
#pragma unroll
        for (int s = 0; s < 20; ++s) {
            int nv = 0;
            if (s + PW < 20) { tk[s + PW][0] = bload16<SC1>(rs, lo16, WS_UF + (T0 * 20 + s + PW) * 1024); tk[s + PW][1] = bload16<SC1>(rs, lo16, WS_UF + ((T0 + 1) * 20 + s + PW) * 1024); nv += 2; }
            if (s == 6) {
#pragma unroll
                for (int i = 0; i < 4; ++i) f2v[i] = flag_load(ws, WS_F2 + (l + 64 * i) * FS);
            }
            if (s == 10) {
                if (!__all(f2v[0] == tag && f2v[1] == tag && f2v[2] == tag && f2v[3] == tag)) {
                    unsigned off[4];
#pragma unroll
                    for (int k = 0; k < 4; ++k) off[k] = WS_F2 + (l + 64 * k) * FS;
                    poll_words<4>(ws, off, true, tag);
                }
            }
            if (s >= 10 && s < 16) { const int f = s - 10; w2f[f / 5][f % 5] = bload16<SC1>(rs, lo16, w2b + f * 1024); nv += 1; }
            if (s >= 16) {
#pragma unroll
                for (int k = 0; k < 3; ++k) { const int f = 6 + (s - 16) * 3 + k; w2f[f / 5][f % 5] = bload16<SC1>(rs, lo16, w2b + f * 1024); }
                nv += 3;
            }
            if (s == 14 || s == 15) {
#pragma unroll
                for (int e = 0; e < 4; ++e) sp[s - 14][e] = bload16<SC1>(rs, (2 * qd + (e >> 1)) * 512 + (e & 1) * 256 + t * 16, WS_ST + (T0 + s - 14) * 4096);
                nv += 4;
            }
            if (s + 1 < 20) {
#pragma unroll
                for (int n = 0; n < 7; ++n) wf[(s + 1) & 1][n] = LD16(((s + 1) * 7 + n) * 1024 + lo16);
            }
#pragma unroll
            for (int n = 0; n < 7; ++n) { acc[0][n] = mfma(wf[s & 1][n], tk[s][0], acc[0][n]); acc[1][n] = mfma(wf[s & 1][n], tk[s][1], acc[1][n]); }
#pragma unroll
            for (int n = 0; n < 7; ++n) { if (s + 1 < 20) SGB(0x100, 1); SGB(0x8, 2); if (n < nv) SGB(0x20, 1); }
            SB();
            if (s == 0) STAMP(21);
            if (s == 9) STAMP(23);
        }
        STAMP(12);
        float al[2], be[2], gm[2], de[2];
        {
            const f32x4 r0 = LDF4(L3_GSC + 6144 + 84 * 16), r1 = LDF4(L3_GSC + 6144 + 88 * 16);
            const float Gg = r0[0], Ggs = r0[1], Gs = r0[2], Sss = r1[1], Ss = r1[2];
#pragma unroll
            for (int tt = 0; tt < 2; ++tt) {
                const f32x4 a0 = asf4(sp[tt][0]) + asf4(sp[tt][2]), b0 = asf4(sp[tt][1]) + asf4(sp[tt][3]);
                const float s0 = red_add_q(a0[0]), s1 = red_add_q(a0[1]), s2 = red_add_q(a0[2]), s3 = red_add_q(a0[3]), s4v = red_add_q(b0[0]), s5 = red_add_q(b0[1]);
                const float iD = 1.f / 640.f;
                const float m1 = s0 * iD, v1 = fmaxf(s1 * iD - m1 * m1, 0.f), r1v = rsqrtf(v1 + 1e-5f);
                const float m2 = (r1v * (s2 - m1 * Gs) + Ss) * iD;
                const float e2 = (r1v * r1v * (s4v - 2.f * m1 * s3 + m1 * m1 * Gg) + 2.f * r1v * (s5 - m1 * Ggs) + Sss) * iD;
                const float r2v = rsqrtf(fmaxf(e2 - m2 * m2, 0.f) + 1e-5f);
                al[tt] = r1v * r2v; be[tt] = -r1v * r2v * m1; gm[tt] = r2v; de[tt] = -r2v * m2;
            }
        }
        u32x2 hb[2][7];
#pragma unroll
        for (int n = 0; n < 7; ++n) {
#pragma unroll
            for (int k = 0; k < 3; ++k) { const int f = 18 + n * 3 + k; w2f[f / 5][f % 5] = bload16<SC1>(rs, lo16, w2b + f * 1024); }
            const f32x4 G = LDF4(L3_GSC + 6144 + (n * 12 + qd) * 16), Sv = LDF4(L3_GSC + 6144 + (n * 12 + 4 + qd) * 16), C = LDF4(L3_GSC + 6144 + (n * 12 + 8 + qd) * 16);
            const float bb[4] = {lo_f(b1v[n][0]), hi_f(b1v[n][0]), lo_f(b1v[n][1]), hi_f(b1v[n][1])};
#pragma unroll
            for (int tt = 0; tt < 2; ++tt) {
                float hv[4];
#pragma unroll
                for (int r = 0; r < 4; ++r) {
                    float z = fmaf(gm[tt], Sv[r], bb[r]); z = fmaf(be[tt], G[r], z); z = fmaf(de[tt], C[r], z); z = fmaf(al[tt], acc[tt][n][r], z);
                    const float u = z * fmaf(z * z, -0.044715f * 1.5957691216057308f * 1.4426950408889634f, -1.5957691216057308f * 1.4426950408889634f);
                    hv[r] = z * __builtin_amdgcn_rcpf(1.f + __builtin_amdgcn_exp2f(u));
                }
                hb[tt][n] = (u32x2){pack2(hv[0], hv[1]), pack2(hv[2], hv[3])};
            }
            SGB(0x20, 1); SGB(0x1, 30); SGB(0x20, 1); SGB(0x1, 30); SGB(0x20, 1); SGB(0x1, 40);
            SB();
        }
#pragma unroll
        for (int tt = 0; tt < 2; ++tt) {
            const unsigned hbase = WS_HF + (T0 + tt) * 54 * 1024;
#pragma unroll
            for (int pr = 0; pr < 3; ++pr)
                bstore16<SC1>((u32x4){hb[tt][2 * pr][0], hb[tt][2 * pr][1], hb[tt][2 * pr + 1][0], hb[tt][2 * pr + 1][1]}, rs, lo16, hbase + (3 * j3 + pr) * 1024);
            if (j3 < 12) bstore8<SC1>(hb[tt][6], rs, lo16 + 8 * (j3 & 1), hbase + (48 + (j3 >> 1)) * 1024);
        }
        STAMP(13);
        publish(ws, WS_F3 + (g3 * 16 + j3) * FS, tag);
    }
    STAMP(14);

    {
        LANE_IDS();
        f32x4 gb[5]; u32x2 b2v[5];
#pragma unroll
        for (int f = 39; f < 70; ++f) w2f[f / 5][f % 5] = bload16<SC1>(rs, lo16, w2b + (f < 65 ? f : (cnt4 - 1) * 5 + f % 5) * 1024);
        SB();
        if (w == 0) {
            unsigned off[1] = {WS_F3 + ((P2 >> 1) * 16 + (l & 15)) * FS};
            poll_words<1>(ws, off, l < 16, tag);
        }
        BAR();
        STAMP(15);
        constexpr int PD = 3;
        u32x4 tk[14][4];
        const unsigned tokb = __builtin_amdgcn_readfirstlane(WS_HF + (4 * P2 * 54 + s4) * 1024);
        auto issue = [&](int i) {
            const int s = i < 13 ? i : cnt4 - 1;
#pragma unroll
            for (int a = 0; a < 4; ++a) tk[i][a] = bload16<SC1>(rs, lo16, tokb + (a * 54 + s) * 1024);
        };
#pragma unroll
        for (int i = 0; i < PD; ++i) issue(i);
        f32x4 acc[4][5];
#pragma unroll
        for (int a = 0; a < 4; ++a)
#pragma unroll
            for (int n = 0; n < 5; ++n) acc[a][n] = (f32x4){0, 0, 0, 0};
        SB();
#pragma unroll
        for (int i = 0; i < 14; ++i) {
            if (i < 13 || w < 2) {
                if (i + PD < 14) issue(i + PD);
                if (i == 11) {
#pragma unroll
                    for (int n = 0; n < 5; ++n) {
                        const int f = 80 * c2 + 16 * n;
                        gb[n] = asf4(bload16<SC1>(rs, qd * 16, WS_MODV + (3200 + f) * 4));
                        b2v[n] = *(const GAS u32x2*)(p.b2 + f + 4 * qd);
                    }
                }
#pragma unroll
                for (int a = 0; a < 4; ++a)
#pragma unroll
                    for (int n = 0; n < 5; ++n) acc[a][n] = mfma(w2f[i][n], tk[i][a], acc[a][n]);
#pragma unroll
                for (int a = 0; a < 4; ++a) { SGB(0x8, 2); if (i == 11) SGB(0x20, 3); else SGB(0x20, 1); SGB(0x8, 3); }
            }
            SB();
            if (i == 0) STAMP(25);
            if (i == 9) STAMP(27);
        }
        STAMP(16);
#pragma unroll
        for (int a = 0; a < 4; ++a) {
            if (a != w) {
                const int src = w < a ? w : w - 1;
#pragma unroll
                for (int n = 0; n < 5; ++n) STF4(((a * 3 + src) * 5 + n) * 1024 + lo16, acc[a][n]);
            }
        }
        BAR();
        constexpr int L4_OUT = 61440;
#pragma unroll
        for (int n = 0; n < 5; ++n) {
            f32x4 v = w == 0 ? acc[0][n] : w == 1 ? acc[1][n] : w == 2 ? acc[2][n] : acc[3][n];
#pragma unroll
            for (int u = 0; u < 3; ++u) v += LDF4(((w * 3 + u) * 5 + n) * 1024 + lo16);
            const float x1[4] = {lo_f(x1b[n][0]), hi_f(x1b[n][0]), lo_f(x1b[n][1]), hi_f(x1b[n][1])};
            const float bv[4] = {lo_f(b2v[n][0]), hi_f(b2v[n][0]), lo_f(b2v[n][1]), hi_f(b2v[n][1])};
            float o[4];
#pragma unroll
            for (int r = 0; r < 4; ++r) o[r] = fmaf(gb[n][r], v[r] + bv[r], x1[r]);
            ST8(L4_OUT + (16 * w + t) * 176 + n * 32 + qd * 8, ((u32x2){pack2(o[0], o[1]), pack2(o[2], o[3])}));
        }
        asm volatile("s_waitcnt lgkmcnt(0)" ::: "memory");
#pragma unroll
        for (int k = 0; k < 3; ++k) {
            const int ch = l + 64 * k;
            if (ch < 160) {
                const int row = ch / 10, cc = ch % 10;
                const u32x4 v = LD16(L4_OUT + (16 * w + row) * 176 + cc * 16);
                *(GAS u32x4*)(p.out + (size_t)(16 * (4 * P2 + w) + row) * 640 + 80 * c2 + cc * 8) = v;
            }
        }
    }
    STAMP(17);
    asm volatile(".Ladaln_end:" ::: "memory");
}

extern "C" void kernel_launch(void* const* d_in, const int* in_sizes, int n_in, void* d_out, int out_size, void* d_ws, size_t ws_size, hipStream_t stream) {
    Params p;
    p.x = (const u16*)d_in[0]; p.wqkv = (const u16*)d_in[1]; p.wo = (const u16*)d_in[2]; p.w1 = (const u16*)d_in[4]; p.b1 = (const u16*)d_in[5];
    p.w2 = (const u16*)d_in[6]; p.b2 = (const u16*)d_in[7]; p.c = (const u16*)d_in[8]; p.wm = (const u16*)d_in[9];
    p.out = (u16*)d_out; p.ws = (char*)d_ws;
    (void)hipFuncSetAttribute((const void*)adaLN_block_kernel, hipFuncAttributeMaxDynamicSharedMemorySize, LDS_BYTES);
    adaLN_block_kernel<<<256, 256, LDS_BYTES, stream>>>(p);
}
```

```cpp
#include <hip/hip_runtime.h>
#include <stdint.h>

typedef unsigned short u16;
typedef float f32x4 __attribute__((ext_vector_type(4)));
typedef float f32x2 __attribute__((ext_vector_type(2)));
typedef __bf16 bf16x8 __attribute__((ext_vector_type(8)));
typedef __bf16 bf16x2 __attribute__((ext_vector_type(2)));
typedef unsigned u32x4 __attribute__((ext_vector_type(4)));
typedef unsigned u32x2 __attribute__((ext_vector_type(2)));
typedef short s16x4 __attribute__((ext_vector_type(4)));
typedef __amdgpu_buffer_rsrc_t rsrc_t;
#define LAS __attribute__((address_space(3)))
#define GAS __attribute__((address_space(1)))
#define DI __device__ __forceinline__

static constexpr unsigned FS = 64;
static constexpr unsigned WS_EPOCH = 0;
static constexpr unsigned WS_TMO   = 256;
static constexpr unsigned WS_FA    = 1024;
static constexpr unsigned WS_FC    = WS_FA + 160 * FS;
static constexpr unsigned WS_FD    = WS_FC + 128 * FS;
static constexpr unsigned WS_F1    = WS_FD + 1024;
static constexpr unsigned WS_F2    = WS_F1 + 256 * FS;
static constexpr unsigned WS_F3    = WS_F2 + 256 * FS;
static constexpr unsigned WS_FEND  = WS_F3 + 256 * FS;
static constexpr unsigned WS_MODV = 131072;
static constexpr unsigned WS_BP   = WS_MODV + 16384;
static constexpr unsigned WS_XNF  = WS_BP + 20 * 16 * 128 * 4;
static constexpr unsigned WS_WQF  = WS_XNF + 128 * 20 * 1024;
static constexpr unsigned WS_WOF  = WS_WQF + 16 * 20 * 8 * 1024;
static constexpr unsigned WS_W1F  = WS_WOF + 8 * 20 * 5 * 1024;
static constexpr unsigned WS_W2F  = WS_W1F + 108 * 20 * 1024;
static constexpr unsigned WS_OF   = WS_W2F + 8 * 54 * 5 * 1024;
static constexpr unsigned WS_UF   = WS_OF + 128 * 20 * 1024;
static constexpr unsigned WS_ST   = WS_UF + 128 * 20 * 1024;
static constexpr unsigned WS_HF   = WS_ST + 128 * 8 * 512;
static constexpr unsigned WS_END  = WS_HF + 128 * 54 * 1024;
static_assert(WS_FEND <= WS_MODV, "flags overflow");

#define B_DELAY 0
#define ROT1 0
#define ROT2 0
#define ROT3 0
#define ROT4 0
static constexpr int LDS_BYTES = 163840;
static constexpr int SC1 = 16;
static constexpr unsigned SPIN_CAP = 40000;

#define STAMP(i)
#define LSTAMP(i)
#define LSTEP(i)
#define LSTAMP_DECL
#define LSTAMP_DUMP

struct Params {
    const u16* x; const u16* wqkv; const u16* wo; const u16* w1; const u16* b1; const u16* w2; const u16* b2; const u16* c; const u16* wm;
    u16* out; char* ws;
};

DI float lo_f(unsigned d) { return __uint_as_float(d << 16); }
DI float hi_f(unsigned d) { return __uint_as_float(d & 0xffff0000u); }
DI unsigned pack2(float a, float b) { f32x2 v = {a, b}; return __builtin_bit_cast(unsigned, __builtin_convertvector(v, bf16x2)); }
DI float rbf(float a) { return lo_f(pack2(a, 0.f)); }
DI u32x2 pack4(const f32x4& v) { return (u32x2){pack2(v[0], v[1]), pack2(v[2], v[3])}; }
DI f32x4 mfma(u32x4 a, u32x4 b, f32x4 c) { return __builtin_amdgcn_mfma_f32_16x16x32_bf16(__builtin_bit_cast(bf16x8, a), __builtin_bit_cast(bf16x8, b), c, 0, 0, 0); }
DI float red_add_q(float v) {
    auto a = __builtin_amdgcn_permlane16_swap(__float_as_uint(v), __float_as_uint(v), false, false);
    v = __uint_as_float(a[0]) + __uint_as_float(a[1]);
    auto b = __builtin_amdgcn_permlane32_swap(__float_as_uint(v), __float_as_uint(v), false, false);
    return __uint_as_float(b[0]) + __uint_as_float(b[1]);
}
DI float red_max_q(float v) {
    auto a = __builtin_amdgcn_permlane16_swap(__float_as_uint(v), __float_as_uint(v), false, false);
    v = fmaxf(__uint_as_float(a[0]), __uint_as_float(a[1]));
    auto b = __builtin_amdgcn_permlane32_swap(__float_as_uint(v), __float_as_uint(v), false, false);
    return fmaxf(__uint_as_float(b[0]), __uint_as_float(b[1]));
}
template <int C> DI float dpp_add(float v) { return v + __builtin_bit_cast(float, __builtin_amdgcn_update_dpp(0, __builtin_bit_cast(int, v), C, 0xf, 0xf, true)); }
DI float red16(float v) { v = dpp_add<0xB1>(v); v = dpp_add<0x4E>(v); v = dpp_add<0x141>(v); return dpp_add<0x140>(v); }
DI unsigned swap32(unsigned v) { auto a = __builtin_amdgcn_permlane32_swap(v, v, false, false); return (threadIdx.x & 32) ? a[0] : a[1]; }
DI rsrc_t mk_rsrc(const void* p, unsigned bytes) { return __builtin_amdgcn_make_buffer_rsrc((void*)p, 0, bytes, 0x00020000); }
template <int AUX> DI u32x4 bload16(rsrc_t r, unsigned voff, unsigned soff) { return __builtin_bit_cast(u32x4, __builtin_amdgcn_raw_buffer_load_b128(r, voff, soff, AUX)); }
template <int AUX> DI u32x2 bload8(rsrc_t r, unsigned voff, unsigned soff) { return __builtin_bit_cast(u32x2, __builtin_amdgcn_raw_buffer_load_b64(r, voff, soff, AUX)); }
template <int AUX> DI unsigned bload4(rsrc_t r, unsigned voff, unsigned soff) { return __builtin_amdgcn_raw_buffer_load_b32(r, voff, soff, AUX); }
template <int AUX> DI void bstore16(u32x4 v, rsrc_t r, unsigned voff, unsigned soff) { __builtin_amdgcn_raw_buffer_store_b128(v, r, voff, soff, AUX); }
template <int AUX> DI void bstore8(u32x2 v, rsrc_t r, unsigned voff, unsigned soff) { __builtin_amdgcn_raw_buffer_store_b64(v, r, voff, soff, AUX); }
template <int AUX> DI void bstore4(unsigned v, rsrc_t r, unsigned voff, unsigned soff) { __builtin_amdgcn_raw_buffer_store_b32(v, r, voff, soff, AUX); }
DI f32x4 asf4(u32x4 v) { return __builtin_bit_cast(f32x4, v); }
DI u32x4 asu4(f32x4 v) { return __builtin_bit_cast(u32x4, v); }
#define LANE_IDS() int tz_ = threadIdx.x; asm volatile("" : "+v"(tz_)); const int tid = tz_, l = tid & 63, t = l & 15, qd = l >> 4; const unsigned lo16 = l * 16; (void)t; (void)qd; (void)lo16
#define SB() __builtin_amdgcn_sched_barrier(0)
#define BAR() asm volatile("s_waitcnt lgkmcnt(0)\n\ts_barrier" ::: "memory")
#define DRAIN() asm volatile("s_waitcnt vmcnt(0)" ::: "memory")
#define SGB(mask, n) __builtin_amdgcn_sched_group_barrier(mask, n, 0)
#define LD16(off) (*(const LAS u32x4*)((const LAS char*)lds + (off)))
#define LD8(off) (*(const LAS u32x2*)((const LAS char*)lds + (off)))
#define LDF4(off) (*(const LAS f32x4*)((const LAS char*)lds + (off)))
#define LDF(off) (*(const LAS float*)((const LAS char*)lds + (off)))
#define ST16(off, v) (*(LAS u32x4*)((LAS char*)lds + (off)) = (v))
#define ST8(off, v) (*(LAS u32x2*)((LAS char*)lds + (off)) = (v))
#define STF4(off, v) (*(LAS f32x4*)((LAS char*)lds + (off)) = (v))
#define STF(off, v) (*(LAS float*)((LAS char*)lds + (off)) = (v))
DI u32x2 tr_read(const char* lds, unsigned off) { return __builtin_bit_cast(u32x2, __builtin_amdgcn_ds_read_tr16_b64_v4i16((LAS s16x4*)((LAS char*)lds + off))); }

DI void flag_store(char* ws, unsigned off, unsigned tag) { __hip_atomic_store((GAS unsigned*)(ws + off), tag, __ATOMIC_RELAXED, __HIP_MEMORY_SCOPE_AGENT); }
DI unsigned flag_load(const char* ws, unsigned off) { return __hip_atomic_load((const GAS unsigned*)(ws + off), __ATOMIC_RELAXED, __HIP_MEMORY_SCOPE_AGENT); }
DI void publish(char* ws, unsigned off, unsigned tag) { DRAIN(); BAR(); if (threadIdx.x == 0) flag_store(ws, off, tag); }
template <int N> DI void poll_words(char* ws, const unsigned (&off)[N], bool active, unsigned tag) {
    for (unsigned spins = 0;; ++spins) {
        bool ok = true;
#pragma unroll
        for (int k = 0; k < N; ++k) ok &= flag_load(ws, off[k]) == tag;
        if (__all(ok || !active)) break;
        if (spins > SPIN_CAP) { if ((threadIdx.x & 63) == 0) flag_store(ws, WS_TMO, tag); break; }
        __builtin_amdgcn_s_sleep(1);
    }
}

DI void check_all_blocks(char* ws, rsrc_t rs, unsigned base, u32x4 v, unsigned tag) {
    asm volatile("" : "+v"(v));
    for (unsigned spins = 0; !__all(v[0] == tag && v[1] == tag && v[2] == tag && v[3] == tag); ++spins) {
        if (spins > SPIN_CAP) { if ((threadIdx.x & 63) == 0) flag_store(ws, WS_TMO, tag); break; }
        __builtin_amdgcn_s_sleep(1);
        v = bload16<SC1>(rs, (threadIdx.x & 63) * 16, base);
        asm volatile("" : "+v"(v) :: "memory");
    }
}

DI void rows_wo(int s, int qd, int& ra, int& rb) { if (s < 16) { ra = 40 * s + 4 * qd; rb = ra + 20; } else { ra = 40 * (4 * (s - 16) + qd) + 16; rb = ra + 20; } }
DI void tiles_p3(int s, int& ta, int& tb) { if (s < 16) { ta = 5 * (s >> 1) + 2 * (s & 1); tb = ta + 1; } else { ta = 10 * (s - 16) + 4; tb = ta + 5; } }
DI void tiles_p4(int s, int& ta, int& tb) { if (s < 48) { int j = s / 3, pr = s % 3; int st = j < 12 ? 7 * j : 84 + 6 * (j - 12); ta = st + 2 * pr; tb = ta + 1; } else { int m = s - 48; ta = 14 * m + 6; tb = ta + 7; } }

static constexpr int L0_SC = 81920, L0_GS = 83200, L0_CRAW = 83968, L0_RW = 86016, L0_D = 101376, L0_XI = 134144, L0_XS = 156160;

template <int AUX> DI void dma(char* lds, unsigned lds_off, rsrc_t r, unsigned voff, unsigned soff) {
    __builtin_amdgcn_raw_ptr_buffer_load_lds(r, (LAS void*)((LAS char*)lds + lds_off), 16, voff, soff, 0, AUX);
}
DI void dma16(char* lds, unsigned lds_off, rsrc_t r, unsigned voff, unsigned soff) {
    const unsigned dst = (unsigned)(size_t)(LAS char*)lds + lds_off;
    asm volatile("s_mov_b32 m0, %0\n\ts_nop 0\n\tbuffer_load_dwordx4 %1, %2, %3 offen lds" :: "s"(dst), "v"(voff), "s"(r), "s"(soff) : "memory");
}
DI void dma16s(char* lds, unsigned lds_off, rsrc_t r, unsigned voff, unsigned soff) { dma<SC1>(lds, lds_off, r, voff, soff); }
#define WAITV(n) asm volatile("s_waitcnt vmcnt(%0)" :: "n"(n) : "memory")

struct DTask { unsigned dst; int kind, r5; };
DI DTask dtask_issue(const Params& p, char* lds, unsigned scr, int id, int w, int l) {
    DTask d; int ra0, rb0, rstep, cg; unsigned pitch; rsrc_t r;
    if (id < 370) {
        const bool o = id < 100; const int i2 = o ? id : id - 100, s = i2 / 5; cg = i2 - 5 * s; pitch = 1280;
        if (o) { r = mk_rsrc(p.wo, 640 * 1280); if (s < 16) { ra0 = 40 * s; rstep = 4; } else { ra0 = 160 * (s - 16) + 16; rstep = 40; } rb0 = ra0 + 20; }
        else { r = mk_rsrc(p.w2, 1728 * 1280); int ta, tb; tiles_p4(s, ta, tb); ra0 = 16 * ta; rb0 = 16 * tb; rstep = 4; }
        const int c0 = (8 * cg) / 5;
        d.kind = 0; d.r5 = 8 * cg - 5 * c0; d.dst = (o ? WS_WOF + (c0 * 20 + s) * 5120 : WS_W2F + (c0 * 54 + s) * 5120) | (o ? 0u : 1u);
    } else {
        const int i2 = id < 650 ? id - 370 : 0, s = i2 / 14; cg = i2 - 14 * s; pitch = 3456; r = mk_rsrc(p.w1, 640 * 3456);
        int ta, tb; tiles_p3(s, ta, tb); ra0 = 16 * ta; rb0 = 16 * tb; rstep = 4;
        d.kind = id < 650 ? 1 : 2; d.r5 = cg == 13 ? 4 : 8; d.dst = WS_W1F + (8 * cg * 20 + s) * 1024;
    }
    const int q = l >> 4, cp = l & 15;
    const unsigned v0 = q * pitch + ((cp ^ (q << 1)) * 16), v1 = q * pitch + ((cp ^ ((4 + q) << 1)) * 16);
    const unsigned sa = ra0 * pitch + cg * 256, sb = rb0 * pitch + cg * 256, st = rstep * pitch;
    if (d.kind != 2) {
#pragma unroll
        for (int i = 0; i < 8; ++i) dma16(lds, scr + w * 8192 + i * 1024, r, (i & 1) ? v1 : v0, (i < 4 ? sa : sb) + (i & 3) * st);
    }
    return d;
}
struct DOut { unsigned dst[8]; u32x4 f[8]; };
DI void dtask_read(const DTask& d, DOut& o, char* lds, unsigned scr, int w, int l) {
    const int qd = l >> 4, q = (l >> 2) & 3, pp = l & 3, r7 = 4 * (qd & 1) + q;
    const unsigned a0 = scr + w * 8192 + (4 * qd + q) * 256 + pp * 8;
    const bool k0 = d.kind == 0;
    const unsigned r5 = k0 ? d.r5 : 0, stride = k0 ? 1024 : 20480, jump = k0 ? ((d.dst & 1u) ? 54 * 5120 - 5120 : 20 * 5120 - 5120) : 0;
    const int lim = k0 ? 8 : (d.kind == 1 ? d.r5 : 0);
    const unsigned d0 = (d.dst & ~1u) + r5 * 1024;
#pragma unroll
    for (int nt = 0; nt < 8; ++nt) {
        const unsigned v = d0 + nt * stride + ((r5 + nt >= 5 ? 1u : 0u) + (r5 + nt >= 10 ? 1u : 0u)) * jump;
        o.dst[nt] = __builtin_amdgcn_readfirstlane(nt < lim ? v : 0x7ff00000u);
        const u32x2 ua = tr_read(lds, a0 + ((nt ^ r7) * 32)), ub = tr_read(lds, a0 + 4096 + ((nt ^ r7) * 32));
        o.f[nt] = (u32x4){ua[0], ua[1], ub[0], ub[1]};
    }
}
template <int NT> DI void dtask_frag(const DOut& o, rsrc_t rs, int l) { bstore16<SC1>(o.f[NT], rs, l * 16, o.dst[NT]); }

DI void wm_dma(const Params& p, char* lds, int w, int l, unsigned colA, unsigned colB, int rot) {
    const rsrc_t rw = mk_rsrc(p.wm, 640 * 3840 * 2);
    const int rl = l >> 3, cp = l & 7;
    const unsigned sg = ((rl >> 1) & 1);
    const unsigned c0 = cp ^ (sg << 1), c1 = cp ^ ((sg | 2) << 1);
    const unsigned v0 = rl * 7680 + (c0 < 4 ? colA + 8 * c0 : colB + 8 * (c0 - 4)) * 2, v1 = rl * 7680 + (c1 < 4 ? colA + 8 * c1 : colB + 8 * (c1 - 4)) * 2;
#pragma unroll
    for (int i = 0; i < 20; ++i) {
        int piece = 4 * i + w + rot; piece = piece >= 80 ? piece - 80 : piece;
        dma16(lds, piece * 1024, rw, (piece & 1) ? v1 : v0, piece * 8 * 7680);
    }
}
DI void c_dma(const Params& p, char* lds, int w, int l) {
    dma16(lds, L0_CRAW + (w & 1) * 1024, mk_rsrc(p.c, 1280), ((w & 1) * 64 + l) * 16, 0);
}
DI float mod_gemv(const Params& p, char* lds, int w, int l, int tid, int did, DTask& dt) {
    WAITV(20);
    if (tid < 80) {
        const u32x4 creg = LD16(L0_CRAW + tid * 16);
        u32x4 o;
#pragma unroll
        for (int d = 0; d < 4; ++d) { const float a = lo_f(creg[d]), b = hi_f(creg[d]); o[d] = pack2(a * __builtin_amdgcn_rcpf(1.f + __expf(-a)), b * __builtin_amdgcn_rcpf(1.f + __expf(-b))); }
        ST16(L0_SC + tid * 16, o);
    }
    WAITV(0);
    dt = dtask_issue(p, lds, L0_D, did, w, l);
    BAR();
    const int qd = l >> 4, q = (l >> 2) & 3, pp = l & 3;
    f32x4 acc = {0, 0, 0, 0}, acc2 = {0, 0, 0, 0};
    const int r0 = 8 * qd + q;
    const unsigned sg = ((r0 >> 1) & 1) | (((r0 >> 3) & 1) << 1);
    const unsigned a0 = r0 * 128 + ((w ^ sg) * 32) + pp * 8;
#pragma unroll
    for (int s = 0; s < 20; s += 2) {
        const u32x2 ba = tr_read(lds, a0 + s * 4096), bb = tr_read(lds, a0 + s * 4096 + 512);
        const u32x2 bc = tr_read(lds, a0 + s * 4096 + 4096), bd = tr_read(lds, a0 + s * 4096 + 4608);
        acc = mfma(LD16(L0_SC + s * 64 + qd * 16), (u32x4){ba[0], ba[1], bb[0], bb[1]}, acc);
        acc2 = mfma(LD16(L0_SC + s * 64 + 64 + qd * 16), (u32x4){bc[0], bc[1], bd[0], bd[1]}, acc2);
    }
    return rbf(acc[0] + acc2[0]);
}
DI void x_dma(const Params& p, char* lds, unsigned img, int T, int w, int l) {
    const rsrc_t rx = mk_rsrc(p.x, 2048 * 1280);
    const int r4 = l >> 4, row = 4 * w + r4;
    const unsigned voff = row * 1280 + (((l & 15) ^ row) * 16);
#pragma unroll
    for (int cb = 0; cb < 5; ++cb) dma16(lds, img + (w * 5 + cb) * 1024, rx, voff, T * 16 * 1280 + cb * 256);
}
DI void x_stats(char* lds, unsigned img, unsigned stat, int w, int l) {
    u32x4 v[5];
#pragma unroll
    for (int cb = 0; cb < 5; ++cb) v[cb] = LD16(img + (w * 5 + cb) * 1024 + l * 16);
    float sum = 0.f;
#pragma unroll
    for (int i = 0; i < 5; ++i)
#pragma unroll
        for (int d = 0; d < 4; ++d) sum += lo_f(v[i][d]) + hi_f(v[i][d]);
    sum = red16(sum);
    const float mean = sum * (1.f / 640.f);
    float vs = 0.f;
#pragma unroll
    for (int i = 0; i < 5; ++i)
#pragma unroll
        for (int d = 0; d < 4; ++d) { const float a = lo_f(v[i][d]) - mean, c2 = hi_f(v[i][d]) - mean; vs += a * a + c2 * c2; }
    vs = red16(vs);
    const float rstd = rsqrtf(vs * (1.f / 640.f) + 1e-5f);
    if ((l & 15) == 0) { STF(stat + (4 * w + (l >> 4)) * 8, mean); STF(stat + (4 * w + (l >> 4)) * 8 + 4, rstd); }
}
DI unsigned ximg_off(int t, int byte_in_row) { return ((t >> 2) * 5 + (byte_in_row >> 8)) * 1024 + (t & 3) * 256 + ((((byte_in_row >> 4) & 15) ^ t) * 16) + (byte_in_row & 15); }
DI void x_frags(char* lds, unsigned img, unsigned stat, rsrc_t rs, int T, int w, int l) {
    const int t = l & 15, qd = l >> 4;
    const float mean = LDF(stat + t * 8), rstd = LDF(stat + t * 8 + 4);
#pragma unroll
    for (int i = 0; i < 5; ++i) {
        const int s = w + 4 * i;
        const u32x4 v = LD16(img + ximg_off(t, s * 64 + qd * 16));
        u32x4 o;
#pragma unroll
        for (int d = 0; d < 4; ++d) o[d] = pack2((lo_f(v[d]) - mean) * rstd, (hi_f(v[d]) - mean) * rstd);
        bstore16<SC1>(o, rs, l * 16, WS_XNF + (T * 20 + s) * 1024);
    }
}
struct TileKV { u32x2 ka0, ka1, k2, va0, va1, z; };
DI void attn_unit(const u32x2 (&qa)[2], const u32x2& xq, const TileKV& cur, const TileKV& prev, bool prev_ok, int t, int qd, u32x4& omain, u32x2& oleft) {
    const float cs = 0.22360679774997896f * 1.4426950408889634f;
    f32x4 oa[2], oz[2];
#pragma unroll
    for (int h = 0; h < 2; ++h) {
        const u32x4 qf = {qa[h][0], qa[h][1], qd == h ? xq[0] : 0u, qd == h ? xq[1] : 0u};
        const u32x2 kcp = h == 0 ? cur.ka0 : cur.ka1, kpp = h == 0 ? prev.ka0 : prev.ka1;
        const f32x4 z4 = {0.f, 0.f, 0.f, 0.f};
        f32x4 sp = mfma((u32x4){kpp[0], kpp[1], prev.k2[0], prev.k2[1]}, qf, z4);
        f32x4 sc = mfma((u32x4){kcp[0], kcp[1], cur.k2[0], cur.k2[1]}, qf, z4);
        float e[8]; float mx = -3.0e38f;
#pragma unroll
        for (int r = 0; r < 4; ++r) {
            const int dp = t + 16 - 4 * qd - r, dc = t - 4 * qd - r;
            const bool vp = prev_ok && dp <= 14 && !(dp & 1), vc = dc >= 0 && !(dc & 1);
            e[r] = vp ? sp[r] * cs : -3.0e38f; e[4 + r] = vc ? sc[r] * cs : -3.0e38f;
            mx = fmaxf(mx, fmaxf(e[r], e[4 + r]));
        }
        mx = red_max_q(mx);
        float sum = 0.f;
#pragma unroll
        for (int r = 0; r < 8; ++r) { e[r] = __builtin_amdgcn_exp2f(e[r] - mx); sum += e[r]; }
        sum = red_add_q(sum);
        const float inv = __builtin_amdgcn_rcpf(sum);
        const u32x4 pf = {pack2(e[0] * inv, e[1] * inv), pack2(e[2] * inv, e[3] * inv), pack2(e[4] * inv, e[5] * inv), pack2(e[6] * inv, e[7] * inv)};
        const u32x2 vc2 = h == 0 ? cur.va0 : cur.va1, vp2 = h == 0 ? prev.va0 : prev.va1;
        oa[h] = mfma((u32x4){vp2[0], vp2[1], vc2[0], vc2[1]}, pf, z4);
        oz[h] = mfma((u32x4){prev.z[0], prev.z[1], cur.z[0], cur.z[1]}, pf, z4);
    }
    const u32x2 a0 = pack4(oa[0]), a1 = pack4(oa[1]);
    omain = (u32x4){a0[0], a0[1], a1[0], a1[1]};
    const u32x2 l0 = pack4(oz[0]), l1 = pack4(oz[1]);
    oleft = qd == 0 ? l0 : l1;
}

__global__ __launch_bounds__(256, 1) void adaLN_block_kernel(Params p) {
    extern __shared__ __attribute__((aligned(16))) char lds[];
    const int b = blockIdx.x, w = __builtin_amdgcn_readfirstlane(threadIdx.x >> 6);
    const int xcd = b & 7, bi = b >> 3;
    char* ws = p.ws;
    const rsrc_t rs = mk_rsrc(ws, WS_END);
    STAMP(0);
    LSTAMP_DECL;
    {
        unsigned long long pc; unsigned len;
        asm volatile("s_getpc_b64 %0\n.Ladaln_pc:\n\ts_mov_b32 %1, .Ladaln_end-.Ladaln_pc" : "=s"(pc), "=s"(len));
        unsigned off = (bi * 256 + threadIdx.x) * 64u;
        off = off < len ? off : 0u;
        const unsigned long long a = pc + off;
        const unsigned dst = (unsigned)(size_t)(LAS char*)lds + LDS_BYTES - 1024 + w * 256;
        asm volatile("s_mov_b32 m0, %0\n\ts_nop 0\n\tglobal_load_lds_dword %1, off" :: "s"(dst), "v"(a) : "memory");
    }
    const unsigned epoch = *(const __attribute__((address_space(4))) unsigned*)(size_t)(ws + WS_EPOCH);
    unsigned tag;
#define MAKE_TAG() do { unsigned e_ = epoch; asm volatile("" : "+s"(e_)); tag = e_ + 0x9E3779B9u; } while (0)

    DOut dout;
    {
        LANE_IDS();
        const int nA = xcd < 4 ? 24 : 16;
        const int did = w == 0 ? 1000 : b * 3 + w - 1;
        DTask dt;
        if (bi < nA) {
            const int s = xcd + 8 * (bi >> 3), jg = bi & 7;
            c_dma(p, lds, w, l);
            {
                const rsrc_t rq = mk_rsrc(p.wqkv, 640 * 3840);
#pragma unroll
                for (int m = 0; m < 4; ++m) {
                    const int n = 64 * (4 * m + w) + l, rho = n / 30, c = n % 30;
                    const int k = (rho & 19) | ((rho & 4) << 1) | ((rho & 8) >> 1);
                    if (4 * m + w < 15) dma16(lds, L0_RW + (4 * m + w) * 1024, rq, k * 3840 + (c / 10) * 1280 + jg * 160 + (c % 10) * 16, s * 32 * 3840);
                }
            }
            wm_dma(p, lds, w, l, 640 + 32 * s, 32 * s, 10 * jg);
            MAKE_TAG();
            STAMP(18);
            const float v = mod_gemv(p, lds, w, l, tid, did, dt);
            STAMP(19);
            const float gv = w < 2 ? rbf(1.f + v) : v;
            if (l < 16) { STF(L0_GS + (w * 16 + l) * 4, gv); *(LAS u16*)((LAS char*)lds + L0_GS + 256 + (w * 16 + l) * 2) = (u16)(__float_as_uint(gv) >> 16); }
            BAR();
            const f32x4 g0 = LDF4(L0_GS + qd * 32), g1 = LDF4(L0_GS + qd * 32 + 16);
            const u32x4 sfr = LD16(L0_GS + 256 + 64 + qd * 16);
            const int q = (l >> 2) & 3, pp = l & 3, jj = w >> 1, j = 2 * jg + jj;
            const unsigned rbase = L0_RW + (16 * (qd >> 1) + 4 * (qd & 1) + q) * 480 + 80 * jj;
#pragma unroll
            for (int i = 0; i < 4; ++i) {
                const int nt = 4 * (w & 1) + i;
                unsigned po;
                if (nt < 4) po = (nt >> 1) * 160 + (nt & 1) * 40 + 8 * pp;
                else if (nt == 4) po = (pp >> 1) * 160 + (pp & 1) * 40 + 32;
                else if (nt < 7) po = 320 + (nt - 5) * 40 + 8 * pp;
                else po = 320 + (pp & 1) * 40 + 32;
                u32x2 ua = tr_read(lds, rbase + po), ub = tr_read(lds, rbase + po + 8 * 480);
                if (nt == 7 && t >= 8) { ua = (u32x2){0u, 0u}; ub = ua; }
                const f32x4 z4 = {0, 0, 0, 0};
                const f32x4 bias = mfma((u32x4){ua[0], ua[1], ub[0], ub[1]}, sfr, z4);
                const u32x4 o = {pack2(lo_f(ua[0]) * g0[0], hi_f(ua[0]) * g0[1]), pack2(lo_f(ua[1]) * g0[2], hi_f(ua[1]) * g0[3]),
                                 pack2(lo_f(ub[0]) * g1[0], hi_f(ub[0]) * g1[1]), pack2(lo_f(ub[1]) * g1[2], hi_f(ub[1]) * g1[3])};
                bstore16<SC1>(o, rs, lo16, WS_WQF + ((j * 20 + s) * 8 + nt) * 1024);
                if (t == 0) bstore16<SC1>(asu4(bias), rs, qd * 16, WS_BP + ((s * 16 + j) * 128 + nt * 16) * 4);
            }
            STAMP(20);
            publish(ws, WS_FA + (jg * 20 + s) * FS, tag);
        } else if (bi < nA + 5) {
            const int idxB = xcd * 5 + (bi - nA);
            for (int i = 0; i < B_DELAY; ++i) __builtin_amdgcn_s_sleep(8);
            c_dma(p, lds, w, l);
            wm_dma(p, lds, w, l, 1280 + 64 * idxB, 1280 + 64 * idxB + 32, 0);
            MAKE_TAG();
            STAMP(18);
            float v = mod_gemv(p, lds, w, l, tid, did, dt);
            STAMP(19);
            const int col = 1280 + 64 * idxB + 16 * w + t;
            if (col >= 2560 && col < 3200) v = rbf(1.f + v);
            if (l < 16) bstore4<SC1>(__float_as_uint(v), rs, l * 4, WS_MODV + (1280 + 64 * idxB + 16 * w) * 4);
        } else {
            const int idx = xcd < 4 ? xcd * 3 + (bi - 29) : 12 + (xcd - 4) * 11 + (bi - 21);
            const int Ts = idx < 16 ? 3 * idx : 48 + 2 * (idx - 16), nT = idx < 16 ? 3 : 2;
#pragma unroll
            for (int k = 0; k < 3; ++k) x_dma(p, lds, k * 20480, Ts + (k < nT ? k : 0), w, l);
            MAKE_TAG();
            STAMP(18);
            WAITV(10); x_stats(lds, 0, L0_XS, w, l);
            WAITV(5); x_stats(lds, 20480, L0_XS + 128, w, l);
            WAITV(0);
            dt = dtask_issue(p, lds, L0_D, did, w, l);
            x_stats(lds, 40960, L0_XS + 256, w, l);
            BAR();
            STAMP(19);
#pragma unroll
            for (int k = 0; k < 3; ++k) if (k < nT) x_frags(lds, k * 20480, L0_XS + k * 128, rs, Ts + k, w, l);
            DRAIN(); BAR();
            if (tid < nT) flag_store(ws, WS_FC + (Ts + tid) * FS, tag);
            STAMP(20);
        }
        if (bi >= nA && bi < nA + 5) WAITV(0);
        if (w > 0) dtask_read(dt, dout, lds, L0_D, w, l);
        else {
#pragma unroll
            for (int i = 0; i < 8; ++i) { dout.dst[i] = 0x7ff00000u; dout.f[i] = (u32x4){0u, 0u, 0u, 0u}; }
        }
        STAMP(1);
    }

    const int g1 = 4 * (xcd >> 1) + (bi >> 3), j1 = 8 * (xcd & 1) + (bi & 7);
    const int P2 = 4 * xcd + (bi >> 3), c2 = bi & 7;
    const int g3 = g1, j3 = j1;
    const int rot2 = ROT2, rot3 = ROT3;
    auto wrap20 = [](int x) { return x >= 20 ? x - 20 : x; };
    const int t3 = j3 < 12 ? 7 * j3 : 84 + 6 * (j3 - 12), cnt3 = j3 < 12 ? 7 : 6;
    constexpr int L2_WO = 61440, L2_X = 46080;
    {
        LANE_IDS();
        const int T0 = 8 * g1 + 2 * w, TH = g1 == 0 ? 0 : 8 * g1 - 1;
        const unsigned xb0 = WS_XNF + T0 * 20480, xbh = WS_XNF + TH * 20480 + (w >> 1) * 1024, wb = WS_WQF + j1 * 163840 + w * 4096;
        const unsigned wob = WS_WOF + c2 * 102400;
        u32x4 xr[10][4], xh[10];
        const int rot1 = ROT1;
        auto gr = [&](int G) { const int x = G + rot1; return x >= 10 ? x - 10 : x; };
        auto w_dma = [&](int G, int k) { dma16s(lds, (G & 3) * 16384 + w * 4096 + k * 1024, rs, lo16, wb + gr(G) * 16384 + k * 1024); };
        auto x_ld = [&](int G, int i) { xr[G][i] = bload16<SC1>(rs, lo16, xb0 + (i & 1) * 20480 + (2 * gr(G) + (i >> 1)) * 1024); };
        if (w == 0) {
            unsigned off[1] = {l == 0 ? WS_FC + TH * FS : WS_FC + (8 * g1 + ((l - 1) & 7)) * FS};
            poll_words<1>(ws, off, l < 9, tag);
        }
        BAR();
#pragma unroll
        for (int G = 0; G < 4; ++G) {
#pragma unroll
            for (int i = 0; i < 4; ++i) x_ld(G, i);
            xh[G] = bload16<SC1>(rs, lo16, xbh + gr(G) * 2048);
        }
        SB();
        if (w == 0) {
            unsigned off[1] = {WS_FA + ((j1 >> 1) * 20 + (l < 20 ? l : 0)) * FS};
            poll_words<1>(ws, off, l < 20, tag);
        }
        BAR();
        STAMP(2);
#pragma unroll
        for (int G = 0; G < 3; ++G)
#pragma unroll
            for (int k = 0; k < 4; ++k) w_dma(G, k);
        WAITV(4);
        BAR();
        STAMP(3);
        f32x4 acc[2][8], hacc[3];
#pragma unroll
        for (int i = 0; i < 8; ++i) { acc[0][i] = (f32x4){0, 0, 0, 0}; acc[1][i] = (f32x4){0, 0, 0, 0}; }
#pragma unroll
        for (int i = 0; i < 3; ++i) hacc[i] = (f32x4){0, 0, 0, 0};
        u32x4 wf[2][8];
#pragma unroll
        for (int n = 0; n < 8; ++n) wf[0][n] = LD16(n * 1024 + lo16);
        const unsigned hoff = (w >> 1) * 8192 + ((w & 1) ? 5 : 2) * 1024 + lo16;
        unsigned bpv[20];
        SB();
#pragma unroll
        for (int G = 0; G < 10; ++G) {
            u32x4 hw[3];
#pragma unroll
            for (int st = 0; st < 2; ++st) {
                const int s = 2 * G + st;
#pragma unroll
                for (int n = 0; n < 8; ++n) {
                    if (s + 1 < 20) wf[(s + 1) & 1][n] = LD16((((s + 1) >> 1) & 3) * 16384 + ((s + 1) & 1) * 8192 + n * 1024 + lo16);
                    if (st == 0 && n < 3) hw[n] = LD16((G & 3) * 16384 + hoff + n * 1024);
                    if (n < 5) { acc[0][n] = mfma(wf[s & 1][n], xr[G][st * 2], acc[0][n]); acc[1][n] = mfma(wf[s & 1][n], xr[G][st * 2 + 1], acc[1][n]); }
                    else { acc[0][n] = mfma(xr[G][st * 2], wf[s & 1][n], acc[0][n]); acc[1][n] = mfma(xr[G][st * 2 + 1], wf[s & 1][n], acc[1][n]); }
                    if (st == 1 && n >= 5) hacc[n - 5] = mfma(hw[n - 5], xh[G], hacc[n - 5]);
                    if (st == 0) {
                        if (G + 3 < 10 && (n & 1) == 0) w_dma(G + 3, n >> 1);
                        if (G + 4 < 10 && n == 7) xh[G + 4] = bload16<SC1>(rs, lo16, xbh + gr(G + 4) * 2048);
                    } else {
                        if (G + 4 < 10 && (n & 1) == 0) x_ld(G + 4, n >> 1);
                    }
                    if (G == 8 && st == 0 && n < 5) {
#pragma unroll
                        for (int k = 0; k < 4; ++k) bpv[4 * n + k] = bload4<SC1>(rs, (tid & 127) * 4, WS_BP + ((4 * n + k) * 16 + j1) * 512);
                    }
                    if (n == 3) {
                        if (s == 12) dtask_frag<0>(dout, rs, l);
                        if (s == 13) dtask_frag<1>(dout, rs, l);
                        if (s == 14) dtask_frag<2>(dout, rs, l);
                        if (s == 15) dtask_frag<3>(dout, rs, l);
                        if (s == 16) dtask_frag<4>(dout, rs, l);
                        if (s == 17) dtask_frag<5>(dout, rs, l);
                        if (s == 18) dtask_frag<6>(dout, rs, l);
                        if (s == 19) dtask_frag<7>(dout, rs, l);
                    }
                    SB();
                }
            }
            if (G == 0) WAITV(9); else if (G <= 5) WAITV(14); else if (G == 6) WAITV(11); else if (G == 7) WAITV(3);
            BAR();
        }
        STAMP(4);
        constexpr int L1_BIAS = 6144, L1_HP = 0, L1_EX = 8192, L1_VT = 6656;
        {
            float bs = 0.f;
#pragma unroll
            for (int i = 0; i < 20; ++i) bs += __uint_as_float(bpv[i]);
            if (tid < 128) STF(L1_BIAS + tid * 4, bs);
        }
        if (w >= 2) {
#pragma unroll
            for (int i = 0; i < 3; ++i) STF4(L1_HP + ((w & 1) * 3 + i) * 1024 + lo16, hacc[i]);
        }
        BAR();
        f32x4 bq[5]; float bv[3];
#pragma unroll
        for (int n = 0; n < 5; ++n) bq[n] = LDF4(L1_BIAS + (n * 16 + 4 * qd) * 4);
#pragma unroll
        for (int n = 0; n < 3; ++n) bv[n] = LDF(L1_BIAS + ((5 + n) * 16 + t) * 4);
        TileKV kv[2]; u32x2 qa[2][2], xq[2];
#pragma unroll
        for (int tt = 0; tt < 2; ++tt) {
            qa[tt][0] = pack4(acc[tt][0] + bq[0]); qa[tt][1] = pack4(acc[tt][1] + bq[1]);
            kv[tt].ka0 = pack4(acc[tt][2] + bq[2]); kv[tt].ka1 = pack4(acc[tt][3] + bq[3]);
            xq[tt] = pack4(acc[tt][4] + bq[4]);
            kv[tt].k2[0] = swap32(xq[tt][0]); kv[tt].k2[1] = swap32(xq[tt][1]);
            kv[tt].va0 = pack4(acc[tt][5] + bv[0]); kv[tt].va1 = pack4(acc[tt][6] + bv[1]); kv[tt].z = pack4(acc[tt][7] + bv[2]);
        }
        if (w < 3) {
            const unsigned e = L1_EX + (w + 1) * 3072 + lo16;
            ST16(e, ((u32x4){kv[1].ka0[0], kv[1].ka0[1], kv[1].ka1[0], kv[1].ka1[1]}));
            ST16(e + 1024, ((u32x4){kv[1].k2[0], kv[1].k2[1], kv[1].va0[0], kv[1].va0[1]}));
            ST16(e + 2048, ((u32x4){kv[1].va1[0], kv[1].va1[1], kv[1].z[0], kv[1].z[1]}));
        }
        if (w == 0) {
            const u32x2 k0 = pack4(hacc[0] + LDF4(L1_HP + lo16) + bq[2]), k1 = pack4(hacc[1] + LDF4(L1_HP + 1024 + lo16) + bq[3]);
            const u32x2 xx = pack4(hacc[2] + LDF4(L1_HP + 2048 + lo16) + bq[4]);
            ST16(L1_EX + lo16, ((u32x4){k0[0], k0[1], k1[0], k1[1]}));
            ST8(L1_EX + 1024 + lo16, ((u32x2){swap32(xx[0]), swap32(xx[1])}));
        } else if (w == 1) {
#pragma unroll
            for (int i = 0; i < 3; ++i) {
                const f32x4 v = hacc[i] + LDF4(L1_HP + (3 + i) * 1024 + lo16) + LDF4(L1_BIAS + ((5 + i) * 16 + 4 * qd) * 4);
                const u32x2 pk = pack4(v);
                LAS u16* d = (LAS u16*)((LAS char*)lds + L1_VT + i * 512 + (4 * qd) * 32 + t * 2);
                d[0] = (u16)pk[0]; d[16] = (u16)(pk[0] >> 16); d[32] = (u16)pk[1]; d[48] = (u16)(pk[1] >> 16);
            }
        }
        const unsigned ob = WS_OF + T0 * 20480;
        u32x4 om; u32x2 ol;
        attn_unit(qa[1], xq[1], kv[1], kv[0], true, t, qd, om, ol);
        bstore16<SC1>(om, rs, lo16, ob + 20480 + j1 * 1024);
        bstore8<SC1>(ol, rs, qd < 2 ? ((j1 & 3) * 16 + t) * 16 + 8 * qd : 0x7ff00000u, ob + 20480 + (16 + (j1 >> 2)) * 1024);
        WAITV(2);
        BAR();
        if (tid == 0) flag_store(ws, WS_FD + b * 4, tag);
        TileKV pv;
        {
            const unsigned e = L1_EX + w * 3072 + lo16;
            const u32x4 a = LD16(e), bb = LD16(e + 1024), cc = LD16(e + 2048);
            pv.ka0 = (u32x2){a[0], a[1]}; pv.ka1 = (u32x2){a[2], a[3]}; pv.k2 = (u32x2){bb[0], bb[1]}; pv.va0 = (u32x2){bb[2], bb[3]};
            pv.va1 = (u32x2){cc[0], cc[1]}; pv.z = (u32x2){cc[2], cc[3]};
            if (w == 0) { pv.va0 = LD8(L1_VT + t * 32 + qd * 8); pv.va1 = LD8(L1_VT + 512 + t * 32 + qd * 8); pv.z = LD8(L1_VT + 1024 + t * 32 + qd * 8); }
        }
        attn_unit(qa[0], xq[0], kv[0], pv, !(g1 == 0 && w == 0), t, qd, om, ol);
        bstore16<SC1>(om, rs, lo16, ob + j1 * 1024);
        if (qd < 2) bstore8<SC1>(ol, rs, ((j1 & 3) * 16 + t) * 16 + 8 * qd, ob + (16 + (j1 >> 2)) * 1024);
        STAMP(5);
        const u32x4 fdv = bload16<SC1>(rs, lo16, WS_FD);
        publish(ws, WS_F1 + (g1 * 16 + j1) * FS, tag);
        if (w == 0) check_all_blocks(ws, rs, WS_FD, fdv, tag);
        BAR();
        if (b == 0 && tid == 0) flag_store(ws, WS_EPOCH, tag);
#pragma unroll
        for (int k = 0; k < 25; ++k) dma16s(lds, L2_WO + w * 25600 + k * 1024, rs, lo16, wob + wrap20(5 * w + k / 5 + rot2) * 5120 + (k % 5) * 1024);
        {
            const rsrc_t rx = mk_rsrc(p.x, 2048 * 1280);
#pragma unroll
            for (int k = 0; k < 3; ++k) {
                const int n = (w * 3 + k) * 64 + l, row = n / 10, cc = n - 10 * row;
                if (w * 3 + k < 10) dma16(lds, L2_X + (w * 3 + k) * 1024, rx, row * 1280 + cc * 16, P2 * 64 * 1280 + c2 * 160);
            }
        }
    }
    STAMP(6);

    u32x2 x1b[5], b1v[7];
    u32x4 w2f[14][5], vraw[5][2];
    const int s4 = __builtin_amdgcn_readfirstlane(w < 2 ? 14 * w : 28 + 13 * (w - 2)), cnt4 = __builtin_amdgcn_readfirstlane(w < 2 ? 14 : 13);
    const unsigned w2b = __builtin_amdgcn_readfirstlane(WS_W2F + (c2 * 54 + s4) * 5 * 1024);
    const int rot4 = __builtin_amdgcn_readfirstlane(ROT4);
    auto step4 = [&](int i) { const int x = (i < 13 ? i : cnt4 - 1) + rot4; return x >= cnt4 ? x - cnt4 : x; };
    auto w2_ld = [&](int f, unsigned lo) { w2f[f / 5][f % 5] = bload16<SC1>(rs, lo, w2b + step4(f / 5) * 5120 + (f % 5) * 1024); };
    {
        LANE_IDS();
        const int T = 4 * P2 + w;
        constexpr int L2_MV = 57344;
        const u32x4 mvl = bload16<SC1>(rs, (1280 + ((tid / 20) % 3) * 640 + 80 * c2 + (tid % 20) * 4) * 4, WS_MODV);
        SB();
        if (tid < 60) ST16(L2_MV + tid * 16, mvl);
        if (w == 0) {
            unsigned off[1] = {WS_F1 + ((P2 >> 1) * 16 + (l & 15)) * FS};
            poll_words<1>(ws, off, l < 16, tag);
        }
        DRAIN();
        BAR();
        STAMP(7);
        u32x2 xv[5];
#pragma unroll
        for (int n = 0; n < 5; ++n) xv[n] = LD8(L2_X + (16 * w + t) * 160 + n * 32 + qd * 8);
        constexpr int PW = 8;
        u32x4 tk[20];
#pragma unroll
        for (int s = 0; s < PW; ++s) tk[s] = bload16<SC1>(rs, lo16, WS_OF + (T * 20 + wrap20(s + rot2)) * 1024);
        f32x4 acc[5];
#pragma unroll
        for (int n = 0; n < 5; ++n) acc[n] = (f32x4){0, 0, 0, 0};
        u32x4 wf[2][5];
#pragma unroll
        for (int n = 0; n < 5; ++n) wf[0][n] = LD16(L2_WO + n * 1024 + lo16);
        SB();
        LSTAMP(40);
#pragma unroll
        for (int s = 0; s < 20; ++s) {
            if (s + PW < 20) tk[s + PW] = bload16<SC1>(rs, lo16, WS_OF + (T * 20 + wrap20(s + PW + rot2)) * 1024);
            if (s + 1 < 20) {
#pragma unroll
                for (int n = 0; n < 5; ++n) wf[(s + 1) & 1][n] = LD16(L2_WO + ((s + 1) * 5 + n) * 1024 + lo16);
            }
#pragma unroll
            for (int n = 0; n < 5; ++n) acc[n] = mfma(wf[s & 1][n], tk[s], acc[n]);
#pragma unroll
            for (int n = 0; n < 5; ++n) { if (s + 1 < 20) SGB(0x100, 1); SGB(0x8, 1); if (n == 1 && s + PW < 20) SGB(0x20, 1); }
            SB();
            if ((s & 3) == 3 || s == 0) LSTEP(41 + (s + 1) / 4);
        }
        STAMP(8);
        BAR();
        const unsigned w1src = WS_W1F + t3 * 20480, w1last = (cnt3 == 7 ? 6 : 0) * 20480;
        auto w1_dma = [&](int base_step, int i) {
            const int n = i % 7;
            dma16s(lds, base_step * 7168 + i * 1024, rs, lo16, w1src + (n == 6 ? w1last : n * 20480) + wrap20(base_step + i / 7 + rot3) * 1024);
        };
        float st[6] = {0, 0, 0, 0, 0, 0};
        u32x2 ub[5];
        const unsigned ufb = WS_UF + T * 20480;
#pragma unroll
        for (int n = 0; n < 5; ++n) {
            const f32x4 ga_n = LDF4(L2_MV + (16 * n + 4 * qd) * 4), s2_n = LDF4(L2_MV + 320 + (16 * n + 4 * qd) * 4), g2_n = LDF4(L2_MV + 640 + (16 * n + 4 * qd) * 4);
            float x1[4] = {lo_f(xv[n][0]), hi_f(xv[n][0]), lo_f(xv[n][1]), hi_f(xv[n][1])};
#pragma unroll
            for (int r = 0; r < 4; ++r) x1[r] = fmaf(ga_n[r], acc[n][r], x1[r]);
            x1b[n] = (u32x2){pack2(x1[0], x1[1]), pack2(x1[2], x1[3])};
            const float xr[4] = {lo_f(x1b[n][0]), hi_f(x1b[n][0]), lo_f(x1b[n][1]), hi_f(x1b[n][1])};
            ub[n] = (u32x2){pack2(xr[0] * g2_n[0], xr[1] * g2_n[1]), pack2(xr[2] * g2_n[2], xr[3] * g2_n[3])};
            const float ur[4] = {lo_f(ub[n][0]), hi_f(ub[n][0]), lo_f(ub[n][1]), hi_f(ub[n][1])};
#pragma unroll
            for (int r = 0; r < 4; ++r) {
                st[0] += xr[r]; st[1] = fmaf(xr[r], xr[r], st[1]); st[2] += ur[r]; st[3] = fmaf(ur[r], g2_n[r], st[3]);
                st[4] = fmaf(ur[r], ur[r], st[4]); st[5] = fmaf(ur[r], s2_n[r], st[5]);
            }
            if (n == 1) bstore16<SC1>((u32x4){ub[0][0], ub[0][1], ub[1][0], ub[1][1]}, rs, lo16, ufb + (2 * c2) * 1024);
            if (n == 3) bstore16<SC1>((u32x4){ub[2][0], ub[2][1], ub[3][0], ub[3][1]}, rs, lo16, ufb + (2 * c2 + 1) * 1024);
            if (n == 4) bstore8<SC1>(ub[4], rs, lo16 + 8 * (c2 & 1), ufb + (16 + (c2 >> 1)) * 1024);
        }
#pragma unroll
        for (int i = 0; i < 6; ++i) st[i] = red_add_q(st[i]);
        if (qd < 2) bstore16<SC1>(asu4(qd == 0 ? (f32x4){st[0], st[1], st[2], st[3]} : (f32x4){st[4], st[5], 0.f, 0.f}), rs, qd * 256 + t * 16, WS_ST + (T * 8 + c2) * 512);
#pragma unroll
        for (int n = 0; n < 5; ++n) asm volatile("" : "+v"(x1b[n]));
        STAMP(9);
        publish(ws, WS_F2 + (P2 * 8 + c2) * FS, tag);
#pragma unroll
        for (int i = 0; i < 5; ++i) {
            int ta, tb; tiles_p3(wrap20(5 * w + i + rot3), ta, tb);
            const unsigned sec = WS_MODV + (t == 0 ? 2560 : 1920) * 4 + qd * 16;
            vraw[i][0] = bload16<SC1>(rs, sec + ta * 64, 0); vraw[i][1] = bload16<SC1>(rs, sec + tb * 64, 0);
        }
#pragma unroll
        for (int n = 0; n < 7; ++n) b1v[n] = *(const GAS u32x2*)(p.b1 + 16 * (t3 + (n < cnt3 ? n : 0)) + 4 * qd);
        SB();
#pragma unroll
        for (int i = 0; i < 14; ++i) w1_dma(2 * w, i);
#pragma unroll
        for (int i = 0; i < 21; ++i) w1_dma(8 + 3 * w, i);
    }
    STAMP(10);

    {
        LANE_IDS();
        constexpr int L3_GSC = 143360;
        const int T0 = 8 * g3 + 2 * w;
        if (w == 0) {
            unsigned off[1] = {WS_F2 + ((2 * g3 + ((l >> 3) & 1)) * 8 + (l & 7)) * FS};
            poll_words<1>(ws, off, l < 16, tag);
        }
        DRAIN();
        BAR();
        STAMP(11);
        constexpr int PW = 4;
        u32x4 tk[20][2];
        u32x4 sp[2][4];
#pragma unroll
        for (int s = 0; s < PW; ++s) { const unsigned o = WS_UF + (T0 * 20 + wrap20(s + rot3)) * 1024; tk[s][0] = bload16<SC1>(rs, lo16, o); tk[s][1] = bload16<SC1>(rs, lo16, o + 20480); }
        SB();
        LSTAMP(62);
        {
            f32x4 va[7], gram = {0, 0, 0, 0};
            u32x4 vf[5], wv[2][7];
#pragma unroll
            for (int n = 0; n < 7; ++n) { va[n] = (f32x4){0, 0, 0, 0}; wv[0][n] = LD16((5 * w * 7 + n) * 1024 + lo16); }
#pragma unroll
            for (int i = 0; i < 5; ++i) {
                const f32x4 a = asf4(vraw[i][0]), c = asf4(vraw[i][1]);
                const unsigned cst = t == 2 ? 0x3f803f80u : 0u;
                const unsigned d0 = pack2(a[0], a[1]), d1 = pack2(a[2], a[3]), d2 = pack2(c[0], c[1]), d3 = pack2(c[2], c[3]);
                vf[i] = (u32x4){t < 2 ? d0 : cst, t < 2 ? d1 : cst, t < 2 ? d2 : cst, t < 2 ? d3 : cst};
            }
            SB();
#pragma unroll
            for (int i = 0; i < 5; ++i) {
                if (i + 1 < 5) {
#pragma unroll
                    for (int n = 0; n < 7; ++n) wv[(i + 1) & 1][n] = LD16(((5 * w + i + 1) * 7 + n) * 1024 + lo16);
                }
                SB();
#pragma unroll
                for (int n = 0; n < 7; ++n) va[n] = mfma(wv[i & 1][n], vf[i], va[n]);
                gram = mfma(vf[i], vf[i], gram);
                SB();
            }
            if (t < 3) {
#pragma unroll
                for (int n = 0; n < 7; ++n) STF4(L3_GSC + ((w * 8 + n) * 3 + t) * 64 + qd * 16, va[n]);
                if (qd == 0) STF4(L3_GSC + ((w * 8 + 7) * 3 + t) * 64, gram);
            }
        }
        SB();
        f32x4 acc[2][7];
#pragma unroll
        for (int n = 0; n < 7; ++n) { acc[0][n] = (f32x4){0, 0, 0, 0}; acc[1][n] = (f32x4){0, 0, 0, 0}; }
        u32x4 wf[2][7];
#pragma unroll
        for (int n = 0; n < 7; ++n) wf[0][n] = LD16(n * 1024 + lo16);
        SB();
        LSTAMP(0);
#pragma unroll
        for (int s = 0; s < 20; ++s) {
            int nv = 0;
            if (s + PW < 20) { const unsigned o = WS_UF + (T0 * 20 + wrap20(s + PW + rot3)) * 1024; tk[s + PW][0] = bload16<SC1>(rs, lo16, o); tk[s + PW][1] = bload16<SC1>(rs, lo16, o + 20480); nv += 2; }
            if (s < 16) { w2_ld(s, lo16); nv += 1; }
            if (s >= 16) {
#pragma unroll
                for (int k = 0; k < 3; ++k) w2_ld(16 + (s - 16) * 3 + k, lo16);
                nv += 3;
            }
            if (s == 14 || s == 15) {
#pragma unroll
                for (int e = 0; e < 4; ++e) sp[s - 14][e] = bload16<SC1>(rs, (2 * qd + (e >> 1)) * 512 + (e & 1) * 256 + t * 16, WS_ST + (T0 + s - 14) * 4096);
                nv += 4;
            }
            if (s + 1 < 20) {
#pragma unroll
                for (int n = 0; n < 7; ++n) wf[(s + 1) & 1][n] = LD16(((s + 1) * 7 + n) * 1024 + lo16);
            }
#pragma unroll
            for (int n = 0; n < 7; ++n) { acc[0][n] = mfma(wf[s & 1][n], tk[s][0], acc[0][n]); acc[1][n] = mfma(wf[s & 1][n], tk[s][1], acc[1][n]); }
#pragma unroll
            for (int n = 0; n < 7; ++n) { if (s + 1 < 20) SGB(0x100, 1); SGB(0x8, 2); if (n < nv) SGB(0x20, 1); }
            SB();
            if (s == 2) BAR();
            if (s == 3 && tid < 96) {
                const int n8 = tid / 12, r12 = tid % 12;
                f32x4 a = LDF4(L3_GSC + (n8 * 12 + r12) * 16);
#pragma unroll
                for (int u = 1; u < 4; ++u) a += LDF4(L3_GSC + (u * 96 + n8 * 12 + r12) * 16);
                STF4(L3_GSC + 6144 + (n8 * 12 + r12) * 16, a);
            }
            if (s == 8) BAR();
            SB();
            LSTEP(1 + s);
        }
        STAMP(12);
        float al[2], be[2], gm[2], de[2];
        {
            const f32x4 r0 = LDF4(L3_GSC + 6144 + 84 * 16), r1 = LDF4(L3_GSC + 6144 + 88 * 16);
            const float Gg = r0[0], Ggs = r0[1], Gs = r0[2], Sss = r1[1], Ss = r1[2];
#pragma unroll
            for (int tt = 0; tt < 2; ++tt) {
                const f32x4 a0 = asf4(sp[tt][0]) + asf4(sp[tt][2]), b0 = asf4(sp[tt][1]) + asf4(sp[tt][3]);
                const float s0 = red_add_q(a0[0]), s1 = red_add_q(a0[1]), s2 = red_add_q(a0[2]), s3 = red_add_q(a0[3]), s4v = red_add_q(b0[0]), s5 = red_add_q(b0[1]);
                const float iD = 1.f / 640.f;
                const float m1 = s0 * iD, v1 = fmaxf(s1 * iD - m1 * m1, 0.f), r1v = rsqrtf(v1 + 1e-5f);
                const float m2 = (r1v * (s2 - m1 * Gs) + Ss) * iD;
                const float e2 = (r1v * r1v * (s4v - 2.f * m1 * s3 + m1 * m1 * Gg) + 2.f * r1v * (s5 - m1 * Ggs) + Sss) * iD;
                const float r2v = rsqrtf(fmaxf(e2 - m2 * m2, 0.f) + 1e-5f);
                al[tt] = r1v * r2v; be[tt] = -r1v * r2v * m1; gm[tt] = r2v; de[tt] = -r2v * m2;
            }
        }
        u32x2 hb[2][7];
#pragma unroll
        for (int n = 0; n < 7; ++n) {
#pragma unroll
            for (int k = 0; k < 5; ++k) w2_ld(28 + n * 5 + k, lo16);
            const f32x4 G = LDF4(L3_GSC + 6144 + (n * 12 + qd) * 16), Sv = LDF4(L3_GSC + 6144 + (n * 12 + 4 + qd) * 16), C = LDF4(L3_GSC + 6144 + (n * 12 + 8 + qd) * 16);
            const float bb[4] = {lo_f(b1v[n][0]), hi_f(b1v[n][0]), lo_f(b1v[n][1]), hi_f(b1v[n][1])};
#pragma unroll
            for (int tt = 0; tt < 2; ++tt) {
                float hv[4];
#pragma unroll
                for (int r = 0; r < 4; ++r) {
                    float z = fmaf(gm[tt], Sv[r], bb[r]); z = fmaf(be[tt], G[r], z); z = fmaf(de[tt], C[r], z); z = fmaf(al[tt], acc[tt][n][r], z);
                    const float u = z * fmaf(z * z, -0.044715f * 1.5957691216057308f * 1.4426950408889634f, -1.5957691216057308f * 1.4426950408889634f);
                    hv[r] = z * __builtin_amdgcn_rcpf(1.f + __builtin_amdgcn_exp2f(u));
                }
                hb[tt][n] = (u32x2){pack2(hv[0], hv[1]), pack2(hv[2], hv[3])};
            }
            SGB(0x20, 1); SGB(0x1, 20); SGB(0x20, 1); SGB(0x1, 20); SGB(0x20, 1); SGB(0x1, 20); SGB(0x20, 1); SGB(0x1, 20); SGB(0x20, 1); SGB(0x1, 40);
            SB();
#pragma unroll
            for (int tt = 0; tt < 2; ++tt) {
                const unsigned hbase = WS_HF + (T0 + tt) * 54 * 1024;
                if (n & 1) bstore16<SC1>((u32x4){hb[tt][n - 1][0], hb[tt][n - 1][1], hb[tt][n][0], hb[tt][n][1]}, rs, lo16, hbase + (3 * j3 + (n >> 1)) * 1024);
                if (n == 6 && j3 < 12) bstore8<SC1>(hb[tt][6], rs, lo16 + 8 * (j3 & 1), hbase + (48 + (j3 >> 1)) * 1024);
            }
            SB();
        }
        STAMP(13);
        publish(ws, WS_F3 + (g3 * 16 + j3) * FS, tag);
    }
    STAMP(14);

    {
        LANE_IDS();
        f32x4 gb[5]; u32x2 b2v[5];
#pragma unroll
        for (int f = 63; f < 70; ++f) w2_ld(f, lo16);
        SB();
        if (w == 0) {
            unsigned off[1] = {WS_F3 + ((P2 >> 1) * 16 + (l & 15)) * FS};
            poll_words<1>(ws, off, l < 16, tag);
        }
        BAR();
        STAMP(15);
        constexpr int PD = 3;
        u32x4 tk[14][4];
        const unsigned tokb = __builtin_amdgcn_readfirstlane(WS_HF + (4 * P2 * 54 + s4) * 1024);
        auto issue = [&](int i) {
            const int s = step4(i);
#pragma unroll
            for (int a = 0; a < 4; ++a) tk[i][a] = bload16<SC1>(rs, lo16, tokb + (a * 54 + s) * 1024);
        };
#pragma unroll
        for (int i = 0; i < PD; ++i) issue(i);
        f32x4 acc[4][5];
#pragma unroll
        for (int a = 0; a < 4; ++a)
#pragma unroll
            for (int n = 0; n < 5; ++n) acc[a][n] = (f32x4){0, 0, 0, 0};
        SB();
        LSTAMP(24);
#pragma unroll
        for (int i = 0; i < 14; ++i) {
            if (i < 13 || w < 2) {
                if (i + PD < 14) issue(i + PD);
                if (i == 11) {
#pragma unroll
                    for (int n = 0; n < 5; ++n) {
                        const int f = 80 * c2 + 16 * n;
                        gb[n] = asf4(bload16<SC1>(rs, qd * 16, WS_MODV + (3200 + f) * 4));
                        b2v[n] = *(const GAS u32x2*)(p.b2 + f + 4 * qd);
                    }
                }
#pragma unroll
                for (int a = 0; a < 4; ++a)
#pragma unroll
                    for (int n = 0; n < 5; ++n) acc[a][n] = mfma(w2f[i][n], tk[i][a], acc[a][n]);
#pragma unroll
                for (int a = 0; a < 4; ++a) { SGB(0x8, 2); if (i == 11) SGB(0x20, 3); else SGB(0x20, 1); SGB(0x8, 3); }
            }
            SB();
            LSTEP(25 + i);
        }
        STAMP(16);
#pragma unroll
        for (int a = 0; a < 4; ++a) {
            if (a != w) {
                const int src = w < a ? w : w - 1;
#pragma unroll
                for (int n = 0; n < 5; ++n) STF4(((a * 3 + src) * 5 + n) * 1024 + lo16, acc[a][n]);
            }
        }
        BAR();
        constexpr int L4_OUT = 61440;
#pragma unroll
        for (int n = 0; n < 5; ++n) {
            f32x4 v = w == 0 ? acc[0][n] : w == 1 ? acc[1][n] : w == 2 ? acc[2][n] : acc[3][n];
#pragma unroll
            for (int u = 0; u < 3; ++u) v += LDF4(((w * 3 + u) * 5 + n) * 1024 + lo16);
            const float x1[4] = {lo_f(x1b[n][0]), hi_f(x1b[n][0]), lo_f(x1b[n][1]), hi_f(x1b[n][1])};
            const float bv[4] = {lo_f(b2v[n][0]), hi_f(b2v[n][0]), lo_f(b2v[n][1]), hi_f(b2v[n][1])};
            float o[4];
#pragma unroll
            for (int r = 0; r < 4; ++r) o[r] = fmaf(gb[n][r], v[r] + bv[r], x1[r]);
            ST8(L4_OUT + (16 * w + t) * 176 + n * 32 + qd * 8, ((u32x2){pack2(o[0], o[1]), pack2(o[2], o[3])}));
        }
        asm volatile("s_waitcnt lgkmcnt(0)" ::: "memory");
#pragma unroll
        for (int k = 0; k < 3; ++k) {
            const int ch = l + 64 * k;
            if (ch < 160) {
                const int row = ch / 10, cc = ch % 10;
                const u32x4 v = LD16(L4_OUT + (16 * w + row) * 176 + cc * 16);
                *(GAS u32x4*)(p.out + (size_t)(16 * (4 * P2 + w) + row) * 640 + 80 * c2 + cc * 8) = v;
            }
        }
    }
    STAMP(17);
    LSTAMP_DUMP;
    asm volatile(".Ladaln_end:" ::: "memory");
}

extern "C" void kernel_launch(void* const* d_in, const int* in_sizes, int n_in, void* d_out, int out_size, void* d_ws, size_t ws_size, hipStream_t stream) {
    Params p;
    p.x = (const u16*)d_in[0]; p.wqkv = (const u16*)d_in[1]; p.wo = (const u16*)d_in[2]; p.w1 = (const u16*)d_in[4]; p.b1 = (const u16*)d_in[5];
    p.w2 = (const u16*)d_in[6]; p.b2 = (const u16*)d_in[7]; p.c = (const u16*)d_in[8]; p.wm = (const u16*)d_in[9];
    p.out = (u16*)d_out; p.ws = (char*)d_ws;
    (void)hipFuncSetAttribute((const void*)adaLN_block_kernel, hipFuncAttributeMaxDynamicSharedMemorySize, LDS_BYTES);
    adaLN_block_kernel<<<256, 256, LDS_BYTES, stream>>>(p);
}
```

```cpp
#define DIAG 1
#define DIAGB 0
#include <hip/hip_runtime.h>
#include <stdint.h>

typedef unsigned short u16;
typedef float f32x4 __attribute__((ext_vector_type(4)));
typedef float f32x2 __attribute__((ext_vector_type(2)));
typedef __bf16 bf16x8 __attribute__((ext_vector_type(8)));
typedef __bf16 bf16x2 __attribute__((ext_vector_type(2)));
typedef unsigned u32x4 __attribute__((ext_vector_type(4)));
typedef unsigned u32x2 __attribute__((ext_vector_type(2)));
typedef short s16x4 __attribute__((ext_vector_type(4)));
typedef __amdgpu_buffer_rsrc_t rsrc_t;
#define LAS __attribute__((address_space(3)))
#define GAS __attribute__((address_space(1)))
#define DI __device__ __forceinline__

static constexpr unsigned FS = 64;
static constexpr unsigned WS_EPOCH = 0;
static constexpr unsigned WS_TMO   = 256;
static constexpr unsigned WS_FA    = 1024;
static constexpr unsigned WS_FC    = WS_FA + 160 * FS;
static constexpr unsigned WS_FD    = WS_FC + 128 * FS;
static constexpr unsigned WS_F1    = WS_FD + 1024;
static constexpr unsigned WS_F2    = WS_F1 + 256 * FS;
static constexpr unsigned WS_F3    = WS_F2 + 256 * FS;
static constexpr unsigned WS_FEND  = WS_F3 + 256 * FS;
static constexpr unsigned WS_MODV = 131072;
static constexpr unsigned WS_BP   = WS_MODV + 16384;
static constexpr unsigned WS_XNF  = WS_BP + 20 * 16 * 128 * 4;
static constexpr unsigned WS_WQF  = WS_XNF + 128 * 20 * 1024;
static constexpr unsigned WS_WOF  = WS_WQF + 16 * 20 * 8 * 1024;
static constexpr unsigned WS_W1F  = WS_WOF + 8 * 20 * 5 * 1024;
static constexpr unsigned WS_W2F  = WS_W1F + 108 * 20 * 1024;
static constexpr unsigned WS_OF   = WS_W2F + 8 * 54 * 5 * 1024;
static constexpr unsigned WS_UF   = WS_OF + 128 * 20 * 1024;
static constexpr unsigned WS_ST   = WS_UF + 128 * 20 * 1024;
static constexpr unsigned WS_HF   = WS_ST + 128 * 8 * 512;
static constexpr unsigned WS_END  = WS_HF + 128 * 54 * 1024;
static_assert(WS_FEND <= WS_MODV, "flags overflow");

#define B_DELAY 0
#define ROT1 0
#define ROT2 0
#define ROT3 0
#define ROT4 0
static constexpr int LDS_BYTES = 163840;
static constexpr int SC1 = 16;
static constexpr unsigned SPIN_CAP = 40000;

#define STAMP(i) do { if ((i) == 0) diag_t0 = __builtin_amdgcn_s_memrealtime(); if ((i) == DIAG) diag_t1 = __builtin_amdgcn_s_memrealtime(); } while (0)
#define LSTAMP(i)
#define LSTEP(i)
#define LSTAMP_DECL
#define LSTAMP_DUMP do { if (blockIdx.x == DIAGB) { const unsigned long long te = __builtin_amdgcn_s_memrealtime() + 20 * (diag_t1 - diag_t0); while (__builtin_amdgcn_s_memrealtime() < te) __builtin_amdgcn_s_sleep(4); } } while (0)

struct Params {
    const u16* x; const u16* wqkv; const u16* wo; const u16* w1; const u16* b1; const u16* w2; const u16* b2; const u16* c; const u16* wm;
    u16* out; char* ws;
};

DI float lo_f(unsigned d) { return __uint_as_float(d << 16); }
DI float hi_f(unsigned d) { return __uint_as_float(d & 0xffff0000u); }
DI unsigned pack2(float a, float b) { f32x2 v = {a, b}; return __builtin_bit_cast(unsigned, __builtin_convertvector(v, bf16x2)); }
DI float rbf(float a) { return lo_f(pack2(a, 0.f)); }
DI u32x2 pack4(const f32x4& v) { return (u32x2){pack2(v[0], v[1]), pack2(v[2], v[3])}; }
DI f32x4 mfma(u32x4 a, u32x4 b, f32x4 c) { return __builtin_amdgcn_mfma_f32_16x16x32_bf16(__builtin_bit_cast(bf16x8, a), __builtin_bit_cast(bf16x8, b), c, 0, 0, 0); }
DI float red_add_q(float v) {
    auto a = __builtin_amdgcn_permlane16_swap(__float_as_uint(v), __float_as_uint(v), false, false);
    v = __uint_as_float(a[0]) + __uint_as_float(a[1]);
    auto b = __builtin_amdgcn_permlane32_swap(__float_as_uint(v), __float_as_uint(v), false, false);
    return __uint_as_float(b[0]) + __uint_as_float(b[1]);
}
DI float red_max_q(float v) {
    auto a = __builtin_amdgcn_permlane16_swap(__float_as_uint(v), __float_as_uint(v), false, false);
    v = fmaxf(__uint_as_float(a[0]), __uint_as_float(a[1]));
    auto b = __builtin_amdgcn_permlane32_swap(__float_as_uint(v), __float_as_uint(v), false, false);
    return fmaxf(__uint_as_float(b[0]), __uint_as_float(b[1]));
}
template <int C> DI float dpp_add(float v) { return v + __builtin_bit_cast(float, __builtin_amdgcn_update_dpp(0, __builtin_bit_cast(int, v), C, 0xf, 0xf, true)); }
DI float red16(float v) { v = dpp_add<0xB1>(v); v = dpp_add<0x4E>(v); v = dpp_add<0x141>(v); return dpp_add<0x140>(v); }
DI unsigned swap32(unsigned v) { auto a = __builtin_amdgcn_permlane32_swap(v, v, false, false); return (threadIdx.x & 32) ? a[0] : a[1]; }
DI rsrc_t mk_rsrc(const void* p, unsigned bytes) { return __builtin_amdgcn_make_buffer_rsrc((void*)p, 0, bytes, 0x00020000); }
template <int AUX> DI u32x4 bload16(rsrc_t r, unsigned voff, unsigned soff) { return __builtin_bit_cast(u32x4, __builtin_amdgcn_raw_buffer_load_b128(r, voff, soff, AUX)); }
template <int AUX> DI u32x2 bload8(rsrc_t r, unsigned voff, unsigned soff) { return __builtin_bit_cast(u32x2, __builtin_amdgcn_raw_buffer_load_b64(r, voff, soff, AUX)); }
template <int AUX> DI unsigned bload4(rsrc_t r, unsigned voff, unsigned soff) { return __builtin_amdgcn_raw_buffer_load_b32(r, voff, soff, AUX); }
template <int AUX> DI void bstore16(u32x4 v, rsrc_t r, unsigned voff, unsigned soff) { __builtin_amdgcn_raw_buffer_store_b128(v, r, voff, soff, AUX); }
template <int AUX> DI void bstore8(u32x2 v, rsrc_t r, unsigned voff, unsigned soff) { __builtin_amdgcn_raw_buffer_store_b64(v, r, voff, soff, AUX); }
template <int AUX> DI void bstore4(unsigned v, rsrc_t r, unsigned voff, unsigned soff) { __builtin_amdgcn_raw_buffer_store_b32(v, r, voff, soff, AUX); }
DI f32x4 asf4(u32x4 v) { return __builtin_bit_cast(f32x4, v); }
DI u32x4 asu4(f32x4 v) { return __builtin_bit_cast(u32x4, v); }
#define LANE_IDS() int tz_ = threadIdx.x; asm volatile("" : "+v"(tz_)); const int tid = tz_, l = tid & 63, t = l & 15, qd = l >> 4; const unsigned lo16 = l * 16; (void)t; (void)qd; (void)lo16
#define SB() __builtin_amdgcn_sched_barrier(0)
#define BAR() asm volatile("s_waitcnt lgkmcnt(0)\n\ts_barrier" ::: "memory")
#define DRAIN() asm volatile("s_waitcnt vmcnt(0)" ::: "memory")
#define SGB(mask, n) __builtin_amdgcn_sched_group_barrier(mask, n, 0)
#define LD16(off) (*(const LAS u32x4*)((const LAS char*)lds + (off)))
#define LD8(off) (*(const LAS u32x2*)((const LAS char*)lds + (off)))
#define LDF4(off) (*(const LAS f32x4*)((const LAS char*)lds + (off)))
#define LDF(off) (*(const LAS float*)((const LAS char*)lds + (off)))
#define ST16(off, v) (*(LAS u32x4*)((LAS char*)lds + (off)) = (v))
#define ST8(off, v) (*(LAS u32x2*)((LAS char*)lds + (off)) = (v))
#define STF4(off, v) (*(LAS f32x4*)((LAS char*)lds + (off)) = (v))
#define STF(off, v) (*(LAS float*)((LAS char*)lds + (off)) = (v))
DI u32x2 tr_read(const char* lds, unsigned off) { return __builtin_bit_cast(u32x2, __builtin_amdgcn_ds_read_tr16_b64_v4i16((LAS s16x4*)((LAS char*)lds + off))); }

DI void flag_store(char* ws, unsigned off, unsigned tag) { __hip_atomic_store((GAS unsigned*)(ws + off), tag, __ATOMIC_RELAXED, __HIP_MEMORY_SCOPE_AGENT); }
DI unsigned flag_load(const char* ws, unsigned off) { return __hip_atomic_load((const GAS unsigned*)(ws + off), __ATOMIC_RELAXED, __HIP_MEMORY_SCOPE_AGENT); }
DI void publish(char* ws, unsigned off, unsigned tag) { DRAIN(); BAR(); if (threadIdx.x == 0) flag_store(ws, off, tag); }
template <int N> DI void poll_words(char* ws, const unsigned (&off)[N], bool active, unsigned tag) {
    for (unsigned spins = 0;; ++spins) {
        bool ok = true;
#pragma unroll
        for (int k = 0; k < N; ++k) ok &= flag_load(ws, off[k]) == tag;
        if (__all(ok || !active)) break;
        if (spins > SPIN_CAP) { if ((threadIdx.x & 63) == 0) flag_store(ws, WS_TMO, tag); break; }
        __builtin_amdgcn_s_sleep(1);
    }
}

DI void check_all_blocks(char* ws, rsrc_t rs, unsigned base, u32x4 v, unsigned tag) {
    asm volatile("" : "+v"(v));
    for (unsigned spins = 0; !__all(v[0] == tag && v[1] == tag && v[2] == tag && v[3] == tag); ++spins) {
        if (spins > SPIN_CAP) { if ((threadIdx.x & 63) == 0) flag_store(ws, WS_TMO, tag); break; }
        __builtin_amdgcn_s_sleep(1);
        v = bload16<SC1>(rs, (threadIdx.x & 63) * 16, base);
        asm volatile("" : "+v"(v) :: "memory");
    }
}

DI void rows_wo(int s, int qd, int& ra, int& rb) { if (s < 16) { ra = 40 * s + 4 * qd; rb = ra + 20; } else { ra = 40 * (4 * (s - 16) + qd) + 16; rb = ra + 20; } }
DI void tiles_p3(int s, int& ta, int& tb) { if (s < 16) { ta = 5 * (s >> 1) + 2 * (s & 1); tb = ta + 1; } else { ta = 10 * (s - 16) + 4; tb = ta + 5; } }
DI void tiles_p4(int s, int& ta, int& tb) { if (s < 48) { int j = s / 3, pr = s % 3; int st = j < 12 ? 7 * j : 84 + 6 * (j - 12); ta = st + 2 * pr; tb = ta + 1; } else { int m = s - 48; ta = 14 * m + 6; tb = ta + 7; } }

static constexpr int L0_SC = 81920, L0_GS = 83200, L0_CRAW = 83968, L0_RW = 86016, L0_D = 101376, L0_XI = 134144, L0_XS = 156160;

template <int AUX> DI void dma(char* lds, unsigned lds_off, rsrc_t r, unsigned voff, unsigned soff) {
    __builtin_amdgcn_raw_ptr_buffer_load_lds(r, (LAS void*)((LAS char*)lds + lds_off), 16, voff, soff, 0, AUX);
}
DI void dma16(char* lds, unsigned lds_off, rsrc_t r, unsigned voff, unsigned soff) {
    const unsigned dst = (unsigned)(size_t)(LAS char*)lds + lds_off;
    asm volatile("s_mov_b32 m0, %0\n\ts_nop 0\n\tbuffer_load_dwordx4 %1, %2, %3 offen lds" :: "s"(dst), "v"(voff), "s"(r), "s"(soff) : "memory");
}
DI void dma16s(char* lds, unsigned lds_off, rsrc_t r, unsigned voff, unsigned soff) { dma<SC1>(lds, lds_off, r, voff, soff); }
#define WAITV(n) asm volatile("s_waitcnt vmcnt(%0)" :: "n"(n) : "memory")

struct DTask { unsigned dst; int kind, r5; };
DI DTask dtask_issue(const Params& p, char* lds, unsigned scr, int id, int w, int l) {
    DTask d; int ra0, rb0, rstep, cg; unsigned pitch; rsrc_t r;
    if (id < 370) {
        const bool o = id < 100; const int i2 = o ? id : id - 100, s = i2 / 5; cg = i2 - 5 * s; pitch = 1280;
        if (o) { r = mk_rsrc(p.wo, 640 * 1280); if (s < 16) { ra0 = 40 * s; rstep = 4; } else { ra0 = 160 * (s - 16) + 16; rstep = 40; } rb0 = ra0 + 20; }
        else { r = mk_rsrc(p.w2, 1728 * 1280); int ta, tb; tiles_p4(s, ta, tb); ra0 = 16 * ta; rb0 = 16 * tb; rstep = 4; }
        const int c0 = (8 * cg) / 5;
        d.kind = 0; d.r5 = 8 * cg - 5 * c0; d.dst = (o ? WS_WOF + (c0 * 20 + s) * 5120 : WS_W2F + (c0 * 54 + s) * 5120) | (o ? 0u : 1u);
    } else {
        const int i2 = id < 650 ? id - 370 : 0, s = i2 / 14; cg = i2 - 14 * s; pitch = 3456; r = mk_rsrc(p.w1, 640 * 3456);
        int ta, tb; tiles_p3(s, ta, tb); ra0 = 16 * ta; rb0 = 16 * tb; rstep = 4;
        d.kind = id < 650 ? 1 : 2; d.r5 = cg == 13 ? 4 : 8; d.dst = WS_W1F + (8 * cg * 20 + s) * 1024;
    }
    const int q = l >> 4, cp = l & 15;
    const unsigned v0 = q * pitch + ((cp ^ (q << 1)) * 16), v1 = q * pitch + ((cp ^ ((4 + q) << 1)) * 16);
    const unsigned sa = ra0 * pitch + cg * 256, sb = rb0 * pitch + cg * 256, st = rstep * pitch;
    if (d.kind != 2) {
#pragma unroll
        for (int i = 0; i < 8; ++i) dma16(lds, scr + w * 8192 + i * 1024, r, (i & 1) ? v1 : v0, (i < 4 ? sa : sb) + (i & 3) * st);
    }
    return d;
}
struct DOut { unsigned dst[8]; u32x4 f[8]; };
DI void dtask_read(const DTask& d, DOut& o, char* lds, unsigned scr, int w, int l) {
    const int qd = l >> 4, q = (l >> 2) & 3, pp = l & 3, r7 = 4 * (qd & 1) + q;
    const unsigned a0 = scr + w * 8192 + (4 * qd + q) * 256 + pp * 8;
    const bool k0 = d.kind == 0;
    const unsigned r5 = k0 ? d.r5 : 0, stride = k0 ? 1024 : 20480, jump = k0 ? ((d.dst & 1u) ? 54 * 5120 - 5120 : 20 * 5120 - 5120) : 0;
    const int lim = k0 ? 8 : (d.kind == 1 ? d.r5 : 0);
    const unsigned d0 = (d.dst & ~1u) + r5 * 1024;
#pragma unroll
    for (int nt = 0; nt < 8; ++nt) {
        const unsigned v = d0 + nt * stride + ((r5 + nt >= 5 ? 1u : 0u) + (r5 + nt >= 10 ? 1u : 0u)) * jump;
        o.dst[nt] = __builtin_amdgcn_readfirstlane(nt < lim ? v : 0x7ff00000u);
        const u32x2 ua = tr_read(lds, a0 + ((nt ^ r7) * 32)), ub = tr_read(lds, a0 + 4096 + ((nt ^ r7) * 32));
        o.f[nt] = (u32x4){ua[0], ua[1], ub[0], ub[1]};
    }
}
template <int NT> DI void dtask_frag(const DOut& o, rsrc_t rs, int l) { bstore16<SC1>(o.f[NT], rs, l * 16, o.dst[NT]); }

DI void wm_dma(const Params& p, char* lds, int w, int l, unsigned colA, unsigned colB, int rot) {
    const rsrc_t rw = mk_rsrc(p.wm, 640 * 3840 * 2);
    const int rl = l >> 3, cp = l & 7;
    const unsigned sg = ((rl >> 1) & 1);
    const unsigned c0 = cp ^ (sg << 1), c1 = cp ^ ((sg | 2) << 1);
    const unsigned v0 = rl * 7680 + (c0 < 4 ? colA + 8 * c0 : colB + 8 * (c0 - 4)) * 2, v1 = rl * 7680 + (c1 < 4 ? colA + 8 * c1 : colB + 8 * (c1 - 4)) * 2;
#pragma unroll
    for (int i = 0; i < 20; ++i) {
        int piece = 4 * i + w + rot; piece = piece >= 80 ? piece - 80 : piece;
        dma16(lds, piece * 1024, rw, (piece & 1) ? v1 : v0, piece * 8 * 7680);
    }
}
DI void c_dma(const Params& p, char* lds, int w, int l) {
    dma16(lds, L0_CRAW + (w & 1) * 1024, mk_rsrc(p.c, 1280), ((w & 1) * 64 + l) * 16, 0);
}
DI float mod_gemv(const Params& p, char* lds, int w, int l, int tid, int did, DTask& dt) {
    WAITV(20);
    if (tid < 80) {
        const u32x4 creg = LD16(L0_CRAW + tid * 16);
        u32x4 o;
#pragma unroll
        for (int d = 0; d < 4; ++d) { const float a = lo_f(creg[d]), b = hi_f(creg[d]); o[d] = pack2(a * __builtin_amdgcn_rcpf(1.f + __expf(-a)), b * __builtin_amdgcn_rcpf(1.f + __expf(-b))); }
        ST16(L0_SC + tid * 16, o);
    }
    WAITV(0);
    dt = dtask_issue(p, lds, L0_D, did, w, l);
    BAR();
    const int qd = l >> 4, q = (l >> 2) & 3, pp = l & 3;
    f32x4 acc = {0, 0, 0, 0}, acc2 = {0, 0, 0, 0};
    const int r0 = 8 * qd + q;
    const unsigned sg = ((r0 >> 1) & 1) | (((r0 >> 3) & 1) << 1);
    const unsigned a0 = r0 * 128 + ((w ^ sg) * 32) + pp * 8;
#pragma unroll
    for (int s = 0; s < 20; s += 2) {
        const u32x2 ba = tr_read(lds, a0 + s * 4096), bb = tr_read(lds, a0 + s * 4096 + 512);
        const u32x2 bc = tr_read(lds, a0 + s * 4096 + 4096), bd = tr_read(lds, a0 + s * 4096 + 4608);
        acc = mfma(LD16(L0_SC + s * 64 + qd * 16), (u32x4){ba[0], ba[1], bb[0], bb[1]}, acc);
        acc2 = mfma(LD16(L0_SC + s * 64 + 64 + qd * 16), (u32x4){bc[0], bc[1], bd[0], bd[1]}, acc2);
    }
    return rbf(acc[0] + acc2[0]);
}
DI void x_dma(const Params& p, char* lds, unsigned img, int T, int w, int l) {
    const rsrc_t rx = mk_rsrc(p.x, 2048 * 1280);
    const int r4 = l >> 4, row = 4 * w + r4;
    const unsigned voff = row * 1280 + (((l & 15) ^ row) * 16);
#pragma unroll
    for (int cb = 0; cb < 5; ++cb) dma16(lds, img + (w * 5 + cb) * 1024, rx, voff, T * 16 * 1280 + cb * 256);
}
DI void x_stats(char* lds, unsigned img, unsigned stat, int w, int l) {
    u32x4 v[5];
#pragma unroll
    for (int cb = 0; cb < 5; ++cb) v[cb] = LD16(img + (w * 5 + cb) * 1024 + l * 16);
    float sum = 0.f;
#pragma unroll
    for (int i = 0; i < 5; ++i)
#pragma unroll
        for (int d = 0; d < 4; ++d) sum += lo_f(v[i][d]) + hi_f(v[i][d]);
    sum = red16(sum);
    const float mean = sum * (1.f / 640.f);
    float vs = 0.f;
#pragma unroll
    for (int i = 0; i < 5; ++i)
#pragma unroll
        for (int d = 0; d < 4; ++d) { const float a = lo_f(v[i][d]) - mean, c2 = hi_f(v[i][d]) - mean; vs += a * a + c2 * c2; }
    vs = red16(vs);
    const float rstd = rsqrtf(vs * (1.f / 640.f) + 1e-5f);
    if ((l & 15) == 0) { STF(stat + (4 * w + (l >> 4)) * 8, mean); STF(stat + (4 * w + (l >> 4)) * 8 + 4, rstd); }
}
DI unsigned ximg_off(int t, int byte_in_row) { return ((t >> 2) * 5 + (byte_in_row >> 8)) * 1024 + (t & 3) * 256 + ((((byte_in_row >> 4) & 15) ^ t) * 16) + (byte_in_row & 15); }
DI void x_frags(char* lds, unsigned img, unsigned stat, rsrc_t rs, int T, int w, int l) {
    const int t = l & 15, qd = l >> 4;
    const float mean = LDF(stat + t * 8), rstd = LDF(stat + t * 8 + 4);
#pragma unroll
    for (int i = 0; i < 5; ++i) {
        const int s = w + 4 * i;
        const u32x4 v = LD16(img + ximg_off(t, s * 64 + qd * 16));
        u32x4 o;
#pragma unroll
        for (int d = 0; d < 4; ++d) o[d] = pack2((lo_f(v[d]) - mean) * rstd, (hi_f(v[d]) - mean) * rstd);
        bstore16<SC1>(o, rs, l * 16, WS_XNF + (T * 20 + s) * 1024);
    }
}
struct TileKV { u32x2 ka0, ka1, k2, va0, va1, z; };
DI void attn_unit(const u32x2 (&qa)[2], const u32x2& xq, const TileKV& cur, const TileKV& prev, bool prev_ok, int t, int qd, u32x4& omain, u32x2& oleft) {
    const float cs = 0.22360679774997896f * 1.4426950408889634f;
    f32x4 oa[2], oz[2];
#pragma unroll
    for (int h = 0; h < 2; ++h) {
        const u32x4 qf = {qa[h][0], qa[h][1], qd == h ? xq[0] : 0u, qd == h ? xq[1] : 0u};
        const u32x2 kcp = h == 0 ? cur.ka0 : cur.ka1, kpp = h == 0 ? prev.ka0 : prev.ka1;
        const f32x4 z4 = {0.f, 0.f, 0.f, 0.f};
        f32x4 sp = mfma((u32x4){kpp[0], kpp[1], prev.k2[0], prev.k2[1]}, qf, z4);
        f32x4 sc = mfma((u32x4){kcp[0], kcp[1], cur.k2[0], cur.k2[1]}, qf, z4);
        float e[8]; float mx = -3.0e38f;
#pragma unroll
        for (int r = 0; r < 4; ++r) {
            const int dp = t + 16 - 4 * qd - r, dc = t - 4 * qd - r;
            const bool vp = prev_ok && dp <= 14 && !(dp & 1), vc = dc >= 0 && !(dc & 1);
            e[r] = vp ? sp[r] * cs : -3.0e38f; e[4 + r] = vc ? sc[r] * cs : -3.0e38f;
            mx = fmaxf(mx, fmaxf(e[r], e[4 + r]));
        }
        mx = red_max_q(mx);
        float sum = 0.f;
#pragma unroll
        for (int r = 0; r < 8; ++r) { e[r] = __builtin_amdgcn_exp2f(e[r] - mx); sum += e[r]; }
        sum = red_add_q(sum);
        const float inv = __builtin_amdgcn_rcpf(sum);
        const u32x4 pf = {pack2(e[0] * inv, e[1] * inv), pack2(e[2] * inv, e[3] * inv), pack2(e[4] * inv, e[5] * inv), pack2(e[6] * inv, e[7] * inv)};
        const u32x2 vc2 = h == 0 ? cur.va0 : cur.va1, vp2 = h == 0 ? prev.va0 : prev.va1;
        oa[h] = mfma((u32x4){vp2[0], vp2[1], vc2[0], vc2[1]}, pf, z4);
        oz[h] = mfma((u32x4){prev.z[0], prev.z[1], cur.z[0], cur.z[1]}, pf, z4);
    }
    const u32x2 a0 = pack4(oa[0]), a1 = pack4(oa[1]);
    omain = (u32x4){a0[0], a0[1], a1[0], a1[1]};
    const u32x2 l0 = pack4(oz[0]), l1 = pack4(oz[1]);
    oleft = qd == 0 ? l0 : l1;
}

__global__ __launch_bounds__(256, 1) void adaLN_block_kernel(Params p) {
    extern __shared__ __attribute__((aligned(16))) char lds[];
    const int b = blockIdx.x, w = __builtin_amdgcn_readfirstlane(threadIdx.x >> 6);
    const int xcd = b & 7, bi = b >> 3;
    char* ws = p.ws;
    const rsrc_t rs = mk_rsrc(ws, WS_END);
    unsigned long long diag_t0 = 0, diag_t1 = 0;
    STAMP(0);
    LSTAMP_DECL;
    {
        unsigned long long pc; unsigned len;
        asm volatile("s_getpc_b64 %0\n.Ladaln_pc:\n\ts_mov_b32 %1, .Ladaln_end-.Ladaln_pc" : "=s"(pc), "=s"(len));
        unsigned off = (bi * 256 + threadIdx.x) * 64u;
        off = off < len ? off : 0u;
        const unsigned long long a = pc + off;
        const unsigned dst = (unsigned)(size_t)(LAS char*)lds + LDS_BYTES - 1024 + w * 256;
        asm volatile("s_mov_b32 m0, %0\n\ts_nop 0\n\tglobal_load_lds_dword %1, off" :: "s"(dst), "v"(a) : "memory");
    }
    const unsigned epoch = *(const __attribute__((address_space(4))) unsigned*)(size_t)(ws + WS_EPOCH);
    unsigned tag;
#define MAKE_TAG() do { unsigned e_ = epoch; asm volatile("" : "+s"(e_)); tag = e_ + 0x9E3779B9u; } while (0)

    DOut dout;
    {
        LANE_IDS();
        const int nA = xcd < 4 ? 24 : 16;
        const int did = w == 0 ? 1000 : b * 3 + w - 1;
        DTask dt;
        if (bi < nA) {
            const int s = xcd + 8 * (bi >> 3), jg = bi & 7;
            c_dma(p, lds, w, l);
            {
                const rsrc_t rq = mk_rsrc(p.wqkv, 640 * 3840);
#pragma unroll
                for (int m = 0; m < 4; ++m) {
                    const int n = 64 * (4 * m + w) + l, rho = n / 30, c = n % 30;
                    const int k = (rho & 19) | ((rho & 4) << 1) | ((rho & 8) >> 1);
                    if (4 * m + w < 15) dma16(lds, L0_RW + (4 * m + w) * 1024, rq, k * 3840 + (c / 10) * 1280 + jg * 160 + (c % 10) * 16, s * 32 * 3840);
                }
            }
            wm_dma(p, lds, w, l, 640 + 32 * s, 32 * s, 10 * jg);
            MAKE_TAG();
            STAMP(18);
            const float v = mod_gemv(p, lds, w, l, tid, did, dt);
            STAMP(19);
            const float gv = w < 2 ? rbf(1.f + v) : v;
            if (l < 16) { STF(L0_GS + (w * 16 + l) * 4, gv); *(LAS u16*)((LAS char*)lds + L0_GS + 256 + (w * 16 + l) * 2) = (u16)(__float_as_uint(gv) >> 16); }
            BAR();
            const f32x4 g0 = LDF4(L0_GS + qd * 32), g1 = LDF4(L0_GS + qd * 32 + 16);
            const u32x4 sfr = LD16(L0_GS + 256 + 64 + qd * 16);
            const int q = (l >> 2) & 3, pp = l & 3, jj = w >> 1, j = 2 * jg + jj;
            const unsigned rbase = L0_RW + (16 * (qd >> 1) + 4 * (qd & 1) + q) * 480 + 80 * jj;
#pragma unroll
            for (int i = 0; i < 4; ++i) {
                const int nt = 4 * (w & 1) + i;
                unsigned po;
                if (nt < 4) po = (nt >> 1) * 160 + (nt & 1) * 40 + 8 * pp;
                else if (nt == 4) po = (pp >> 1) * 160 + (pp & 1) * 40 + 32;
                else if (nt < 7) po = 320 + (nt - 5) * 40 + 8 * pp;
                else po = 320 + (pp & 1) * 40 + 32;
                u32x2 ua = tr_read(lds, rbase + po), ub = tr_read(lds, rbase + po + 8 * 480);
                if (nt == 7 && t >= 8) { ua = (u32x2){0u, 0u}; ub = ua; }
                const f32x4 z4 = {0, 0, 0, 0};
                const f32x4 bias = mfma((u32x4){ua[0], ua[1], ub[0], ub[1]}, sfr, z4);
                const u32x4 o = {pack2(lo_f(ua[0]) * g0[0], hi_f(ua[0]) * g0[1]), pack2(lo_f(ua[1]) * g0[2], hi_f(ua[1]) * g0[3]),
                                 pack2(lo_f(ub[0]) * g1[0], hi_f(ub[0]) * g1[1]), pack2(lo_f(ub[1]) * g1[2], hi_f(ub[1]) * g1[3])};
                bstore16<SC1>(o, rs, lo16, WS_WQF + ((j * 20 + s) * 8 + nt) * 1024);
                if (t == 0) bstore16<SC1>(asu4(bias), rs, qd * 16, WS_BP + ((s * 16 + j) * 128 + nt * 16) * 4);
            }
            STAMP(20);
            publish(ws, WS_FA + (jg * 20 + s) * FS, tag);
        } else if (bi < nA + 5) {
            const int idxB = xcd * 5 + (bi - nA);
            for (int i = 0; i < B_DELAY; ++i) __builtin_amdgcn_s_sleep(8);
            c_dma(p, lds, w, l);
            wm_dma(p, lds, w, l, 1280 + 64 * idxB, 1280 + 64 * idxB + 32, 0);
            MAKE_TAG();
            STAMP(18);
            float v = mod_gemv(p, lds, w, l, tid, did, dt);
            STAMP(19);
            const int col = 1280 + 64 * idxB + 16 * w + t;
            if (col >= 2560 && col < 3200) v = rbf(1.f + v);
            if (l < 16) bstore4<SC1>(__float_as_uint(v), rs, l * 4, WS_MODV + (1280 + 64 * idxB + 16 * w) * 4);
        } else {
            const int idx = xcd < 4 ? xcd * 3 + (bi - 29) : 12 + (xcd - 4) * 11 + (bi - 21);
            const int Ts = idx < 16 ? 3 * idx : 48 + 2 * (idx - 16), nT = idx < 16 ? 3 : 2;
#pragma unroll
            for (int k = 0; k < 3; ++k) x_dma(p, lds, k * 20480, Ts + (k < nT ? k : 0), w, l);
            MAKE_TAG();
            STAMP(18);
            WAITV(10); x_stats(lds, 0, L0_XS, w, l);
            WAITV(5); x_stats(lds, 20480, L0_XS + 128, w, l);
            WAITV(0);
            dt = dtask_issue(p, lds, L0_D, did, w, l);
            x_stats(lds, 40960, L0_XS + 256, w, l);
            BAR();
            STAMP(19);
#pragma unroll
            for (int k = 0; k < 3; ++k) if (k < nT) x_frags(lds, k * 20480, L0_XS + k * 128, rs, Ts + k, w, l);
            DRAIN(); BAR();
            if (tid < nT) flag_store(ws, WS_FC + (Ts + tid) * FS, tag);
            STAMP(20);
        }
        if (bi >= nA && bi < nA + 5) WAITV(0);
        if (w > 0) dtask_read(dt, dout, lds, L0_D, w, l);
        else {
#pragma unroll
            for (int i = 0; i < 8; ++i) { dout.dst[i] = 0x7ff00000u; dout.f[i] = (u32x4){0u, 0u, 0u, 0u}; }
        }
        STAMP(1);
    }

    const int g1 = 4 * (xcd >> 1) + (bi >> 3), j1 = 8 * (xcd & 1) + (bi & 7);
    const int P2 = 4 * xcd + (bi >> 3), c2 = bi & 7;
    const int g3 = g1, j3 = j1;
    const int rot2 = ROT2, rot3 = ROT3;
    auto wrap20 = [](int x) { return x >= 20 ? x - 20 : x; };
    const int t3 = j3 < 12 ? 7 * j3 : 84 + 6 * (j3 - 12), cnt3 = j3 < 12 ? 7 : 6;
    constexpr int L2_WO = 61440, L2_X = 46080;
    {
        LANE_IDS();
        const int T0 = 8 * g1 + 2 * w, TH = g1 == 0 ? 0 : 8 * g1 - 1;
        const unsigned xb0 = WS_XNF + T0 * 20480, xbh = WS_XNF + TH * 20480 + (w >> 1) * 1024, wb = WS_WQF + j1 * 163840 + w * 4096;
        const unsigned wob = WS_WOF + c2 * 102400;
        u32x4 xr[10][4], xh[10];
        const int rot1 = ROT1;
        auto gr = [&](int G) { const int x = G + rot1; return x >= 10 ? x - 10 : x; };
        auto w_dma = [&](int G, int k) { dma16s(lds, (G & 3) * 16384 + w * 4096 + k * 1024, rs, lo16, wb + gr(G) * 16384 + k * 1024); };
        auto x_ld = [&](int G, int i) { xr[G][i] = bload16<SC1>(rs, lo16, xb0 + (i & 1) * 20480 + (2 * gr(G) + (i >> 1)) * 1024); };
        if (w == 0) {
            unsigned off[1] = {l == 0 ? WS_FC + TH * FS : WS_FC + (8 * g1 + ((l - 1) & 7)) * FS};
            poll_words<1>(ws, off, l < 9, tag);
        }
        BAR();
#pragma unroll
        for (int G = 0; G < 4; ++G) {
#pragma unroll
            for (int i = 0; i < 4; ++i) x_ld(G, i);
            xh[G] = bload16<SC1>(rs, lo16, xbh + gr(G) * 2048);
        }
        SB();
        if (w == 0) {
            unsigned off[1] = {WS_FA + ((j1 >> 1) * 20 + (l < 20 ? l : 0)) * FS};
            poll_words<1>(ws, off, l < 20, tag);
        }
        BAR();
        STAMP(2);
#pragma unroll
        for (int G = 0; G < 3; ++G)
#pragma unroll
            for (int k = 0; k < 4; ++k) w_dma(G, k);
        WAITV(4);
        BAR();
        STAMP(3);
        f32x4 acc[2][8], hacc[3];
#pragma unroll
        for (int i = 0; i < 8; ++i) { acc[0][i] = (f32x4){0, 0, 0, 0}; acc[1][i] = (f32x4){0, 0, 0, 0}; }
#pragma unroll
        for (int i = 0; i < 3; ++i) hacc[i] = (f32x4){0, 0, 0, 0};
        u32x4 wf[2][8];
#pragma unroll
        for (int n = 0; n < 8; ++n) wf[0][n] = LD16(n * 1024 + lo16);
        const unsigned hoff = (w >> 1) * 8192 + ((w & 1) ? 5 : 2) * 1024 + lo16;
        unsigned bpv[20];
        SB();
#pragma unroll
        for (int G = 0; G < 10; ++G) {
            u32x4 hw[3];
#pragma unroll
            for (int st = 0; st < 2; ++st) {
                const int s = 2 * G + st;
#pragma unroll
                for (int n = 0; n < 8; ++n) {
                    if (s + 1 < 20) wf[(s + 1) & 1][n] = LD16((((s + 1) >> 1) & 3) * 16384 + ((s + 1) & 1) * 8192 + n * 1024 + lo16);
                    if (st == 0 && n < 3) hw[n] = LD16((G & 3) * 16384 + hoff + n * 1024);
                    if (n < 5) { acc[0][n] = mfma(wf[s & 1][n], xr[G][st * 2], acc[0][n]); acc[1][n] = mfma(wf[s & 1][n], xr[G][st * 2 + 1], acc[1][n]); }
                    else { acc[0][n] = mfma(xr[G][st * 2], wf[s & 1][n], acc[0][n]); acc[1][n] = mfma(xr[G][st * 2 + 1], wf[s & 1][n], acc[1][n]); }
                    if (st == 1 && n >= 5) hacc[n - 5] = mfma(hw[n - 5], xh[G], hacc[n - 5]);
                    if (st == 0) {
                        if (G + 3 < 10 && (n & 1) == 0) w_dma(G + 3, n >> 1);
                        if (G + 4 < 10 && n == 7) xh[G + 4] = bload16<SC1>(rs, lo16, xbh + gr(G + 4) * 2048);
                    } else {
                        if (G + 4 < 10 && (n & 1) == 0) x_ld(G + 4, n >> 1);
                    }
                    if (G == 8 && st == 0 && n < 5) {
#pragma unroll
                        for (int k = 0; k < 4; ++k) bpv[4 * n + k] = bload4<SC1>(rs, (tid & 127) * 4, WS_BP + ((4 * n + k) * 16 + j1) * 512);
                    }
                    if (n == 3) {
                        if (s == 12) dtask_frag<0>(dout, rs, l);
                        if (s == 13) dtask_frag<1>(dout, rs, l);
                        if (s == 14) dtask_frag<2>(dout, rs, l);
                        if (s == 15) dtask_frag<3>(dout, rs, l);
                        if (s == 16) dtask_frag<4>(dout, rs, l);
                        if (s == 17) dtask_frag<5>(dout, rs, l);
                        if (s == 18) dtask_frag<6>(dout, rs, l);
                        if (s == 19) dtask_frag<7>(dout, rs, l);
                    }
                    SB();
                }
            }
            if (G == 0) WAITV(9); else if (G <= 5) WAITV(14); else if (G == 6) WAITV(11); else if (G == 7) WAITV(3);
            BAR();
        }
        STAMP(4);
        constexpr int L1_BIAS = 6144, L1_HP = 0, L1_EX = 8192, L1_VT = 6656;
        {
            float bs = 0.f;
#pragma unroll
            for (int i = 0; i < 20; ++i) bs += __uint_as_float(bpv[i]);
            if (tid < 128) STF(L1_BIAS + tid * 4, bs);
        }
        if (w >= 2) {
#pragma unroll
            for (int i = 0; i < 3; ++i) STF4(L1_HP + ((w & 1) * 3 + i) * 1024 + lo16, hacc[i]);
        }
        BAR();
        f32x4 bq[5]; float bv[3];
#pragma unroll
        for (int n = 0; n < 5; ++n) bq[n] = LDF4(L1_BIAS + (n * 16 + 4 * qd) * 4);
#pragma unroll
        for (int n = 0; n < 3; ++n) bv[n] = LDF(L1_BIAS + ((5 + n) * 16 + t) * 4);
        TileKV kv[2]; u32x2 qa[2][2], xq[2];
#pragma unroll
        for (int tt = 0; tt < 2; ++tt) {
            qa[tt][0] = pack4(acc[tt][0] + bq[0]); qa[tt][1] = pack4(acc[tt][1] + bq[1]);
            kv[tt].ka0 = pack4(acc[tt][2] + bq[2]); kv[tt].ka1 = pack4(acc[tt][3] + bq[3]);
            xq[tt] = pack4(acc[tt][4] + bq[4]);
            kv[tt].k2[0] = swap32(xq[tt][0]); kv[tt].k2[1] = swap32(xq[tt][1]);
            kv[tt].va0 = pack4(acc[tt][5] + bv[0]); kv[tt].va1 = pack4(acc[tt][6] + bv[1]); kv[tt].z = pack4(acc[tt][7] + bv[2]);
        }
        if (w < 3) {
            const unsigned e = L1_EX + (w + 1) * 3072 + lo16;
            ST16(e, ((u32x4){kv[1].ka0[0], kv[1].ka0[1], kv[1].ka1[0], kv[1].ka1[1]}));
            ST16(e + 1024, ((u32x4){kv[1].k2[0], kv[1].k2[1], kv[1].va0[0], kv[1].va0[1]}));
            ST16(e + 2048, ((u32x4){kv[1].va1[0], kv[1].va1[1], kv[1].z[0], kv[1].z[1]}));
        }
        if (w == 0) {
            const u32x2 k0 = pack4(hacc[0] + LDF4(L1_HP + lo16) + bq[2]), k1 = pack4(hacc[1] + LDF4(L1_HP + 1024 + lo16) + bq[3]);
            const u32x2 xx = pack4(hacc[2] + LDF4(L1_HP + 2048 + lo16) + bq[4]);
            ST16(L1_EX + lo16, ((u32x4){k0[0], k0[1], k1[0], k1[1]}));
            ST8(L1_EX + 1024 + lo16, ((u32x2){swap32(xx[0]), swap32(xx[1])}));
        } else if (w == 1) {
#pragma unroll
            for (int i = 0; i < 3; ++i) {
                const f32x4 v = hacc[i] + LDF4(L1_HP + (3 + i) * 1024 + lo16) + LDF4(L1_BIAS + ((5 + i) * 16 + 4 * qd) * 4);
                const u32x2 pk = pack4(v);
                LAS u16* d = (LAS u16*)((LAS char*)lds + L1_VT + i * 512 + (4 * qd) * 32 + t * 2);
                d[0] = (u16)pk[0]; d[16] = (u16)(pk[0] >> 16); d[32] = (u16)pk[1]; d[48] = (u16)(pk[1] >> 16);
            }
        }
        const unsigned ob = WS_OF + T0 * 20480;
        u32x4 om; u32x2 ol;
        attn_unit(qa[1], xq[1], kv[1], kv[0], true, t, qd, om, ol);
        bstore16<SC1>(om, rs, lo16, ob + 20480 + j1 * 1024);
        bstore8<SC1>(ol, rs, qd < 2 ? ((j1 & 3) * 16 + t) * 16 + 8 * qd : 0x7ff00000u, ob + 20480 + (16 + (j1 >> 2)) * 1024);
        WAITV(2);
        BAR();
        if (tid == 0) flag_store(ws, WS_FD + b * 4, tag);
        TileKV pv;
        {
            const unsigned e = L1_EX + w * 3072 + lo16;
            const u32x4 a = LD16(e), bb = LD16(e + 1024), cc = LD16(e + 2048);
            pv.ka0 = (u32x2){a[0], a[1]}; pv.ka1 = (u32x2){a[2], a[3]}; pv.k2 = (u32x2){bb[0], bb[1]}; pv.va0 = (u32x2){bb[2], bb[3]};
            pv.va1 = (u32x2){cc[0], cc[1]}; pv.z = (u32x2){cc[2], cc[3]};
            if (w == 0) { pv.va0 = LD8(L1_VT + t * 32 + qd * 8); pv.va1 = LD8(L1_VT + 512 + t * 32 + qd * 8); pv.z = LD8(L1_VT + 1024 + t * 32 + qd * 8); }
        }
        attn_unit(qa[0], xq[0], kv[0], pv, !(g1 == 0 && w == 0), t, qd, om, ol);
        bstore16<SC1>(om, rs, lo16, ob + j1 * 1024);
        if (qd < 2) bstore8<SC1>(ol, rs, ((j1 & 3) * 16 + t) * 16 + 8 * qd, ob + (16 + (j1 >> 2)) * 1024);
        STAMP(5);
        const u32x4 fdv = bload16<SC1>(rs, lo16, WS_FD);
        publish(ws, WS_F1 + (g1 * 16 + j1) * FS, tag);
        if (w == 0) check_all_blocks(ws, rs, WS_FD, fdv, tag);
        BAR();
        if (b == 0 && tid == 0) flag_store(ws, WS_EPOCH, tag);
#pragma unroll
        for (int k = 0; k < 25; ++k) dma16s(lds, L2_WO + w * 25600 + k * 1024, rs, lo16, wob + wrap20(5 * w + k / 5 + rot2) * 5120 + (k % 5) * 1024);
        {
            const rsrc_t rx = mk_rsrc(p.x, 2048 * 1280);
#pragma unroll
            for (int k = 0; k < 3; ++k) {
                const int n = (w * 3 + k) * 64 + l, row = n / 10, cc = n - 10 * row;
                if (w * 3 + k < 10) dma16(lds, L2_X + (w * 3 + k) * 1024, rx, row * 1280 + cc * 16, P2 * 64 * 1280 + c2 * 160);
            }
        }
    }
    STAMP(6);

    u32x2 x1b[5], b1v[7];
    u32x4 w2f[14][5], vraw[5][2];
    const int s4 = __builtin_amdgcn_readfirstlane(w < 2 ? 14 * w : 28 + 13 * (w - 2)), cnt4 = __builtin_amdgcn_readfirstlane(w < 2 ? 14 : 13);
    const unsigned w2b = __builtin_amdgcn_readfirstlane(WS_W2F + (c2 * 54 + s4) * 5 * 1024);
    const int rot4 = __builtin_amdgcn_readfirstlane(ROT4);
    auto step4 = [&](int i) { const int x = (i < 13 ? i : cnt4 - 1) + rot4; return x >= cnt4 ? x - cnt4 : x; };
    auto w2_ld = [&](int f, unsigned lo) { w2f[f / 5][f % 5] = bload16<SC1>(rs, lo, w2b + step4(f / 5) * 5120 + (f % 5) * 1024); };
    {
        LANE_IDS();
        const int T = 4 * P2 + w;
        constexpr int L2_MV = 57344;
        const u32x4 mvl = bload16<SC1>(rs, (1280 + ((tid / 20) % 3) * 640 + 80 * c2 + (tid % 20) * 4) * 4, WS_MODV);
        SB();
        if (tid < 60) ST16(L2_MV + tid * 16, mvl);
        if (w == 0) {
            unsigned off[1] = {WS_F1 + ((P2 >> 1) * 16 + (l & 15)) * FS};
            poll_words<1>(ws, off, l < 16, tag);
        }
        DRAIN();
        BAR();
        STAMP(7);
        u32x2 xv[5];
#pragma unroll
        for (int n = 0; n < 5; ++n) xv[n] = LD8(L2_X + (16 * w + t) * 160 + n * 32 + qd * 8);
        constexpr int PW = 8;
        u32x4 tk[20];
#pragma unroll
        for (int s = 0; s < PW; ++s) tk[s] = bload16<SC1>(rs, lo16, WS_OF + (T * 20 + wrap20(s + rot2)) * 1024);
        f32x4 acc[5];
#pragma unroll
        for (int n = 0; n < 5; ++n) acc[n] = (f32x4){0, 0, 0, 0};
        u32x4 wf[2][5];
#pragma unroll
        for (int n = 0; n < 5; ++n) wf[0][n] = LD16(L2_WO + n * 1024 + lo16);
        SB();
        LSTAMP(40);
#pragma unroll
        for (int s = 0; s < 20; ++s) {
            if (s + PW < 20) tk[s + PW] = bload16<SC1>(rs, lo16, WS_OF + (T * 20 + wrap20(s + PW + rot2)) * 1024);
            if (s + 1 < 20) {
#pragma unroll
                for (int n = 0; n < 5; ++n) wf[(s + 1) & 1][n] = LD16(L2_WO + ((s + 1) * 5 + n) * 1024 + lo16);
            }
#pragma unroll
            for (int n = 0; n < 5; ++n) acc[n] = mfma(wf[s & 1][n], tk[s], acc[n]);
#pragma unroll
            for (int n = 0; n < 5; ++n) { if (s + 1 < 20) SGB(0x100, 1); SGB(0x8, 1); if (n == 1 && s + PW < 20) SGB(0x20, 1); }
            SB();
            if ((s & 3) == 3 || s == 0) LSTEP(41 + (s + 1) / 4);
        }
        STAMP(8);
        BAR();
        const unsigned w1src = WS_W1F + t3 * 20480, w1last = (cnt3 == 7 ? 6 : 0) * 20480;
        auto w1_dma = [&](int base_step, int i) {
            const int n = i % 7;
            dma16s(lds, base_step * 7168 + i * 1024, rs, lo16, w1src + (n == 6 ? w1last : n * 20480) + wrap20(base_step + i / 7 + rot3) * 1024);
        };
        float st[6] = {0, 0, 0, 0, 0, 0};
        u32x2 ub[5];
        const unsigned ufb = WS_UF + T * 20480;
#pragma unroll
        for (int n = 0; n < 5; ++n) {
            const f32x4 ga_n = LDF4(L2_MV + (16 * n + 4 * qd) * 4), s2_n = LDF4(L2_MV + 320 + (16 * n + 4 * qd) * 4), g2_n = LDF4(L2_MV + 640 + (16 * n + 4 * qd) * 4);
            float x1[4] = {lo_f(xv[n][0]), hi_f(xv[n][0]), lo_f(xv[n][1]), hi_f(xv[n][1])};
#pragma unroll
            for (int r = 0; r < 4; ++r) x1[r] = fmaf(ga_n[r], acc[n][r], x1[r]);
            x1b[n] = (u32x2){pack2(x1[0], x1[1]), pack2(x1[2], x1[3])};
            const float xr[4] = {lo_f(x1b[n][0]), hi_f(x1b[n][0]), lo_f(x1b[n][1]), hi_f(x1b[n][1])};
            ub[n] = (u32x2){pack2(xr[0] * g2_n[0], xr[1] * g2_n[1]), pack2(xr[2] * g2_n[2], xr[3] * g2_n[3])};
            const float ur[4] = {lo_f(ub[n][0]), hi_f(ub[n][0]), lo_f(ub[n][1]), hi_f(ub[n][1])};
#pragma unroll
            for (int r = 0; r < 4; ++r) {
                st[0] += xr[r]; st[1] = fmaf(xr[r], xr[r], st[1]); st[2] += ur[r]; st[3] = fmaf(ur[r], g2_n[r], st[3]);
                st[4] = fmaf(ur[r], ur[r], st[4]); st[5] = fmaf(ur[r], s2_n[r], st[5]);
            }
            if (n == 1) bstore16<SC1>((u32x4){ub[0][0], ub[0][1], ub[1][0], ub[1][1]}, rs, lo16, ufb + (2 * c2) * 1024);
            if (n == 3) bstore16<SC1>((u32x4){ub[2][0], ub[2][1], ub[3][0], ub[3][1]}, rs, lo16, ufb + (2 * c2 + 1) * 1024);
            if (n == 4) bstore8<SC1>(ub[4], rs, lo16 + 8 * (c2 & 1), ufb + (16 + (c2 >> 1)) * 1024);
        }
#pragma unroll
        for (int i = 0; i < 6; ++i) st[i] = red_add_q(st[i]);
        if (qd < 2) bstore16<SC1>(asu4(qd == 0 ? (f32x4){st[0], st[1], st[2], st[3]} : (f32x4){st[4], st[5], 0.f, 0.f}), rs, qd * 256 + t * 16, WS_ST + (T * 8 + c2) * 512);
#pragma unroll
        for (int n = 0; n < 5; ++n) asm volatile("" : "+v"(x1b[n]));
        STAMP(9);
        publish(ws, WS_F2 + (P2 * 8 + c2) * FS, tag);
#pragma unroll
        for (int i = 0; i < 5; ++i) {
            int ta, tb; tiles_p3(wrap20(5 * w + i + rot3), ta, tb);
            const unsigned sec = WS_MODV + (t == 0 ? 2560 : 1920) * 4 + qd * 16;
            vraw[i][0] = bload16<SC1>(rs, sec + ta * 64, 0); vraw[i][1] = bload16<SC1>(rs, sec + tb * 64, 0);
        }
#pragma unroll
        for (int n = 0; n < 7; ++n) b1v[n] = *(const GAS u32x2*)(p.b1 + 16 * (t3 + (n < cnt3 ? n : 0)) + 4 * qd);
        SB();
#pragma unroll
        for (int i = 0; i < 14; ++i) w1_dma(2 * w, i);
#pragma unroll
        for (int i = 0; i < 21; ++i) w1_dma(8 + 3 * w, i);
    }
    STAMP(10);

    {
        LANE_IDS();
        constexpr int L3_GSC = 143360;
        const int T0 = 8 * g3 + 2 * w;
        if (w == 0) {
            unsigned off[1] = {WS_F2 + ((2 * g3 + ((l >> 3) & 1)) * 8 + (l & 7)) * FS};
            poll_words<1>(ws, off, l < 16, tag);
        }
        DRAIN();
        BAR();
        STAMP(11);
        constexpr int PW = 4;
        u32x4 tk[20][2];
        u32x4 sp[2][4];
#pragma unroll
        for (int s = 0; s < PW; ++s) { const unsigned o = WS_UF + (T0 * 20 + wrap20(s + rot3)) * 1024; tk[s][0] = bload16<SC1>(rs, lo16, o); tk[s][1] = bload16<SC1>(rs, lo16, o + 20480); }
        SB();
        LSTAMP(62);
        {
            f32x4 va[7], gram = {0, 0, 0, 0};
            u32x4 vf[5], wv[2][7];
#pragma unroll
            for (int n = 0; n < 7; ++n) { va[n] = (f32x4){0, 0, 0, 0}; wv[0][n] = LD16((5 * w * 7 + n) * 1024 + lo16); }
#pragma unroll
            for (int i = 0; i < 5; ++i) {
                const f32x4 a = asf4(vraw[i][0]), c = asf4(vraw[i][1]);
                const unsigned cst = t == 2 ? 0x3f803f80u : 0u;
                const unsigned d0 = pack2(a[0], a[1]), d1 = pack2(a[2], a[3]), d2 = pack2(c[0], c[1]), d3 = pack2(c[2], c[3]);
                vf[i] = (u32x4){t < 2 ? d0 : cst, t < 2 ? d1 : cst, t < 2 ? d2 : cst, t < 2 ? d3 : cst};
            }
            SB();
#pragma unroll
            for (int i = 0; i < 5; ++i) {
                if (i + 1 < 5) {
#pragma unroll
                    for (int n = 0; n < 7; ++n) wv[(i + 1) & 1][n] = LD16(((5 * w + i + 1) * 7 + n) * 1024 + lo16);
                }
                SB();
#pragma unroll
                for (int n = 0; n < 7; ++n) va[n] = mfma(wv[i & 1][n], vf[i], va[n]);
                gram = mfma(vf[i], vf[i], gram);
                SB();
            }
            if (t < 3) {
#pragma unroll
                for (int n = 0; n < 7; ++n) STF4(L3_GSC + ((w * 8 + n) * 3 + t) * 64 + qd * 16, va[n]);
                if (qd == 0) STF4(L3_GSC + ((w * 8 + 7) * 3 + t) * 64, gram);
            }
        }
        SB();
        f32x4 acc[2][7];
#pragma unroll
        for (int n = 0; n < 7; ++n) { acc[0][n] = (f32x4){0, 0, 0, 0}; acc[1][n] = (f32x4){0, 0, 0, 0}; }
        u32x4 wf[2][7];
#pragma unroll
        for (int n = 0; n < 7; ++n) wf[0][n] = LD16(n * 1024 + lo16);
        SB();
        LSTAMP(0);
#pragma unroll
        for (int s = 0; s < 20; ++s) {
            int nv = 0;
            if (s + PW < 20) { const unsigned o = WS_UF + (T0 * 20 + wrap20(s + PW + rot3)) * 1024; tk[s + PW][0] = bload16<SC1>(rs, lo16, o); tk[s + PW][1] = bload16<SC1>(rs, lo16, o + 20480); nv += 2; }
            if (s < 16) { w2_ld(s, lo16); nv += 1; }
            if (s >= 16) {
#pragma unroll
                for (int k = 0; k < 3; ++k) w2_ld(16 + (s - 16) * 3 + k, lo16);
                nv += 3;
            }
            if (s == 14 || s == 15) {
#pragma unroll
                for (int e = 0; e < 4; ++e) sp[s - 14][e] = bload16<SC1>(rs, (2 * qd + (e >> 1)) * 512 + (e & 1) * 256 + t * 16, WS_ST + (T0 + s - 14) * 4096);
                nv += 4;
            }
            if (s + 1 < 20) {
#pragma unroll
                for (int n = 0; n < 7; ++n) wf[(s + 1) & 1][n] = LD16(((s + 1) * 7 + n) * 1024 + lo16);
            }
#pragma unroll
            for (int n = 0; n < 7; ++n) { acc[0][n] = mfma(wf[s & 1][n], tk[s][0], acc[0][n]); acc[1][n] = mfma(wf[s & 1][n], tk[s][1], acc[1][n]); }
#pragma unroll
            for (int n = 0; n < 7; ++n) { if (s + 1 < 20) SGB(0x100, 1); SGB(0x8, 2); if (n < nv) SGB(0x20, 1); }
            SB();
            if (s == 2) BAR();
            if (s == 3 && tid < 96) {
                const int n8 = tid / 12, r12 = tid % 12;
                f32x4 a = LDF4(L3_GSC + (n8 * 12 + r12) * 16);
#pragma unroll
                for (int u = 1; u < 4; ++u) a += LDF4(L3_GSC + (u * 96 + n8 * 12 + r12) * 16);
                STF4(L3_GSC + 6144 + (n8 * 12 + r12) * 16, a);
            }
            if (s == 8) BAR();
            SB();
            LSTEP(1 + s);
        }
        STAMP(12);
        float al[2], be[2], gm[2], de[2];
        {
            const f32x4 r0 = LDF4(L3_GSC + 6144 + 84 * 16), r1 = LDF4(L3_GSC + 6144 + 88 * 16);
            const float Gg = r0[0], Ggs = r0[1], Gs = r0[2], Sss = r1[1], Ss = r1[2];
#pragma unroll
            for (int tt = 0; tt < 2; ++tt) {
                const f32x4 a0 = asf4(sp[tt][0]) + asf4(sp[tt][2]), b0 = asf4(sp[tt][1]) + asf4(sp[tt][3]);
                const float s0 = red_add_q(a0[0]), s1 = red_add_q(a0[1]), s2 = red_add_q(a0[2]), s3 = red_add_q(a0[3]), s4v = red_add_q(b0[0]), s5 = red_add_q(b0[1]);
                const float iD = 1.f / 640.f;
                const float m1 = s0 * iD, v1 = fmaxf(s1 * iD - m1 * m1, 0.f), r1v = rsqrtf(v1 + 1e-5f);
                const float m2 = (r1v * (s2 - m1 * Gs) + Ss) * iD;
                const float e2 = (r1v * r1v * (s4v - 2.f * m1 * s3 + m1 * m1 * Gg) + 2.f * r1v * (s5 - m1 * Ggs) + Sss) * iD;
                const float r2v = rsqrtf(fmaxf(e2 - m2 * m2, 0.f) + 1e-5f);
                al[tt] = r1v * r2v; be[tt] = -r1v * r2v * m1; gm[tt] = r2v; de[tt] = -r2v * m2;
            }
        }
        u32x2 hb[2][7];
#pragma unroll
        for (int n = 0; n < 7; ++n) {
#pragma unroll
            for (int k = 0; k < 5; ++k) w2_ld(28 + n * 5 + k, lo16);
            const f32x4 G = LDF4(L3_GSC + 6144 + (n * 12 + qd) * 16), Sv = LDF4(L3_GSC + 6144 + (n * 12 + 4 + qd) * 16), C = LDF4(L3_GSC + 6144 + (n * 12 + 8 + qd) * 16);
            const float bb[4] = {lo_f(b1v[n][0]), hi_f(b1v[n][0]), lo_f(b1v[n][1]), hi_f(b1v[n][1])};
#pragma unroll
            for (int tt = 0; tt < 2; ++tt) {
                float hv[4];
#pragma unroll
                for (int r = 0; r < 4; ++r) {
                    float z = fmaf(gm[tt], Sv[r], bb[r]); z = fmaf(be[tt], G[r], z); z = fmaf(de[tt], C[r], z); z = fmaf(al[tt], acc[tt][n][r], z);
                    const float u = z * fmaf(z * z, -0.044715f * 1.5957691216057308f * 1.4426950408889634f, -1.5957691216057308f * 1.4426950408889634f);
                    hv[r] = z * __builtin_amdgcn_rcpf(1.f + __builtin_amdgcn_exp2f(u));
                }
                hb[tt][n] = (u32x2){pack2(hv[0], hv[1]), pack2(hv[2], hv[3])};
            }
            SGB(0x20, 1); SGB(0x1, 20); SGB(0x20, 1); SGB(0x1, 20); SGB(0x20, 1); SGB(0x1, 20); SGB(0x20, 1); SGB(0x1, 20); SGB(0x20, 1); SGB(0x1, 40);
            SB();
#pragma unroll
            for (int tt = 0; tt < 2; ++tt) {
                const unsigned hbase = WS_HF + (T0 + tt) * 54 * 1024;
                if (n & 1) bstore16<SC1>((u32x4){hb[tt][n - 1][0], hb[tt][n - 1][1], hb[tt][n][0], hb[tt][n][1]}, rs, lo16, hbase + (3 * j3 + (n >> 1)) * 1024);
                if (n == 6 && j3 < 12) bstore8<SC1>(hb[tt][6], rs, lo16 + 8 * (j3 & 1), hbase + (48 + (j3 >> 1)) * 1024);
            }
            SB();
        }
        STAMP(13);
        publish(ws, WS_F3 + (g3 * 16 + j3) * FS, tag);
    }
    STAMP(14);

    {
        LANE_IDS();
        f32x4 gb[5]; u32x2 b2v[5];
#pragma unroll
        for (int f = 63; f < 70; ++f) w2_ld(f, lo16);
        SB();
        if (w == 0) {
            unsigned off[1] = {WS_F3 + ((P2 >> 1) * 16 + (l & 15)) * FS};
            poll_words<1>(ws, off, l < 16, tag);
        }
        BAR();
        STAMP(15);
        constexpr int PD = 3;
        u32x4 tk[14][4];
        const unsigned tokb = __builtin_amdgcn_readfirstlane(WS_HF + (4 * P2 * 54 + s4) * 1024);
        auto issue = [&](int i) {
            const int s = step4(i);
#pragma unroll
            for (int a = 0; a < 4; ++a) tk[i][a] = bload16<SC1>(rs, lo16, tokb + (a * 54 + s) * 1024);
        };
#pragma unroll
        for (int i = 0; i < PD; ++i) issue(i);
        f32x4 acc[4][5];
#pragma unroll
        for (int a = 0; a < 4; ++a)
#pragma unroll
            for (int n = 0; n < 5; ++n) acc[a][n] = (f32x4){0, 0, 0, 0};
        SB();
        LSTAMP(24);
#pragma unroll
        for (int i = 0; i < 14; ++i) {
            if (i < 13 || w < 2) {
                if (i + PD < 14) issue(i + PD);
                if (i == 11) {
#pragma unroll
                    for (int n = 0; n < 5; ++n) {
                        const int f = 80 * c2 + 16 * n;
                        gb[n] = asf4(bload16<SC1>(rs, qd * 16, WS_MODV + (3200 + f) * 4));
                        b2v[n] = *(const GAS u32x2*)(p.b2 + f + 4 * qd);
                    }
                }
#pragma unroll
                for (int a = 0; a < 4; ++a)
#pragma unroll
                    for (int n = 0; n < 5; ++n) acc[a][n] = mfma(w2f[i][n], tk[i][a], acc[a][n]);
#pragma unroll
                for (int a = 0; a < 4; ++a) { SGB(0x8, 2); if (i == 11) SGB(0x20, 3); else SGB(0x20, 1); SGB(0x8, 3); }
            }
            SB();
            LSTEP(25 + i);
        }
        STAMP(16);
#pragma unroll
        for (int a = 0; a < 4; ++a) {
            if (a != w) {
                const int src = w < a ? w : w - 1;
#pragma unroll
                for (int n = 0; n < 5; ++n) STF4(((a * 3 + src) * 5 + n) * 1024 + lo16, acc[a][n]);
            }
        }
        BAR();
        constexpr int L4_OUT = 61440;
#pragma unroll
        for (int n = 0; n < 5; ++n) {
            f32x4 v = w == 0 ? acc[0][n] : w == 1 ? acc[1][n] : w == 2 ? acc[2][n] : acc[3][n];
#pragma unroll
            for (int u = 0; u < 3; ++u) v += LDF4(((w * 3 + u) * 5 + n) * 1024 + lo16);
            const float x1[4] = {lo_f(x1b[n][0]), hi_f(x1b[n][0]), lo_f(x1b[n][1]), hi_f(x1b[n][1])};
            const float bv[4] = {lo_f(b2v[n][0]), hi_f(b2v[n][0]), lo_f(b2v[n][1]), hi_f(b2v[n][1])};
            float o[4];
#pragma unroll
            for (int r = 0; r < 4; ++r) o[r] = fmaf(gb[n][r], v[r] + bv[r], x1[r]);
            ST8(L4_OUT + (16 * w + t) * 176 + n * 32 + qd * 8, ((u32x2){pack2(o[0], o[1]), pack2(o[2], o[3])}));
        }
        asm volatile("s_waitcnt lgkmcnt(0)" ::: "memory");
#pragma unroll
        for (int k = 0; k < 3; ++k) {
            const int ch = l + 64 * k;
            if (ch < 160) {
                const int row = ch / 10, cc = ch % 10;
                const u32x4 v = LD16(L4_OUT + (16 * w + row) * 176 + cc * 16);
                *(GAS u32x4*)(p.out + (size_t)(16 * (4 * P2 + w) + row) * 640 + 80 * c2 + cc * 8) = v;
            }
        }
    }
    STAMP(17);
    LSTAMP_DUMP;
    asm volatile(".Ladaln_end:" ::: "memory");
}

extern "C" void kernel_launch(void* const* d_in, const int* in_sizes, int n_in, void* d_out, int out_size, void* d_ws, size_t ws_size, hipStream_t stream) {
    Params p;
    p.x = (const u16*)d_in[0]; p.wqkv = (const u16*)d_in[1]; p.wo = (const u16*)d_in[2]; p.w1 = (const u16*)d_in[4]; p.b1 = (const u16*)d_in[5];
    p.w2 = (const u16*)d_in[6]; p.b2 = (const u16*)d_in[7]; p.c = (const u16*)d_in[8]; p.wm = (const u16*)d_in[9];
    p.out = (u16*)d_out; p.ws = (char*)d_ws;
    (void)hipFuncSetAttribute((const void*)adaLN_block_kernel, hipFuncAttributeMaxDynamicSharedMemorySize, LDS_BYTES);
    adaLN_block_kernel<<<256, 256, LDS_BYTES, stream>>>(p);
}
```

```cpp
#define DIAG 100
#define DIAGB 128
#include <hip/hip_runtime.h>
#include <stdint.h>

typedef unsigned short u16;
typedef float f32x4 __attribute__((ext_vector_type(4)));
typedef float f32x2 __attribute__((ext_vector_type(2)));
typedef __bf16 bf16x8 __attribute__((ext_vector_type(8)));
typedef __bf16 bf16x2 __attribute__((ext_vector_type(2)));
typedef unsigned u32x4 __attribute__((ext_vector_type(4)));
typedef unsigned u32x2 __attribute__((ext_vector_type(2)));
typedef short s16x4 __attribute__((ext_vector_type(4)));
typedef __amdgpu_buffer_rsrc_t rsrc_t;
#define LAS __attribute__((address_space(3)))
#define GAS __attribute__((address_space(1)))
#define DI __device__ __forceinline__

static constexpr unsigned FS = 64;
static constexpr unsigned WS_EPOCH = 0;
static constexpr unsigned WS_TMO   = 256;
static constexpr unsigned WS_FA    = 1024;
static constexpr unsigned WS_FC    = WS_FA + 160 * FS;
static constexpr unsigned WS_FD    = WS_FC + 128 * FS;
static constexpr unsigned WS_F1    = WS_FD + 1024;
static constexpr unsigned WS_F2    = WS_F1 + 256 * FS;
static constexpr unsigned WS_F3    = WS_F2 + 256 * FS;
static constexpr unsigned WS_FEND  = WS_F3 + 256 * FS;
static constexpr unsigned WS_MODV = 131072;
static constexpr unsigned WS_BP   = WS_MODV + 16384;
static constexpr unsigned WS_XNF  = WS_BP + 20 * 16 * 128 * 4;
static constexpr unsigned WS_WQF  = WS_XNF + 128 * 20 * 1024;
static constexpr unsigned WS_WOF  = WS_WQF + 16 * 20 * 8 * 1024;
static constexpr unsigned WS_W1F  = WS_WOF + 8 * 20 * 5 * 1024;
static constexpr unsigned WS_W2F  = WS_W1F + 108 * 20 * 1024;
static constexpr unsigned WS_OF   = WS_W2F + 8 * 54 * 5 * 1024;
static constexpr unsigned WS_UF   = WS_OF + 128 * 20 * 1024;
static constexpr unsigned WS_ST   = WS_UF + 128 * 20 * 1024;
static constexpr unsigned WS_HF   = WS_ST + 128 * 8 * 512;
static constexpr unsigned WS_END  = WS_HF + 128 * 54 * 1024;
static_assert(WS_FEND <= WS_MODV, "flags overflow");

#define B_DELAY 0
#define ROT1 0
#define ROT2 0
#define ROT3 0
#define ROT4 0
static constexpr int LDS_BYTES = 163840;
static constexpr int SC1 = 16;
static constexpr unsigned SPIN_CAP = 40000;

#define STAMP(i) do { if ((i) == 0) diag_t0 = __builtin_amdgcn_s_memrealtime(); if ((i) == DIAG) diag_t1 = __builtin_amdgcn_s_memrealtime(); } while (0)
#define LSTAMP(i)
#define LSTEP(i)
#define LSTAMP_DECL
#define LSTAMP_DUMP do { if (blockIdx.x == DIAGB) { if (DIAG == 100) { diag_t1 = diag_t0; diag_t0 = __hip_atomic_load((const GAS unsigned long long*)(p.ws + 512), __ATOMIC_RELAXED, __HIP_MEMORY_SCOPE_AGENT); if (diag_t0 > diag_t1) diag_t0 = diag_t1; } const unsigned long long te = __builtin_amdgcn_s_memrealtime() + 20 * (diag_t1 - diag_t0); while (__builtin_amdgcn_s_memrealtime() < te) __builtin_amdgcn_s_sleep(4); } } while (0)

struct Params {
    const u16* x; const u16* wqkv; const u16* wo; const u16* w1; const u16* b1; const u16* w2; const u16* b2; const u16* c; const u16* wm;
    u16* out; char* ws;
};

DI float lo_f(unsigned d) { return __uint_as_float(d << 16); }
DI float hi_f(unsigned d) { return __uint_as_float(d & 0xffff0000u); }
DI unsigned pack2(float a, float b) { f32x2 v = {a, b}; return __builtin_bit_cast(unsigned, __builtin_convertvector(v, bf16x2)); }
DI float rbf(float a) { return lo_f(pack2(a, 0.f)); }
DI u32x2 pack4(const f32x4& v) { return (u32x2){pack2(v[0], v[1]), pack2(v[2], v[3])}; }
DI f32x4 mfma(u32x4 a, u32x4 b, f32x4 c) { return __builtin_amdgcn_mfma_f32_16x16x32_bf16(__builtin_bit_cast(bf16x8, a), __builtin_bit_cast(bf16x8, b), c, 0, 0, 0); }
DI float red_add_q(float v) {
    auto a = __builtin_amdgcn_permlane16_swap(__float_as_uint(v), __float_as_uint(v), false, false);
    v = __uint_as_float(a[0]) + __uint_as_float(a[1]);
    auto b = __builtin_amdgcn_permlane32_swap(__float_as_uint(v), __float_as_uint(v), false, false);
    return __uint_as_float(b[0]) + __uint_as_float(b[1]);
}
DI float red_max_q(float v) {
    auto a = __builtin_amdgcn_permlane16_swap(__float_as_uint(v), __float_as_uint(v), false, false);
    v = fmaxf(__uint_as_float(a[0]), __uint_as_float(a[1]));
    auto b = __builtin_amdgcn_permlane32_swap(__float_as_uint(v), __float_as_uint(v), false, false);
    return fmaxf(__uint_as_float(b[0]), __uint_as_float(b[1]));
}
template <int C> DI float dpp_add(float v) { return v + __builtin_bit_cast(float, __builtin_amdgcn_update_dpp(0, __builtin_bit_cast(int, v), C, 0xf, 0xf, true)); }
DI float red16(float v) { v = dpp_add<0xB1>(v); v = dpp_add<0x4E>(v); v = dpp_add<0x141>(v); return dpp_add<0x140>(v); }
DI unsigned swap32(unsigned v) { auto a = __builtin_amdgcn_permlane32_swap(v, v, false, false); return (threadIdx.x & 32) ? a[0] : a[1]; }
DI rsrc_t mk_rsrc(const void* p, unsigned bytes) { return __builtin_amdgcn_make_buffer_rsrc((void*)p, 0, bytes, 0x00020000); }
template <int AUX> DI u32x4 bload16(rsrc_t r, unsigned voff, unsigned soff) { return __builtin_bit_cast(u32x4, __builtin_amdgcn_raw_buffer_load_b128(r, voff, soff, AUX)); }
template <int AUX> DI u32x2 bload8(rsrc_t r, unsigned voff, unsigned soff) { return __builtin_bit_cast(u32x2, __builtin_amdgcn_raw_buffer_load_b64(r, voff, soff, AUX)); }
template <int AUX> DI unsigned bload4(rsrc_t r, unsigned voff, unsigned soff) { return __builtin_amdgcn_raw_buffer_load_b32(r, voff, soff, AUX); }
template <int AUX> DI void bstore16(u32x4 v, rsrc_t r, unsigned voff, unsigned soff) { __builtin_amdgcn_raw_buffer_store_b128(v, r, voff, soff, AUX); }
template <int AUX> DI void bstore8(u32x2 v, rsrc_t r, unsigned voff, unsigned soff) { __builtin_amdgcn_raw_buffer_store_b64(v, r, voff, soff, AUX); }
template <int AUX> DI void bstore4(unsigned v, rsrc_t r, unsigned voff, unsigned soff) { __builtin_amdgcn_raw_buffer_store_b32(v, r, voff, soff, AUX); }
DI f32x4 asf4(u32x4 v) { return __builtin_bit_cast(f32x4, v); }
DI u32x4 asu4(f32x4 v) { return __builtin_bit_cast(u32x4, v); }
#define LANE_IDS() int tz_ = threadIdx.x; asm volatile("" : "+v"(tz_)); const int tid = tz_, l = tid & 63, t = l & 15, qd = l >> 4; const unsigned lo16 = l * 16; (void)t; (void)qd; (void)lo16
#define SB() __builtin_amdgcn_sched_barrier(0)
#define BAR() asm volatile("s_waitcnt lgkmcnt(0)\n\ts_barrier" ::: "memory")
#define DRAIN() asm volatile("s_waitcnt vmcnt(0)" ::: "memory")
#define SGB(mask, n) __builtin_amdgcn_sched_group_barrier(mask, n, 0)
#define LD16(off) (*(const LAS u32x4*)((const LAS char*)lds + (off)))
#define LD8(off) (*(const LAS u32x2*)((const LAS char*)lds + (off)))
#define LDF4(off) (*(const LAS f32x4*)((const LAS char*)lds + (off)))
#define LDF(off) (*(const LAS float*)((const LAS char*)lds + (off)))
#define ST16(off, v) (*(LAS u32x4*)((LAS char*)lds + (off)) = (v))
#define ST8(off, v) (*(LAS u32x2*)((LAS char*)lds + (off)) = (v))
#define STF4(off, v) (*(LAS f32x4*)((LAS char*)lds + (off)) = (v))
#define STF(off, v) (*(LAS float*)((LAS char*)lds + (off)) = (v))
DI u32x2 tr_read(const char* lds, unsigned off) { return __builtin_bit_cast(u32x2, __builtin_amdgcn_ds_read_tr16_b64_v4i16((LAS s16x4*)((LAS char*)lds + off))); }

DI void flag_store(char* ws, unsigned off, unsigned tag) { __hip_atomic_store((GAS unsigned*)(ws + off), tag, __ATOMIC_RELAXED, __HIP_MEMORY_SCOPE_AGENT); }
DI unsigned flag_load(const char* ws, unsigned off) { return __hip_atomic_load((const GAS unsigned*)(ws + off), __ATOMIC_RELAXED, __HIP_MEMORY_SCOPE_AGENT); }
DI void publish(char* ws, unsigned off, unsigned tag) { DRAIN(); BAR(); if (threadIdx.x == 0) flag_store(ws, off, tag); }
template <int N> DI void poll_words(char* ws, const unsigned (&off)[N], bool active, unsigned tag) {
    for (unsigned spins = 0;; ++spins) {
        bool ok = true;
#pragma unroll
        for (int k = 0; k < N; ++k) ok &= flag_load(ws, off[k]) == tag;
        if (__all(ok || !active)) break;
        if (spins > SPIN_CAP) { if ((threadIdx.x & 63) == 0) flag_store(ws, WS_TMO, tag); break; }
        __builtin_amdgcn_s_sleep(1);
    }
}

DI void check_all_blocks(char* ws, rsrc_t rs, unsigned base, u32x4 v, unsigned tag) {
    asm volatile("" : "+v"(v));
    for (unsigned spins = 0; !__all(v[0] == tag && v[1] == tag && v[2] == tag && v[3] == tag); ++spins) {
        if (spins > SPIN_CAP) { if ((threadIdx.x & 63) == 0) flag_store(ws, WS_TMO, tag); break; }
        __builtin_amdgcn_s_sleep(1);
        v = bload16<SC1>(rs, (threadIdx.x & 63) * 16, base);
        asm volatile("" : "+v"(v) :: "memory");
    }
}

DI void rows_wo(int s, int qd, int& ra, int& rb) { if (s < 16) { ra = 40 * s + 4 * qd; rb = ra + 20; } else { ra = 40 * (4 * (s - 16) + qd) + 16; rb = ra + 20; } }
DI void tiles_p3(int s, int& ta, int& tb) { if (s < 16) { ta = 5 * (s >> 1) + 2 * (s & 1); tb = ta + 1; } else { ta = 10 * (s - 16) + 4; tb = ta + 5; } }
DI void tiles_p4(int s, int& ta, int& tb) { if (s < 48) { int j = s / 3, pr = s % 3; int st = j < 12 ? 7 * j : 84 + 6 * (j - 12); ta = st + 2 * pr; tb = ta + 1; } else { int m = s - 48; ta = 14 * m + 6; tb = ta + 7; } }

static constexpr int L0_SC = 81920, L0_GS = 83200, L0_CRAW = 83968, L0_RW = 86016, L0_D = 101376, L0_XI = 134144, L0_XS = 156160;

template <int AUX> DI void dma(char* lds, unsigned lds_off, rsrc_t r, unsigned voff, unsigned soff) {
    __builtin_amdgcn_raw_ptr_buffer_load_lds(r, (LAS void*)((LAS char*)lds + lds_off), 16, voff, soff, 0, AUX);
}
DI void dma16(char* lds, unsigned lds_off, rsrc_t r, unsigned voff, unsigned soff) {
    const unsigned dst = (unsigned)(size_t)(LAS char*)lds + lds_off;
    asm volatile("s_mov_b32 m0, %0\n\ts_nop 0\n\tbuffer_load_dwordx4 %1, %2, %3 offen lds" :: "s"(dst), "v"(voff), "s"(r), "s"(soff) : "memory");
}
DI void dma16s(char* lds, unsigned lds_off, rsrc_t r, unsigned voff, unsigned soff) { dma<SC1>(lds, lds_off, r, voff, soff); }
#define WAITV(n) asm volatile("s_waitcnt vmcnt(%0)" :: "n"(n) : "memory")

struct DTask { unsigned dst; int kind, r5; };
DI DTask dtask_issue(const Params& p, char* lds, unsigned scr, int id, int w, int l) {
    DTask d; int ra0, rb0, rstep, cg; unsigned pitch; rsrc_t r;
    if (id < 370) {
        const bool o = id < 100; const int i2 = o ? id : id - 100, s = i2 / 5; cg = i2 - 5 * s; pitch = 1280;
        if (o) { r = mk_rsrc(p.wo, 640 * 1280); if (s < 16) { ra0 = 40 * s; rstep = 4; } else { ra0 = 160 * (s - 16) + 16; rstep = 40; } rb0 = ra0 + 20; }
        else { r = mk_rsrc(p.w2, 1728 * 1280); int ta, tb; tiles_p4(s, ta, tb); ra0 = 16 * ta; rb0 = 16 * tb; rstep = 4; }
        const int c0 = (8 * cg) / 5;
        d.kind = 0; d.r5 = 8 * cg - 5 * c0; d.dst = (o ? WS_WOF + (c0 * 20 + s) * 5120 : WS_W2F + (c0 * 54 + s) * 5120) | (o ? 0u : 1u);
    } else {
        const int i2 = id < 650 ? id - 370 : 0, s = i2 / 14; cg = i2 - 14 * s; pitch = 3456; r = mk_rsrc(p.w1, 640 * 3456);
        int ta, tb; tiles_p3(s, ta, tb); ra0 = 16 * ta; rb0 = 16 * tb; rstep = 4;
        d.kind = id < 650 ? 1 : 2; d.r5 = cg == 13 ? 4 : 8; d.dst = WS_W1F + (8 * cg * 20 + s) * 1024;
    }
    const int q = l >> 4, cp = l & 15;
    const unsigned v0 = q * pitch + ((cp ^ (q << 1)) * 16), v1 = q * pitch + ((cp ^ ((4 + q) << 1)) * 16);
    const unsigned sa = ra0 * pitch + cg * 256, sb = rb0 * pitch + cg * 256, st = rstep * pitch;
    if (d.kind != 2) {
#pragma unroll
        for (int i = 0; i < 8; ++i) dma16(lds, scr + w * 8192 + i * 1024, r, (i & 1) ? v1 : v0, (i < 4 ? sa : sb) + (i & 3) * st);
    }
    return d;
}
struct DOut { unsigned dst[8]; u32x4 f[8]; };
DI void dtask_read(const DTask& d, DOut& o, char* lds, unsigned scr, int w, int l) {
    const int qd = l >> 4, q = (l >> 2) & 3, pp = l & 3, r7 = 4 * (qd & 1) + q;
    const unsigned a0 = scr + w * 8192 + (4 * qd + q) * 256 + pp * 8;
    const bool k0 = d.kind == 0;
    const unsigned r5 = k0 ? d.r5 : 0, stride = k0 ? 1024 : 20480, jump = k0 ? ((d.dst & 1u) ? 54 * 5120 - 5120 : 20 * 5120 - 5120) : 0;
    const int lim = k0 ? 8 : (d.kind == 1 ? d.r5 : 0);
    const unsigned d0 = (d.dst & ~1u) + r5 * 1024;
#pragma unroll
    for (int nt = 0; nt < 8; ++nt) {
        const unsigned v = d0 + nt * stride + ((r5 + nt >= 5 ? 1u : 0u) + (r5 + nt >= 10 ? 1u : 0u)) * jump;
        o.dst[nt] = __builtin_amdgcn_readfirstlane(nt < lim ? v : 0x7ff00000u);
        const u32x2 ua = tr_read(lds, a0 + ((nt ^ r7) * 32)), ub = tr_read(lds, a0 + 4096 + ((nt ^ r7) * 32));
        o.f[nt] = (u32x4){ua[0], ua[1], ub[0], ub[1]};
    }
}
template <int NT> DI void dtask_frag(const DOut& o, rsrc_t rs, int l) { bstore16<SC1>(o.f[NT], rs, l * 16, o.dst[NT]); }

DI void wm_dma(const Params& p, char* lds, int w, int l, unsigned colA, unsigned colB, int rot) {
    const rsrc_t rw = mk_rsrc(p.wm, 640 * 3840 * 2);
    const int rl = l >> 3, cp = l & 7;
    const unsigned sg = ((rl >> 1) & 1);
    const unsigned c0 = cp ^ (sg << 1), c1 = cp ^ ((sg | 2) << 1);
    const unsigned v0 = rl * 7680 + (c0 < 4 ? colA + 8 * c0 : colB + 8 * (c0 - 4)) * 2, v1 = rl * 7680 + (c1 < 4 ? colA + 8 * c1 : colB + 8 * (c1 - 4)) * 2;
#pragma unroll
    for (int i = 0; i < 20; ++i) {
        int piece = 4 * i + w + rot; piece = piece >= 80 ? piece - 80 : piece;
        dma16(lds, piece * 1024, rw, (piece & 1) ? v1 : v0, piece * 8 * 7680);
    }
}
DI void c_dma(const Params& p, char* lds, int w, int l) {
    dma16(lds, L0_CRAW + (w & 1) * 1024, mk_rsrc(p.c, 1280), ((w & 1) * 64 + l) * 16, 0);
}
DI float mod_gemv(const Params& p, char* lds, int w, int l, int tid, int did, DTask& dt) {
    WAITV(20);
    if (tid < 80) {
        const u32x4 creg = LD16(L0_CRAW + tid * 16);
        u32x4 o;
#pragma unroll
        for (int d = 0; d < 4; ++d) { const float a = lo_f(creg[d]), b = hi_f(creg[d]); o[d] = pack2(a * __builtin_amdgcn_rcpf(1.f + __expf(-a)), b * __builtin_amdgcn_rcpf(1.f + __expf(-b))); }
        ST16(L0_SC + tid * 16, o);
    }
    WAITV(0);
    dt = dtask_issue(p, lds, L0_D, did, w, l);
    BAR();
    const int qd = l >> 4, q = (l >> 2) & 3, pp = l & 3;
    f32x4 acc = {0, 0, 0, 0}, acc2 = {0, 0, 0, 0};
    const int r0 = 8 * qd + q;
    const unsigned sg = ((r0 >> 1) & 1) | (((r0 >> 3) & 1) << 1);
    const unsigned a0 = r0 * 128 + ((w ^ sg) * 32) + pp * 8;
#pragma unroll
    for (int s = 0; s < 20; s += 2) {
        const u32x2 ba = tr_read(lds, a0 + s * 4096), bb = tr_read(lds, a0 + s * 4096 + 512);
        const u32x2 bc = tr_read(lds, a0 + s * 4096 + 4096), bd = tr_read(lds, a0 + s * 4096 + 4608);
        acc = mfma(LD16(L0_SC + s * 64 + qd * 16), (u32x4){ba[0], ba[1], bb[0], bb[1]}, acc);
        acc2 = mfma(LD16(L0_SC + s * 64 + 64 + qd * 16), (u32x4){bc[0], bc[1], bd[0], bd[1]}, acc2);
    }
    return rbf(acc[0] + acc2[0]);
}
DI void x_dma(const Params& p, char* lds, unsigned img, int T, int w, int l) {
    const rsrc_t rx = mk_rsrc(p.x, 2048 * 1280);
    const int r4 = l >> 4, row = 4 * w + r4;
    const unsigned voff = row * 1280 + (((l & 15) ^ row) * 16);
#pragma unroll
    for (int cb = 0; cb < 5; ++cb) dma16(lds, img + (w * 5 + cb) * 1024, rx, voff, T * 16 * 1280 + cb * 256);
}
DI void x_stats(char* lds, unsigned img, unsigned stat, int w, int l) {
    u32x4 v[5];
#pragma unroll
    for (int cb = 0; cb < 5; ++cb) v[cb] = LD16(img + (w * 5 + cb) * 1024 + l * 16);
    float sum = 0.f;
#pragma unroll
    for (int i = 0; i < 5; ++i)
#pragma unroll
        for (int d = 0; d < 4; ++d) sum += lo_f(v[i][d]) + hi_f(v[i][d]);
    sum = red16(sum);
    const float mean = sum * (1.f / 640.f);
    float vs = 0.f;
#pragma unroll
    for (int i = 0; i < 5; ++i)
#pragma unroll
        for (int d = 0; d < 4; ++d) { const float a = lo_f(v[i][d]) - mean, c2 = hi_f(v[i][d]) - mean; vs += a * a + c2 * c2; }
    vs = red16(vs);
    const float rstd = rsqrtf(vs * (1.f / 640.f) + 1e-5f);
    if ((l & 15) == 0) { STF(stat + (4 * w + (l >> 4)) * 8, mean); STF(stat + (4 * w + (l >> 4)) * 8 + 4, rstd); }
}
DI unsigned ximg_off(int t, int byte_in_row) { return ((t >> 2) * 5 + (byte_in_row >> 8)) * 1024 + (t & 3) * 256 + ((((byte_in_row >> 4) & 15) ^ t) * 16) + (byte_in_row & 15); }
DI void x_frags(char* lds, unsigned img, unsigned stat, rsrc_t rs, int T, int w, int l) {
    const int t = l & 15, qd = l >> 4;
    const float mean = LDF(stat + t * 8), rstd = LDF(stat + t * 8 + 4);
#pragma unroll
    for (int i = 0; i < 5; ++i) {
        const int s = w + 4 * i;
        const u32x4 v = LD16(img + ximg_off(t, s * 64 + qd * 16));
        u32x4 o;
#pragma unroll
        for (int d = 0; d < 4; ++d) o[d] = pack2((lo_f(v[d]) - mean) * rstd, (hi_f(v[d]) - mean) * rstd);
        bstore16<SC1>(o, rs, l * 16, WS_XNF + (T * 20 + s) * 1024);
    }
}
struct TileKV { u32x2 ka0, ka1, k2, va0, va1, z; };
DI void attn_unit(const u32x2 (&qa)[2], const u32x2& xq, const TileKV& cur, const TileKV& prev, bool prev_ok, int t, int qd, u32x4& omain, u32x2& oleft) {
    const float cs = 0.22360679774997896f * 1.4426950408889634f;
    f32x4 oa[2], oz[2];
#pragma unroll
    for (int h = 0; h < 2; ++h) {
        const u32x4 qf = {qa[h][0], qa[h][1], qd == h ? xq[0] : 0u, qd == h ? xq[1] : 0u};
        const u32x2 kcp = h == 0 ? cur.ka0 : cur.ka1, kpp = h == 0 ? prev.ka0 : prev.ka1;
        const f32x4 z4 = {0.f, 0.f, 0.f, 0.f};
        f32x4 sp = mfma((u32x4){kpp[0], kpp[1], prev.k2[0], prev.k2[1]}, qf, z4);
        f32x4 sc = mfma((u32x4){kcp[0], kcp[1], cur.k2[0], cur.k2[1]}, qf, z4);
        float e[8]; float mx = -3.0e38f;
#pragma unroll
        for (int r = 0; r < 4; ++r) {
            const int dp = t + 16 - 4 * qd - r, dc = t - 4 * qd - r;
            const bool vp = prev_ok && dp <= 14 && !(dp & 1), vc = dc >= 0 && !(dc & 1);
            e[r] = vp ? sp[r] * cs : -3.0e38f; e[4 + r] = vc ? sc[r] * cs : -3.0e38f;
            mx = fmaxf(mx, fmaxf(e[r], e[4 + r]));
        }
        mx = red_max_q(mx);
        float sum = 0.f;
#pragma unroll
        for (int r = 0; r < 8; ++r) { e[r] = __builtin_amdgcn_exp2f(e[r] - mx); sum += e[r]; }
        sum = red_add_q(sum);
        const float inv = __builtin_amdgcn_rcpf(sum);
        const u32x4 pf = {pack2(e[0] * inv, e[1] * inv), pack2(e[2] * inv, e[3] * inv), pack2(e[4] * inv, e[5] * inv), pack2(e[6] * inv, e[7] * inv)};
        const u32x2 vc2 = h == 0 ? cur.va0 : cur.va1, vp2 = h == 0 ? prev.va0 : prev.va1;
        oa[h] = mfma((u32x4){vp2[0], vp2[1], vc2[0], vc2[1]}, pf, z4);
        oz[h] = mfma((u32x4){prev.z[0], prev.z[1], cur.z[0], cur.z[1]}, pf, z4);
    }
    const u32x2 a0 = pack4(oa[0]), a1 = pack4(oa[1]);
    omain = (u32x4){a0[0], a0[1], a1[0], a1[1]};
    const u32x2 l0 = pack4(oz[0]), l1 = pack4(oz[1]);
    oleft = qd == 0 ? l0 : l1;
}

__global__ __launch_bounds__(256, 1) void adaLN_block_kernel(Params p) {
    extern __shared__ __attribute__((aligned(16))) char lds[];
    const int b = blockIdx.x, w = __builtin_amdgcn_readfirstlane(threadIdx.x >> 6);
    const int xcd = b & 7, bi = b >> 3;
    char* ws = p.ws;
    const rsrc_t rs = mk_rsrc(ws, WS_END);
    unsigned long long diag_t0 = 0, diag_t1 = 0;
    STAMP(0);
    if (DIAG == 100 && blockIdx.x == 0 && threadIdx.x == 0) __hip_atomic_store((GAS unsigned long long*)(p.ws + 512), diag_t0, __ATOMIC_RELAXED, __HIP_MEMORY_SCOPE_AGENT);
    LSTAMP_DECL;
    {
        unsigned long long pc; unsigned len;
        asm volatile("s_getpc_b64 %0\n.Ladaln_pc:\n\ts_mov_b32 %1, .Ladaln_end-.Ladaln_pc" : "=s"(pc), "=s"(len));
        unsigned off = (bi * 256 + threadIdx.x) * 64u;
        off = off < len ? off : 0u;
        const unsigned long long a = pc + off;
        const unsigned dst = (unsigned)(size_t)(LAS char*)lds + LDS_BYTES - 1024 + w * 256;
        asm volatile("s_mov_b32 m0, %0\n\ts_nop 0\n\tglobal_load_lds_dword %1, off" :: "s"(dst), "v"(a) : "memory");
    }
    const unsigned epoch = *(const __attribute__((address_space(4))) unsigned*)(size_t)(ws + WS_EPOCH);
    unsigned tag;
#define MAKE_TAG() do { unsigned e_ = epoch; asm volatile("" : "+s"(e_)); tag = e_ + 0x9E3779B9u; } while (0)

    DOut dout;
    {
        LANE_IDS();
        const int nA = xcd < 4 ? 24 : 16;
        const int did = w == 0 ? 1000 : b * 3 + w - 1;
        DTask dt;
        if (bi < nA) {
            const int s = xcd + 8 * (bi >> 3), jg = bi & 7;
            c_dma(p, lds, w, l);
            {
                const rsrc_t rq = mk_rsrc(p.wqkv, 640 * 3840);
#pragma unroll
                for (int m = 0; m < 4; ++m) {
                    const int n = 64 * (4 * m + w) + l, rho = n / 30, c = n % 30;
                    const int k = (rho & 19) | ((rho & 4) << 1) | ((rho & 8) >> 1);
                    if (4 * m + w < 15) dma16(lds, L0_RW + (4 * m + w) * 1024, rq, k * 3840 + (c / 10) * 1280 + jg * 160 + (c % 10) * 16, s * 32 * 3840);
                }
            }
            wm_dma(p, lds, w, l, 640 + 32 * s, 32 * s, 10 * jg);
            MAKE_TAG();
            STAMP(18);
            const float v = mod_gemv(p, lds, w, l, tid, did, dt);
            STAMP(19);
            const float gv = w < 2 ? rbf(1.f + v) : v;
            if (l < 16) { STF(L0_GS + (w * 16 + l) * 4, gv); *(LAS u16*)((LAS char*)lds + L0_GS + 256 + (w * 16 + l) * 2) = (u16)(__float_as_uint(gv) >> 16); }
            BAR();
            const f32x4 g0 = LDF4(L0_GS + qd * 32), g1 = LDF4(L0_GS + qd * 32 + 16);
            const u32x4 sfr = LD16(L0_GS + 256 + 64 + qd * 16);
            const int q = (l >> 2) & 3, pp = l & 3, jj = w >> 1, j = 2 * jg + jj;
            const unsigned rbase = L0_RW + (16 * (qd >> 1) + 4 * (qd & 1) + q) * 480 + 80 * jj;
#pragma unroll
            for (int i = 0; i < 4; ++i) {
                const int nt = 4 * (w & 1) + i;
                unsigned po;
                if (nt < 4) po = (nt >> 1) * 160 + (nt & 1) * 40 + 8 * pp;
                else if (nt == 4) po = (pp >> 1) * 160 + (pp & 1) * 40 + 32;
                else if (nt < 7) po = 320 + (nt - 5) * 40 + 8 * pp;
                else po = 320 + (pp & 1) * 40 + 32;
                u32x2 ua = tr_read(lds, rbase + po), ub = tr_read(lds, rbase + po + 8 * 480);
                if (nt == 7 && t >= 8) { ua = (u32x2){0u, 0u}; ub = ua; }
                const f32x4 z4 = {0, 0, 0, 0};
                const f32x4 bias = mfma((u32x4){ua[0], ua[1], ub[0], ub[1]}, sfr, z4);
                const u32x4 o = {pack2(lo_f(ua[0]) * g0[0], hi_f(ua[0]) * g0[1]), pack2(lo_f(ua[1]) * g0[2], hi_f(ua[1]) * g0[3]),
                                 pack2(lo_f(ub[0]) * g1[0], hi_f(ub[0]) * g1[1]), pack2(lo_f(ub[1]) * g1[2], hi_f(ub[1]) * g1[3])};
                bstore16<SC1>(o, rs, lo16, WS_WQF + ((j * 20 + s) * 8 + nt) * 1024);
                if (t == 0) bstore16<SC1>(asu4(bias), rs, qd * 16, WS_BP + ((s * 16 + j) * 128 + nt * 16) * 4);
            }
            STAMP(20);
            publish(ws, WS_FA + (jg * 20 + s) * FS, tag);
        } else if (bi < nA + 5) {
            const int idxB = xcd * 5 + (bi - nA);
            for (int i = 0; i < B_DELAY; ++i) __builtin_amdgcn_s_sleep(8);
            c_dma(p, lds, w, l);
            wm_dma(p, lds, w, l, 1280 + 64 * idxB, 1280 + 64 * idxB + 32, 0);
            MAKE_TAG();
            STAMP(18);
            float v = mod_gemv(p, lds, w, l, tid, did, dt);
            STAMP(19);
            const int col = 1280 + 64 * idxB + 16 * w + t;
            if (col >= 2560 && col < 3200) v = rbf(1.f + v);
            if (l < 16) bstore4<SC1>(__float_as_uint(v), rs, l * 4, WS_MODV + (1280 + 64 * idxB + 16 * w) * 4);
        } else {
            const int idx = xcd < 4 ? xcd * 3 + (bi - 29) : 12 + (xcd - 4) * 11 + (bi - 21);
            const int Ts = idx < 16 ? 3 * idx : 48 + 2 * (idx - 16), nT = idx < 16 ? 3 : 2;
#pragma unroll
            for (int k = 0; k < 3; ++k) x_dma(p, lds, k * 20480, Ts + (k < nT ? k : 0), w, l);
            MAKE_TAG();
            STAMP(18);
            WAITV(10); x_stats(lds, 0, L0_XS, w, l);
            WAITV(5); x_stats(lds, 20480, L0_XS + 128, w, l);
            WAITV(0);
            dt = dtask_issue(p, lds, L0_D, did, w, l);
            x_stats(lds, 40960, L0_XS + 256, w, l);
            BAR();
            STAMP(19);
#pragma unroll
            for (int k = 0; k < 3; ++k) if (k < nT) x_frags(lds, k * 20480, L0_XS + k * 128, rs, Ts + k, w, l);
            DRAIN(); BAR();
            if (tid < nT) flag_store(ws, WS_FC + (Ts + tid) * FS, tag);
            STAMP(20);
        }
        if (bi >= nA && bi < nA + 5) WAITV(0);
        if (w > 0) dtask_read(dt, dout, lds, L0_D, w, l);
        else {
#pragma unroll
            for (int i = 0; i < 8; ++i) { dout.dst[i] = 0x7ff00000u; dout.f[i] = (u32x4){0u, 0u, 0u, 0u}; }
        }
        STAMP(1);
    }

    const int g1 = 4 * (xcd >> 1) + (bi >> 3), j1 = 8 * (xcd & 1) + (bi & 7);
    const int P2 = 4 * xcd + (bi >> 3), c2 = bi & 7;
    const int g3 = g1, j3 = j1;
    const int rot2 = ROT2, rot3 = ROT3;
    auto wrap20 = [](int x) { return x >= 20 ? x - 20 : x; };
    const int t3 = j3 < 12 ? 7 * j3 : 84 + 6 * (j3 - 12), cnt3 = j3 < 12 ? 7 : 6;
    constexpr int L2_WO = 61440, L2_X = 46080;
    {
        LANE_IDS();
        const int T0 = 8 * g1 + 2 * w, TH = g1 == 0 ? 0 : 8 * g1 - 1;
        const unsigned xb0 = WS_XNF + T0 * 20480, xbh = WS_XNF + TH * 20480 + (w >> 1) * 1024, wb = WS_WQF + j1 * 163840 + w * 4096;
        const unsigned wob = WS_WOF + c2 * 102400;
        u32x4 xr[10][4], xh[10];
        const int rot1 = ROT1;
        auto gr = [&](int G) { const int x = G + rot1; return x >= 10 ? x - 10 : x; };
        auto w_dma = [&](int G, int k) { dma16s(lds, (G & 3) * 16384 + w * 4096 + k * 1024, rs, lo16, wb + gr(G) * 16384 + k * 1024); };
        auto x_ld = [&](int G, int i) { xr[G][i] = bload16<SC1>(rs, lo16, xb0 + (i & 1) * 20480 + (2 * gr(G) + (i >> 1)) * 1024); };
        if (w == 0) {
            unsigned off[1] = {l == 0 ? WS_FC + TH * FS : WS_FC + (8 * g1 + ((l - 1) & 7)) * FS};
            poll_words<1>(ws, off, l < 9, tag);
        }
        BAR();
#pragma unroll
        for (int G = 0; G < 4; ++G) {
#pragma unroll
            for (int i = 0; i < 4; ++i) x_ld(G, i);
            xh[G] = bload16<SC1>(rs, lo16, xbh + gr(G) * 2048);
        }
        SB();
        if (w == 0) {
            unsigned off[1] = {WS_FA + ((j1 >> 1) * 20 + (l < 20 ? l : 0)) * FS};
            poll_words<1>(ws, off, l < 20, tag);
        }
        BAR();
        STAMP(2);
#pragma unroll
        for (int G = 0; G < 3; ++G)
#pragma unroll
            for (int k = 0; k < 4; ++k) w_dma(G, k);
        WAITV(4);
        BAR();
        STAMP(3);
        f32x4 acc[2][8], hacc[3];
#pragma unroll
        for (int i = 0; i < 8; ++i) { acc[0][i] = (f32x4){0, 0, 0, 0}; acc[1][i] = (f32x4){0, 0, 0, 0}; }
#pragma unroll
        for (int i = 0; i < 3; ++i) hacc[i] = (f32x4){0, 0, 0, 0};
        u32x4 wf[2][8];
#pragma unroll
        for (int n = 0; n < 8; ++n) wf[0][n] = LD16(n * 1024 + lo16);
        const unsigned hoff = (w >> 1) * 8192 + ((w & 1) ? 5 : 2) * 1024 + lo16;
        unsigned bpv[20];
        SB();
#pragma unroll
        for (int G = 0; G < 10; ++G) {
            u32x4 hw[3];
#pragma unroll
            for (int st = 0; st < 2; ++st) {
                const int s = 2 * G + st;
#pragma unroll
                for (int n = 0; n < 8; ++n) {
                    if (s + 1 < 20) wf[(s + 1) & 1][n] = LD16((((s + 1) >> 1) & 3) * 16384 + ((s + 1) & 1) * 8192 + n * 1024 + lo16);
                    if (st == 0 && n < 3) hw[n] = LD16((G & 3) * 16384 + hoff + n * 1024);
                    if (n < 5) { acc[0][n] = mfma(wf[s & 1][n], xr[G][st * 2], acc[0][n]); acc[1][n] = mfma(wf[s & 1][n], xr[G][st * 2 + 1], acc[1][n]); }
                    else { acc[0][n] = mfma(xr[G][st * 2], wf[s & 1][n], acc[0][n]); acc[1][n] = mfma(xr[G][st * 2 + 1], wf[s & 1][n], acc[1][n]); }
                    if (st == 1 && n >= 5) hacc[n - 5] = mfma(hw[n - 5], xh[G], hacc[n - 5]);
                    if (st == 0) {
                        if (G + 3 < 10 && (n & 1) == 0) w_dma(G + 3, n >> 1);
                        if (G + 4 < 10 && n == 7) xh[G + 4] = bload16<SC1>(rs, lo16, xbh + gr(G + 4) * 2048);
                    } else {
                        if (G + 4 < 10 && (n & 1) == 0) x_ld(G + 4, n >> 1);
                    }
                    if (G == 8 && st == 0 && n < 5) {
#pragma unroll
                        for (int k = 0; k < 4; ++k) bpv[4 * n + k] = bload4<SC1>(rs, (tid & 127) * 4, WS_BP + ((4 * n + k) * 16 + j1) * 512);
                    }
                    if (n == 3) {
                        if (s == 12) dtask_frag<0>(dout, rs, l);
                        if (s == 13) dtask_frag<1>(dout, rs, l);
                        if (s == 14) dtask_frag<2>(dout, rs, l);
                        if (s == 15) dtask_frag<3>(dout, rs, l);
                        if (s == 16) dtask_frag<4>(dout, rs, l);
                        if (s == 17) dtask_frag<5>(dout, rs, l);
                        if (s == 18) dtask_frag<6>(dout, rs, l);
                        if (s == 19) dtask_frag<7>(dout, rs, l);
                    }
                    SB();
                }
            }
            if (G == 0) WAITV(9); else if (G <= 5) WAITV(14); else if (G == 6) WAITV(11); else if (G == 7) WAITV(3);
            BAR();
        }
        STAMP(4);
        constexpr int L1_BIAS = 6144, L1_HP = 0, L1_EX = 8192, L1_VT = 6656;
        {
            float bs = 0.f;
#pragma unroll
            for (int i = 0; i < 20; ++i) bs += __uint_as_float(bpv[i]);
            if (tid < 128) STF(L1_BIAS + tid * 4, bs);
        }
        if (w >= 2) {
#pragma unroll
            for (int i = 0; i < 3; ++i) STF4(L1_HP + ((w & 1) * 3 + i) * 1024 + lo16, hacc[i]);
        }
        BAR();
        f32x4 bq[5]; float bv[3];
#pragma unroll
        for (int n = 0; n < 5; ++n) bq[n] = LDF4(L1_BIAS + (n * 16 + 4 * qd) * 4);
#pragma unroll
        for (int n = 0; n < 3; ++n) bv[n] = LDF(L1_BIAS + ((5 + n) * 16 + t) * 4);
        TileKV kv[2]; u32x2 qa[2][2], xq[2];
#pragma unroll
        for (int tt = 0; tt < 2; ++tt) {
            qa[tt][0] = pack4(acc[tt][0] + bq[0]); qa[tt][1] = pack4(acc[tt][1] + bq[1]);
            kv[tt].ka0 = pack4(acc[tt][2] + bq[2]); kv[tt].ka1 = pack4(acc[tt][3] + bq[3]);
            xq[tt] = pack4(acc[tt][4] + bq[4]);
            kv[tt].k2[0] = swap32(xq[tt][0]); kv[tt].k2[1] = swap32(xq[tt][1]);
            kv[tt].va0 = pack4(acc[tt][5] + bv[0]); kv[tt].va1 = pack4(acc[tt][6] + bv[1]); kv[tt].z = pack4(acc[tt][7] + bv[2]);
        }
        if (w < 3) {
            const unsigned e = L1_EX + (w + 1) * 3072 + lo16;
            ST16(e, ((u32x4){kv[1].ka0[0], kv[1].ka0[1], kv[1].ka1[0], kv[1].ka1[1]}));
            ST16(e + 1024, ((u32x4){kv[1].k2[0], kv[1].k2[1], kv[1].va0[0], kv[1].va0[1]}));
            ST16(e + 2048, ((u32x4){kv[1].va1[0], kv[1].va1[1], kv[1].z[0], kv[1].z[1]}));
        }
        if (w == 0) {
            const u32x2 k0 = pack4(hacc[0] + LDF4(L1_HP + lo16) + bq[2]), k1 = pack4(hacc[1] + LDF4(L1_HP + 1024 + lo16) + bq[3]);
            const u32x2 xx = pack4(hacc[2] + LDF4(L1_HP + 2048 + lo16) + bq[4]);
            ST16(L1_EX + lo16, ((u32x4){k0[0], k0[1], k1[0], k1[1]}));
            ST8(L1_EX + 1024 + lo16, ((u32x2){swap32(xx[0]), swap32(xx[1])}));
        } else if (w == 1) {
#pragma unroll
            for (int i = 0; i < 3; ++i) {
                const f32x4 v = hacc[i] + LDF4(L1_HP + (3 + i) * 1024 + lo16) + LDF4(L1_BIAS + ((5 + i) * 16 + 4 * qd) * 4);
                const u32x2 pk = pack4(v);
                LAS u16* d = (LAS u16*)((LAS char*)lds + L1_VT + i * 512 + (4 * qd) * 32 + t * 2);
                d[0] = (u16)pk[0]; d[16] = (u16)(pk[0] >> 16); d[32] = (u16)pk[1]; d[48] = (u16)(pk[1] >> 16);
            }
        }
        const unsigned ob = WS_OF + T0 * 20480;
        u32x4 om; u32x2 ol;
        attn_unit(qa[1], xq[1], kv[1], kv[0], true, t, qd, om, ol);
        bstore16<SC1>(om, rs, lo16, ob + 20480 + j1 * 1024);
        bstore8<SC1>(ol, rs, qd < 2 ? ((j1 & 3) * 16 + t) * 16 + 8 * qd : 0x7ff00000u, ob + 20480 + (16 + (j1 >> 2)) * 1024);
        WAITV(2);
        BAR();
        if (tid == 0) flag_store(ws, WS_FD + b * 4, tag);
        TileKV pv;
        {
            const unsigned e = L1_EX + w * 3072 + lo16;
            const u32x4 a = LD16(e), bb = LD16(e + 1024), cc = LD16(e + 2048);
            pv.ka0 = (u32x2){a[0], a[1]}; pv.ka1 = (u32x2){a[2], a[3]}; pv.k2 = (u32x2){bb[0], bb[1]}; pv.va0 = (u32x2){bb[2], bb[3]};
            pv.va1 = (u32x2){cc[0], cc[1]}; pv.z = (u32x2){cc[2], cc[3]};
            if (w == 0) { pv.va0 = LD8(L1_VT + t * 32 + qd * 8); pv.va1 = LD8(L1_VT + 512 + t * 32 + qd * 8); pv.z = LD8(L1_VT + 1024 + t * 32 + qd * 8); }
        }
        attn_unit(qa[0], xq[0], kv[0], pv, !(g1 == 0 && w == 0), t, qd, om, ol);
        bstore16<SC1>(om, rs, lo16, ob + j1 * 1024);
        if (qd < 2) bstore8<SC1>(ol, rs, ((j1 & 3) * 16 + t) * 16 + 8 * qd, ob + (16 + (j1 >> 2)) * 1024);
        STAMP(5);
        const u32x4 fdv = bload16<SC1>(rs, lo16, WS_FD);
        publish(ws, WS_F1 + (g1 * 16 + j1) * FS, tag);
        if (w == 0) check_all_blocks(ws, rs, WS_FD, fdv, tag);
        BAR();
        if (b == 0 && tid == 0) flag_store(ws, WS_EPOCH, tag);
#pragma unroll
        for (int k = 0; k < 25; ++k) dma16s(lds, L2_WO + w * 25600 + k * 1024, rs, lo16, wob + wrap20(5 * w + k / 5 + rot2) * 5120 + (k % 5) * 1024);
        {
            const rsrc_t rx = mk_rsrc(p.x, 2048 * 1280);
#pragma unroll
            for (int k = 0; k < 3; ++k) {
                const int n = (w * 3 + k) * 64 + l, row = n / 10, cc = n - 10 * row;
                if (w * 3 + k < 10) dma16(lds, L2_X + (w * 3 + k) * 1024, rx, row * 1280 + cc * 16, P2 * 64 * 1280 + c2 * 160);
            }
        }
    }
    STAMP(6);

    u32x2 x1b[5], b1v[7];
    u32x4 w2f[14][5], vraw[5][2];
    const int s4 = __builtin_amdgcn_readfirstlane(w < 2 ? 14 * w : 28 + 13 * (w - 2)), cnt4 = __builtin_amdgcn_readfirstlane(w < 2 ? 14 : 13);
    const unsigned w2b = __builtin_amdgcn_readfirstlane(WS_W2F + (c2 * 54 + s4) * 5 * 1024);
    const int rot4 = __builtin_amdgcn_readfirstlane(ROT4);
    auto step4 = [&](int i) { const int x = (i < 13 ? i : cnt4 - 1) + rot4; return x >= cnt4 ? x - cnt4 : x; };
    auto w2_ld = [&](int f, unsigned lo) { w2f[f / 5][f % 5] = bload16<SC1>(rs, lo, w2b + step4(f / 5) * 5120 + (f % 5) * 1024); };
    {
        LANE_IDS();
        const int T = 4 * P2 + w;
        constexpr int L2_MV = 57344;
        const u32x4 mvl = bload16<SC1>(rs, (1280 + ((tid / 20) % 3) * 640 + 80 * c2 + (tid % 20) * 4) * 4, WS_MODV);
        SB();
        if (tid < 60) ST16(L2_MV + tid * 16, mvl);
        if (w == 0) {
            unsigned off[1] = {WS_F1 + ((P2 >> 1) * 16 + (l & 15)) * FS};
            poll_words<1>(ws, off, l < 16, tag);
        }
        DRAIN();
        BAR();
        STAMP(7);
        u32x2 xv[5];
#pragma unroll
        for (int n = 0; n < 5; ++n) xv[n] = LD8(L2_X + (16 * w + t) * 160 + n * 32 + qd * 8);
        constexpr int PW = 8;
        u32x4 tk[20];
#pragma unroll
        for (int s = 0; s < PW; ++s) tk[s] = bload16<SC1>(rs, lo16, WS_OF + (T * 20 + wrap20(s + rot2)) * 1024);
        f32x4 acc[5];
#pragma unroll
        for (int n = 0; n < 5; ++n) acc[n] = (f32x4){0, 0, 0, 0};
        u32x4 wf[2][5];
#pragma unroll
        for (int n = 0; n < 5; ++n) wf[0][n] = LD16(L2_WO + n * 1024 + lo16);
        SB();
        LSTAMP(40);
#pragma unroll
        for (int s = 0; s < 20; ++s) {
            if (s + PW < 20) tk[s + PW] = bload16<SC1>(rs, lo16, WS_OF + (T * 20 + wrap20(s + PW + rot2)) * 1024);
            if (s + 1 < 20) {
#pragma unroll
                for (int n = 0; n < 5; ++n) wf[(s + 1) & 1][n] = LD16(L2_WO + ((s + 1) * 5 + n) * 1024 + lo16);
            }
#pragma unroll
            for (int n = 0; n < 5; ++n) acc[n] = mfma(wf[s & 1][n], tk[s], acc[n]);
#pragma unroll
            for (int n = 0; n < 5; ++n) { if (s + 1 < 20) SGB(0x100, 1); SGB(0x8, 1); if (n == 1 && s + PW < 20) SGB(0x20, 1); }
            SB();
            if ((s & 3) == 3 || s == 0) LSTEP(41 + (s + 1) / 4);
        }
        STAMP(8);
        BAR();
        const unsigned w1src = WS_W1F + t3 * 20480, w1last = (cnt3 == 7 ? 6 : 0) * 20480;
        auto w1_dma = [&](int base_step, int i) {
            const int n = i % 7;
            dma16s(lds, base_step * 7168 + i * 1024, rs, lo16, w1src + (n == 6 ? w1last : n * 20480) + wrap20(base_step + i / 7 + rot3) * 1024);
        };
        float st[6] = {0, 0, 0, 0, 0, 0};
        u32x2 ub[5];
        const unsigned ufb = WS_UF + T * 20480;
#pragma unroll
        for (int n = 0; n < 5; ++n) {
            const f32x4 ga_n = LDF4(L2_MV + (16 * n + 4 * qd) * 4), s2_n = LDF4(L2_MV + 320 + (16 * n + 4 * qd) * 4), g2_n = LDF4(L2_MV + 640 + (16 * n + 4 * qd) * 4);
            float x1[4] = {lo_f(xv[n][0]), hi_f(xv[n][0]), lo_f(xv[n][1]), hi_f(xv[n][1])};
#pragma unroll
            for (int r = 0; r < 4; ++r) x1[r] = fmaf(ga_n[r], acc[n][r], x1[r]);
            x1b[n] = (u32x2){pack2(x1[0], x1[1]), pack2(x1[2], x1[3])};
            const float xr[4] = {lo_f(x1b[n][0]), hi_f(x1b[n][0]), lo_f(x1b[n][1]), hi_f(x1b[n][1])};
            ub[n] = (u32x2){pack2(xr[0] * g2_n[0], xr[1] * g2_n[1]), pack2(xr[2] * g2_n[2], xr[3] * g2_n[3])};
            const float ur[4] = {lo_f(ub[n][0]), hi_f(ub[n][0]), lo_f(ub[n][1]), hi_f(ub[n][1])};
#pragma unroll
            for (int r = 0; r < 4; ++r) {
                st[0] += xr[r]; st[1] = fmaf(xr[r], xr[r], st[1]); st[2] += ur[r]; st[3] = fmaf(ur[r], g2_n[r], st[3]);
                st[4] = fmaf(ur[r], ur[r], st[4]); st[5] = fmaf(ur[r], s2_n[r], st[5]);
            }
            if (n == 1) bstore16<SC1>((u32x4){ub[0][0], ub[0][1], ub[1][0], ub[1][1]}, rs, lo16, ufb + (2 * c2) * 1024);
            if (n == 3) bstore16<SC1>((u32x4){ub[2][0], ub[2][1], ub[3][0], ub[3][1]}, rs, lo16, ufb + (2 * c2 + 1) * 1024);
            if (n == 4) bstore8<SC1>(ub[4], rs, lo16 + 8 * (c2 & 1), ufb + (16 + (c2 >> 1)) * 1024);
        }
#pragma unroll
        for (int i = 0; i < 6; ++i) st[i] = red_add_q(st[i]);
        if (qd < 2) bstore16<SC1>(asu4(qd == 0 ? (f32x4){st[0], st[1], st[2], st[3]} : (f32x4){st[4], st[5], 0.f, 0.f}), rs, qd * 256 + t * 16, WS_ST + (T * 8 + c2) * 512);
#pragma unroll
        for (int n = 0; n < 5; ++n) asm volatile("" : "+v"(x1b[n]));
        STAMP(9);
        publish(ws, WS_F2 + (P2 * 8 + c2) * FS, tag);
#pragma unroll
        for (int i = 0; i < 5; ++i) {
            int ta, tb; tiles_p3(wrap20(5 * w + i + rot3), ta, tb);
            const unsigned sec = WS_MODV + (t == 0 ? 2560 : 1920) * 4 + qd * 16;
            vraw[i][0] = bload16<SC1>(rs, sec + ta * 64, 0); vraw[i][1] = bload16<SC1>(rs, sec + tb * 64, 0);
        }
#pragma unroll
        for (int n = 0; n < 7; ++n) b1v[n] = *(const GAS u32x2*)(p.b1 + 16 * (t3 + (n < cnt3 ? n : 0)) + 4 * qd);
        SB();
#pragma unroll
        for (int i = 0; i < 14; ++i) w1_dma(2 * w, i);
#pragma unroll
        for (int i = 0; i < 21; ++i) w1_dma(8 + 3 * w, i);
    }
    STAMP(10);

    {
        LANE_IDS();
        constexpr int L3_GSC = 143360;
        const int T0 = 8 * g3 + 2 * w;
        if (w == 0) {
            unsigned off[1] = {WS_F2 + ((2 * g3 + ((l >> 3) & 1)) * 8 + (l & 7)) * FS};
            poll_words<1>(ws, off, l < 16, tag);
        }
        DRAIN();
        BAR();
        STAMP(11);
        constexpr int PW = 4;
        u32x4 tk[20][2];
        u32x4 sp[2][4];
#pragma unroll
        for (int s = 0; s < PW; ++s) { const unsigned o = WS_UF + (T0 * 20 + wrap20(s + rot3)) * 1024; tk[s][0] = bload16<SC1>(rs, lo16, o); tk[s][1] = bload16<SC1>(rs, lo16, o + 20480); }
        SB();
        LSTAMP(62);
        {
            f32x4 va[7], gram = {0, 0, 0, 0};
            u32x4 vf[5], wv[2][7];
#pragma unroll
            for (int n = 0; n < 7; ++n) { va[n] = (f32x4){0, 0, 0, 0}; wv[0][n] = LD16((5 * w * 7 + n) * 1024 + lo16); }
#pragma unroll
            for (int i = 0; i < 5; ++i) {
                const f32x4 a = asf4(vraw[i][0]), c = asf4(vraw[i][1]);
                const unsigned cst = t == 2 ? 0x3f803f80u : 0u;
                const unsigned d0 = pack2(a[0], a[1]), d1 = pack2(a[2], a[3]), d2 = pack2(c[0], c[1]), d3 = pack2(c[2], c[3]);
                vf[i] = (u32x4){t < 2 ? d0 : cst, t < 2 ? d1 : cst, t < 2 ? d2 : cst, t < 2 ? d3 : cst};
            }
            SB();
#pragma unroll
            for (int i = 0; i < 5; ++i) {
                if (i + 1 < 5) {
#pragma unroll
                    for (int n = 0; n < 7; ++n) wv[(i + 1) & 1][n] = LD16(((5 * w + i + 1) * 7 + n) * 1024 + lo16);
                }
                SB();
#pragma unroll
                for (int n = 0; n < 7; ++n) va[n] = mfma(wv[i & 1][n], vf[i], va[n]);
                gram = mfma(vf[i], vf[i], gram);
                SB();
            }
            if (t < 3) {
#pragma unroll
                for (int n = 0; n < 7; ++n) STF4(L3_GSC + ((w * 8 + n) * 3 + t) * 64 + qd * 16, va[n]);
                if (qd == 0) STF4(L3_GSC + ((w * 8 + 7) * 3 + t) * 64, gram);
            }
        }
        SB();
        f32x4 acc[2][7];
#pragma unroll
        for (int n = 0; n < 7; ++n) { acc[0][n] = (f32x4){0, 0, 0, 0}; acc[1][n] = (f32x4){0, 0, 0, 0}; }
        u32x4 wf[2][7];
#pragma unroll
        for (int n = 0; n < 7; ++n) wf[0][n] = LD16(n * 1024 + lo16);
        SB();
        LSTAMP(0);
#pragma unroll
        for (int s = 0; s < 20; ++s) {
            int nv = 0;
            if (s + PW < 20) { const unsigned o = WS_UF + (T0 * 20 + wrap20(s + PW + rot3)) * 1024; tk[s + PW][0] = bload16<SC1>(rs, lo16, o); tk[s + PW][1] = bload16<SC1>(rs, lo16, o + 20480); nv += 2; }
            if (s < 16) { w2_ld(s, lo16); nv += 1; }
            if (s >= 16) {
#pragma unroll
                for (int k = 0; k < 3; ++k) w2_ld(16 + (s - 16) * 3 + k, lo16);
                nv += 3;
            }
            if (s == 14 || s == 15) {
#pragma unroll
                for (int e = 0; e < 4; ++e) sp[s - 14][e] = bload16<SC1>(rs, (2 * qd + (e >> 1)) * 512 + (e & 1) * 256 + t * 16, WS_ST + (T0 + s - 14) * 4096);
                nv += 4;
            }
            if (s + 1 < 20) {
#pragma unroll
                for (int n = 0; n < 7; ++n) wf[(s + 1) & 1][n] = LD16(((s + 1) * 7 + n) * 1024 + lo16);
            }
#pragma unroll
            for (int n = 0; n < 7; ++n) { acc[0][n] = mfma(wf[s & 1][n], tk[s][0], acc[0][n]); acc[1][n] = mfma(wf[s & 1][n], tk[s][1], acc[1][n]); }
#pragma unroll
            for (int n = 0; n < 7; ++n) { if (s + 1 < 20) SGB(0x100, 1); SGB(0x8, 2); if (n < nv) SGB(0x20, 1); }
            SB();
            if (s == 2) BAR();
            if (s == 3 && tid < 96) {
                const int n8 = tid / 12, r12 = tid % 12;
                f32x4 a = LDF4(L3_GSC + (n8 * 12 + r12) * 16);
#pragma unroll
                for (int u = 1; u < 4; ++u) a += LDF4(L3_GSC + (u * 96 + n8 * 12 + r12) * 16);
                STF4(L3_GSC + 6144 + (n8 * 12 + r12) * 16, a);
            }
            if (s == 8) BAR();
            SB();
            LSTEP(1 + s);
        }
        STAMP(12);
        float al[2], be[2], gm[2], de[2];
        {
            const f32x4 r0 = LDF4(L3_GSC + 6144 + 84 * 16), r1 = LDF4(L3_GSC + 6144 + 88 * 16);
            const float Gg = r0[0], Ggs = r0[1], Gs = r0[2], Sss = r1[1], Ss = r1[2];
#pragma unroll
            for (int tt = 0; tt < 2; ++tt) {
                const f32x4 a0 = asf4(sp[tt][0]) + asf4(sp[tt][2]), b0 = asf4(sp[tt][1]) + asf4(sp[tt][3]);
                const float s0 = red_add_q(a0[0]), s1 = red_add_q(a0[1]), s2 = red_add_q(a0[2]), s3 = red_add_q(a0[3]), s4v = red_add_q(b0[0]), s5 = red_add_q(b0[1]);
                const float iD = 1.f / 640.f;
                const float m1 = s0 * iD, v1 = fmaxf(s1 * iD - m1 * m1, 0.f), r1v = rsqrtf(v1 + 1e-5f);
                const float m2 = (r1v * (s2 - m1 * Gs) + Ss) * iD;
                const float e2 = (r1v * r1v * (s4v - 2.f * m1 * s3 + m1 * m1 * Gg) + 2.f * r1v * (s5 - m1 * Ggs) + Sss) * iD;
                const float r2v = rsqrtf(fmaxf(e2 - m2 * m2, 0.f) + 1e-5f);
                al[tt] = r1v * r2v; be[tt] = -r1v * r2v * m1; gm[tt] = r2v; de[tt] = -r2v * m2;
            }
        }
        u32x2 hb[2][7];
#pragma unroll
        for (int n = 0; n < 7; ++n) {
#pragma unroll
            for (int k = 0; k < 5; ++k) w2_ld(28 + n * 5 + k, lo16);
            const f32x4 G = LDF4(L3_GSC + 6144 + (n * 12 + qd) * 16), Sv = LDF4(L3_GSC + 6144 + (n * 12 + 4 + qd) * 16), C = LDF4(L3_GSC + 6144 + (n * 12 + 8 + qd) * 16);
            const float bb[4] = {lo_f(b1v[n][0]), hi_f(b1v[n][0]), lo_f(b1v[n][1]), hi_f(b1v[n][1])};
#pragma unroll
            for (int tt = 0; tt < 2; ++tt) {
                float hv[4];
#pragma unroll
                for (int r = 0; r < 4; ++r) {
                    float z = fmaf(gm[tt], Sv[r], bb[r]); z = fmaf(be[tt], G[r], z); z = fmaf(de[tt], C[r], z); z = fmaf(al[tt], acc[tt][n][r], z);
                    const float u = z * fmaf(z * z, -0.044715f * 1.5957691216057308f * 1.4426950408889634f, -1.5957691216057308f * 1.4426950408889634f);
                    hv[r] = z * __builtin_amdgcn_rcpf(1.f + __builtin_amdgcn_exp2f(u));
                }
                hb[tt][n] = (u32x2){pack2(hv[0], hv[1]), pack2(hv[2], hv[3])};
            }
            SGB(0x20, 1); SGB(0x1, 20); SGB(0x20, 1); SGB(0x1, 20); SGB(0x20, 1); SGB(0x1, 20); SGB(0x20, 1); SGB(0x1, 20); SGB(0x20, 1); SGB(0x1, 40);
            SB();
#pragma unroll
            for (int tt = 0; tt < 2; ++tt) {
                const unsigned hbase = WS_HF + (T0 + tt) * 54 * 1024;
                if (n & 1) bstore16<SC1>((u32x4){hb[tt][n - 1][0], hb[tt][n - 1][1], hb[tt][n][0], hb[tt][n][1]}, rs, lo16, hbase + (3 * j3 + (n >> 1)) * 1024);
                if (n == 6 && j3 < 12) bstore8<SC1>(hb[tt][6], rs, lo16 + 8 * (j3 & 1), hbase + (48 + (j3 >> 1)) * 1024);
            }
            SB();
        }
        STAMP(13);
        publish(ws, WS_F3 + (g3 * 16 + j3) * FS, tag);
    }
    STAMP(14);

    {
        LANE_IDS();
        f32x4 gb[5]; u32x2 b2v[5];
#pragma unroll
        for (int f = 63; f < 70; ++f) w2_ld(f, lo16);
        SB();
        if (w == 0) {
            unsigned off[1] = {WS_F3 + ((P2 >> 1) * 16 + (l & 15)) * FS};
            poll_words<1>(ws, off, l < 16, tag);
        }
        BAR();
        STAMP(15);
        constexpr int PD = 3;
        u32x4 tk[14][4];
        const unsigned tokb = __builtin_amdgcn_readfirstlane(WS_HF + (4 * P2 * 54 + s4) * 1024);
        auto issue = [&](int i) {
            const int s = step4(i);
#pragma unroll
            for (int a = 0; a < 4; ++a) tk[i][a] = bload16<SC1>(rs, lo16, tokb + (a * 54 + s) * 1024);
        };
#pragma unroll
        for (int i = 0; i < PD; ++i) issue(i);
        f32x4 acc[4][5];
#pragma unroll
        for (int a = 0; a < 4; ++a)
#pragma unroll
            for (int n = 0; n < 5; ++n) acc[a][n] = (f32x4){0, 0, 0, 0};
        SB();
        LSTAMP(24);
#pragma unroll
        for (int i = 0; i < 14; ++i) {
            if (i < 13 || w < 2) {
                if (i + PD < 14) issue(i + PD);
                if (i == 11) {
#pragma unroll
                    for (int n = 0; n < 5; ++n) {
                        const int f = 80 * c2 + 16 * n;
                        gb[n] = asf4(bload16<SC1>(rs, qd * 16, WS_MODV + (3200 + f) * 4));
                        b2v[n] = *(const GAS u32x2*)(p.b2 + f + 4 * qd);
                    }
                }
#pragma unroll
                for (int a = 0; a < 4; ++a)
#pragma unroll
                    for (int n = 0; n < 5; ++n) acc[a][n] = mfma(w2f[i][n], tk[i][a], acc[a][n]);
#pragma unroll
                for (int a = 0; a < 4; ++a) { SGB(0x8, 2); if (i == 11) SGB(0x20, 3); else SGB(0x20, 1); SGB(0x8, 3); }
            }
            SB();
            LSTEP(25 + i);
        }
        STAMP(16);
#pragma unroll
        for (int a = 0; a < 4; ++a) {
            if (a != w) {
                const int src = w < a ? w : w - 1;
#pragma unroll
                for (int n = 0; n < 5; ++n) STF4(((a * 3 + src) * 5 + n) * 1024 + lo16, acc[a][n]);
            }
        }
        BAR();
        constexpr int L4_OUT = 61440;
#pragma unroll
        for (int n = 0; n < 5; ++n) {
            f32x4 v = w == 0 ? acc[0][n] : w == 1 ? acc[1][n] : w == 2 ? acc[2][n] : acc[3][n];
#pragma unroll
            for (int u = 0; u < 3; ++u) v += LDF4(((w * 3 + u) * 5 + n) * 1024 + lo16);
            const float x1[4] = {lo_f(x1b[n][0]), hi_f(x1b[n][0]), lo_f(x1b[n][1]), hi_f(x1b[n][1])};
            const float bv[4] = {lo_f(b2v[n][0]), hi_f(b2v[n][0]), lo_f(b2v[n][1]), hi_f(b2v[n][1])};
            float o[4];
#pragma unroll
            for (int r = 0; r < 4; ++r) o[r] = fmaf(gb[n][r], v[r] + bv[r], x1[r]);
            ST8(L4_OUT + (16 * w + t) * 176 + n * 32 + qd * 8, ((u32x2){pack2(o[0], o[1]), pack2(o[2], o[3])}));
        }
        asm volatile("s_waitcnt lgkmcnt(0)" ::: "memory");
#pragma unroll
        for (int k = 0; k < 3; ++k) {
            const int ch = l + 64 * k;
            if (ch < 160) {
                const int row = ch / 10, cc = ch % 10;
                const u32x4 v = LD16(L4_OUT + (16 * w + row) * 176 + cc * 16);
                *(GAS u32x4*)(p.out + (size_t)(16 * (4 * P2 + w) + row) * 640 + 80 * c2 + cc * 8) = v;
            }
        }
    }
    STAMP(17);
    LSTAMP_DUMP;
    asm volatile(".Ladaln_end:" ::: "memory");
}

extern "C" void kernel_launch(void* const* d_in, const int* in_sizes, int n_in, void* d_out, int out_size, void* d_ws, size_t ws_size, hipStream_t stream) {
    Params p;
    p.x = (const u16*)d_in[0]; p.wqkv = (const u16*)d_in[1]; p.wo = (const u16*)d_in[2]; p.w1 = (const u16*)d_in[4]; p.b1 = (const u16*)d_in[5];
    p.w2 = (const u16*)d_in[6]; p.b2 = (const u16*)d_in[7]; p.c = (const u16*)d_in[8]; p.wm = (const u16*)d_in[9];
    p.out = (u16*)d_out; p.ws = (char*)d_ws;
    (void)hipFuncSetAttribute((const void*)adaLN_block_kernel, hipFuncAttributeMaxDynamicSharedMemorySize, LDS_BYTES);
    adaLN_block_kernel<<<256, 256, LDS_BYTES, stream>>>(p);
}
```

```cpp
#include <hip/hip_runtime.h>
#include <stdint.h>

typedef unsigned short u16;
typedef float f32x4 __attribute__((ext_vector_type(4)));
typedef float f32x2 __attribute__((ext_vector_type(2)));
typedef __bf16 bf16x8 __attribute__((ext_vector_type(8)));
typedef __bf16 bf16x2 __attribute__((ext_vector_type(2)));
typedef unsigned u32x4 __attribute__((ext_vector_type(4)));
typedef unsigned u32x2 __attribute__((ext_vector_type(2)));
typedef short s16x4 __attribute__((ext_vector_type(4)));
typedef __amdgpu_buffer_rsrc_t rsrc_t;
#define LAS __attribute__((address_space(3)))
#define GAS __attribute__((address_space(1)))
#define DI __device__ __forceinline__

static constexpr unsigned FS = 64;
static constexpr unsigned WS_EPOCH = 0;
static constexpr unsigned WS_TMO   = 256;
static constexpr unsigned WS_FA    = 1024;
static constexpr unsigned WS_FC    = WS_FA + 160 * FS;
static constexpr unsigned WS_FD    = WS_FC + 128 * FS;
static constexpr unsigned WS_F1    = WS_FD + 1024;
static constexpr unsigned WS_F2    = WS_F1 + 256 * FS;
static constexpr unsigned WS_F3    = WS_F2 + 256 * FS;
static constexpr unsigned WS_FEND  = WS_F3 + 256 * FS;
static constexpr unsigned WS_MODV = 131072;
static constexpr unsigned WS_BP   = WS_MODV + 16384;
static constexpr unsigned WS_XNF  = WS_BP + 20 * 16 * 128 * 4;
static constexpr unsigned WS_WQF  = WS_XNF + 128 * 20 * 1024;
static constexpr unsigned WS_WOF  = WS_WQF + 16 * 20 * 8 * 1024;
static constexpr unsigned WS_W1F  = WS_WOF + 8 * 20 * 5 * 1024;
static constexpr unsigned WS_W2F  = WS_W1F + 108 * 20 * 1024;
static constexpr unsigned WS_OF   = WS_W2F + 8 * 54 * 5 * 1024;
static constexpr unsigned WS_UF   = WS_OF + 128 * 20 * 1024;
static constexpr unsigned WS_ST   = WS_UF + 128 * 20 * 1024;
static constexpr unsigned WS_HF   = WS_ST + 128 * 8 * 512;
static constexpr unsigned WS_END  = WS_HF + 128 * 54 * 1024;
static_assert(WS_FEND <= WS_MODV, "flags overflow");

#define B_DELAY 0
#define OUT_AUX 0
#define ROT1 0
#define ROT2 0
#define ROT3 0
#define ROT4 0
static constexpr int LDS_BYTES = 163840;
static constexpr int SC1 = 16;
static constexpr unsigned SPIN_CAP = 40000;

#define STAMP(i)
#define LSTAMP(i)
#define LSTEP(i)
#define LSTAMP_DECL
#define LSTAMP_DUMP

struct Params {
    const u16* x; const u16* wqkv; const u16* wo; const u16* w1; const u16* b1; const u16* w2; const u16* b2; const u16* c; const u16* wm;
    u16* out; char* ws;
};

DI float lo_f(unsigned d) { return __uint_as_float(d << 16); }
DI float hi_f(unsigned d) { return __uint_as_float(d & 0xffff0000u); }
DI unsigned pack2(float a, float b) { f32x2 v = {a, b}; return __builtin_bit_cast(unsigned, __builtin_convertvector(v, bf16x2)); }
DI float rbf(float a) { return lo_f(pack2(a, 0.f)); }
DI u32x2 pack4(const f32x4& v) { return (u32x2){pack2(v[0], v[1]), pack2(v[2], v[3])}; }
DI f32x4 mfma(u32x4 a, u32x4 b, f32x4 c) { return __builtin_amdgcn_mfma_f32_16x16x32_bf16(__builtin_bit_cast(bf16x8, a), __builtin_bit_cast(bf16x8, b), c, 0, 0, 0); }
DI float red_add_q(float v) {
    auto a = __builtin_amdgcn_permlane16_swap(__float_as_uint(v), __float_as_uint(v), false, false);
    v = __uint_as_float(a[0]) + __uint_as_float(a[1]);
    auto b = __builtin_amdgcn_permlane32_swap(__float_as_uint(v), __float_as_uint(v), false, false);
    return __uint_as_float(b[0]) + __uint_as_float(b[1]);
}
DI float red_max_q(float v) {
    auto a = __builtin_amdgcn_permlane16_swap(__float_as_uint(v), __float_as_uint(v), false, false);
    v = fmaxf(__uint_as_float(a[0]), __uint_as_float(a[1]));
    auto b = __builtin_amdgcn_permlane32_swap(__float_as_uint(v), __float_as_uint(v), false, false);
    return fmaxf(__uint_as_float(b[0]), __uint_as_float(b[1]));
}
template <int C> DI float dpp_add(float v) { return v + __builtin_bit_cast(float, __builtin_amdgcn_update_dpp(0, __builtin_bit_cast(int, v), C, 0xf, 0xf, true)); }
DI float red16(float v) { v = dpp_add<0xB1>(v); v = dpp_add<0x4E>(v); v = dpp_add<0x141>(v); return dpp_add<0x140>(v); }
DI unsigned swap32(unsigned v) { auto a = __builtin_amdgcn_permlane32_swap(v, v, false, false); return (threadIdx.x & 32) ? a[0] : a[1]; }
DI rsrc_t mk_rsrc(const void* p, unsigned bytes) { return __builtin_amdgcn_make_buffer_rsrc((void*)p, 0, bytes, 0x00020000); }
template <int AUX> DI u32x4 bload16(rsrc_t r, unsigned voff, unsigned soff) { return __builtin_bit_cast(u32x4, __builtin_amdgcn_raw_buffer_load_b128(r, voff, soff, AUX)); }
template <int AUX> DI u32x2 bload8(rsrc_t r, unsigned voff, unsigned soff) { return __builtin_bit_cast(u32x2, __builtin_amdgcn_raw_buffer_load_b64(r, voff, soff, AUX)); }
template <int AUX> DI unsigned bload4(rsrc_t r, unsigned voff, unsigned soff) { return __builtin_amdgcn_raw_buffer_load_b32(r, voff, soff, AUX); }
template <int AUX> DI void bstore16(u32x4 v, rsrc_t r, unsigned voff, unsigned soff) { __builtin_amdgcn_raw_buffer_store_b128(v, r, voff, soff, AUX); }
template <int AUX> DI void bstore8(u32x2 v, rsrc_t r, unsigned voff, unsigned soff) { __builtin_amdgcn_raw_buffer_store_b64(v, r, voff, soff, AUX); }
template <int AUX> DI void bstore4(unsigned v, rsrc_t r, unsigned voff, unsigned soff) { __builtin_amdgcn_raw_buffer_store_b32(v, r, voff, soff, AUX); }
DI f32x4 asf4(u32x4 v) { return __builtin_bit_cast(f32x4, v); }
DI u32x4 asu4(f32x4 v) { return __builtin_bit_cast(u32x4, v); }
#define LANE_IDS() int tz_ = threadIdx.x; asm volatile("" : "+v"(tz_)); const int tid = tz_, l = tid & 63, t = l & 15, qd = l >> 4; const unsigned lo16 = l * 16; (void)t; (void)qd; (void)lo16
#define OPAQUE(x) asm volatile("" : "+v"(x))
#define SB() __builtin_amdgcn_sched_barrier(0)
#define BAR() asm volatile("s_waitcnt lgkmcnt(0)\n\ts_barrier" ::: "memory")
#define DRAIN() asm volatile("s_waitcnt vmcnt(0)" ::: "memory")
#define SGB(mask, n) __builtin_amdgcn_sched_group_barrier(mask, n, 0)
#define LD16(off) (*(const LAS u32x4*)((const LAS char*)lds + (off)))
#define LD8(off) (*(const LAS u32x2*)((const LAS char*)lds + (off)))
#define LDF4(off) (*(const LAS f32x4*)((const LAS char*)lds + (off)))
#define LDF(off) (*(const LAS float*)((const LAS char*)lds + (off)))
#define ST16(off, v) (*(LAS u32x4*)((LAS char*)lds + (off)) = (v))
#define ST8(off, v) (*(LAS u32x2*)((LAS char*)lds + (off)) = (v))
#define STF4(off, v) (*(LAS f32x4*)((LAS char*)lds + (off)) = (v))
#define STF(off, v) (*(LAS float*)((LAS char*)lds + (off)) = (v))
DI u32x2 tr_read(const char* lds, unsigned off) { return __builtin_bit_cast(u32x2, __builtin_amdgcn_ds_read_tr16_b64_v4i16((LAS s16x4*)((LAS char*)lds + off))); }

DI void flag_store(char* ws, unsigned off, unsigned tag) { __hip_atomic_store((GAS unsigned*)(ws + off), tag, __ATOMIC_RELAXED, __HIP_MEMORY_SCOPE_AGENT); }
DI unsigned flag_load(const char* ws, unsigned off) { return __hip_atomic_load((const GAS unsigned*)(ws + off), __ATOMIC_RELAXED, __HIP_MEMORY_SCOPE_AGENT); }
DI void flag_put(rsrc_t rs, unsigned off, unsigned tag, bool me) { bstore4<SC1>(tag, rs, me ? off : 0x7ff00000u, 0); }
DI void publish(rsrc_t rs, unsigned off, unsigned tag) { DRAIN(); BAR(); flag_put(rs, off, tag, threadIdx.x == 0); }
template <int N> DI void poll_words(char* ws, const unsigned (&off)[N], bool active, unsigned tag) {
#pragma nounroll
    for (unsigned spins = 0;; ++spins) {
        bool ok = true;
#pragma unroll
        for (int k = 0; k < N; ++k) ok &= flag_load(ws, off[k]) == tag;
        if (__all(ok || !active)) break;
        if (spins > SPIN_CAP) { if ((threadIdx.x & 63) == 0) flag_store(ws, WS_TMO, tag); break; }
        __builtin_amdgcn_s_sleep(1);
    }
}

DI void check_all_blocks(char* ws, rsrc_t rs, unsigned base, u32x4 v, unsigned tag) {
    asm volatile("" : "+v"(v));
#pragma nounroll
    for (unsigned spins = 0; !__all(v[0] == tag && v[1] == tag && v[2] == tag && v[3] == tag); ++spins) {
        if (spins > SPIN_CAP) { if ((threadIdx.x & 63) == 0) flag_store(ws, WS_TMO, tag); break; }
        __builtin_amdgcn_s_sleep(1);
        v = bload16<SC1>(rs, (threadIdx.x & 63) * 16, base);
        asm volatile("" : "+v"(v) :: "memory");
    }
}

DI void rows_wo(int s, int qd, int& ra, int& rb) { if (s < 16) { ra = 40 * s + 4 * qd; rb = ra + 20; } else { ra = 40 * (4 * (s - 16) + qd) + 16; rb = ra + 20; } }
DI void tiles_p3(int s, int& ta, int& tb) { if (s < 16) { ta = 5 * (s >> 1) + 2 * (s & 1); tb = ta + 1; } else { ta = 10 * (s - 16) + 4; tb = ta + 5; } }
DI void tiles_p4(int s, int& ta, int& tb) { if (s < 48) { int j = s / 3, pr = s % 3; int st = j < 12 ? 7 * j : 84 + 6 * (j - 12); ta = st + 2 * pr; tb = ta + 1; } else { int m = s - 48; ta = 14 * m + 6; tb = ta + 7; } }

static constexpr int L0_SC = 81920, L0_GS = 83200, L0_CRAW = 83968, L0_RW = 86016, L0_D = 101376, L0_XI = 134144, L0_XS = 156160;

template <int AUX> DI void dma(char* lds, unsigned lds_off, rsrc_t r, unsigned voff, unsigned soff) {
    __builtin_amdgcn_raw_ptr_buffer_load_lds(r, (LAS void*)((LAS char*)lds + lds_off), 16, voff, soff, 0, AUX);
}
DI void dma16(char* lds, unsigned lds_off, rsrc_t r, unsigned voff, unsigned soff) {
    const unsigned dst = (unsigned)(size_t)(LAS char*)lds + lds_off;
    asm volatile("s_mov_b32 m0, %0\n\ts_nop 0\n\tbuffer_load_dwordx4 %1, %2, %3 offen lds" :: "s"(dst), "v"(voff), "s"(r), "s"(soff) : "memory");
}
DI void dma16s(char* lds, unsigned lds_off, rsrc_t r, unsigned voff, unsigned soff) { dma<SC1>(lds, lds_off, r, voff, soff); }
#define WAITV(n) asm volatile("s_waitcnt vmcnt(%0)" :: "n"(n) : "memory")

struct DPlan { rsrc_t r; unsigned v0, v1, sa, sb, st; bool on; };
struct DOut { unsigned dst[8]; u32x4 f[8]; };
DI void dtask_plan(const Params& p, int id, int l, DPlan& d, DOut& o) {
    int ra0, rb0, rstep, cg, lim; unsigned pitch, d0, r5, stride, jump;
    if (id < 370) {
        const bool wo = id < 100; const int i2 = wo ? id : id - 100, s = i2 / 5; cg = i2 - 5 * s; pitch = 1280;
        if (wo) { d.r = mk_rsrc(p.wo, 640 * 1280); if (s < 16) { ra0 = 40 * s; rstep = 4; } else { ra0 = 160 * (s - 16) + 16; rstep = 40; } rb0 = ra0 + 20; }
        else { d.r = mk_rsrc(p.w2, 1728 * 1280); int ta, tb; tiles_p4(s, ta, tb); ra0 = 16 * ta; rb0 = 16 * tb; rstep = 4; }
        const int c0 = (8 * cg) / 5;
        r5 = 8 * cg - 5 * c0; d0 = (wo ? WS_WOF + (c0 * 20 + s) * 5120 : WS_W2F + (c0 * 54 + s) * 5120) + r5 * 1024;
        stride = 1024; jump = wo ? 19 * 5120 : 53 * 5120; lim = 8;
    } else {
        const int i2 = id < 650 ? id - 370 : 0, s = i2 / 14; cg = i2 - 14 * s; pitch = 3456; d.r = mk_rsrc(p.w1, 640 * 3456);
        int ta, tb; tiles_p3(s, ta, tb); ra0 = 16 * ta; rb0 = 16 * tb; rstep = 4;
        r5 = 0; d0 = WS_W1F + (8 * cg * 20 + s) * 1024; stride = 20480; jump = 0; lim = id < 650 ? (cg == 13 ? 4 : 8) : 0;
    }
    const int q = l >> 4, cp = l & 15;
    d.v0 = q * pitch + ((cp ^ (q << 1)) * 16); d.v1 = q * pitch + ((cp ^ ((4 + q) << 1)) * 16);
    d.sa = ra0 * pitch + cg * 256; d.sb = rb0 * pitch + cg * 256; d.st = rstep * pitch; d.on = lim > 0;
#pragma unroll
    for (int nt = 0; nt < 8; ++nt) {
        const unsigned v = d0 + nt * stride + ((r5 + nt >= 5 ? 1u : 0u) + (r5 + nt >= 10 ? 1u : 0u)) * jump;
        o.dst[nt] = __builtin_amdgcn_readfirstlane(nt < lim ? v : 0x7ff00000u);
    }
}
DI void dtask_go(const DPlan& d, char* lds, unsigned scr, int w) {
    if (d.on) {
#pragma unroll
        for (int i = 0; i < 8; ++i) dma16(lds, scr + w * 8192 + i * 1024, d.r, (i & 1) ? d.v1 : d.v0, (i < 4 ? d.sa : d.sb) + (i & 3) * d.st);
    }
}
DI void dtask_read(DOut& o, char* lds, unsigned scr, int w, int l) {
    const int qd = l >> 4, q = (l >> 2) & 3, pp = l & 3, r7 = 4 * (qd & 1) + q;
    const unsigned a0 = scr + w * 8192 + (4 * qd + q) * 256 + pp * 8;
#pragma unroll
    for (int nt = 0; nt < 8; ++nt) {
        const u32x2 ua = tr_read(lds, a0 + ((nt ^ r7) * 32)), ub = tr_read(lds, a0 + 4096 + ((nt ^ r7) * 32));
        o.f[nt] = (u32x4){ua[0], ua[1], ub[0], ub[1]};
    }
}
template <int NT> DI void dtask_frag(const DOut& o, rsrc_t rs, int l) { bstore16<SC1>(o.f[NT], rs, l * 16, o.dst[NT]); }

template <int I0, int I1> DI void wm_dma(const Params& p, char* lds, int w, int l, unsigned colA, unsigned colB, int rot) {
    const rsrc_t rw = mk_rsrc(p.wm, 640 * 3840 * 2);
    const int rl = l >> 3, cp = l & 7;
    const unsigned sg = ((rl >> 1) & 1);
    const unsigned c0 = cp ^ (sg << 1), c1 = cp ^ ((sg | 2) << 1);
    const unsigned v0 = rl * 7680 + (c0 < 4 ? colA + 8 * c0 : colB + 8 * (c0 - 4)) * 2, v1 = rl * 7680 + (c1 < 4 ? colA + 8 * c1 : colB + 8 * (c1 - 4)) * 2;
#pragma unroll
    for (int i = I0; i < I1; ++i) {
        int piece = 4 * i + w + rot; piece = piece >= 80 ? piece - 80 : piece;
        dma16(lds, piece * 1024, rw, (piece & 1) ? v1 : v0, piece * 8 * 7680);
    }
}
DI void c_dma(const Params& p, char* lds, int w, int l) {
    dma16(lds, L0_CRAW + (w & 1) * 1024, mk_rsrc(p.c, 1280), ((w & 1) * 64 + l) * 16, 0);
}
DI void silu_table(char* lds, int tid) {
    if (tid < 80) {
        const u32x4 creg = LD16(L0_CRAW + tid * 16);
        u32x4 o;
#pragma unroll
        for (int d = 0; d < 4; ++d) { const float a = lo_f(creg[d]), b = hi_f(creg[d]); o[d] = pack2(a * __builtin_amdgcn_rcpf(1.f + __expf(-a)), b * __builtin_amdgcn_rcpf(1.f + __expf(-b))); }
        ST16(L0_SC + tid * 16, o);
    }
}
DI float mod_gemv(char* lds, int w, int l) {
    const int qd = l >> 4, q = (l >> 2) & 3, pp = l & 3;
    f32x4 acc = {0, 0, 0, 0}, acc2 = {0, 0, 0, 0};
    const int r0 = 8 * qd + q;
    const unsigned sg = ((r0 >> 1) & 1) | (((r0 >> 3) & 1) << 1);
    unsigned a0 = r0 * 128 + ((w ^ sg) * 32) + pp * 8, a1 = a0 + 40960, sb = L0_SC + qd * 16;
    OPAQUE(a0); OPAQUE(a1); OPAQUE(sb);
    u32x2 tb[2][4]; u32x4 sc[2][2];
    auto rd = [&](int pr) {
        const unsigned ab = pr < 5 ? a0 : a1; const int po = (pr < 5 ? pr : pr - 5) * 8192;
        tb[pr & 1][0] = tr_read(lds, ab + po); tb[pr & 1][1] = tr_read(lds, ab + po + 512); tb[pr & 1][2] = tr_read(lds, ab + po + 4096); tb[pr & 1][3] = tr_read(lds, ab + po + 4608);
        sc[pr & 1][0] = LD16(sb + pr * 128); sc[pr & 1][1] = LD16(sb + pr * 128 + 64);
    };
    rd(0);
#pragma unroll
    for (int pr = 0; pr < 10; ++pr) {
        if (pr + 1 < 10) rd(pr + 1);
        SB();
        acc = mfma(sc[pr & 1][0], (u32x4){tb[pr & 1][0][0], tb[pr & 1][0][1], tb[pr & 1][1][0], tb[pr & 1][1][1]}, acc);
        acc2 = mfma(sc[pr & 1][1], (u32x4){tb[pr & 1][2][0], tb[pr & 1][2][1], tb[pr & 1][3][0], tb[pr & 1][3][1]}, acc2);
        SB();
    }
    return rbf(acc[0] + acc2[0]);
}
DI void x_dma(const Params& p, char* lds, unsigned img, int T, int w, int l) {
    const rsrc_t rx = mk_rsrc(p.x, 2048 * 1280);
    const int r4 = l >> 4, row = 4 * w + r4;
    const unsigned voff = row * 1280 + (((l & 15) ^ row) * 16);
#pragma unroll
    for (int cb = 0; cb < 5; ++cb) dma16(lds, img + (w * 5 + cb) * 1024, rx, voff, T * 16 * 1280 + cb * 256);
}
DI void x_stats(char* lds, unsigned img, unsigned stat, int w, int l) {
    u32x4 v[5];
#pragma unroll
    for (int cb = 0; cb < 5; ++cb) v[cb] = LD16(img + (w * 5 + cb) * 1024 + l * 16);
    float sum = 0.f;
#pragma unroll
    for (int i = 0; i < 5; ++i)
#pragma unroll
        for (int d = 0; d < 4; ++d) sum += lo_f(v[i][d]) + hi_f(v[i][d]);
    sum = red16(sum);
    const float mean = sum * (1.f / 640.f);
    float vs = 0.f;
#pragma unroll
    for (int i = 0; i < 5; ++i)
#pragma unroll
        for (int d = 0; d < 4; ++d) { const float a = lo_f(v[i][d]) - mean, c2 = hi_f(v[i][d]) - mean; vs += a * a + c2 * c2; }
    vs = red16(vs);
    const float rstd = rsqrtf(vs * (1.f / 640.f) + 1e-5f);
    if ((l & 15) == 0) { STF(stat + (4 * w + (l >> 4)) * 8, mean); STF(stat + (4 * w + (l >> 4)) * 8 + 4, rstd); }
}
DI unsigned ximg_off(int t, int byte_in_row) { return ((t >> 2) * 5 + (byte_in_row >> 8)) * 1024 + (t & 3) * 256 + ((((byte_in_row >> 4) & 15) ^ t) * 16) + (byte_in_row & 15); }
DI void x_frags(char* lds, unsigned img, unsigned stat, rsrc_t rs, int T, int w, int l) {
    const int t = l & 15, qd = l >> 4;
    const float mean = LDF(stat + t * 8), rstd = LDF(stat + t * 8 + 4);
#pragma unroll
    for (int i = 0; i < 5; ++i) {
        const int s = w + 4 * i;
        const u32x4 v = LD16(img + ximg_off(t, s * 64 + qd * 16));
        u32x4 o;
#pragma unroll
        for (int d = 0; d < 4; ++d) o[d] = pack2((lo_f(v[d]) - mean) * rstd, (hi_f(v[d]) - mean) * rstd);
        bstore16<SC1>(o, rs, l * 16, WS_XNF + (T * 20 + s) * 1024);
    }
}
struct TileKV { u32x2 ka0, ka1, k2, va0, va1, z; };
DI void attn_unit(const u32x2 (&qa)[2], const u32x2& xq, const TileKV& cur, const TileKV& prev, bool prev_ok, int t, int qd, u32x4& omain, u32x2& oleft) {
    const float cs = 0.22360679774997896f * 1.4426950408889634f;
    f32x4 oa[2], oz[2];
#pragma unroll
    for (int h = 0; h < 2; ++h) {
        const u32x4 qf = {qa[h][0], qa[h][1], qd == h ? xq[0] : 0u, qd == h ? xq[1] : 0u};
        const u32x2 kcp = h == 0 ? cur.ka0 : cur.ka1, kpp = h == 0 ? prev.ka0 : prev.ka1;
        const f32x4 z4 = {0.f, 0.f, 0.f, 0.f};
        f32x4 sp = mfma((u32x4){kpp[0], kpp[1], prev.k2[0], prev.k2[1]}, qf, z4);
        f32x4 sc = mfma((u32x4){kcp[0], kcp[1], cur.k2[0], cur.k2[1]}, qf, z4);
        float e[8]; float mx = -3.0e38f;
#pragma unroll
        for (int r = 0; r < 4; ++r) {
            const int dp = t + 16 - 4 * qd - r, dc = t - 4 * qd - r;
            const bool vp = prev_ok && dp <= 14 && !(dp & 1), vc = dc >= 0 && !(dc & 1);
            e[r] = vp ? sp[r] * cs : -3.0e38f; e[4 + r] = vc ? sc[r] * cs : -3.0e38f;
            mx = fmaxf(mx, fmaxf(e[r], e[4 + r]));
        }
        mx = red_max_q(mx);
        float sum = 0.f;
#pragma unroll
        for (int r = 0; r < 8; ++r) { e[r] = __builtin_amdgcn_exp2f(e[r] - mx); sum += e[r]; }
        sum = red_add_q(sum);
        const float inv = __builtin_amdgcn_rcpf(sum);
        const u32x4 pf = {pack2(e[0] * inv, e[1] * inv), pack2(e[2] * inv, e[3] * inv), pack2(e[4] * inv, e[5] * inv), pack2(e[6] * inv, e[7] * inv)};
        const u32x2 vc2 = h == 0 ? cur.va0 : cur.va1, vp2 = h == 0 ? prev.va0 : prev.va1;
        oa[h] = mfma((u32x4){vp2[0], vp2[1], vc2[0], vc2[1]}, pf, z4);
        oz[h] = mfma((u32x4){prev.z[0], prev.z[1], cur.z[0], cur.z[1]}, pf, z4);
    }
    const u32x2 a0 = pack4(oa[0]), a1 = pack4(oa[1]);
    omain = (u32x4){a0[0], a0[1], a1[0], a1[1]};
    const u32x2 l0 = pack4(oz[0]), l1 = pack4(oz[1]);
    oleft = qd == 0 ? l0 : l1;
}

__global__ __launch_bounds__(256, 1) void adaLN_block_kernel(Params p) {
    extern __shared__ __attribute__((aligned(16))) char lds[];
    const int b = blockIdx.x, w = __builtin_amdgcn_readfirstlane(threadIdx.x >> 6);
    const int xcd = b & 7, bi = b >> 3;
    char* ws = p.ws;
    const rsrc_t rs = mk_rsrc(ws, WS_END);
    STAMP(0);
    LSTAMP_DECL;
    {
        unsigned long long pc; unsigned len;
        asm volatile("s_getpc_b64 %0\n.Ladaln_pc:\n\ts_mov_b32 %1, .Ladaln_end-.Ladaln_pc" : "=s"(pc), "=s"(len));
        unsigned off = (bi * 256 + threadIdx.x) * 64u;
        off = off < len ? off : 0u;
        const unsigned long long a = pc + off;
        const unsigned dst = (unsigned)(size_t)(LAS char*)lds + LDS_BYTES - 1024 + w * 256;
        asm volatile("s_mov_b32 m0, %0\n\ts_nop 0\n\tglobal_load_lds_dword %1, off" :: "s"(dst), "v"(a) : "memory");
    }
    const unsigned epoch = *(const __attribute__((address_space(4))) unsigned*)(size_t)(ws + WS_EPOCH);
    unsigned tag;
#define MAKE_TAG() do { unsigned e_ = epoch; asm volatile("" : "+s"(e_)); tag = e_ + 0x9E3779B9u; } while (0)

    DOut dout;
    {
        LANE_IDS();
        const bool isA = bi < 20, isB = !isA && bi < 25, isC = !isA && !isB;
        const int sA = bi < 16 ? xcd + 8 * (bi >> 3) : 16 + (xcd >> 1), jg = bi < 16 ? (bi & 7) : 4 * (xcd & 1) + (bi - 16), idxB = xcd * 5 + (bi - 20);
        const int aidx = jg * 20 + sA;
        const int idxC = xcd * 7 + (bi - 25);
        const int Ts = isB ? idxB : (idxC < 32 ? 40 + 2 * idxC : 72 + idxC), nT = (isC && idxC < 32) ? 2 : 1;
        const int did = w == 0 ? 1000 : b * 3 + w - 1;
        DPlan dp;
        if (!isA) {
            x_dma(p, lds, L0_XI, Ts, w, l);
            if (nT == 2) x_dma(p, lds, 0, Ts + 1, w, l);
            MAKE_TAG();
            dtask_plan(p, did, l, dp, dout);
            STAMP(18);
            if (nT == 2) { WAITV(5); x_stats(lds, L0_XI, L0_XS, w, l); WAITV(0); x_stats(lds, 0, L0_XS + 128, w, l); }
            else { WAITV(0); x_stats(lds, L0_XI, L0_XS, w, l); }
            BAR();
            x_frags(lds, L0_XI, L0_XS, rs, Ts, w, l);
            if (nT == 2) x_frags(lds, 0, L0_XS + 128, rs, Ts + 1, w, l);
            if (isB) {
                c_dma(p, lds, w, l);
                wm_dma<0, 20>(p, lds, w, l, 1280 + 64 * idxB, 1280 + 64 * idxB + 32, 0);
                WAITV(21);
            } else {
                dtask_go(dp, lds, L0_D, w);
                if (dp.on) WAITV(8); else WAITV(0);
            }
            BAR();
            flag_put(rs, WS_FC + (Ts + (tid & 1)) * FS, tag, tid < nT);
            STAMP(20);
        }
        if (!isC) {
            if (isA) {
                c_dma(p, lds, w, l);
                wm_dma<0, 10>(p, lds, w, l, 640 + 32 * sA, 32 * sA, 10 * jg);
                WAITV(10);
                silu_table(lds, tid);
                wm_dma<10, 20>(p, lds, w, l, 640 + 32 * sA, 32 * sA, 10 * jg);
                const rsrc_t rq = mk_rsrc(p.wqkv, 640 * 3840);
#pragma unroll
                for (int m = 0; m < 4; ++m) {
                    const int n = 64 * (4 * m + w) + l, rho = n / 30, c = n % 30;
                    const int k = (rho & 19) | ((rho & 4) << 1) | ((rho & 8) >> 1);
                    if (4 * m + w < 15) dma16(lds, L0_RW + (4 * m + w) * 1024, rq, k * 3840 + (c / 10) * 1280 + jg * 160 + (c % 10) * 16, sA * 32 * 3840);
                }
                MAKE_TAG();
                STAMP(18);
            } else {
                WAITV(21);
                silu_table(lds, tid);
            }
            WAITV(0);
            BAR();
            float v = mod_gemv(lds, w, l);
            STAMP(19);
            if (isA) {
                const float gv = w < 2 ? rbf(1.f + v) : v;
                if (l < 16) { STF(L0_GS + (w * 16 + l) * 4, gv); *(LAS u16*)((LAS char*)lds + L0_GS + 256 + (w * 16 + l) * 2) = (u16)(__float_as_uint(gv) >> 16); }
                const int q = (l >> 2) & 3, pp = l & 3, jj = w >> 1, j = 2 * jg + jj;
                const unsigned rbase = L0_RW + (16 * (qd >> 1) + 4 * (qd & 1) + q) * 480 + 80 * jj;
                u32x2 ua[4], ub[4];
#pragma unroll
                for (int i = 0; i < 4; ++i) {
                    const unsigned lo = i < 2 ? i * 40 + 8 * pp : 160 + (i - 2) * 40 + 8 * pp;
                    const unsigned hi = i == 0 ? (pp >> 1) * 160 + (pp & 1) * 40 + 32 : i < 3 ? 320 + (i - 1) * 40 + 8 * pp : 320 + (pp & 1) * 40 + 32;
                    const unsigned po = (w & 1) ? hi : lo;
                    ua[i] = tr_read(lds, rbase + po); ub[i] = tr_read(lds, rbase + po + 8 * 480);
                }
                if ((w & 1) && t >= 8) { ua[3] = (u32x2){0u, 0u}; ub[3] = ua[3]; }
                BAR();
                const f32x4 g0 = LDF4(L0_GS + qd * 32), g1 = LDF4(L0_GS + qd * 32 + 16);
                const u32x4 sfr = LD16(L0_GS + 256 + 64 + qd * 16);
#pragma unroll
                for (int i = 0; i < 4; ++i) {
                    const int nt = 4 * (w & 1) + i;
                    const u32x4 o = {pack2(lo_f(ua[i][0]) * g0[0], hi_f(ua[i][0]) * g0[1]), pack2(lo_f(ua[i][1]) * g0[2], hi_f(ua[i][1]) * g0[3]),
                                     pack2(lo_f(ub[i][0]) * g1[0], hi_f(ub[i][0]) * g1[1]), pack2(lo_f(ub[i][1]) * g1[2], hi_f(ub[i][1]) * g1[3])};
                    bstore16<SC1>(o, rs, lo16, WS_WQF + ((j * 20 + sA) * 8 + nt) * 1024);
                }
                {
                    const f32x4 z4 = {0, 0, 0, 0};
                    f32x4 bias = mfma((u32x4){ua[0][0], ua[0][1], ub[0][0], ub[0][1]}, sfr, z4);
#pragma unroll
                    for (int i = 1; i < 4; ++i) { const f32x4 bi4 = mfma((u32x4){ua[i][0], ua[i][1], ub[i][0], ub[i][1]}, sfr, z4); bias = t == i ? bi4 : bias; }
                    bstore16<SC1>(asu4(bias), rs, t < 4 ? t * 64 + qd * 16 : 0x7ff00000u, WS_BP + ((sA * 16 + j) * 128 + (w & 1) * 64) * 4);
                }
                STAMP(20);
                dtask_plan(p, did, l, dp, dout);
            } else {
                const int col = 1280 + 64 * idxB + 16 * w + t;
                if (col >= 2560 && col < 3200) v = rbf(1.f + v);
                bstore4<SC1>(__float_as_uint(v), rs, l < 16 ? l * 4 : 0x7ff00000u, WS_MODV + (1280 + 64 * idxB + 16 * w) * 4);
            }
            dtask_go(dp, lds, L0_D, w);
            if (dp.on) WAITV(8); else WAITV(0);
            BAR();
            flag_put(rs, WS_FA + aidx * FS, tag, isA && tid == 0);
        }
        STAMP(1);
    }

    const int g1 = 4 * (xcd >> 1) + (bi >> 3), j1 = 8 * (xcd & 1) + (bi & 7);
    const int P2 = 4 * xcd + (bi >> 3), c2 = bi & 7;
    const int g3 = g1, j3 = j1;
    const int rot2 = ROT2, rot3 = ROT3;
    auto wrap20 = [](int x) { return x >= 20 ? x - 20 : x; };
    const int t3 = j3 < 12 ? 7 * j3 : 84 + 6 * (j3 - 12), cnt3 = j3 < 12 ? 7 : 6;
    u32x4 w2f[14][5];
    const int s4 = __builtin_amdgcn_readfirstlane(w < 2 ? 14 * w : 28 + 13 * (w - 2)), cnt4 = __builtin_amdgcn_readfirstlane(w < 2 ? 14 : 13);
    const unsigned w2b = __builtin_amdgcn_readfirstlane(WS_W2F + (c2 * 54 + s4) * 5 * 1024);
    const int rot4 = __builtin_amdgcn_readfirstlane(ROT4);
    auto step4 = [&](int i) { const int x = (i < 13 ? i : cnt4 - 1) + rot4; return x >= cnt4 ? x - cnt4 : x; };
    auto w2_ld = [&](int f, unsigned lo) { w2f[f / 5][f % 5] = bload16<SC1>(rs, lo, w2b + step4(f / 5) * 5120 + (f % 5) * 1024); };
    constexpr int L2_WO = 61440, L2_X = 46080;
    {
        LANE_IDS();
        const int T0 = 8 * g1 + 2 * w, TH = g1 == 0 ? 0 : 8 * g1 - 1;
        const unsigned xb0 = WS_XNF + T0 * 20480, xbh = WS_XNF + TH * 20480 + (w >> 1) * 1024, wb = WS_WQF + j1 * 163840 + w * 4096;
        const unsigned wob = WS_WOF + c2 * 102400;
        u32x4 xr[10][4], xh[10];
        const int rot1 = ROT1;
        auto gr = [&](int G) { const int x = G + rot1; return x >= 10 ? x - 10 : x; };
        auto w_dma = [&](int G, int k) { dma16s(lds, (G & 3) * 16384 + w * 4096 + k * 1024, rs, lo16, wb + gr(G) * 16384 + k * 1024); };
        auto x_ld = [&](int G, int i) { xr[G][i] = bload16<SC1>(rs, lo16, xb0 + (i & 1) * 20480 + (2 * gr(G) + (i >> 1)) * 1024); };
        if (w == 0) {
            unsigned off[1] = {l == 0 ? WS_FC + TH * FS : WS_FC + (8 * g1 + ((l - 1) & 7)) * FS};
            poll_words<1>(ws, off, l < 9, tag);
        }
        BAR();
#pragma unroll
        for (int G = 0; G < 4; ++G) {
#pragma unroll
            for (int i = 0; i < 4; ++i) x_ld(G, i);
            xh[G] = bload16<SC1>(rs, lo16, xbh + gr(G) * 2048);
        }
        SB();
        if (w == 0) {
            unsigned off[1] = {WS_FA + ((j1 >> 1) * 20 + (l < 20 ? l : 0)) * FS};
            poll_words<1>(ws, off, l < 20, tag);
        }
        DRAIN();
        if (w > 0) dtask_read(dout, lds, L0_D, w, l);
        else {
#pragma unroll
            for (int i = 0; i < 8; ++i) dout.f[i] = (u32x4){0u, 0u, 0u, 0u};
        }
        BAR();
        STAMP(2);
#pragma unroll
        for (int G = 0; G < 3; ++G)
#pragma unroll
            for (int k = 0; k < 4; ++k) w_dma(G, k);
        WAITV(4);
        BAR();
        STAMP(3);
        f32x4 acc[2][8], hacc[3];
#pragma unroll
        for (int i = 0; i < 8; ++i) { acc[0][i] = (f32x4){0, 0, 0, 0}; acc[1][i] = (f32x4){0, 0, 0, 0}; }
#pragma unroll
        for (int i = 0; i < 3; ++i) hacc[i] = (f32x4){0, 0, 0, 0};
        u32x4 wf[2][8];
#pragma unroll
        for (int n = 0; n < 8; ++n) wf[0][n] = LD16(n * 1024 + lo16);
        const unsigned hoff = (w >> 1) * 8192 + ((w & 1) ? 5 : 2) * 1024 + lo16;
        unsigned bpv[20];
        SB();
#pragma unroll
        for (int G = 0; G < 10; ++G) {
            u32x4 hw[3];
#pragma unroll
            for (int st = 0; st < 2; ++st) {
                const int s = 2 * G + st;
#pragma unroll
                for (int n = 0; n < 8; ++n) {
                    if (s + 1 < 20) wf[(s + 1) & 1][n] = LD16((((s + 1) >> 1) & 3) * 16384 + ((s + 1) & 1) * 8192 + n * 1024 + lo16);
                    if (st == 0 && n < 3) hw[n] = LD16((G & 3) * 16384 + hoff + n * 1024);
                    if (n < 5) { acc[0][n] = mfma(wf[s & 1][n], xr[G][st * 2], acc[0][n]); acc[1][n] = mfma(wf[s & 1][n], xr[G][st * 2 + 1], acc[1][n]); }
                    else { acc[0][n] = mfma(xr[G][st * 2], wf[s & 1][n], acc[0][n]); acc[1][n] = mfma(xr[G][st * 2 + 1], wf[s & 1][n], acc[1][n]); }
                    if (st == 1 && n >= 5) hacc[n - 5] = mfma(hw[n - 5], xh[G], hacc[n - 5]);
                    if (st == 0) {
                        if (G + 3 < 10 && (n & 1) == 0) w_dma(G + 3, n >> 1);
                        if (G + 4 < 10 && n == 7) xh[G + 4] = bload16<SC1>(rs, lo16, xbh + gr(G + 4) * 2048);
                    } else {
                        if (G + 4 < 10 && (n & 1) == 0) x_ld(G + 4, n >> 1);
                    }
                    if (G == 8 && st == 0 && n < 5) {
#pragma unroll
                        for (int k = 0; k < 4; ++k) bpv[4 * n + k] = bload4<SC1>(rs, (tid & 127) * 4, WS_BP + ((4 * n + k) * 16 + j1) * 512);
                    }
                    if (n == 3) {
                        if (s == 8) dtask_frag<0>(dout, rs, l);
                        if (s == 9) dtask_frag<1>(dout, rs, l);
                        if (s == 10) dtask_frag<2>(dout, rs, l);
                        if (s == 11) dtask_frag<3>(dout, rs, l);
                        if (s == 12) dtask_frag<4>(dout, rs, l);
                        if (s == 13) dtask_frag<5>(dout, rs, l);
                        if (s == 14) dtask_frag<6>(dout, rs, l);
                        if (s == 15) dtask_frag<7>(dout, rs, l);
                    }
                    SB();
                }
            }
            if (G == 0) WAITV(9); else if (G <= 3) WAITV(14); else if (G == 4) WAITV(16); else if (G == 5) WAITV(17); else if (G == 6) WAITV(12); else if (G == 7) WAITV(3); else if (G == 8) WAITV(20);
            BAR();
            if (G == 8) flag_put(rs, WS_FD + b * 4, tag, tid == 0);
        }
        STAMP(4);
        constexpr int L1_BIAS = 6144, L1_HP = 0, L1_EX = 8192, L1_VT = 6656;
        {
            float bs = 0.f;
#pragma unroll
            for (int i = 0; i < 20; ++i) bs += __uint_as_float(bpv[i]);
            if (tid < 128) STF(L1_BIAS + tid * 4, bs);
        }
        if (w >= 2) {
#pragma unroll
            for (int i = 0; i < 3; ++i) STF4(L1_HP + ((w & 1) * 3 + i) * 1024 + lo16, hacc[i]);
        }
        BAR();
        f32x4 bq[5]; float bv[3];
#pragma unroll
        for (int n = 0; n < 5; ++n) bq[n] = LDF4(L1_BIAS + (n * 16 + 4 * qd) * 4);
#pragma unroll
        for (int n = 0; n < 3; ++n) bv[n] = LDF(L1_BIAS + ((5 + n) * 16 + t) * 4);
        TileKV kv[2]; u32x2 qa[2][2], xq[2];
#pragma unroll
        for (int tt = 0; tt < 2; ++tt) {
            qa[tt][0] = pack4(acc[tt][0] + bq[0]); qa[tt][1] = pack4(acc[tt][1] + bq[1]);
            kv[tt].ka0 = pack4(acc[tt][2] + bq[2]); kv[tt].ka1 = pack4(acc[tt][3] + bq[3]);
            xq[tt] = pack4(acc[tt][4] + bq[4]);
            kv[tt].k2[0] = swap32(xq[tt][0]); kv[tt].k2[1] = swap32(xq[tt][1]);
            kv[tt].va0 = pack4(acc[tt][5] + bv[0]); kv[tt].va1 = pack4(acc[tt][6] + bv[1]); kv[tt].z = pack4(acc[tt][7] + bv[2]);
        }
        if (w < 3) {
            const unsigned e = L1_EX + (w + 1) * 3072 + lo16;
            ST16(e, ((u32x4){kv[1].ka0[0], kv[1].ka0[1], kv[1].ka1[0], kv[1].ka1[1]}));
            ST16(e + 1024, ((u32x4){kv[1].k2[0], kv[1].k2[1], kv[1].va0[0], kv[1].va0[1]}));
            ST16(e + 2048, ((u32x4){kv[1].va1[0], kv[1].va1[1], kv[1].z[0], kv[1].z[1]}));
        }
        if (w == 0) {
            const u32x2 k0 = pack4(hacc[0] + LDF4(L1_HP + lo16) + bq[2]), k1 = pack4(hacc[1] + LDF4(L1_HP + 1024 + lo16) + bq[3]);
            const u32x2 xx = pack4(hacc[2] + LDF4(L1_HP + 2048 + lo16) + bq[4]);
            ST16(L1_EX + lo16, ((u32x4){k0[0], k0[1], k1[0], k1[1]}));
            ST8(L1_EX + 1024 + lo16, ((u32x2){swap32(xx[0]), swap32(xx[1])}));
        } else if (w == 1) {
#pragma unroll
            for (int i = 0; i < 3; ++i) {
                const f32x4 v = hacc[i] + LDF4(L1_HP + (3 + i) * 1024 + lo16) + LDF4(L1_BIAS + ((5 + i) * 16 + 4 * qd) * 4);
                const u32x2 pk = pack4(v);
                LAS u16* d = (LAS u16*)((LAS char*)lds + L1_VT + i * 512 + (4 * qd) * 32 + t * 2);
                d[0] = (u16)pk[0]; d[16] = (u16)(pk[0] >> 16); d[32] = (u16)pk[1]; d[48] = (u16)(pk[1] >> 16);
            }
        }
        const unsigned ob = WS_OF + T0 * 20480;
        u32x4 om; u32x2 ol;
        const u32x4 fdv = bload16<SC1>(rs, lo16, WS_FD);
        SB();
        attn_unit(qa[1], xq[1], kv[1], kv[0], true, t, qd, om, ol);
        SB();
        if (w == 0) {
            check_all_blocks(ws, rs, WS_FD, fdv, tag);
            if (b == 0 && l == 0) flag_store(ws, WS_EPOCH, tag);
        }
        bstore16<SC1>(om, rs, lo16, ob + 20480 + j1 * 1024);
        if (qd < 2) bstore8<SC1>(ol, rs, ((j1 & 3) * 16 + t) * 16 + 8 * qd, ob + 20480 + (16 + (j1 >> 2)) * 1024);
        BAR();
        TileKV pv;
        {
            const unsigned e = L1_EX + w * 3072 + lo16;
            const u32x4 a = LD16(e), bb = LD16(e + 1024), cc = LD16(e + 2048);
            pv.ka0 = (u32x2){a[0], a[1]}; pv.ka1 = (u32x2){a[2], a[3]}; pv.k2 = (u32x2){bb[0], bb[1]}; pv.va0 = (u32x2){bb[2], bb[3]};
            pv.va1 = (u32x2){cc[0], cc[1]}; pv.z = (u32x2){cc[2], cc[3]};
            if (w == 0) { pv.va0 = LD8(L1_VT + t * 32 + qd * 8); pv.va1 = LD8(L1_VT + 512 + t * 32 + qd * 8); pv.z = LD8(L1_VT + 1024 + t * 32 + qd * 8); }
        }
        SB();
#pragma unroll
        for (int f = 0; f < 12; ++f) w2_ld(f, lo16);
        attn_unit(qa[0], xq[0], kv[0], pv, !(g1 == 0 && w == 0), t, qd, om, ol);
#pragma unroll
        for (int f = 0; f < 12; ++f) { SGB(0x1, 17); SGB(0x20, 1); }
        SB();
        bstore16<SC1>(om, rs, lo16, ob + j1 * 1024);
        if (qd < 2) bstore8<SC1>(ol, rs, ((j1 & 3) * 16 + t) * 16 + 8 * qd, ob + (16 + (j1 >> 2)) * 1024);
        STAMP(5);
        publish(rs, WS_F1 + (g1 * 16 + j1) * FS, tag);
#pragma unroll
        for (int k = 0; k < 25; ++k) dma16s(lds, L2_WO + w * 25600 + k * 1024, rs, lo16, wob + wrap20(5 * w + k / 5 + rot2) * 5120 + (k % 5) * 1024);
        {
            const rsrc_t rx = mk_rsrc(p.x, 2048 * 1280);
#pragma unroll
            for (int k = 0; k < 3; ++k) {
                const int n = (w * 3 + k) * 64 + l, row = n / 10, cc = n - 10 * row;
                if (w * 3 + k < 10) dma16(lds, L2_X + (w * 3 + k) * 1024, rx, row * 1280 + cc * 16, P2 * 64 * 1280 + c2 * 160);
            }
        }
    }
    STAMP(6);

    u32x2 x1b[5], b1v[7];
    u32x4 vraw[5][2];
    {
        LANE_IDS();
        const int T = 4 * P2 + w;
        constexpr int L2_MV = 57344;
        const u32x4 mvl = bload16<SC1>(rs, (1280 + ((tid / 20) % 3) * 640 + 80 * c2 + (tid % 20) * 4) * 4, WS_MODV);
        SB();
        if (tid < 60) ST16(L2_MV + tid * 16, mvl);
        if (w == 0) {
            unsigned off[1] = {WS_F1 + ((P2 >> 1) * 16 + (l & 15)) * FS};
            poll_words<1>(ws, off, l < 16, tag);
        }
        DRAIN();
        BAR();
        STAMP(7);
        u32x2 xv[5];
#pragma unroll
        for (int n = 0; n < 5; ++n) xv[n] = LD8(L2_X + (16 * w + t) * 160 + n * 32 + qd * 8);
        constexpr int PW = 8;
        u32x4 tk[20];
#pragma unroll
        for (int s = 0; s < PW; ++s) tk[s] = bload16<SC1>(rs, lo16, WS_OF + (T * 20 + wrap20(s + rot2)) * 1024);
        f32x4 acc[5];
#pragma unroll
        for (int n = 0; n < 5; ++n) acc[n] = (f32x4){0, 0, 0, 0};
        unsigned wb0 = L2_WO + lo16, wb1 = L2_WO + 51200 + lo16; OPAQUE(wb0); OPAQUE(wb1);
        auto wo_rd = [&](int s, int n) { return s < 10 ? LD16(wb0 + (s * 5 + n) * 1024) : LD16(wb1 + ((s - 10) * 5 + n) * 1024); };
        u32x4 wf[3][5];
#pragma unroll
        for (int n = 0; n < 5; ++n) { wf[0][n] = wo_rd(0, n); wf[1][n] = wo_rd(1, n); }
        SB();
        LSTAMP(40);
#pragma unroll
        for (int s = 0; s < 20; ++s) {
            if (s + PW < 20) tk[s + PW] = bload16<SC1>(rs, lo16, WS_OF + (T * 20 + wrap20(s + PW + rot2)) * 1024);
            if (s + 2 < 20) {
#pragma unroll
                for (int n = 0; n < 5; ++n) wf[(s + 2) % 3][n] = wo_rd(s + 2, n);
            }
#pragma unroll
            for (int n = 0; n < 5; ++n) acc[n] = mfma(wf[s % 3][n], tk[s], acc[n]);
#pragma unroll
            for (int n = 0; n < 5; ++n) { if (s + 2 < 20) SGB(0x100, 1); SGB(0x8, 1); if (n == 1 && s + PW < 20) SGB(0x20, 1); }
            SB();
            if ((s & 3) == 3 || s == 0) LSTEP(41 + (s + 1) / 4);
        }
        STAMP(8);
        BAR();
        const unsigned w1src = WS_W1F + t3 * 20480, w1last = (cnt3 == 7 ? 6 : 0) * 20480;
        auto w1_dma = [&](int base_step, int i) {
            const int n = i % 7;
            dma16s(lds, base_step * 7168 + i * 1024, rs, lo16, w1src + (n == 6 ? w1last : n * 20480) + wrap20(base_step + i / 7 + rot3) * 1024);
        };
        float st[6] = {0, 0, 0, 0, 0, 0};
        u32x2 ub[5];
        const unsigned ufb = WS_UF + T * 20480;
#pragma unroll
        for (int n = 0; n < 5; ++n) {
            const f32x4 ga_n = LDF4(L2_MV + (16 * n + 4 * qd) * 4), s2_n = LDF4(L2_MV + 320 + (16 * n + 4 * qd) * 4), g2_n = LDF4(L2_MV + 640 + (16 * n + 4 * qd) * 4);
            float x1[4] = {lo_f(xv[n][0]), hi_f(xv[n][0]), lo_f(xv[n][1]), hi_f(xv[n][1])};
#pragma unroll
            for (int r = 0; r < 4; ++r) x1[r] = fmaf(ga_n[r], acc[n][r], x1[r]);
            x1b[n] = (u32x2){pack2(x1[0], x1[1]), pack2(x1[2], x1[3])};
            const float xr[4] = {lo_f(x1b[n][0]), hi_f(x1b[n][0]), lo_f(x1b[n][1]), hi_f(x1b[n][1])};
            ub[n] = (u32x2){pack2(xr[0] * g2_n[0], xr[1] * g2_n[1]), pack2(xr[2] * g2_n[2], xr[3] * g2_n[3])};
            const float ur[4] = {lo_f(ub[n][0]), hi_f(ub[n][0]), lo_f(ub[n][1]), hi_f(ub[n][1])};
#pragma unroll
            for (int r = 0; r < 4; ++r) {
                st[0] += xr[r]; st[1] = fmaf(xr[r], xr[r], st[1]); st[2] += ur[r]; st[3] = fmaf(ur[r], g2_n[r], st[3]);
                st[4] = fmaf(ur[r], ur[r], st[4]); st[5] = fmaf(ur[r], s2_n[r], st[5]);
            }
            if (n == 1) bstore16<SC1>((u32x4){ub[0][0], ub[0][1], ub[1][0], ub[1][1]}, rs, lo16, ufb + (2 * c2) * 1024);
            if (n == 3) bstore16<SC1>((u32x4){ub[2][0], ub[2][1], ub[3][0], ub[3][1]}, rs, lo16, ufb + (2 * c2 + 1) * 1024);
            if (n == 4) bstore8<SC1>(ub[4], rs, lo16 + 8 * (c2 & 1), ufb + (16 + (c2 >> 1)) * 1024);
#pragma unroll
            for (int k = 0; k < 3; ++k) if (3 * n + k < 14) w1_dma(2 * w, 3 * n + k);
            SGB(0x20, 1); SGB(0x1, 18); SGB(0x20, 1); SGB(0x1, 18); SGB(0x20, 1); SGB(0x1, 24);
            SB();
        }
#pragma unroll
        for (int i = 0; i < 6; ++i) st[i] = red_add_q(st[i]);
        if (qd < 2) bstore16<SC1>(asu4(qd == 0 ? (f32x4){st[0], st[1], st[2], st[3]} : (f32x4){st[4], st[5], 0.f, 0.f}), rs, qd * 256 + t * 16, WS_ST + (T * 8 + c2) * 512);
#pragma unroll
        for (int n = 0; n < 5; ++n) asm volatile("" : "+v"(x1b[n]));
        STAMP(9);
        publish(rs, WS_F2 + (P2 * 8 + c2) * FS, tag);
#pragma unroll
        for (int i = 0; i < 5; ++i) {
            int ta, tb; tiles_p3(wrap20(5 * w + i + rot3), ta, tb);
            const unsigned sec = WS_MODV + (t == 0 ? 2560 : 1920) * 4 + qd * 16;
            vraw[i][0] = bload16<SC1>(rs, sec + ta * 64, 0); vraw[i][1] = bload16<SC1>(rs, sec + tb * 64, 0);
        }
#pragma unroll
        for (int n = 0; n < 7; ++n) b1v[n] = *(const GAS u32x2*)(p.b1 + 16 * (t3 + (n < cnt3 ? n : 0)) + 4 * qd);
        SB();
#pragma unroll
        for (int i = 0; i < 21; ++i) w1_dma(8 + 3 * w, i);
    }
    STAMP(10);

    {
        LANE_IDS();
        constexpr int L3_GSC = 143360;
        const int T0 = 8 * g3 + 2 * w;
        if (w == 0) {
            unsigned off[1] = {WS_F2 + ((2 * g3 + ((l >> 3) & 1)) * 8 + (l & 7)) * FS};
            poll_words<1>(ws, off, l < 16, tag);
        }
        DRAIN();
        BAR();
        STAMP(11);
        constexpr int PW = 4;
        u32x4 tk[20][2];
        u32x4 sp[2][4];
#pragma unroll
        for (int s = 0; s < PW; ++s) { const unsigned o = WS_UF + (T0 * 20 + wrap20(s + rot3)) * 1024; tk[s][0] = bload16<SC1>(rs, lo16, o); tk[s][1] = bload16<SC1>(rs, lo16, o + 20480); }
        SB();
        LSTAMP(62);
        {
            f32x4 va[7], gram = {0, 0, 0, 0};
            u32x4 vf[5], wv[2][7];
#pragma unroll
            for (int n = 0; n < 7; ++n) { va[n] = (f32x4){0, 0, 0, 0}; wv[0][n] = LD16((5 * w * 7 + n) * 1024 + lo16); }
#pragma unroll
            for (int i = 0; i < 5; ++i) {
                const f32x4 a = asf4(vraw[i][0]), c = asf4(vraw[i][1]);
                const unsigned cst = t == 2 ? 0x3f803f80u : 0u;
                const unsigned d0 = pack2(a[0], a[1]), d1 = pack2(a[2], a[3]), d2 = pack2(c[0], c[1]), d3 = pack2(c[2], c[3]);
                vf[i] = (u32x4){t < 2 ? d0 : cst, t < 2 ? d1 : cst, t < 2 ? d2 : cst, t < 2 ? d3 : cst};
            }
            SB();
#pragma unroll
            for (int i = 0; i < 5; ++i) {
                if (i + 1 < 5) {
#pragma unroll
                    for (int n = 0; n < 7; ++n) wv[(i + 1) & 1][n] = LD16(((5 * w + i + 1) * 7 + n) * 1024 + lo16);
                }
                SB();
#pragma unroll
                for (int n = 0; n < 7; ++n) va[n] = mfma(wv[i & 1][n], vf[i], va[n]);
                gram = mfma(vf[i], vf[i], gram);
                SB();
            }
            if (t < 3) {
#pragma unroll
                for (int n = 0; n < 7; ++n) STF4(L3_GSC + ((w * 8 + n) * 3 + t) * 64 + qd * 16, va[n]);
                if (qd == 0) STF4(L3_GSC + ((w * 8 + 7) * 3 + t) * 64, gram);
            }
        }
        SB();
        f32x4 acc[2][7];
#pragma unroll
        for (int n = 0; n < 7; ++n) { acc[0][n] = (f32x4){0, 0, 0, 0}; acc[1][n] = (f32x4){0, 0, 0, 0}; }
        unsigned wb0 = lo16, wb1 = 57344 + lo16, wb2 = 114688 + lo16; OPAQUE(wb0); OPAQUE(wb1); OPAQUE(wb2);
        auto w1_rd = [&](int s, int n) { return s < 8 ? LD16(wb0 + (s * 7 + n) * 1024) : s < 16 ? LD16(wb1 + ((s - 8) * 7 + n) * 1024) : LD16(wb2 + ((s - 16) * 7 + n) * 1024); };
        u32x4 wf[2][7];
#pragma unroll
        for (int n = 0; n < 7; ++n) wf[0][n] = w1_rd(0, n);
        SB();
        LSTAMP(0);
#pragma unroll
        for (int s = 0; s < 20; ++s) {
            int nv = 0;
            if (s + PW < 20) { const unsigned o = WS_UF + (T0 * 20 + wrap20(s + PW + rot3)) * 1024; tk[s + PW][0] = bload16<SC1>(rs, lo16, o); tk[s + PW][1] = bload16<SC1>(rs, lo16, o + 20480); nv += 2; }
            if (s < 16) { w2_ld(12 + s, lo16); nv += 1; }
            if (s >= 16) {
#pragma unroll
                for (int k = 0; k < 3; ++k) w2_ld(28 + (s - 16) * 3 + k, lo16);
                nv += 3;
            }
            if (s == 14 || s == 15) {
#pragma unroll
                for (int e = 0; e < 4; ++e) sp[s - 14][e] = bload16<SC1>(rs, (2 * qd + (e >> 1)) * 512 + (e & 1) * 256 + t * 16, WS_ST + (T0 + s - 14) * 4096);
                nv += 4;
            }
            if (s + 1 < 20) {
#pragma unroll
                for (int n = 0; n < 7; ++n) wf[(s + 1) & 1][n] = w1_rd(s + 1, n);
            }
#pragma unroll
            for (int n = 0; n < 7; ++n) { acc[0][n] = mfma(wf[s & 1][n], tk[s][0], acc[0][n]); acc[1][n] = mfma(wf[s & 1][n], tk[s][1], acc[1][n]); }
#pragma unroll
            for (int n = 0; n < 7; ++n) { if (s + 1 < 20) SGB(0x100, 1); SGB(0x8, 2); if (n < nv) SGB(0x20, 1); }
            SB();
            if (s == 2) BAR();
            if (s == 3 && tid < 96) {
                const int n8 = tid / 12, r12 = tid % 12;
                f32x4 a = LDF4(L3_GSC + (n8 * 12 + r12) * 16);
#pragma unroll
                for (int u = 1; u < 4; ++u) a += LDF4(L3_GSC + (u * 96 + n8 * 12 + r12) * 16);
                STF4(L3_GSC + 6144 + (n8 * 12 + r12) * 16, a);
            }
            if (s == 8) BAR();
            SB();
            LSTEP(1 + s);
        }
        STAMP(12);
        float al[2], be[2], gm[2], de[2];
        {
            const f32x4 r0 = LDF4(L3_GSC + 6144 + 84 * 16), r1 = LDF4(L3_GSC + 6144 + 88 * 16);
            const float Gg = r0[0], Ggs = r0[1], Gs = r0[2], Sss = r1[1], Ss = r1[2];
#pragma unroll
            for (int tt = 0; tt < 2; ++tt) {
                const f32x4 a0 = asf4(sp[tt][0]) + asf4(sp[tt][2]), b0 = asf4(sp[tt][1]) + asf4(sp[tt][3]);
                const float s0 = red_add_q(a0[0]), s1 = red_add_q(a0[1]), s2 = red_add_q(a0[2]), s3 = red_add_q(a0[3]), s4v = red_add_q(b0[0]), s5 = red_add_q(b0[1]);
                const float iD = 1.f / 640.f;
                const float m1 = s0 * iD, v1 = fmaxf(s1 * iD - m1 * m1, 0.f), r1v = rsqrtf(v1 + 1e-5f);
                const float m2 = (r1v * (s2 - m1 * Gs) + Ss) * iD;
                const float e2 = (r1v * r1v * (s4v - 2.f * m1 * s3 + m1 * m1 * Gg) + 2.f * r1v * (s5 - m1 * Ggs) + Sss) * iD;
                const float r2v = rsqrtf(fmaxf(e2 - m2 * m2, 0.f) + 1e-5f);
                al[tt] = r1v * r2v; be[tt] = -r1v * r2v * m1; gm[tt] = r2v; de[tt] = -r2v * m2;
            }
        }
        u32x2 hb[2][7];
        unsigned gsb = L3_GSC + 6144 + qd * 16; OPAQUE(gsb);
        f32x4 gsc[2][3];
#pragma unroll
        for (int k = 0; k < 3; ++k) gsc[0][k] = LDF4(gsb + 64 * k);
#pragma unroll
        for (int n = 0; n < 7; ++n) {
#pragma unroll
            for (int k = 0; k < 2; ++k) w2_ld(40 + n * 2 + k, lo16);
            if (n + 1 < 7) {
#pragma unroll
                for (int k = 0; k < 3; ++k) gsc[(n + 1) & 1][k] = LDF4(gsb + (n + 1) * 192 + 64 * k);
            }
            const f32x4 G = gsc[n & 1][0], Sv = gsc[n & 1][1], C = gsc[n & 1][2];
            const float bb[4] = {lo_f(b1v[n][0]), hi_f(b1v[n][0]), lo_f(b1v[n][1]), hi_f(b1v[n][1])};
#pragma unroll
            for (int tt = 0; tt < 2; ++tt) {
                float hv[4];
#pragma unroll
                for (int r = 0; r < 4; ++r) {
                    float z = fmaf(gm[tt], Sv[r], bb[r]); z = fmaf(be[tt], G[r], z); z = fmaf(de[tt], C[r], z); z = fmaf(al[tt], acc[tt][n][r], z);
                    const float u = z * fmaf(z * z, -0.044715f * 1.5957691216057308f * 1.4426950408889634f, -1.5957691216057308f * 1.4426950408889634f);
                    hv[r] = z * __builtin_amdgcn_rcpf(1.f + __builtin_amdgcn_exp2f(u));
                }
                hb[tt][n] = (u32x2){pack2(hv[0], hv[1]), pack2(hv[2], hv[3])};
            }
            SGB(0x20, 1); SGB(0x1, 40); SGB(0x20, 1); SGB(0x1, 60);
            SB();
#pragma unroll
            for (int tt = 0; tt < 2; ++tt) {
                const unsigned hbase = WS_HF + (T0 + tt) * 54 * 1024;
                if (n & 1) bstore16<SC1>((u32x4){hb[tt][n - 1][0], hb[tt][n - 1][1], hb[tt][n][0], hb[tt][n][1]}, rs, lo16, hbase + (3 * j3 + (n >> 1)) * 1024);
                if (n == 6 && j3 < 12) bstore8<SC1>(hb[tt][6], rs, lo16 + 8 * (j3 & 1), hbase + (48 + (j3 >> 1)) * 1024);
            }
            SB();
        }
        STAMP(13);
        publish(rs, WS_F3 + (g3 * 16 + j3) * FS, tag);
    }
    STAMP(14);

    {
        LANE_IDS();
        f32x4 gb[5]; u32x2 b2v[5];
#pragma unroll
        for (int f = 54; f < 70; ++f) w2_ld(f, lo16);
        SB();
        if (w == 0) {
            unsigned off[1] = {WS_F3 + ((P2 >> 1) * 16 + (l & 15)) * FS};
            poll_words<1>(ws, off, l < 16, tag);
        }
        BAR();
        STAMP(15);
        constexpr int PD = 3;
        u32x4 tk[14][4];
        const unsigned tokb = __builtin_amdgcn_readfirstlane(WS_HF + (4 * P2 * 54 + s4) * 1024);
        auto issue = [&](int i) {
            const int s = step4(i);
#pragma unroll
            for (int a = 0; a < 4; ++a) tk[i][a] = bload16<SC1>(rs, lo16, tokb + (((a + w) & 3) * 54 + s) * 1024);
        };
#pragma unroll
        for (int i = 0; i < PD; ++i) issue(i);
        f32x4 acc[4][5];
#pragma unroll
        for (int a = 0; a < 4; ++a)
#pragma unroll
            for (int n = 0; n < 5; ++n) acc[a][n] = (f32x4){0, 0, 0, 0};
        SB();
        LSTAMP(24);
#pragma unroll
        for (int i = 0; i < 14; ++i) {
            if (i < 13 || w < 2) {
                if (i + PD < 14) issue(i + PD);
                if (i == 11) {
#pragma unroll
                    for (int n = 0; n < 5; ++n) {
                        const int f = 80 * c2 + 16 * n;
                        gb[n] = asf4(bload16<SC1>(rs, qd * 16, WS_MODV + (3200 + f) * 4));
                        b2v[n] = *(const GAS u32x2*)(p.b2 + f + 4 * qd);
                    }
                }
#pragma unroll
                for (int a = 0; a < 4; ++a)
#pragma unroll
                    for (int n = 0; n < 5; ++n) acc[a][n] = mfma(w2f[i][n], tk[i][a], acc[a][n]);
#pragma unroll
                for (int a = 0; a < 4; ++a) { SGB(0x8, 2); if (i == 11) SGB(0x20, 3); else SGB(0x20, 1); SGB(0x8, 3); }
            }
            SB();
            LSTEP(25 + i);
        }
        STAMP(16);
#pragma unroll
        for (int a = 1; a < 4; ++a) {
            unsigned d = ((((a + w) & 3) * 3 + (3 - a)) * 5) * 1024 + lo16; OPAQUE(d);
#pragma unroll
            for (int n = 0; n < 5; ++n) STF4(d + n * 1024, acc[a][n]);
        }
        BAR();
        constexpr int L4_OUT = 61440;
        unsigned rb = w * 15360 + lo16; OPAQUE(rb);
#pragma unroll
        for (int n = 0; n < 5; ++n) {
            f32x4 v = acc[0][n];
#pragma unroll
            for (int u = 0; u < 3; ++u) v += LDF4(rb + (u * 5 + n) * 1024);
            const float x1[4] = {lo_f(x1b[n][0]), hi_f(x1b[n][0]), lo_f(x1b[n][1]), hi_f(x1b[n][1])};
            const float bv[4] = {lo_f(b2v[n][0]), hi_f(b2v[n][0]), lo_f(b2v[n][1]), hi_f(b2v[n][1])};
            float o[4];
#pragma unroll
            for (int r = 0; r < 4; ++r) o[r] = fmaf(gb[n][r], v[r] + bv[r], x1[r]);
            ST8(L4_OUT + (16 * w + t) * 176 + n * 32 + qd * 8, ((u32x2){pack2(o[0], o[1]), pack2(o[2], o[3])}));
        }
        asm volatile("s_waitcnt lgkmcnt(0)" ::: "memory");
        const rsrc_t ro = mk_rsrc(p.out, 2048 * 1280);
#pragma unroll
        for (int k = 0; k < 3; ++k) {
            const int ch = l + 64 * k;
            if (ch < 160) {
                const int row = ch / 10, cc = ch % 10;
                const u32x4 v = LD16(L4_OUT + (16 * w + row) * 176 + cc * 16);
                bstore16<OUT_AUX>(v, ro, row * 1280 + cc * 16, (16 * (4 * P2 + w)) * 1280 + c2 * 160);
            }
        }
    }
    STAMP(17);
    LSTAMP_DUMP;
    asm volatile(".Ladaln_end:" ::: "memory");
}

extern "C" void kernel_launch(void* const* d_in, const int* in_sizes, int n_in, void* d_out, int out_size, void* d_ws, size_t ws_size, hipStream_t stream) {
    Params p;
    p.x = (const u16*)d_in[0]; p.wqkv = (const u16*)d_in[1]; p.wo = (const u16*)d_in[2]; p.w1 = (const u16*)d_in[4]; p.b1 = (const u16*)d_in[5];
    p.w2 = (const u16*)d_in[6]; p.b2 = (const u16*)d_in[7]; p.c = (const u16*)d_in[8]; p.wm = (const u16*)d_in[9];
    p.out = (u16*)d_out; p.ws = (char*)d_ws;
    (void)hipFuncSetAttribute((const void*)adaLN_block_kernel, hipFuncAttributeMaxDynamicSharedMemorySize, LDS_BYTES);
    adaLN_block_kernel<<<256, 256, LDS_BYTES, stream>>>(p);
}
```

```cpp
#include <hip/hip_runtime.h>
#include <stdint.h>

typedef unsigned short u16;
typedef float f32x4 __attribute__((ext_vector_type(4)));
typedef float f32x2 __attribute__((ext_vector_type(2)));
typedef __bf16 bf16x8 __attribute__((ext_vector_type(8)));
typedef __bf16 bf16x2 __attribute__((ext_vector_type(2)));
typedef unsigned u32x4 __attribute__((ext_vector_type(4)));
typedef unsigned u32x2 __attribute__((ext_vector_type(2)));
typedef short s16x4 __attribute__((ext_vector_type(4)));
typedef __amdgpu_buffer_rsrc_t rsrc_t;
#define LAS __attribute__((address_space(3)))
#define GAS __attribute__((address_space(1)))
#define DI __device__ __forceinline__

static constexpr unsigned FS = 64;
static constexpr unsigned WS_EPOCH = 0;
static constexpr unsigned WS_TMO   = 256;
static constexpr unsigned WS_FA    = 1024;
static constexpr unsigned WS_FC    = WS_FA + 160 * FS;
static constexpr unsigned WS_FD    = WS_FC + 128 * FS;
static constexpr unsigned WS_F1    = WS_FD + 1024;
static constexpr unsigned WS_F2    = WS_F1 + 256 * FS;
static constexpr unsigned WS_F3    = WS_F2 + 256 * FS;
static constexpr unsigned WS_FEND  = WS_F3 + 256 * FS;
static constexpr unsigned WS_MODV = 131072;
static constexpr unsigned WS_BP   = WS_MODV + 16384;
static constexpr unsigned WS_XNF  = WS_BP + 20 * 16 * 128 * 4;
static constexpr unsigned WS_WQF  = WS_XNF + 128 * 20 * 1024;
static constexpr unsigned WS_WOF  = WS_WQF + 16 * 20 * 8 * 1024;
static constexpr unsigned WS_W1F  = WS_WOF + 8 * 20 * 5 * 1024;
static constexpr unsigned WS_W2F  = WS_W1F + 108 * 20 * 1024;
static constexpr unsigned WS_OF   = WS_W2F + 8 * 54 * 5 * 1024;
static constexpr unsigned WS_UF   = WS_OF + 128 * 20 * 1024;
static constexpr unsigned WS_ST   = WS_UF + 128 * 20 * 1024;
static constexpr unsigned WS_HF   = WS_ST + 128 * 8 * 512;
static constexpr unsigned WS_END  = WS_HF + 128 * 54 * 1024;
static_assert(WS_FEND <= WS_MODV, "flags overflow");

#define B_DELAY 0
#define OUT_AUX 0
#define ROT1 0
#define ROT2 0
#define ROT3 0
#define ROT4 0
static constexpr int LDS_BYTES = 163840;
static constexpr int SC1 = 16;
static constexpr unsigned SPIN_CAP = 40000;

#define STAMP(i)
#define LSTAMP(i)
#define LSTEP(i)
#define LSTAMP_DECL
#define LSTAMP_DUMP

struct Params {
    const u16* x; const u16* wqkv; const u16* wo; const u16* w1; const u16* b1; const u16* w2; const u16* b2; const u16* c; const u16* wm;
    u16* out; char* ws;
};

DI float lo_f(unsigned d) { return __uint_as_float(d << 16); }
DI float hi_f(unsigned d) { return __uint_as_float(d & 0xffff0000u); }
DI unsigned pack2(float a, float b) { f32x2 v = {a, b}; return __builtin_bit_cast(unsigned, __builtin_convertvector(v, bf16x2)); }
DI float rbf(float a) { return lo_f(pack2(a, 0.f)); }
DI u32x2 pack4(const f32x4& v) { return (u32x2){pack2(v[0], v[1]), pack2(v[2], v[3])}; }
DI f32x4 mfma(u32x4 a, u32x4 b, f32x4 c) { return __builtin_amdgcn_mfma_f32_16x16x32_bf16(__builtin_bit_cast(bf16x8, a), __builtin_bit_cast(bf16x8, b), c, 0, 0, 0); }
DI float red_add_q(float v) {
    auto a = __builtin_amdgcn_permlane16_swap(__float_as_uint(v), __float_as_uint(v), false, false);
    v = __uint_as_float(a[0]) + __uint_as_float(a[1]);
    auto b = __builtin_amdgcn_permlane32_swap(__float_as_uint(v), __float_as_uint(v), false, false);
    return __uint_as_float(b[0]) + __uint_as_float(b[1]);
}
DI float red_max_q(float v) {
    auto a = __builtin_amdgcn_permlane16_swap(__float_as_uint(v), __float_as_uint(v), false, false);
    v = fmaxf(__uint_as_float(a[0]), __uint_as_float(a[1]));
    auto b = __builtin_amdgcn_permlane32_swap(__float_as_uint(v), __float_as_uint(v), false, false);
    return fmaxf(__uint_as_float(b[0]), __uint_as_float(b[1]));
}
template <int C> DI float dpp_add(float v) { return v + __builtin_bit_cast(float, __builtin_amdgcn_update_dpp(0, __builtin_bit_cast(int, v), C, 0xf, 0xf, true)); }
DI float red16(float v) { v = dpp_add<0xB1>(v); v = dpp_add<0x4E>(v); v = dpp_add<0x141>(v); return dpp_add<0x140>(v); }
DI unsigned swap32(unsigned v) { auto a = __builtin_amdgcn_permlane32_swap(v, v, false, false); return (threadIdx.x & 32) ? a[0] : a[1]; }
DI rsrc_t mk_rsrc(const void* p, unsigned bytes) { return __builtin_amdgcn_make_buffer_rsrc((void*)p, 0, bytes, 0x00020000); }
template <int AUX> DI u32x4 bload16(rsrc_t r, unsigned voff, unsigned soff) { return __builtin_bit_cast(u32x4, __builtin_amdgcn_raw_buffer_load_b128(r, voff, soff, AUX)); }
template <int AUX> DI u32x2 bload8(rsrc_t r, unsigned voff, unsigned soff) { return __builtin_bit_cast(u32x2, __builtin_amdgcn_raw_buffer_load_b64(r, voff, soff, AUX)); }
template <int AUX> DI unsigned bload4(rsrc_t r, unsigned voff, unsigned soff) { return __builtin_amdgcn_raw_buffer_load_b32(r, voff, soff, AUX); }
template <int AUX> DI void bstore16(u32x4 v, rsrc_t r, unsigned voff, unsigned soff) { __builtin_amdgcn_raw_buffer_store_b128(v, r, voff, soff, AUX); }
template <int AUX> DI void bstore8(u32x2 v, rsrc_t r, unsigned voff, unsigned soff) { __builtin_amdgcn_raw_buffer_store_b64(v, r, voff, soff, AUX); }
template <int AUX> DI void bstore4(unsigned v, rsrc_t r, unsigned voff, unsigned soff) { __builtin_amdgcn_raw_buffer_store_b32(v, r, voff, soff, AUX); }
DI f32x4 asf4(u32x4 v) { return __builtin_bit_cast(f32x4, v); }
DI u32x4 asu4(f32x4 v) { return __builtin_bit_cast(u32x4, v); }
#define LANE_IDS() int tz_ = threadIdx.x; asm volatile("" : "+v"(tz_)); const int tid = tz_, l = tid & 63, t = l & 15, qd = l >> 4; const unsigned lo16 = l * 16; (void)t; (void)qd; (void)lo16
#define OPAQUE(x) asm volatile("" : "+v"(x))
#define SB() __builtin_amdgcn_sched_barrier(0)
#define BAR() asm volatile("s_waitcnt lgkmcnt(0)\n\ts_barrier" ::: "memory")
#define DRAIN() asm volatile("s_waitcnt vmcnt(0)" ::: "memory")
#define SGB(mask, n) __builtin_amdgcn_sched_group_barrier(mask, n, 0)
#define LD16(off) (*(const LAS u32x4*)((const LAS char*)lds + (off)))
#define LD8(off) (*(const LAS u32x2*)((const LAS char*)lds + (off)))
#define LDF4(off) (*(const LAS f32x4*)((const LAS char*)lds + (off)))
#define LDF(off) (*(const LAS float*)((const LAS char*)lds + (off)))
#define ST16(off, v) (*(LAS u32x4*)((LAS char*)lds + (off)) = (v))
#define ST8(off, v) (*(LAS u32x2*)((LAS char*)lds + (off)) = (v))
#define STF4(off, v) (*(LAS f32x4*)((LAS char*)lds + (off)) = (v))
#define STF(off, v) (*(LAS float*)((LAS char*)lds + (off)) = (v))
DI u32x2 tr_read(const char* lds, unsigned off) { return __builtin_bit_cast(u32x2, __builtin_amdgcn_ds_read_tr16_b64_v4i16((LAS s16x4*)((LAS char*)lds + off))); }

DI void flag_store(char* ws, unsigned off, unsigned tag) { __hip_atomic_store((GAS unsigned*)(ws + off), tag, __ATOMIC_RELAXED, __HIP_MEMORY_SCOPE_AGENT); }
DI unsigned flag_load(const char* ws, unsigned off) { return __hip_atomic_load((const GAS unsigned*)(ws + off), __ATOMIC_RELAXED, __HIP_MEMORY_SCOPE_AGENT); }
DI void flag_put(rsrc_t rs, unsigned off, unsigned tag, bool me) { bstore4<SC1>(tag, rs, me ? off : 0x7ff00000u, 0); }
DI void publish(rsrc_t rs, unsigned off, unsigned tag) { DRAIN(); BAR(); flag_put(rs, off, tag, threadIdx.x == 0); }
template <int N> DI void poll_words(char* ws, const unsigned (&off)[N], bool active, unsigned tag) {
#pragma nounroll
    for (unsigned spins = 0;; ++spins) {
        bool ok = true;
#pragma unroll
        for (int k = 0; k < N; ++k) ok &= flag_load(ws, off[k]) == tag;
        if (__all(ok || !active)) break;
        if (spins > SPIN_CAP) { if ((threadIdx.x & 63) == 0) flag_store(ws, WS_TMO, tag); break; }
        __builtin_amdgcn_s_sleep(1);
    }
}

DI void check_all_blocks(char* ws, rsrc_t rs, unsigned base, u32x4 v, unsigned tag) {
    asm volatile("" : "+v"(v));
#pragma nounroll
    for (unsigned spins = 0; !__all(v[0] == tag && v[1] == tag && v[2] == tag && v[3] == tag); ++spins) {
        if (spins > SPIN_CAP) { if ((threadIdx.x & 63) == 0) flag_store(ws, WS_TMO, tag); break; }
        __builtin_amdgcn_s_sleep(1);
        v = bload16<SC1>(rs, (threadIdx.x & 63) * 16, base);
        asm volatile("" : "+v"(v) :: "memory");
    }
}

DI void rows_wo(int s, int qd, int& ra, int& rb) { if (s < 16) { ra = 40 * s + 4 * qd; rb = ra + 20; } else { ra = 40 * (4 * (s - 16) + qd) + 16; rb = ra + 20; } }
DI void tiles_p3(int s, int& ta, int& tb) { if (s < 16) { ta = 5 * (s >> 1) + 2 * (s & 1); tb = ta + 1; } else { ta = 10 * (s - 16) + 4; tb = ta + 5; } }
DI void tiles_p4(int s, int& ta, int& tb) { if (s < 48) { int j = s / 3, pr = s % 3; int st = j < 12 ? 7 * j : 84 + 6 * (j - 12); ta = st + 2 * pr; tb = ta + 1; } else { int m = s - 48; ta = 14 * m + 6; tb = ta + 7; } }

static constexpr int L0_SC = 81920, L0_GS = 83200, L0_CRAW = 83968, L0_RW = 86016, L0_D = 101376, L0_XI = 134144, L0_XS = 156160;

template <int AUX> DI void dma(char* lds, unsigned lds_off, rsrc_t r, unsigned voff, unsigned soff) {
    __builtin_amdgcn_raw_ptr_buffer_load_lds(r, (LAS void*)((LAS char*)lds + lds_off), 16, voff, soff, 0, AUX);
}
DI void dma16(char* lds, unsigned lds_off, rsrc_t r, unsigned voff, unsigned soff) {
    const unsigned dst = (unsigned)(size_t)(LAS char*)lds + lds_off;
    asm volatile("s_mov_b32 m0, %0\n\ts_nop 0\n\tbuffer_load_dwordx4 %1, %2, %3 offen lds" :: "s"(dst), "v"(voff), "s"(r), "s"(soff) : "memory");
}
DI void dma16s(char* lds, unsigned lds_off, rsrc_t r, unsigned voff, unsigned soff) { dma<SC1>(lds, lds_off, r, voff, soff); }
#define WAITV(n) asm volatile("s_waitcnt vmcnt(%0)" :: "n"(n) : "memory")

struct DPlan { rsrc_t r; unsigned v0, v1, sa, sb, st; bool on; };
struct DOut { unsigned dst[8]; u32x4 f[8]; };
DI void dtask_plan(const Params& p, int id, int l, DPlan& d, DOut& o) {
    int ra0, rb0, rstep, cg, lim; unsigned pitch, d0, r5, stride, jump;
    if (id < 370) {
        const bool wo = id < 100; const int i2 = wo ? id : id - 100, s = i2 / 5; cg = i2 - 5 * s; pitch = 1280;
        if (wo) { d.r = mk_rsrc(p.wo, 640 * 1280); if (s < 16) { ra0 = 40 * s; rstep = 4; } else { ra0 = 160 * (s - 16) + 16; rstep = 40; } rb0 = ra0 + 20; }
        else { d.r = mk_rsrc(p.w2, 1728 * 1280); int ta, tb; tiles_p4(s, ta, tb); ra0 = 16 * ta; rb0 = 16 * tb; rstep = 4; }
        const int c0 = (8 * cg) / 5;
        r5 = 8 * cg - 5 * c0; d0 = (wo ? WS_WOF + (c0 * 20 + s) * 5120 : WS_W2F + (c0 * 54 + s) * 5120) + r5 * 1024;
        stride = 1024; jump = wo ? 19 * 5120 : 53 * 5120; lim = 8;
    } else {
        const int i2 = id < 650 ? id - 370 : 0, s = i2 / 14; cg = i2 - 14 * s; pitch = 3456; d.r = mk_rsrc(p.w1, 640 * 3456);
        int ta, tb; tiles_p3(s, ta, tb); ra0 = 16 * ta; rb0 = 16 * tb; rstep = 4;
        r5 = 0; d0 = WS_W1F + (8 * cg * 20 + s) * 1024; stride = 20480; jump = 0; lim = id < 650 ? (cg == 13 ? 4 : 8) : 0;
    }
    const int q = l >> 4, cp = l & 15;
    d.v0 = q * pitch + ((cp ^ (q << 1)) * 16); d.v1 = q * pitch + ((cp ^ ((4 + q) << 1)) * 16);
    d.sa = ra0 * pitch + cg * 256; d.sb = rb0 * pitch + cg * 256; d.st = rstep * pitch; d.on = lim > 0;
#pragma unroll
    for (int nt = 0; nt < 8; ++nt) {
        const unsigned v = d0 + nt * stride + ((r5 + nt >= 5 ? 1u : 0u) + (r5 + nt >= 10 ? 1u : 0u)) * jump;
        o.dst[nt] = __builtin_amdgcn_readfirstlane(nt < lim ? v : 0x7ff00000u);
    }
}
DI void dtask_go(const DPlan& d, char* lds, unsigned scr, int w) {
    if (d.on) {
#pragma unroll
        for (int i = 0; i < 8; ++i) dma16(lds, scr + w * 8192 + i * 1024, d.r, (i & 1) ? d.v1 : d.v0, (i < 4 ? d.sa : d.sb) + (i & 3) * d.st);
    }
}
DI void dtask_read(DOut& o, char* lds, unsigned scr, int w, int l) {
    const int qd = l >> 4, q = (l >> 2) & 3, pp = l & 3, r7 = 4 * (qd & 1) + q;
    const unsigned a0 = scr + w * 8192 + (4 * qd + q) * 256 + pp * 8;
#pragma unroll
    for (int nt = 0; nt < 8; ++nt) {
        const u32x2 ua = tr_read(lds, a0 + ((nt ^ r7) * 32)), ub = tr_read(lds, a0 + 4096 + ((nt ^ r7) * 32));
        o.f[nt] = (u32x4){ua[0], ua[1], ub[0], ub[1]};
    }
}
template <int NT> DI void dtask_frag(const DOut& o, rsrc_t rs, int l) { bstore16<SC1>(o.f[NT], rs, l * 16, o.dst[NT]); }

template <int I0, int I1> DI void wm_dma(const Params& p, char* lds, int w, int l, unsigned colA, unsigned colB, int rot) {
    const rsrc_t rw = mk_rsrc(p.wm, 640 * 3840 * 2);
    const int rl = l >> 3, cp = l & 7;
    const unsigned sg = ((rl >> 1) & 1);
    const unsigned c0 = cp ^ (sg << 1), c1 = cp ^ ((sg | 2) << 1);
    const unsigned v0 = rl * 7680 + (c0 < 4 ? colA + 8 * c0 : colB + 8 * (c0 - 4)) * 2, v1 = rl * 7680 + (c1 < 4 ? colA + 8 * c1 : colB + 8 * (c1 - 4)) * 2;
#pragma unroll
    for (int i = I0; i < I1; ++i) {
        int piece = 4 * i + w + rot; piece = piece >= 80 ? piece - 80 : piece;
        dma16(lds, piece * 1024, rw, (piece & 1) ? v1 : v0, piece * 8 * 7680);
    }
}
DI void c_dma(const Params& p, char* lds, int w, int l) {
    dma16(lds, L0_CRAW + (w & 1) * 1024, mk_rsrc(p.c, 1280), ((w & 1) * 64 + l) * 16, 0);
}
DI void silu_table(char* lds, int tid) {
    if (tid < 80) {
        const u32x4 creg = LD16(L0_CRAW + tid * 16);
        u32x4 o;
#pragma unroll
        for (int d = 0; d < 4; ++d) { const float a = lo_f(creg[d]), b = hi_f(creg[d]); o[d] = pack2(a * __builtin_amdgcn_rcpf(1.f + __expf(-a)), b * __builtin_amdgcn_rcpf(1.f + __expf(-b))); }
        ST16(L0_SC + tid * 16, o);
    }
}
DI float mod_gemv(char* lds, int w, int l) {
    const int qd = l >> 4, q = (l >> 2) & 3, pp = l & 3;
    f32x4 acc = {0, 0, 0, 0}, acc2 = {0, 0, 0, 0};
    const int r0 = 8 * qd + q;
    const unsigned sg = ((r0 >> 1) & 1) | (((r0 >> 3) & 1) << 1);
    unsigned a0 = r0 * 128 + ((w ^ sg) * 32) + pp * 8, a1 = a0 + 40960, sb = L0_SC + qd * 16;
    OPAQUE(a0); OPAQUE(a1); OPAQUE(sb);
    u32x2 tb[2][4]; u32x4 sc[2][2];
    auto rd = [&](int pr) {
        const unsigned ab = pr < 5 ? a0 : a1; const int po = (pr < 5 ? pr : pr - 5) * 8192;
        tb[pr & 1][0] = tr_read(lds, ab + po); tb[pr & 1][1] = tr_read(lds, ab + po + 512); tb[pr & 1][2] = tr_read(lds, ab + po + 4096); tb[pr & 1][3] = tr_read(lds, ab + po + 4608);
        sc[pr & 1][0] = LD16(sb + pr * 128); sc[pr & 1][1] = LD16(sb + pr * 128 + 64);
    };
    rd(0);
#pragma unroll
    for (int pr = 0; pr < 10; ++pr) {
        if (pr + 1 < 10) rd(pr + 1);
        SB();
        acc = mfma(sc[pr & 1][0], (u32x4){tb[pr & 1][0][0], tb[pr & 1][0][1], tb[pr & 1][1][0], tb[pr & 1][1][1]}, acc);
        acc2 = mfma(sc[pr & 1][1], (u32x4){tb[pr & 1][2][0], tb[pr & 1][2][1], tb[pr & 1][3][0], tb[pr & 1][3][1]}, acc2);
        SB();
    }
    return rbf(acc[0] + acc2[0]);
}
DI void x_dma(const Params& p, char* lds, unsigned img, int T, int w, int l) {
    const rsrc_t rx = mk_rsrc(p.x, 2048 * 1280);
    const int r4 = l >> 4, row = 4 * w + r4;
    const unsigned voff = row * 1280 + (((l & 15) ^ row) * 16);
#pragma unroll
    for (int cb = 0; cb < 5; ++cb) dma16(lds, img + (w * 5 + cb) * 1024, rx, voff, T * 16 * 1280 + cb * 256);
}
DI void x_stats(char* lds, unsigned img, unsigned stat, int w, int l) {
    u32x4 v[5];
#pragma unroll
    for (int cb = 0; cb < 5; ++cb) v[cb] = LD16(img + (w * 5 + cb) * 1024 + l * 16);
    float sum = 0.f;
#pragma unroll
    for (int i = 0; i < 5; ++i)
#pragma unroll
        for (int d = 0; d < 4; ++d) sum += lo_f(v[i][d]) + hi_f(v[i][d]);
    sum = red16(sum);
    const float mean = sum * (1.f / 640.f);
    float vs = 0.f;
#pragma unroll
    for (int i = 0; i < 5; ++i)
#pragma unroll
        for (int d = 0; d < 4; ++d) { const float a = lo_f(v[i][d]) - mean, c2 = hi_f(v[i][d]) - mean; vs += a * a + c2 * c2; }
    vs = red16(vs);
    const float rstd = rsqrtf(vs * (1.f / 640.f) + 1e-5f);
    if ((l & 15) == 0) { STF(stat + (4 * w + (l >> 4)) * 8, mean); STF(stat + (4 * w + (l >> 4)) * 8 + 4, rstd); }
}
DI unsigned ximg_off(int t, int byte_in_row) { return ((t >> 2) * 5 + (byte_in_row >> 8)) * 1024 + (t & 3) * 256 + ((((byte_in_row >> 4) & 15) ^ t) * 16) + (byte_in_row & 15); }
DI void x_frags(char* lds, unsigned img, unsigned stat, rsrc_t rs, int T, int w, int l) {
    const int t = l & 15, qd = l >> 4;
    const float mean = LDF(stat + t * 8), rstd = LDF(stat + t * 8 + 4);
#pragma unroll
    for (int i = 0; i < 5; ++i) {
        const int s = w + 4 * i;
        const u32x4 v = LD16(img + ximg_off(t, s * 64 + qd * 16));
        u32x4 o;
#pragma unroll
        for (int d = 0; d < 4; ++d) o[d] = pack2((lo_f(v[d]) - mean) * rstd, (hi_f(v[d]) - mean) * rstd);
        bstore16<SC1>(o, rs, l * 16, WS_XNF + (T * 20 + s) * 1024);
    }
}
struct TileKV { u32x2 ka0, ka1, k2, va0, va1, z; };
struct AttnMask { bool c0, c1, p0, p1, odd; unsigned sel0, sel1; };
DI AttnMask attn_mask(int t, int qd) {
    AttnMask m; const int par = t & 1, dc = t - 4 * qd - par;
    m.odd = par; m.c0 = dc >= 0; m.c1 = dc >= 2; m.p0 = dc + 16 <= 14; m.p1 = dc + 14 <= 14;
    m.sel0 = par ? 0x01000c0cu : 0x0c0c0100u; m.sel1 = par ? 0x03020c0cu : 0x0c0c0302u;
    return m;
}
DI void attn_unit(const u32x2 (&qa)[2], const u32x2& xq, const TileKV& cur, const TileKV& prev, bool prev_ok, const AttnMask& m, int qd, u32x4& omain, u32x2& oleft) {
    const float cs = 0.22360679774997896f * 1.4426950408889634f, ninf = -__builtin_inff();
    f32x4 oa[2], oz[2];
#pragma unroll
    for (int h = 0; h < 2; ++h) {
        const u32x4 qf = {qa[h][0], qa[h][1], qd == h ? xq[0] : 0u, qd == h ? xq[1] : 0u};
        const u32x2 kcp = h == 0 ? cur.ka0 : cur.ka1, kpp = h == 0 ? prev.ka0 : prev.ka1;
        const f32x4 z4 = {0.f, 0.f, 0.f, 0.f};
        const f32x4 sp = mfma((u32x4){kpp[0], kpp[1], prev.k2[0], prev.k2[1]}, qf, z4);
        const f32x4 sc = mfma((u32x4){kcp[0], kcp[1], cur.k2[0], cur.k2[1]}, qf, z4);
        float e0 = m.odd ? sp[1] : sp[0], e1 = m.odd ? sp[3] : sp[2], e2 = m.odd ? sc[1] : sc[0], e3 = m.odd ? sc[3] : sc[2];
        e0 = (m.p0 && prev_ok) ? e0 : ninf; e1 = (m.p1 && prev_ok) ? e1 : ninf; e2 = m.c0 ? e2 : ninf; e3 = m.c1 ? e3 : ninf;
        const float mx = red_max_q(fmaxf(fmaxf(e0, e1), fmaxf(e2, e3))), mc = -mx * cs;
        e0 = __builtin_amdgcn_exp2f(fmaf(e0, cs, mc)); e1 = __builtin_amdgcn_exp2f(fmaf(e1, cs, mc)); e2 = __builtin_amdgcn_exp2f(fmaf(e2, cs, mc)); e3 = __builtin_amdgcn_exp2f(fmaf(e3, cs, mc));
        const float inv = __builtin_amdgcn_rcpf(red_add_q((e0 + e1) + (e2 + e3)));
        const unsigned pp = pack2(e0 * inv, e1 * inv), pc = pack2(e2 * inv, e3 * inv);
        const u32x4 pf = {__builtin_amdgcn_perm(0u, pp, m.sel0), __builtin_amdgcn_perm(0u, pp, m.sel1), __builtin_amdgcn_perm(0u, pc, m.sel0), __builtin_amdgcn_perm(0u, pc, m.sel1)};
        const u32x2 vc2 = h == 0 ? cur.va0 : cur.va1, vp2 = h == 0 ? prev.va0 : prev.va1;
        oa[h] = mfma((u32x4){vp2[0], vp2[1], vc2[0], vc2[1]}, pf, z4);
        oz[h] = mfma((u32x4){prev.z[0], prev.z[1], cur.z[0], cur.z[1]}, pf, z4);
    }
    const u32x2 a0 = pack4(oa[0]), a1 = pack4(oa[1]);
    omain = (u32x4){a0[0], a0[1], a1[0], a1[1]};
    const u32x2 l0 = pack4(oz[0]), l1 = pack4(oz[1]);
    oleft = qd == 0 ? l0 : l1;
}

__global__ __launch_bounds__(256, 1) void adaLN_block_kernel(Params p) {
    extern __shared__ __attribute__((aligned(16))) char lds[];
    const int b = blockIdx.x, w = __builtin_amdgcn_readfirstlane(threadIdx.x >> 6);
    const int xcd = b & 7, bi = b >> 3;
    char* ws = p.ws;
    const rsrc_t rs = mk_rsrc(ws, WS_END);
    STAMP(0);
    LSTAMP_DECL;
    {
        unsigned long long pc; unsigned len;
        asm volatile("s_getpc_b64 %0\n.Ladaln_pc:\n\ts_mov_b32 %1, .Ladaln_end-.Ladaln_pc" : "=s"(pc), "=s"(len));
        unsigned off = (bi * 256 + threadIdx.x) * 64u;
        off = off < len ? off : 0u;
        const unsigned long long a = pc + off;
        const unsigned dst = (unsigned)(size_t)(LAS char*)lds + LDS_BYTES - 1024 + w * 256;
        asm volatile("s_mov_b32 m0, %0\n\ts_nop 0\n\tglobal_load_lds_dword %1, off" :: "s"(dst), "v"(a) : "memory");
    }
    const unsigned epoch = *(const __attribute__((address_space(4))) unsigned*)(size_t)(ws + WS_EPOCH);
    unsigned tag;
#define MAKE_TAG() do { unsigned e_ = epoch; asm volatile("" : "+s"(e_)); tag = e_ + 0x9E3779B9u; } while (0)

    DOut dout;
    {
        LANE_IDS();
        const bool isA = bi < 20, isB = !isA && bi < 25, isC = !isA && !isB;
        const int sA = bi < 16 ? xcd + 8 * (bi >> 3) : 16 + (xcd >> 1), jg = bi < 16 ? (bi & 7) : 4 * (xcd & 1) + (bi - 16), idxB = xcd * 5 + (bi - 20);
        const int aidx = jg * 20 + sA;
        const int idxC = xcd * 7 + (bi - 25);
        const int Ts = isB ? idxB : (idxC < 32 ? 40 + 2 * idxC : 72 + idxC), nT = (isC && idxC < 32) ? 2 : 1;
        const int did = w == 0 ? 1000 : b * 3 + w - 1;
        DPlan dp;
        if (!isA) {
            x_dma(p, lds, L0_XI, Ts, w, l);
            if (nT == 2) x_dma(p, lds, 0, Ts + 1, w, l);
            MAKE_TAG();
            dtask_plan(p, did, l, dp, dout);
            STAMP(18);
            if (nT == 2) { WAITV(5); x_stats(lds, L0_XI, L0_XS, w, l); WAITV(0); x_stats(lds, 0, L0_XS + 128, w, l); }
            else { WAITV(0); x_stats(lds, L0_XI, L0_XS, w, l); }
            BAR();
            x_frags(lds, L0_XI, L0_XS, rs, Ts, w, l);
            if (nT == 2) x_frags(lds, 0, L0_XS + 128, rs, Ts + 1, w, l);
            if (isB) {
                c_dma(p, lds, w, l);
                wm_dma<0, 20>(p, lds, w, l, 1280 + 64 * idxB, 1280 + 64 * idxB + 32, 0);
                WAITV(21);
            } else {
                dtask_go(dp, lds, L0_D, w);
                if (dp.on) WAITV(8); else WAITV(0);
            }
            BAR();
            flag_put(rs, WS_FC + (Ts + (tid & 1)) * FS, tag, tid < nT);
            STAMP(20);
        }
        if (!isC) {
            if (isA) {
                c_dma(p, lds, w, l);
                wm_dma<0, 10>(p, lds, w, l, 640 + 32 * sA, 32 * sA, 10 * jg);
                WAITV(10);
                silu_table(lds, tid);
                wm_dma<10, 20>(p, lds, w, l, 640 + 32 * sA, 32 * sA, 10 * jg);
                const rsrc_t rq = mk_rsrc(p.wqkv, 640 * 3840);
#pragma unroll
                for (int m = 0; m < 4; ++m) {
                    const int n = 64 * (4 * m + w) + l, rho = n / 30, c = n % 30;
                    const int k = (rho & 19) | ((rho & 4) << 1) | ((rho & 8) >> 1);
                    if (4 * m + w < 15) dma16(lds, L0_RW + (4 * m + w) * 1024, rq, k * 3840 + (c / 10) * 1280 + jg * 160 + (c % 10) * 16, sA * 32 * 3840);
                }
                MAKE_TAG();
                STAMP(18);
            } else {
                WAITV(21);
                silu_table(lds, tid);
            }
            WAITV(0);
            BAR();
            float v = mod_gemv(lds, w, l);
            STAMP(19);
            if (isA) {
                const float gv = w < 2 ? rbf(1.f + v) : v;
                if (l < 16) { STF(L0_GS + (w * 16 + l) * 4, gv); *(LAS u16*)((LAS char*)lds + L0_GS + 256 + (w * 16 + l) * 2) = (u16)(__float_as_uint(gv) >> 16); }
                const int q = (l >> 2) & 3, pp = l & 3, jj = w >> 1, j = 2 * jg + jj;
                const unsigned rbase = L0_RW + (16 * (qd >> 1) + 4 * (qd & 1) + q) * 480 + 80 * jj;
                u32x2 ua[4], ub[4];
#pragma unroll
                for (int i = 0; i < 4; ++i) {
                    const unsigned lo = i < 2 ? i * 40 + 8 * pp : 160 + (i - 2) * 40 + 8 * pp;
                    const unsigned hi = i == 0 ? (pp >> 1) * 160 + (pp & 1) * 40 + 32 : i < 3 ? 320 + (i - 1) * 40 + 8 * pp : 320 + (pp & 1) * 40 + 32;
                    const unsigned po = (w & 1) ? hi : lo;
                    ua[i] = tr_read(lds, rbase + po); ub[i] = tr_read(lds, rbase + po + 8 * 480);
                }
                if ((w & 1) && t >= 8) { ua[3] = (u32x2){0u, 0u}; ub[3] = ua[3]; }
                BAR();
                const f32x4 g0 = LDF4(L0_GS + qd * 32), g1 = LDF4(L0_GS + qd * 32 + 16);
                const u32x4 sfr = LD16(L0_GS + 256 + 64 + qd * 16);
#pragma unroll
                for (int i = 0; i < 4; ++i) {
                    const int nt = 4 * (w & 1) + i;
                    const u32x4 o = {pack2(lo_f(ua[i][0]) * g0[0], hi_f(ua[i][0]) * g0[1]), pack2(lo_f(ua[i][1]) * g0[2], hi_f(ua[i][1]) * g0[3]),
                                     pack2(lo_f(ub[i][0]) * g1[0], hi_f(ub[i][0]) * g1[1]), pack2(lo_f(ub[i][1]) * g1[2], hi_f(ub[i][1]) * g1[3])};
                    bstore16<SC1>(o, rs, lo16, WS_WQF + ((j * 20 + sA) * 8 + nt) * 1024);
                }
                {
                    const f32x4 z4 = {0, 0, 0, 0};
                    f32x4 bias = mfma((u32x4){ua[0][0], ua[0][1], ub[0][0], ub[0][1]}, sfr, z4);
#pragma unroll
                    for (int i = 1; i < 4; ++i) { const f32x4 bi4 = mfma((u32x4){ua[i][0], ua[i][1], ub[i][0], ub[i][1]}, sfr, z4); bias = t == i ? bi4 : bias; }
                    bstore16<SC1>(asu4(bias), rs, t < 4 ? t * 64 + qd * 16 : 0x7ff00000u, WS_BP + ((sA * 16 + j) * 128 + (w & 1) * 64) * 4);
                }
                STAMP(20);
                dtask_plan(p, did, l, dp, dout);
            } else {
                const int col = 1280 + 64 * idxB + 16 * w + t;
                if (col >= 2560 && col < 3200) v = rbf(1.f + v);
                bstore4<SC1>(__float_as_uint(v), rs, l < 16 ? l * 4 : 0x7ff00000u, WS_MODV + (1280 + 64 * idxB + 16 * w) * 4);
            }
            dtask_go(dp, lds, L0_D, w);
            if (dp.on) WAITV(8); else WAITV(0);
            BAR();
            flag_put(rs, WS_FA + aidx * FS, tag, isA && tid == 0);
        }
        STAMP(1);
    }

    const int g1 = 4 * (xcd >> 1) + (bi >> 3), j1 = 8 * (xcd & 1) + (bi & 7);
    const int P2 = 4 * xcd + (bi >> 3), c2 = bi & 7;
    const int g3 = g1, j3 = j1;
    const int rot2 = ROT2, rot3 = ROT3;
    auto wrap20 = [](int x) { return x >= 20 ? x - 20 : x; };
    const int t3 = j3 < 12 ? 7 * j3 : 84 + 6 * (j3 - 12), cnt3 = j3 < 12 ? 7 : 6;
    u32x4 w2f[14][5];
    const int s4 = __builtin_amdgcn_readfirstlane(w < 2 ? 14 * w : 28 + 13 * (w - 2)), cnt4 = __builtin_amdgcn_readfirstlane(w < 2 ? 14 : 13);
    const unsigned w2b = __builtin_amdgcn_readfirstlane(WS_W2F + (c2 * 54 + s4) * 5 * 1024);
    const int rot4 = __builtin_amdgcn_readfirstlane(ROT4);
    auto step4 = [&](int i) { const int x = (i < 13 ? i : cnt4 - 1) + rot4; return x >= cnt4 ? x - cnt4 : x; };
    auto w2_ld = [&](int f, unsigned lo) { w2f[f / 5][f % 5] = bload16<SC1>(rs, lo, w2b + step4(f / 5) * 5120 + (f % 5) * 1024); };
    constexpr int L2_WO = 61440, L2_X = 46080;
    {
        LANE_IDS();
        const int T0 = 8 * g1 + 2 * w, TH = g1 == 0 ? 0 : 8 * g1 - 1;
        const unsigned xb0 = WS_XNF + T0 * 20480, xbh = WS_XNF + TH * 20480 + (w >> 1) * 1024, wb = WS_WQF + j1 * 163840 + w * 4096;
        const unsigned wob = WS_WOF + c2 * 102400;
        u32x4 xr[10][4], xh[10];
        const int rot1 = ROT1;
        auto gr = [&](int G) { const int x = G + rot1; return x >= 10 ? x - 10 : x; };
        auto w_dma = [&](int G, int k) { dma16s(lds, (G & 3) * 16384 + w * 4096 + k * 1024, rs, lo16, wb + gr(G) * 16384 + k * 1024); };
        auto x_ld = [&](int G, int i) { xr[G][i] = bload16<SC1>(rs, lo16, xb0 + (i & 1) * 20480 + (2 * gr(G) + (i >> 1)) * 1024); };
        if (w == 0) {
            unsigned off[1] = {l == 0 ? WS_FC + TH * FS : WS_FC + (8 * g1 + ((l - 1) & 7)) * FS};
            poll_words<1>(ws, off, l < 9, tag);
        }
        BAR();
#pragma unroll
        for (int G = 0; G < 4; ++G) {
#pragma unroll
            for (int i = 0; i < 4; ++i) x_ld(G, i);
            xh[G] = bload16<SC1>(rs, lo16, xbh + gr(G) * 2048);
        }
        SB();
        if (w == 0) {
            unsigned off[1] = {WS_FA + ((j1 >> 1) * 20 + (l < 20 ? l : 0)) * FS};
            poll_words<1>(ws, off, l < 20, tag);
        }
        DRAIN();
        if (w > 0) dtask_read(dout, lds, L0_D, w, l);
        else {
#pragma unroll
            for (int i = 0; i < 8; ++i) dout.f[i] = (u32x4){0u, 0u, 0u, 0u};
        }
        BAR();
        STAMP(2);
#pragma unroll
        for (int G = 0; G < 3; ++G)
#pragma unroll
            for (int k = 0; k < 4; ++k) w_dma(G, k);
        WAITV(4);
        BAR();
        STAMP(3);
        f32x4 acc[2][8], hacc[3];
#pragma unroll
        for (int i = 0; i < 8; ++i) { acc[0][i] = (f32x4){0, 0, 0, 0}; acc[1][i] = (f32x4){0, 0, 0, 0}; }
#pragma unroll
        for (int i = 0; i < 3; ++i) hacc[i] = (f32x4){0, 0, 0, 0};
        u32x4 wf[2][8];
#pragma unroll
        for (int n = 0; n < 8; ++n) wf[0][n] = LD16(n * 1024 + lo16);
        const unsigned hoff = (w >> 1) * 8192 + ((w & 1) ? 5 : 2) * 1024 + lo16;
        f32x4 bp4[3]; u32x4 fdv;
        constexpr int L1_BPART = 40960;
        SB();
#pragma unroll
        for (int G = 0; G < 10; ++G) {
            u32x4 hw[3];
#pragma unroll
            for (int st = 0; st < 2; ++st) {
                const int s = 2 * G + st;
#pragma unroll
                for (int n = 0; n < 8; ++n) {
                    if (s + 1 < 20) wf[(s + 1) & 1][n] = LD16((((s + 1) >> 1) & 3) * 16384 + ((s + 1) & 1) * 8192 + n * 1024 + lo16);
                    if (st == 0 && n < 3) hw[n] = LD16((G & 3) * 16384 + hoff + n * 1024);
                    if (n < 5) { acc[0][n] = mfma(wf[s & 1][n], xr[G][st * 2], acc[0][n]); acc[1][n] = mfma(wf[s & 1][n], xr[G][st * 2 + 1], acc[1][n]); }
                    else { acc[0][n] = mfma(xr[G][st * 2], wf[s & 1][n], acc[0][n]); acc[1][n] = mfma(xr[G][st * 2 + 1], wf[s & 1][n], acc[1][n]); }
                    if (st == 1 && n >= 5) hacc[n - 5] = mfma(hw[n - 5], xh[G], hacc[n - 5]);
                    if (st == 0) {
                        if (G + 3 < 10 && (n & 1) == 0) w_dma(G + 3, n >> 1);
                        if (G + 4 < 10 && n == 7) xh[G + 4] = bload16<SC1>(rs, lo16, xbh + gr(G + 4) * 2048);
                    } else {
                        if (G + 4 < 10 && (n & 1) == 0) x_ld(G + 4, n >> 1);
                    }
                    if (G == 7 && st == 0 && n < 3) bp4[n] = asf4(bload16<SC1>(rs, (n < 2 || tid < 128) ? ((tid >> 5) + 8 * n) * 8192 + (tid & 31) * 16 : 0x7ff00000u, WS_BP + j1 * 512));
                    if (G == 9 && st == 0 && n == 0) {
                        f32x4 bs = (bp4[0] + bp4[1]) + bp4[2];
#pragma unroll
                        for (int r = 0; r < 4; ++r) { auto sw = __builtin_amdgcn_permlane32_swap(__float_as_uint(bs[r]), __float_as_uint(bs[r]), false, false); bs[r] = __uint_as_float(sw[0]) + __uint_as_float(sw[1]); }
                        STF4(L1_BPART + w * 1024 + lo16, bs);
                    }
                    if (G == 9 && st == 1 && n == 0) fdv = bload16<SC1>(rs, lo16, WS_FD);
                    if (n == 3) {
                        if (s == 0) dtask_frag<0>(dout, rs, l);
                        if (s == 1) dtask_frag<1>(dout, rs, l);
                        if (s == 2) dtask_frag<2>(dout, rs, l);
                        if (s == 3) dtask_frag<3>(dout, rs, l);
                        if (s == 4) dtask_frag<4>(dout, rs, l);
                        if (s == 5) dtask_frag<5>(dout, rs, l);
                        if (s == 6) dtask_frag<6>(dout, rs, l);
                        if (s == 7) dtask_frag<7>(dout, rs, l);
                    }
                    SB();
                }
            }
            if (G == 0) WAITV(11); else if (G <= 3) WAITV(17); else if (G == 4) WAITV(11); else if (G == 5) WAITV(14); else if (G == 6) WAITV(9); else if (G == 7) WAITV(3);
            BAR();
            if (G == 4) flag_put(rs, WS_FD + b * 4, tag, tid == 0);
        }
        STAMP(4);
        constexpr int L1_BIAS = 6144, L1_HP = 0, L1_EX = 8192, L1_VT = 6656;
        if (w == 0) {
            check_all_blocks(ws, rs, WS_FD, fdv, tag);
            if (b == 0 && l == 0) flag_store(ws, WS_EPOCH, tag);
            if (l < 32) STF4(L1_BIAS + l * 16, (LDF4(L1_BPART + l * 16) + LDF4(L1_BPART + 1024 + l * 16)) + (LDF4(L1_BPART + 2048 + l * 16) + LDF4(L1_BPART + 3072 + l * 16)));
        }
        if (w >= 2) {
#pragma unroll
            for (int i = 0; i < 3; ++i) STF4(L1_HP + ((w & 1) * 3 + i) * 1024 + lo16, hacc[i]);
        }
        BAR();
        auto wo_dma = [&](int k) { dma16s(lds, L2_WO + w * 25600 + k * 1024, rs, lo16, wob + w * 25600 + k * 1024); };
        const rsrc_t rx = mk_rsrc(p.x, 2048 * 1280);
        auto x_dma2 = [&](int k) {
            const int i = w * 3 + k, n = i * 64 + l, row = n / 10, cc = n - 10 * row;
            dma<0>(lds, L2_X + (i < 10 ? i : 10) * 1024, rx, i < 10 ? row * 1280 + cc * 16 : 0x7ff00000u, P2 * 64 * 1280 + c2 * 160);
        };
        f32x4 bq[5]; float bv[3];
#pragma unroll
        for (int n = 0; n < 5; ++n) bq[n] = LDF4(L1_BIAS + (n * 16 + 4 * qd) * 4);
#pragma unroll
        for (int n = 0; n < 3; ++n) bv[n] = LDF(L1_BIAS + ((5 + n) * 16 + t) * 4);
        TileKV kv[2]; u32x2 qa[2][2], xq[2];
#pragma unroll
        for (int k = 0; k < 8; ++k) wo_dma(k);
#pragma unroll
        for (int tt = 0; tt < 2; ++tt) {
            qa[tt][0] = pack4(acc[tt][0] + bq[0]); qa[tt][1] = pack4(acc[tt][1] + bq[1]);
            kv[tt].ka0 = pack4(acc[tt][2] + bq[2]); kv[tt].ka1 = pack4(acc[tt][3] + bq[3]);
            xq[tt] = pack4(acc[tt][4] + bq[4]);
            kv[tt].k2[0] = swap32(xq[tt][0]); kv[tt].k2[1] = swap32(xq[tt][1]);
            kv[tt].va0 = pack4(acc[tt][5] + bv[0]); kv[tt].va1 = pack4(acc[tt][6] + bv[1]); kv[tt].z = pack4(acc[tt][7] + bv[2]);
        }
        {
            const unsigned e = L1_EX + (w + 1) * 3072 + lo16;
            ST16(e, ((u32x4){kv[1].ka0[0], kv[1].ka0[1], kv[1].ka1[0], kv[1].ka1[1]}));
            ST16(e + 1024, ((u32x4){kv[1].k2[0], kv[1].k2[1], kv[1].va0[0], kv[1].va0[1]}));
            ST16(e + 2048, ((u32x4){kv[1].va1[0], kv[1].va1[1], kv[1].z[0], kv[1].z[1]}));
        }
#pragma unroll
        for (int k = 0; k < 8; ++k) { SGB(0x1, 20); SGB(0x20, 1); }
        SB();
        if (w == 0) {
            const u32x2 k0 = pack4(hacc[0] + LDF4(L1_HP + lo16) + bq[2]), k1 = pack4(hacc[1] + LDF4(L1_HP + 1024 + lo16) + bq[3]);
            const u32x2 xx = pack4(hacc[2] + LDF4(L1_HP + 2048 + lo16) + bq[4]);
            ST16(L1_EX + lo16, ((u32x4){k0[0], k0[1], k1[0], k1[1]}));
            ST8(L1_EX + 1024 + lo16, ((u32x2){swap32(xx[0]), swap32(xx[1])}));
        } else if (w == 1) {
#pragma unroll
            for (int i = 0; i < 3; ++i) {
                const f32x4 v = hacc[i] + LDF4(L1_HP + (3 + i) * 1024 + lo16) + LDF4(L1_BIAS + ((5 + i) * 16 + 4 * qd) * 4);
                const u32x2 pk = pack4(v);
                LAS u16* d = (LAS u16*)((LAS char*)lds + L1_VT + i * 512 + (4 * qd) * 32 + t * 2);
                d[0] = (u16)pk[0]; d[16] = (u16)(pk[0] >> 16); d[32] = (u16)pk[1]; d[48] = (u16)(pk[1] >> 16);
            }
        }
        const unsigned ob = WS_OF + T0 * 20480;
        const AttnMask am = attn_mask(t, qd);
        u32x4 om; u32x2 ol;
        SB();
#pragma unroll
        for (int k = 8; k < 17; ++k) wo_dma(k);
        attn_unit(qa[1], xq[1], kv[1], kv[0], true, am, qd, om, ol);
#pragma unroll
        for (int k = 0; k < 9; ++k) { SGB(0x1, 16); SGB(0x20, 1); }
        SB();
        bstore16<SC1>(om, rs, lo16, ob + 20480 + j1 * 1024);
        bstore8<SC1>(ol, rs, qd < 2 ? ((j1 & 3) * 16 + t) * 16 + 8 * qd : 0x7ff00000u, ob + 20480 + (16 + (j1 >> 2)) * 1024);
        BAR();
        TileKV pv;
        {
            const unsigned e = L1_EX + w * 3072 + lo16;
            const u32x4 a = LD16(e), bb = LD16(e + 1024), cc = LD16(e + 2048);
            pv.ka0 = (u32x2){a[0], a[1]}; pv.ka1 = (u32x2){a[2], a[3]}; pv.k2 = (u32x2){bb[0], bb[1]}; pv.va0 = (u32x2){bb[2], bb[3]};
            pv.va1 = (u32x2){cc[0], cc[1]}; pv.z = (u32x2){cc[2], cc[3]};
            if (w == 0) { pv.va0 = LD8(L1_VT + t * 32 + qd * 8); pv.va1 = LD8(L1_VT + 512 + t * 32 + qd * 8); pv.z = LD8(L1_VT + 1024 + t * 32 + qd * 8); }
        }
        SB();
#pragma unroll
        for (int k = 17; k < 25; ++k) wo_dma(k);
#pragma unroll
        for (int k = 0; k < 3; ++k) x_dma2(k);
        attn_unit(qa[0], xq[0], kv[0], pv, !(g1 == 0 && w == 0), am, qd, om, ol);
#pragma unroll
        for (int k = 0; k < 11; ++k) { SGB(0x1, 16); SGB(0x20, 1); }
        SB();
        bstore16<SC1>(om, rs, lo16, ob + j1 * 1024);
        bstore8<SC1>(ol, rs, qd < 2 ? ((j1 & 3) * 16 + t) * 16 + 8 * qd : 0x7ff00000u, ob + (16 + (j1 >> 2)) * 1024);
        STAMP(5);
        publish(rs, WS_F1 + (g1 * 16 + j1) * FS, tag);
    }
    STAMP(6);

    u32x2 x1b[5], b1v[7];
    u32x4 vraw[5][2];
    {
        LANE_IDS();
        const int T = 4 * P2 + w;
        constexpr int L2_MV = 57344;
        const u32x4 mvl = bload16<SC1>(rs, (1280 + ((tid / 20) % 3) * 640 + 80 * c2 + (tid % 20) * 4) * 4, WS_MODV);
#pragma unroll
        for (int i = 0; i < 5; ++i) {
            int ta, tb; tiles_p3(wrap20(5 * w + i + rot3), ta, tb);
            const unsigned sec = WS_MODV + (t == 0 ? 2560 : 1920) * 4 + qd * 16;
            vraw[i][0] = bload16<SC1>(rs, sec + ta * 64, 0); vraw[i][1] = bload16<SC1>(rs, sec + tb * 64, 0);
        }
#pragma unroll
        for (int n = 0; n < 7; ++n) b1v[n] = *(const GAS u32x2*)(p.b1 + 16 * (t3 + (n < cnt3 ? n : 0)) + 4 * qd);
#pragma unroll
        for (int f = 0; f < 12; ++f) w2_ld(f, lo16);
        SB();
        if (tid < 60) ST16(L2_MV + tid * 16, mvl);
        if (w == 0) {
            unsigned off[1] = {WS_F1 + ((P2 >> 1) * 16 + (l & 15)) * FS};
            poll_words<1>(ws, off, l < 16, tag);
        }
        BAR();
        STAMP(7);
        u32x2 xv[5];
#pragma unroll
        for (int n = 0; n < 5; ++n) xv[n] = LD8(L2_X + (16 * w + t) * 160 + n * 32 + qd * 8);
        constexpr int PW = 8;
        u32x4 tk[20];
#pragma unroll
        for (int s = 0; s < PW; ++s) tk[s] = bload16<SC1>(rs, lo16, WS_OF + (T * 20 + wrap20(s + rot2)) * 1024);
        f32x4 acc[5];
#pragma unroll
        for (int n = 0; n < 5; ++n) acc[n] = (f32x4){0, 0, 0, 0};
        unsigned wb0 = L2_WO + lo16, wb1 = L2_WO + 51200 + lo16; OPAQUE(wb0); OPAQUE(wb1);
        auto wo_rd = [&](int s, int n) { return s < 10 ? LD16(wb0 + (s * 5 + n) * 1024) : LD16(wb1 + ((s - 10) * 5 + n) * 1024); };
        u32x4 wf[3][5];
#pragma unroll
        for (int n = 0; n < 5; ++n) { wf[0][n] = wo_rd(0, n); wf[1][n] = wo_rd(1, n); }
        SB();
        LSTAMP(40);
#pragma unroll
        for (int s = 0; s < 20; ++s) {
            if (s + PW < 20) tk[s + PW] = bload16<SC1>(rs, lo16, WS_OF + (T * 20 + wrap20(s + PW + rot2)) * 1024);
            if (s + 2 < 20) {
#pragma unroll
                for (int n = 0; n < 5; ++n) wf[(s + 2) % 3][n] = wo_rd(s + 2, n);
            }
#pragma unroll
            for (int n = 0; n < 5; ++n) acc[n] = mfma(wf[s % 3][n], tk[s], acc[n]);
#pragma unroll
            for (int n = 0; n < 5; ++n) { if (s + 2 < 20) SGB(0x100, 1); SGB(0x8, 1); if (n == 1 && s + PW < 20) SGB(0x20, 1); }
            SB();
            if ((s & 3) == 3 || s == 0) LSTEP(41 + (s + 1) / 4);
        }
        STAMP(8);
        BAR();
        const unsigned w1src = WS_W1F + t3 * 20480, w1last = (cnt3 == 7 ? 6 : 0) * 20480;
        auto w1_dma = [&](int base_step, int i) {
            const int n = i % 7;
            dma16s(lds, base_step * 7168 + i * 1024, rs, lo16, w1src + (n == 6 ? w1last : n * 20480) + wrap20(base_step + i / 7 + rot3) * 1024);
        };
        float st[6] = {0, 0, 0, 0, 0, 0};
        u32x2 ub[5];
        const unsigned ufb = WS_UF + T * 20480;
#pragma unroll
        for (int n = 0; n < 5; ++n) {
            const f32x4 ga_n = LDF4(L2_MV + (16 * n + 4 * qd) * 4), s2_n = LDF4(L2_MV + 320 + (16 * n + 4 * qd) * 4), g2_n = LDF4(L2_MV + 640 + (16 * n + 4 * qd) * 4);
            float x1[4] = {lo_f(xv[n][0]), hi_f(xv[n][0]), lo_f(xv[n][1]), hi_f(xv[n][1])};
#pragma unroll
            for (int r = 0; r < 4; ++r) x1[r] = fmaf(ga_n[r], acc[n][r], x1[r]);
            x1b[n] = (u32x2){pack2(x1[0], x1[1]), pack2(x1[2], x1[3])};
            const float xr[4] = {lo_f(x1b[n][0]), hi_f(x1b[n][0]), lo_f(x1b[n][1]), hi_f(x1b[n][1])};
            ub[n] = (u32x2){pack2(xr[0] * g2_n[0], xr[1] * g2_n[1]), pack2(xr[2] * g2_n[2], xr[3] * g2_n[3])};
            const float ur[4] = {lo_f(ub[n][0]), hi_f(ub[n][0]), lo_f(ub[n][1]), hi_f(ub[n][1])};
#pragma unroll
            for (int r = 0; r < 4; ++r) {
                st[0] += xr[r]; st[1] = fmaf(xr[r], xr[r], st[1]); st[2] += ur[r]; st[3] = fmaf(ur[r], g2_n[r], st[3]);
                st[4] = fmaf(ur[r], ur[r], st[4]); st[5] = fmaf(ur[r], s2_n[r], st[5]);
            }
            if (n == 1) bstore16<SC1>((u32x4){ub[0][0], ub[0][1], ub[1][0], ub[1][1]}, rs, lo16, ufb + (2 * c2) * 1024);
            if (n == 3) bstore16<SC1>((u32x4){ub[2][0], ub[2][1], ub[3][0], ub[3][1]}, rs, lo16, ufb + (2 * c2 + 1) * 1024);
            if (n == 4) bstore8<SC1>(ub[4], rs, lo16 + 8 * (c2 & 1), ufb + (16 + (c2 >> 1)) * 1024);
#pragma unroll
            for (int k = 0; k < 3; ++k) if (3 * n + k < 14) w1_dma(2 * w, 3 * n + k);
            SGB(0x20, 1); SGB(0x1, 18); SGB(0x20, 1); SGB(0x1, 18); SGB(0x20, 1); SGB(0x1, 24);
            SB();
        }
#pragma unroll
        for (int i = 0; i < 6; ++i) st[i] = red_add_q(st[i]);
        if (qd < 2) bstore16<SC1>(asu4(qd == 0 ? (f32x4){st[0], st[1], st[2], st[3]} : (f32x4){st[4], st[5], 0.f, 0.f}), rs, qd * 256 + t * 16, WS_ST + (T * 8 + c2) * 512);
#pragma unroll
        for (int n = 0; n < 5; ++n) asm volatile("" : "+v"(x1b[n]));
        STAMP(9);
        publish(rs, WS_F2 + (P2 * 8 + c2) * FS, tag);
#pragma unroll
        for (int i = 0; i < 21; ++i) w1_dma(8 + 3 * w, i);
    }
    STAMP(10);

    {
        LANE_IDS();
        constexpr int L3_GSC = 143360;
        const int T0 = 8 * g3 + 2 * w;
        u32x4 vf[5];
#pragma unroll
        for (int i = 0; i < 5; ++i) {
            const f32x4 a = asf4(vraw[i][0]), c = asf4(vraw[i][1]);
            const unsigned cst = t == 2 ? 0x3f803f80u : 0u;
            const unsigned d0 = pack2(a[0], a[1]), d1 = pack2(a[2], a[3]), d2 = pack2(c[0], c[1]), d3 = pack2(c[2], c[3]);
            vf[i] = (u32x4){t < 2 ? d0 : cst, t < 2 ? d1 : cst, t < 2 ? d2 : cst, t < 2 ? d3 : cst};
            OPAQUE(vf[i]);
        }
        if (w == 0) {
            unsigned off[1] = {WS_F2 + ((2 * g3 + ((l >> 3) & 1)) * 8 + (l & 7)) * FS};
            poll_words<1>(ws, off, l < 16, tag);
        }
        DRAIN();
        BAR();
        STAMP(11);
        constexpr int PW = 4;
        u32x4 tk[20][2];
        u32x4 spa[2][2]; u32x2 spb[2][2];
        float al[2], be[2], gm[2], de[2], ts[2][6];
        f32x4 red4[4], gr0, gr1;
#pragma unroll
        for (int s = 0; s < PW; ++s) { const unsigned o = WS_UF + (T0 * 20 + wrap20(s + rot3)) * 1024; tk[s][0] = bload16<SC1>(rs, lo16, o); tk[s][1] = bload16<SC1>(rs, lo16, o + 20480); }
        SB();
        LSTAMP(62);
        {
            f32x4 va[7], gram = {0, 0, 0, 0};
            u32x4 wv[2][7];
            unsigned vb = 5 * w * 7168 + lo16; OPAQUE(vb);
#pragma unroll
            for (int n = 0; n < 7; ++n) { va[n] = (f32x4){0, 0, 0, 0}; wv[0][n] = LD16(vb + n * 1024); }
            SB();
#pragma unroll
            for (int i = 0; i < 5; ++i) {
                if (i + 1 < 5) {
#pragma unroll
                    for (int n = 0; n < 7; ++n) wv[(i + 1) & 1][n] = LD16(vb + ((i + 1) * 7 + n) * 1024);
                }
#pragma unroll
                for (int n = 0; n < 7; ++n) va[n] = mfma(wv[i & 1][n], vf[i], va[n]);
                gram = mfma(vf[i], vf[i], gram);
#pragma unroll
                for (int n = 0; n < 7; ++n) { if (i + 1 < 5) SGB(0x100, 1); SGB(0x8, 1); }
                SB();
            }
            if (t < 3) {
#pragma unroll
                for (int n = 0; n < 7; ++n) STF4(L3_GSC + ((w * 8 + n) * 3 + t) * 64 + qd * 16, va[n]);
                if (qd == 0) STF4(L3_GSC + ((w * 8 + 7) * 3 + t) * 64, gram);
            }
        }
        SB();
        f32x4 acc[2][7];
#pragma unroll
        for (int n = 0; n < 7; ++n) { acc[0][n] = (f32x4){0, 0, 0, 0}; acc[1][n] = (f32x4){0, 0, 0, 0}; }
        unsigned wb0 = lo16, wb1 = 57344 + lo16, wb2 = 114688 + lo16; OPAQUE(wb0); OPAQUE(wb1); OPAQUE(wb2);
        auto w1_rd = [&](int s, int n) { return s < 8 ? LD16(wb0 + (s * 7 + n) * 1024) : s < 16 ? LD16(wb1 + ((s - 8) * 7 + n) * 1024) : LD16(wb2 + ((s - 16) * 7 + n) * 1024); };
        u32x4 wf[2][7];
#pragma unroll
        for (int n = 0; n < 7; ++n) wf[0][n] = w1_rd(0, n);
        SB();
        LSTAMP(0);
#pragma unroll
        for (int s = 0; s < 20; ++s) {
            int nv = 0;
            if (s + PW < 20) { const unsigned o = WS_UF + (T0 * 20 + wrap20(s + PW + rot3)) * 1024; tk[s + PW][0] = bload16<SC1>(rs, lo16, o); tk[s + PW][1] = bload16<SC1>(rs, lo16, o + 20480); nv += 2; }
            if (s < 16) { w2_ld(12 + s, lo16); nv += 1; }
            if (s >= 16) {
#pragma unroll
                for (int k = 0; k < 3; ++k) w2_ld(28 + (s - 16) * 3 + k, lo16);
                nv += 3;
            }
            if (s == 10 || s == 11) {
#pragma unroll
                for (int e = 0; e < 2; ++e) {
                    spa[s - 10][e] = bload16<SC1>(rs, (2 * qd + e) * 512 + t * 16, WS_ST + (T0 + s - 10) * 4096);
                    spb[s - 10][e] = bload8<SC1>(rs, (2 * qd + e) * 512 + 256 + t * 16, WS_ST + (T0 + s - 10) * 4096);
                }
                nv += 4;
            }
            int nu = 0;
            if (s == 3) {
                const int r96 = tid < 96 ? tid : 0;
#pragma unroll
                for (int u = 0; u < 4; ++u) red4[u] = LDF4(L3_GSC + (u * 96 + r96) * 16);
            }
            if (s == 4) { STF4(L3_GSC + 6144 + (tid < 96 ? tid : 96 + (tid & 31)) * 16, (red4[0] + red4[1]) + (red4[2] + red4[3])); nu = 1; }
            if (s == 9) { gr0 = LDF4(L3_GSC + 6144 + 84 * 16); gr1 = LDF4(L3_GSC + 6144 + 88 * 16); }
            if (s == 13 || s == 14) {
                const int tt = s - 13;
                const f32x4 a0 = asf4(spa[tt][0]) + asf4(spa[tt][1]);
                const float b00 = __uint_as_float(spb[tt][0][0]) + __uint_as_float(spb[tt][1][0]), b01 = __uint_as_float(spb[tt][0][1]) + __uint_as_float(spb[tt][1][1]);
                ts[tt][0] = red_add_q(a0[0]); ts[tt][1] = red_add_q(a0[1]); ts[tt][2] = red_add_q(a0[2]); ts[tt][3] = red_add_q(a0[3]); ts[tt][4] = red_add_q(b00); ts[tt][5] = red_add_q(b01);
                nu = 4;
            }
            if (s == 15) {
                const float Gg = gr0[0], Ggs = gr0[1], Gs = gr0[2], Sss = gr1[1], Ss = gr1[2], iD = 1.f / 640.f;
#pragma unroll
                for (int tt = 0; tt < 2; ++tt) {
                    const float m1 = ts[tt][0] * iD, v1 = fmaxf(ts[tt][1] * iD - m1 * m1, 0.f), r1v = __builtin_amdgcn_rsqf(v1 + 1e-5f);
                    const float m2 = (r1v * (ts[tt][2] - m1 * Gs) + Ss) * iD;
                    const float e2 = (r1v * r1v * (ts[tt][4] - 2.f * m1 * ts[tt][3] + m1 * m1 * Gg) + 2.f * r1v * (ts[tt][5] - m1 * Ggs) + Sss) * iD;
                    const float r2v = __builtin_amdgcn_rsqf(fmaxf(e2 - m2 * m2, 0.f) + 1e-5f);
                    al[tt] = r1v * r2v; be[tt] = -r1v * r2v * m1; gm[tt] = r2v; de[tt] = -r2v * m2;
                }
                nu = 4;
            }
            if (s + 1 < 20) {
#pragma unroll
                for (int n = 0; n < 7; ++n) wf[(s + 1) & 1][n] = w1_rd(s + 1, n);
            }
#pragma unroll
            for (int n = 0; n < 7; ++n) { acc[0][n] = mfma(wf[s & 1][n], tk[s][0], acc[0][n]); acc[1][n] = mfma(wf[s & 1][n], tk[s][1], acc[1][n]); }
#pragma unroll
            for (int n = 0; n < 7; ++n) {
                if (s + 1 < 20) SGB(0x100, 1);
                SGB(0x8, 1);
                if (nu == 1) SGB(0x402, 1);
                if (nu == 4) SGB(0x402, 4);
                SGB(0x8, 1);
                if (nu == 1) SGB(0x402, 1);
                if (nu == 4) SGB(0x402, 4);
                if (n < nv) SGB(0x20, 1);
            }
            SB();
            if (s == 2 || s == 8) BAR();
            LSTEP(1 + s);
        }
        STAMP(12);
        u32x2 hb[2][7];
        unsigned gsb = L3_GSC + 6144 + qd * 16; OPAQUE(gsb);
        f32x4 gsc[2][3];
#pragma unroll
        for (int k = 0; k < 3; ++k) gsc[0][k] = LDF4(gsb + 64 * k);
#pragma unroll
        for (int n = 0; n < 7; ++n) {
#pragma unroll
            for (int k = 0; k < 2; ++k) w2_ld(40 + n * 2 + k, lo16);
            if (n + 1 < 7) {
#pragma unroll
                for (int k = 0; k < 3; ++k) gsc[(n + 1) & 1][k] = LDF4(gsb + (n + 1) * 192 + 64 * k);
            }
            const f32x4 G = gsc[n & 1][0], Sv = gsc[n & 1][1], C = gsc[n & 1][2];
            const float bb[4] = {lo_f(b1v[n][0]), hi_f(b1v[n][0]), lo_f(b1v[n][1]), hi_f(b1v[n][1])};
#pragma unroll
            for (int tt = 0; tt < 2; ++tt) {
                float hv[4];
#pragma unroll
                for (int r = 0; r < 4; ++r) {
                    float z = fmaf(gm[tt], Sv[r], bb[r]); z = fmaf(be[tt], G[r], z); z = fmaf(de[tt], C[r], z); z = fmaf(al[tt], acc[tt][n][r], z);
                    const float u = z * fmaf(z * z, -0.044715f * 1.5957691216057308f * 1.4426950408889634f, -1.5957691216057308f * 1.4426950408889634f);
                    hv[r] = z * __builtin_amdgcn_rcpf(1.f + __builtin_amdgcn_exp2f(u));
                }
                hb[tt][n] = (u32x2){pack2(hv[0], hv[1]), pack2(hv[2], hv[3])};
            }
            SGB(0x20, 1); SGB(0x1, 40); SGB(0x20, 1); SGB(0x1, 60);
            SB();
#pragma unroll
            for (int tt = 0; tt < 2; ++tt) {
                const unsigned hbase = WS_HF + (T0 + tt) * 54 * 1024;
                if (n & 1) bstore16<SC1>((u32x4){hb[tt][n - 1][0], hb[tt][n - 1][1], hb[tt][n][0], hb[tt][n][1]}, rs, lo16, hbase + (3 * j3 + (n >> 1)) * 1024);
                if (n == 6 && j3 < 12) bstore8<SC1>(hb[tt][6], rs, lo16 + 8 * (j3 & 1), hbase + (48 + (j3 >> 1)) * 1024);
            }
            SB();
        }
        STAMP(13);
        publish(rs, WS_F3 + (g3 * 16 + j3) * FS, tag);
    }
    STAMP(14);

    {
        LANE_IDS();
        f32x4 gb[5]; u32x2 b2v[5];
#pragma unroll
        for (int f = 54; f < 70; ++f) w2_ld(f, lo16);
        SB();
        if (w == 0) {
            unsigned off[1] = {WS_F3 + ((P2 >> 1) * 16 + (l & 15)) * FS};
            poll_words<1>(ws, off, l < 16, tag);
        }
        BAR();
        STAMP(15);
        constexpr int PD = 3;
        u32x4 tk[14][4];
        const unsigned tokb = __builtin_amdgcn_readfirstlane(WS_HF + (4 * P2 * 54 + s4) * 1024);
        auto issue = [&](int i) {
            const int s = step4(i);
#pragma unroll
            for (int a = 0; a < 4; ++a) tk[i][a] = bload16<SC1>(rs, lo16, tokb + (((a + w) & 3) * 54 + s) * 1024);
        };
#pragma unroll
        for (int i = 0; i < PD; ++i) issue(i);
        f32x4 acc[4][5];
#pragma unroll
        for (int a = 0; a < 4; ++a)
#pragma unroll
            for (int n = 0; n < 5; ++n) acc[a][n] = (f32x4){0, 0, 0, 0};
        SB();
        LSTAMP(24);
#pragma unroll
        for (int i = 0; i < 14; ++i) {
            if (i < 13 || w < 2) {
                if (i + PD < 14) issue(i + PD);
                if (i == 11) {
#pragma unroll
                    for (int n = 0; n < 5; ++n) {
                        const int f = 80 * c2 + 16 * n;
                        gb[n] = asf4(bload16<SC1>(rs, qd * 16, WS_MODV + (3200 + f) * 4));
                        b2v[n] = *(const GAS u32x2*)(p.b2 + f + 4 * qd);
                    }
                }
#pragma unroll
                for (int a = 0; a < 4; ++a)
#pragma unroll
                    for (int n = 0; n < 5; ++n) acc[a][n] = mfma(w2f[i][n], tk[i][a], acc[a][n]);
#pragma unroll
                for (int a = 0; a < 4; ++a) { SGB(0x8, 2); if (i == 11) SGB(0x20, 3); else SGB(0x20, 1); SGB(0x8, 3); }
            }
            SB();
            LSTEP(25 + i);
        }
        STAMP(16);
#pragma unroll
        for (int a = 1; a < 4; ++a) {
            unsigned d = ((((a + w) & 3) * 3 + (3 - a)) * 5) * 1024 + lo16; OPAQUE(d);
#pragma unroll
            for (int n = 0; n < 5; ++n) STF4(d + n * 1024, acc[a][n]);
        }
        BAR();
        constexpr int L4_OUT = 61440;
        unsigned rb = w * 15360 + lo16; OPAQUE(rb);
#pragma unroll
        for (int n = 0; n < 5; ++n) {
            f32x4 v = acc[0][n];
#pragma unroll
            for (int u = 0; u < 3; ++u) v += LDF4(rb + (u * 5 + n) * 1024);
            const float x1[4] = {lo_f(x1b[n][0]), hi_f(x1b[n][0]), lo_f(x1b[n][1]), hi_f(x1b[n][1])};
            const float bv[4] = {lo_f(b2v[n][0]), hi_f(b2v[n][0]), lo_f(b2v[n][1]), hi_f(b2v[n][1])};
            float o[4];
#pragma unroll
            for (int r = 0; r < 4; ++r) o[r] = fmaf(gb[n][r], v[r] + bv[r], x1[r]);
            ST8(L4_OUT + (16 * w + t) * 176 + n * 32 + qd * 8, ((u32x2){pack2(o[0], o[1]), pack2(o[2], o[3])}));
        }
        asm volatile("s_waitcnt lgkmcnt(0)" ::: "memory");
        const rsrc_t ro = mk_rsrc(p.out, 2048 * 1280);
#pragma unroll
        for (int k = 0; k < 3; ++k) {
            const int ch = l + 64 * k;
            if (ch < 160) {
                const int row = ch / 10, cc = ch % 10;
                const u32x4 v = LD16(L4_OUT + (16 * w + row) * 176 + cc * 16);
                bstore16<OUT_AUX>(v, ro, row * 1280 + cc * 16, (16 * (4 * P2 + w)) * 1280 + c2 * 160);
            }
        }
    }
    STAMP(17);
    LSTAMP_DUMP;
    asm volatile(".Ladaln_end:" ::: "memory");
}

extern "C" void kernel_launch(void* const* d_in, const int* in_sizes, int n_in, void* d_out, int out_size, void* d_ws, size_t ws_size, hipStream_t stream) {
    Params p;
    p.x = (const u16*)d_in[0]; p.wqkv = (const u16*)d_in[1]; p.wo = (const u16*)d_in[2]; p.w1 = (const u16*)d_in[4]; p.b1 = (const u16*)d_in[5];
    p.w2 = (const u16*)d_in[6]; p.b2 = (const u16*)d_in[7]; p.c = (const u16*)d_in[8]; p.wm = (const u16*)d_in[9];
    p.out = (u16*)d_out; p.ws = (char*)d_ws;
    (void)hipFuncSetAttribute((const void*)adaLN_block_kernel, hipFuncAttributeMaxDynamicSharedMemorySize, LDS_BYTES);
    adaLN_block_kernel<<<256, 256, LDS_BYTES, stream>>>(p);
}
```

```cpp
#define DIAG 2
#define DIAGB 0
#include <hip/hip_runtime.h>
#include <stdint.h>

typedef unsigned short u16;
typedef float f32x4 __attribute__((ext_vector_type(4)));
typedef float f32x2 __attribute__((ext_vector_type(2)));
typedef __bf16 bf16x8 __attribute__((ext_vector_type(8)));
typedef __bf16 bf16x2 __attribute__((ext_vector_type(2)));
typedef unsigned u32x4 __attribute__((ext_vector_type(4)));
typedef unsigned u32x2 __attribute__((ext_vector_type(2)));
typedef short s16x4 __attribute__((ext_vector_type(4)));
typedef __amdgpu_buffer_rsrc_t rsrc_t;
#define LAS __attribute__((address_space(3)))
#define GAS __attribute__((address_space(1)))
#define DI __device__ __forceinline__

static constexpr unsigned FS = 64;
static constexpr unsigned WS_EPOCH = 0;
static constexpr unsigned WS_TMO   = 256;
static constexpr unsigned WS_FA    = 1024;
static constexpr unsigned WS_FC    = WS_FA + 160 * FS;
static constexpr unsigned WS_FD    = WS_FC + 128 * FS;
static constexpr unsigned WS_F1    = WS_FD + 1024;
static constexpr unsigned WS_F2    = WS_F1 + 256 * FS;
static constexpr unsigned WS_F3    = WS_F2 + 256 * FS;
static constexpr unsigned WS_FEND  = WS_F3 + 256 * FS;
static constexpr unsigned WS_MODV = 131072;
static constexpr unsigned WS_MODB = WS_MODV + 16384;
static constexpr unsigned WS_BP   = WS_MODB + 8192;
static constexpr unsigned WS_XNF  = WS_BP + 20 * 16 * 128 * 4;
static constexpr unsigned WS_WQF  = WS_XNF + 128 * 20 * 1024;
static constexpr unsigned WS_WOF  = WS_WQF + 16 * 20 * 8 * 1024;
static constexpr unsigned WS_W1F  = WS_WOF + 8 * 20 * 5 * 1024;
static constexpr unsigned WS_W2F  = WS_W1F + 108 * 20 * 1024;
static constexpr unsigned WS_OF   = WS_W2F + 8 * 54 * 5 * 1024;
static constexpr unsigned WS_UF   = WS_OF + 128 * 20 * 1024;
static constexpr unsigned WS_ST   = WS_UF + 128 * 20 * 1024;
static constexpr unsigned WS_HF   = WS_ST + 128 * 8 * 512;
static constexpr unsigned WS_END  = WS_HF + 128 * 54 * 1024;
static_assert(WS_FEND <= WS_MODV, "flags overflow");

#define B_DELAY 0
#define OUT_AUX 0
#define ROT1 0
#define ROT2 0
#define ROT3 0
#define ROT4 0
static constexpr int LDS_BYTES = 163840;
static constexpr int SC1 = 16;
static constexpr unsigned SPIN_CAP = 40000;

#define STAMP(i) do { if ((i) == 0) diag_t0 = __builtin_amdgcn_s_memrealtime(); if ((i) == DIAG) diag_t1 = __builtin_amdgcn_s_memrealtime(); } while (0)
#define LSTAMP(i)
#define LSTEP(i)
#define LSTAMP_DECL
#define LSTAMP_DUMP do { if (blockIdx.x == DIAGB) { const unsigned long long te = __builtin_amdgcn_s_memrealtime() + 20 * (diag_t1 - diag_t0); while (__builtin_amdgcn_s_memrealtime() < te) __builtin_amdgcn_s_sleep(4); } } while (0)

struct Params {
    const u16* x; const u16* wqkv; const u16* wo; const u16* w1; const u16* b1; const u16* w2; const u16* b2; const u16* c; const u16* wm;
    u16* out; char* ws;
};

DI float lo_f(unsigned d) { return __uint_as_float(d << 16); }
DI float hi_f(unsigned d) { return __uint_as_float(d & 0xffff0000u); }
DI unsigned pack2(float a, float b) { f32x2 v = {a, b}; return __builtin_bit_cast(unsigned, __builtin_convertvector(v, bf16x2)); }
DI float rbf(float a) { return lo_f(pack2(a, 0.f)); }
DI u32x2 pack4(const f32x4& v) { return (u32x2){pack2(v[0], v[1]), pack2(v[2], v[3])}; }
DI f32x4 mfma(u32x4 a, u32x4 b, f32x4 c) { return __builtin_amdgcn_mfma_f32_16x16x32_bf16(__builtin_bit_cast(bf16x8, a), __builtin_bit_cast(bf16x8, b), c, 0, 0, 0); }
DI float red_add_q(float v) {
    auto a = __builtin_amdgcn_permlane16_swap(__float_as_uint(v), __float_as_uint(v), false, false);
    v = __uint_as_float(a[0]) + __uint_as_float(a[1]);
    auto b = __builtin_amdgcn_permlane32_swap(__float_as_uint(v), __float_as_uint(v), false, false);
    return __uint_as_float(b[0]) + __uint_as_float(b[1]);
}
DI float red_max_q(float v) {
    auto a = __builtin_amdgcn_permlane16_swap(__float_as_uint(v), __float_as_uint(v), false, false);
    v = fmaxf(__uint_as_float(a[0]), __uint_as_float(a[1]));
    auto b = __builtin_amdgcn_permlane32_swap(__float_as_uint(v), __float_as_uint(v), false, false);
    return fmaxf(__uint_as_float(b[0]), __uint_as_float(b[1]));
}
template <int C> DI float dpp_add(float v) { return v + __builtin_bit_cast(float, __builtin_amdgcn_update_dpp(0, __builtin_bit_cast(int, v), C, 0xf, 0xf, true)); }
DI float red16(float v) { v = dpp_add<0xB1>(v); v = dpp_add<0x4E>(v); v = dpp_add<0x141>(v); return dpp_add<0x140>(v); }
DI unsigned swap32(unsigned v) { auto a = __builtin_amdgcn_permlane32_swap(v, v, false, false); return (threadIdx.x & 32) ? a[0] : a[1]; }
DI rsrc_t mk_rsrc(const void* p, unsigned bytes) { return __builtin_amdgcn_make_buffer_rsrc((void*)p, 0, bytes, 0x00020000); }
template <int AUX> DI u32x4 bload16(rsrc_t r, unsigned voff, unsigned soff) { return __builtin_bit_cast(u32x4, __builtin_amdgcn_raw_buffer_load_b128(r, voff, soff, AUX)); }
template <int AUX> DI u32x2 bload8(rsrc_t r, unsigned voff, unsigned soff) { return __builtin_bit_cast(u32x2, __builtin_amdgcn_raw_buffer_load_b64(r, voff, soff, AUX)); }
template <int AUX> DI unsigned bload4(rsrc_t r, unsigned voff, unsigned soff) { return __builtin_amdgcn_raw_buffer_load_b32(r, voff, soff, AUX); }
template <int AUX> DI void bstore16(u32x4 v, rsrc_t r, unsigned voff, unsigned soff) { __builtin_amdgcn_raw_buffer_store_b128(v, r, voff, soff, AUX); }
template <int AUX> DI void bstore8(u32x2 v, rsrc_t r, unsigned voff, unsigned soff) { __builtin_amdgcn_raw_buffer_store_b64(v, r, voff, soff, AUX); }
template <int AUX> DI void bstore4(unsigned v, rsrc_t r, unsigned voff, unsigned soff) { __builtin_amdgcn_raw_buffer_store_b32(v, r, voff, soff, AUX); }
DI f32x4 asf4(u32x4 v) { return __builtin_bit_cast(f32x4, v); }
DI u32x4 asu4(f32x4 v) { return __builtin_bit_cast(u32x4, v); }
#define LANE_IDS() int tz_ = threadIdx.x; asm volatile("" : "+v"(tz_)); const int tid = tz_, l = tid & 63, t = l & 15, qd = l >> 4; const unsigned lo16 = l * 16; (void)t; (void)qd; (void)lo16
#define OPAQUE(x) asm volatile("" : "+v"(x))
#define SB() __builtin_amdgcn_sched_barrier(0)
#define BAR() asm volatile("s_waitcnt lgkmcnt(0)\n\ts_barrier" ::: "memory")
#define DRAIN() asm volatile("s_waitcnt vmcnt(0)" ::: "memory")
#define SGB(mask, n) __builtin_amdgcn_sched_group_barrier(mask, n, 0)
#define LD16(off) (*(const LAS u32x4*)((const LAS char*)lds + (off)))
#define LD8(off) (*(const LAS u32x2*)((const LAS char*)lds + (off)))
#define LDF4(off) (*(const LAS f32x4*)((const LAS char*)lds + (off)))
#define LDF(off) (*(const LAS float*)((const LAS char*)lds + (off)))
#define ST16(off, v) (*(LAS u32x4*)((LAS char*)lds + (off)) = (v))
#define ST8(off, v) (*(LAS u32x2*)((LAS char*)lds + (off)) = (v))
#define STF4(off, v) (*(LAS f32x4*)((LAS char*)lds + (off)) = (v))
#define STF(off, v) (*(LAS float*)((LAS char*)lds + (off)) = (v))
DI u32x2 tr_read(const char* lds, unsigned off) { return __builtin_bit_cast(u32x2, __builtin_amdgcn_ds_read_tr16_b64_v4i16((LAS s16x4*)((LAS char*)lds + off))); }

DI void flag_store(char* ws, unsigned off, unsigned tag) { __hip_atomic_store((GAS unsigned*)(ws + off), tag, __ATOMIC_RELAXED, __HIP_MEMORY_SCOPE_AGENT); }
DI unsigned flag_load(const char* ws, unsigned off) { return __hip_atomic_load((const GAS unsigned*)(ws + off), __ATOMIC_RELAXED, __HIP_MEMORY_SCOPE_AGENT); }
DI void flag_put(rsrc_t rs, unsigned off, unsigned tag, bool me) { bstore4<SC1>(tag, rs, me ? off : 0x7ff00000u, 0); }
DI void publish(rsrc_t rs, unsigned off, unsigned tag) { DRAIN(); BAR(); flag_put(rs, off, tag, threadIdx.x == 0); }
template <int N> DI void poll_words(char* ws, const unsigned (&off)[N], bool active, unsigned tag) {
#pragma nounroll
    for (unsigned spins = 0;; ++spins) {
        bool ok = true;
#pragma unroll
        for (int k = 0; k < N; ++k) ok &= flag_load(ws, off[k]) == tag;
        if (__all(ok || !active)) break;
        if (spins > SPIN_CAP) { if ((threadIdx.x & 63) == 0) flag_store(ws, WS_TMO, tag); break; }
        __builtin_amdgcn_s_sleep(1);
    }
}

DI void check_all_blocks(char* ws, rsrc_t rs, unsigned base, u32x4 v, unsigned tag) {
    asm volatile("" : "+v"(v));
#pragma nounroll
    for (unsigned spins = 0; !__all(v[0] == tag && v[1] == tag && v[2] == tag && v[3] == tag); ++spins) {
        if (spins > SPIN_CAP) { if ((threadIdx.x & 63) == 0) flag_store(ws, WS_TMO, tag); break; }
        __builtin_amdgcn_s_sleep(1);
        v = bload16<SC1>(rs, (threadIdx.x & 63) * 16, base);
        asm volatile("" : "+v"(v) :: "memory");
    }
}

DI void rows_wo(int s, int qd, int& ra, int& rb) { if (s < 16) { ra = 40 * s + 4 * qd; rb = ra + 20; } else { ra = 40 * (4 * (s - 16) + qd) + 16; rb = ra + 20; } }
DI void tiles_p3(int s, int& ta, int& tb) { if (s < 16) { ta = 5 * (s >> 1) + 2 * (s & 1); tb = ta + 1; } else { ta = 10 * (s - 16) + 4; tb = ta + 5; } }
DI void tiles_p4(int s, int& ta, int& tb) { if (s < 48) { int j = s / 3, pr = s % 3; int st = j < 12 ? 7 * j : 84 + 6 * (j - 12); ta = st + 2 * pr; tb = ta + 1; } else { int m = s - 48; ta = 14 * m + 6; tb = ta + 7; } }

static constexpr int L0_SC = 81920, L0_GS = 83200, L0_CRAW = 83968, L0_RW = 86016, L0_D = 101376, L0_XI = 134144, L0_XS = 156160;

template <int AUX> DI void dma(char* lds, unsigned lds_off, rsrc_t r, unsigned voff, unsigned soff) {
    __builtin_amdgcn_raw_ptr_buffer_load_lds(r, (LAS void*)((LAS char*)lds + lds_off), 16, voff, soff, 0, AUX);
}
DI void dma16(char* lds, unsigned lds_off, rsrc_t r, unsigned voff, unsigned soff) {
    const unsigned dst = (unsigned)(size_t)(LAS char*)lds + lds_off;
    asm volatile("s_mov_b32 m0, %0\n\ts_nop 0\n\tbuffer_load_dwordx4 %1, %2, %3 offen lds" :: "s"(dst), "v"(voff), "s"(r), "s"(soff) : "memory");
}
DI void dma16s(char* lds, unsigned lds_off, rsrc_t r, unsigned voff, unsigned soff) { dma<SC1>(lds, lds_off, r, voff, soff); }
#define WAITV(n) asm volatile("s_waitcnt vmcnt(%0)" :: "n"(n) : "memory")

struct DPlan { rsrc_t r; unsigned v0, v1, sa, sb, st; bool on; };
struct DOut { unsigned dst[8]; u32x4 f[8]; };
DI void dtask_plan(const Params& p, int id, int l, DPlan& d, DOut& o) {
    int ra0, rb0, rstep, cg, lim; unsigned pitch, d0, r5, stride, jump;
    if (id < 370) {
        const bool wo = id < 100; const int i2 = wo ? id : id - 100, s = i2 / 5; cg = i2 - 5 * s; pitch = 1280;
        if (wo) { d.r = mk_rsrc(p.wo, 640 * 1280); if (s < 16) { ra0 = 40 * s; rstep = 4; } else { ra0 = 160 * (s - 16) + 16; rstep = 40; } rb0 = ra0 + 20; }
        else { d.r = mk_rsrc(p.w2, 1728 * 1280); int ta, tb; tiles_p4(s, ta, tb); ra0 = 16 * ta; rb0 = 16 * tb; rstep = 4; }
        const int c0 = (8 * cg) / 5;
        r5 = 8 * cg - 5 * c0; d0 = (wo ? WS_WOF + (c0 * 20 + s) * 5120 : WS_W2F + (c0 * 54 + s) * 5120) + r5 * 1024;
        stride = 1024; jump = wo ? 19 * 5120 : 53 * 5120; lim = 8;
    } else {
        const int i2 = id < 650 ? id - 370 : 0, s = i2 / 14; cg = i2 - 14 * s; pitch = 3456; d.r = mk_rsrc(p.w1, 640 * 3456);
        int ta, tb; tiles_p3(s, ta, tb); ra0 = 16 * ta; rb0 = 16 * tb; rstep = 4;
        r5 = 0; d0 = WS_W1F + (8 * cg * 20 + s) * 1024; stride = 20480; jump = 0; lim = id < 650 ? (cg == 13 ? 4 : 8) : 0;
    }
    const int q = l >> 4, cp = l & 15;
    d.v0 = q * pitch + ((cp ^ (q << 1)) * 16); d.v1 = q * pitch + ((cp ^ ((4 + q) << 1)) * 16);
    d.sa = ra0 * pitch + cg * 256; d.sb = rb0 * pitch + cg * 256; d.st = rstep * pitch; d.on = lim > 0;
#pragma unroll
    for (int nt = 0; nt < 8; ++nt) {
        const unsigned v = d0 + nt * stride + ((r5 + nt >= 5 ? 1u : 0u) + (r5 + nt >= 10 ? 1u : 0u)) * jump;
        o.dst[nt] = __builtin_amdgcn_readfirstlane(nt < lim ? v : 0x7ff00000u);
    }
}
DI void dtask_go(const DPlan& d, char* lds, unsigned scr, int w) {
    if (d.on) {
#pragma unroll
        for (int i = 0; i < 8; ++i) dma16(lds, scr + w * 8192 + i * 1024, d.r, (i & 1) ? d.v1 : d.v0, (i < 4 ? d.sa : d.sb) + (i & 3) * d.st);
    }
}
DI void dtask_read(DOut& o, char* lds, unsigned scr, int w, int l) {
    const int qd = l >> 4, q = (l >> 2) & 3, pp = l & 3, r7 = 4 * (qd & 1) + q;
    const unsigned a0 = scr + w * 8192 + (4 * qd + q) * 256 + pp * 8;
#pragma unroll
    for (int nt = 0; nt < 8; ++nt) {
        const u32x2 ua = tr_read(lds, a0 + ((nt ^ r7) * 32)), ub = tr_read(lds, a0 + 4096 + ((nt ^ r7) * 32));
        o.f[nt] = (u32x4){ua[0], ua[1], ub[0], ub[1]};
    }
}
template <int NT> DI void dtask_frag(const DOut& o, rsrc_t rs, int l) { bstore16<SC1>(o.f[NT], rs, l * 16, o.dst[NT]); }

template <int I0, int I1> DI void wm_dma(const Params& p, char* lds, int w, int l, unsigned colA, unsigned colB, int rot) {
    const rsrc_t rw = mk_rsrc(p.wm, 640 * 3840 * 2);
    const int rl = l >> 3, cp = l & 7;
    const unsigned sg = ((rl >> 1) & 1);
    const unsigned c0 = cp ^ (sg << 1), c1 = cp ^ ((sg | 2) << 1);
    const unsigned v0 = rl * 7680 + (c0 < 4 ? colA + 8 * c0 : colB + 8 * (c0 - 4)) * 2, v1 = rl * 7680 + (c1 < 4 ? colA + 8 * c1 : colB + 8 * (c1 - 4)) * 2;
#pragma unroll
    for (int i = I0; i < I1; ++i) {
        int piece = 4 * i + w + rot; piece = piece >= 80 ? piece - 80 : piece;
        dma16(lds, piece * 1024, rw, (piece & 1) ? v1 : v0, piece * 8 * 7680);
    }
}
DI void c_dma(const Params& p, char* lds, int w, int l) {
    dma16(lds, L0_CRAW + (w & 1) * 1024, mk_rsrc(p.c, 1280), ((w & 1) * 64 + l) * 16, 0);
}
DI void silu_table(char* lds, int tid) {
    if (tid < 80) {
        const u32x4 creg = LD16(L0_CRAW + tid * 16);
        u32x4 o;
#pragma unroll
        for (int d = 0; d < 4; ++d) { const float a = lo_f(creg[d]), b = hi_f(creg[d]); o[d] = pack2(a * __builtin_amdgcn_rcpf(1.f + __expf(-a)), b * __builtin_amdgcn_rcpf(1.f + __expf(-b))); }
        ST16(L0_SC + tid * 16, o);
    }
}
DI float mod_gemv(char* lds, int w, int l) {
    const int qd = l >> 4, q = (l >> 2) & 3, pp = l & 3;
    f32x4 acc = {0, 0, 0, 0}, acc2 = {0, 0, 0, 0};
    const int r0 = 8 * qd + q;
    const unsigned sg = ((r0 >> 1) & 1) | (((r0 >> 3) & 1) << 1);
    unsigned a0 = r0 * 128 + ((w ^ sg) * 32) + pp * 8, a1 = a0 + 40960, sb = L0_SC + qd * 16;
    OPAQUE(a0); OPAQUE(a1); OPAQUE(sb);
    u32x2 tb[2][4]; u32x4 sc[2][2];
    auto rd = [&](int pr) {
        const unsigned ab = pr < 5 ? a0 : a1; const int po = (pr < 5 ? pr : pr - 5) * 8192;
        tb[pr & 1][0] = tr_read(lds, ab + po); tb[pr & 1][1] = tr_read(lds, ab + po + 512); tb[pr & 1][2] = tr_read(lds, ab + po + 4096); tb[pr & 1][3] = tr_read(lds, ab + po + 4608);
        sc[pr & 1][0] = LD16(sb + pr * 128); sc[pr & 1][1] = LD16(sb + pr * 128 + 64);
    };
    rd(0);
#pragma unroll
    for (int pr = 0; pr < 10; ++pr) {
        if (pr + 1 < 10) rd(pr + 1);
        SB();
        acc = mfma(sc[pr & 1][0], (u32x4){tb[pr & 1][0][0], tb[pr & 1][0][1], tb[pr & 1][1][0], tb[pr & 1][1][1]}, acc);
        acc2 = mfma(sc[pr & 1][1], (u32x4){tb[pr & 1][2][0], tb[pr & 1][2][1], tb[pr & 1][3][0], tb[pr & 1][3][1]}, acc2);
        SB();
    }
    return rbf(acc[0] + acc2[0]);
}
DI void x_dma(const Params& p, char* lds, unsigned img, int T, int w, int l) {
    const rsrc_t rx = mk_rsrc(p.x, 2048 * 1280);
    const int r4 = l >> 4, row = 4 * w + r4;
    const unsigned voff = row * 1280 + (((l & 15) ^ row) * 16);
#pragma unroll
    for (int cb = 0; cb < 5; ++cb) dma16(lds, img + (w * 5 + cb) * 1024, rx, voff, T * 16 * 1280 + cb * 256);
}
DI void x_stats(char* lds, unsigned img, unsigned stat, int w, int l) {
    u32x4 v[5];
#pragma unroll
    for (int cb = 0; cb < 5; ++cb) v[cb] = LD16(img + (w * 5 + cb) * 1024 + l * 16);
    float sum = 0.f;
#pragma unroll
    for (int i = 0; i < 5; ++i)
#pragma unroll
        for (int d = 0; d < 4; ++d) sum += lo_f(v[i][d]) + hi_f(v[i][d]);
    sum = red16(sum);
    const float mean = sum * (1.f / 640.f);
    float vs = 0.f;
#pragma unroll
    for (int i = 0; i < 5; ++i)
#pragma unroll
        for (int d = 0; d < 4; ++d) { const float a = lo_f(v[i][d]) - mean, c2 = hi_f(v[i][d]) - mean; vs += a * a + c2 * c2; }
    vs = red16(vs);
    const float rstd = rsqrtf(vs * (1.f / 640.f) + 1e-5f);
    if ((l & 15) == 0) { STF(stat + (4 * w + (l >> 4)) * 8, mean); STF(stat + (4 * w + (l >> 4)) * 8 + 4, rstd); }
}
DI unsigned ximg_off(int t, int byte_in_row) { return ((t >> 2) * 5 + (byte_in_row >> 8)) * 1024 + (t & 3) * 256 + ((((byte_in_row >> 4) & 15) ^ t) * 16) + (byte_in_row & 15); }
DI void x_frags(char* lds, unsigned img, unsigned stat, rsrc_t rs, int T, int w, int l) {
    const int t = l & 15, qd = l >> 4;
    const float mean = LDF(stat + t * 8), rstd = LDF(stat + t * 8 + 4);
#pragma unroll
    for (int i = 0; i < 5; ++i) {
        const int s = w + 4 * i;
        const u32x4 v = LD16(img + ximg_off(t, s * 64 + qd * 16));
        u32x4 o;
#pragma unroll
        for (int d = 0; d < 4; ++d) o[d] = pack2((lo_f(v[d]) - mean) * rstd, (hi_f(v[d]) - mean) * rstd);
        bstore16<SC1>(o, rs, l * 16, WS_XNF + (T * 20 + s) * 1024);
    }
}
struct TileKV { u32x2 ka0, ka1, k2, va0, va1, z; };
struct AttnMask { bool c0, c1, p0, p1, odd; unsigned sel0, sel1; };
DI AttnMask attn_mask(int t, int qd) {
    AttnMask m; const int par = t & 1, dc = t - 4 * qd - par;
    m.odd = par; m.c0 = dc >= 0; m.c1 = dc >= 2; m.p0 = dc + 16 <= 14; m.p1 = dc + 14 <= 14;
    m.sel0 = par ? 0x01000c0cu : 0x0c0c0100u; m.sel1 = par ? 0x03020c0cu : 0x0c0c0302u;
    return m;
}
template <class BG> DI void attn_unit(const u32x2 (&qa)[2], const u32x2& xq, const TileKV& cur, const TileKV& prev, bool prev_ok, const AttnMask& m, int qd, u32x4& omain, u32x2& oleft, BG&& bg) {
    const float cs = 0.22360679774997896f * 1.4426950408889634f, ninf = -__builtin_inff();
    f32x4 oa[2], oz[2];
#pragma unroll
    for (int h = 0; h < 2; ++h) {
        const u32x4 qf = {qa[h][0], qa[h][1], qd == h ? xq[0] : 0u, qd == h ? xq[1] : 0u};
        const u32x2 kcp = h == 0 ? cur.ka0 : cur.ka1, kpp = h == 0 ? prev.ka0 : prev.ka1;
        const f32x4 z4 = {0.f, 0.f, 0.f, 0.f};
        const f32x4 sp = mfma((u32x4){kpp[0], kpp[1], prev.k2[0], prev.k2[1]}, qf, z4);
        const f32x4 sc = mfma((u32x4){kcp[0], kcp[1], cur.k2[0], cur.k2[1]}, qf, z4);
        bg(5 * h);
        float e0 = m.odd ? sp[1] : sp[0], e1 = m.odd ? sp[3] : sp[2], e2 = m.odd ? sc[1] : sc[0], e3 = m.odd ? sc[3] : sc[2];
        e0 = (m.p0 && prev_ok) ? e0 : ninf; e1 = (m.p1 && prev_ok) ? e1 : ninf; e2 = m.c0 ? e2 : ninf; e3 = m.c1 ? e3 : ninf;
        bg(5 * h + 1);
        const float mx = red_max_q(fmaxf(fmaxf(e0, e1), fmaxf(e2, e3))), mc = -mx * cs;
        bg(5 * h + 2);
        e0 = __builtin_amdgcn_exp2f(fmaf(e0, cs, mc)); e1 = __builtin_amdgcn_exp2f(fmaf(e1, cs, mc)); e2 = __builtin_amdgcn_exp2f(fmaf(e2, cs, mc)); e3 = __builtin_amdgcn_exp2f(fmaf(e3, cs, mc));
        bg(5 * h + 3);
        const float inv = __builtin_amdgcn_rcpf(red_add_q((e0 + e1) + (e2 + e3)));
        bg(5 * h + 4);
        const unsigned pp = pack2(e0 * inv, e1 * inv), pc = pack2(e2 * inv, e3 * inv);
        const u32x4 pf = {__builtin_amdgcn_perm(0u, pp, m.sel0), __builtin_amdgcn_perm(0u, pp, m.sel1), __builtin_amdgcn_perm(0u, pc, m.sel0), __builtin_amdgcn_perm(0u, pc, m.sel1)};
        const u32x2 vc2 = h == 0 ? cur.va0 : cur.va1, vp2 = h == 0 ? prev.va0 : prev.va1;
        oa[h] = mfma((u32x4){vp2[0], vp2[1], vc2[0], vc2[1]}, pf, z4);
        oz[h] = mfma((u32x4){prev.z[0], prev.z[1], cur.z[0], cur.z[1]}, pf, z4);
    }
    const u32x2 a0 = pack4(oa[0]), a1 = pack4(oa[1]);
    omain = (u32x4){a0[0], a0[1], a1[0], a1[1]};
    const u32x2 l0 = pack4(oz[0]), l1 = pack4(oz[1]);
    oleft = qd == 0 ? l0 : l1;
}

__global__ __launch_bounds__(256, 1) void adaLN_block_kernel(Params p) {
    extern __shared__ __attribute__((aligned(16))) char lds[];
    const int b = blockIdx.x, w = __builtin_amdgcn_readfirstlane(threadIdx.x >> 6);
    const int xcd = b & 7, bi = b >> 3;
    char* ws = p.ws;
    const rsrc_t rs = mk_rsrc(ws, WS_END);
    unsigned long long diag_t0 = 0, diag_t1 = 0;
    STAMP(0);
    LSTAMP_DECL;
    {
        unsigned long long pc; unsigned len;
        asm volatile("s_getpc_b64 %0\n.Ladaln_pc:\n\ts_mov_b32 %1, .Ladaln_end-.Ladaln_pc" : "=s"(pc), "=s"(len));
        unsigned off = (bi * 256 + threadIdx.x) * 64u;
        off = off < len ? off : 0u;
        const unsigned long long a = pc + off;
        const unsigned dst = (unsigned)(size_t)(LAS char*)lds + LDS_BYTES - 1024 + w * 256;
        asm volatile("s_mov_b32 m0, %0\n\ts_nop 0\n\tglobal_load_lds_dword %1, off" :: "s"(dst), "v"(a) : "memory");
    }
    const unsigned epoch = *(const __attribute__((address_space(4))) unsigned*)(size_t)(ws + WS_EPOCH);
    unsigned tag;
#define MAKE_TAG() do { unsigned e_ = epoch; asm volatile("" : "+s"(e_)); tag = e_ + 0x9E3779B9u; } while (0)

    DOut dout;
    {
        LANE_IDS();
        const bool isA = bi < 20, isB = !isA && bi < 25, isC = !isA && !isB;
        const int sA = bi < 16 ? xcd + 8 * (bi >> 3) : 16 + (xcd >> 1), jg = bi < 16 ? (bi & 7) : 4 * (xcd & 1) + (bi - 16), idxB = xcd * 5 + (bi - 20);
        const int aidx = jg * 20 + sA;
        const int idxC = xcd * 7 + (bi - 25);
        const int Ts = isB ? idxB : (idxC < 32 ? 40 + 2 * idxC : 72 + idxC), nT = (isC && idxC < 32) ? 2 : 1;
        const int did = w == 0 ? 1000 : b * 3 + w - 1;
        DPlan dp;
        if (!isA) {
            x_dma(p, lds, L0_XI, Ts, w, l);
            if (nT == 2) x_dma(p, lds, 0, Ts + 1, w, l);
            MAKE_TAG();
            dtask_plan(p, did, l, dp, dout);
            STAMP(18);
            if (nT == 2) { WAITV(5); x_stats(lds, L0_XI, L0_XS, w, l); WAITV(0); x_stats(lds, 0, L0_XS + 128, w, l); }
            else { WAITV(0); x_stats(lds, L0_XI, L0_XS, w, l); }
            BAR();
            x_frags(lds, L0_XI, L0_XS, rs, Ts, w, l);
            if (nT == 2) x_frags(lds, 0, L0_XS + 128, rs, Ts + 1, w, l);
            if (isB) {
                c_dma(p, lds, w, l);
                wm_dma<0, 20>(p, lds, w, l, 1280 + 64 * idxB, 1280 + 64 * idxB + 32, 0);
                WAITV(21);
            } else {
                dtask_go(dp, lds, L0_D, w);
                if (dp.on) WAITV(8); else WAITV(0);
            }
            BAR();
            flag_put(rs, WS_FC + (Ts + (tid & 1)) * FS, tag, tid < nT);
            STAMP(20);
        }
        if (!isC) {
            if (isA) {
                c_dma(p, lds, w, l);
                wm_dma<0, 10>(p, lds, w, l, 640 + 32 * sA, 32 * sA, 10 * jg);
                WAITV(10);
                silu_table(lds, tid);
                wm_dma<10, 20>(p, lds, w, l, 640 + 32 * sA, 32 * sA, 10 * jg);
                const rsrc_t rq = mk_rsrc(p.wqkv, 640 * 3840);
#pragma unroll
                for (int m = 0; m < 4; ++m) {
                    const int n = 64 * (4 * m + w) + l, rho = n / 30, c = n % 30;
                    const int k = (rho & 19) | ((rho & 4) << 1) | ((rho & 8) >> 1);
                    if (4 * m + w < 15) dma16(lds, L0_RW + (4 * m + w) * 1024, rq, k * 3840 + (c / 10) * 1280 + jg * 160 + (c % 10) * 16, sA * 32 * 3840);
                }
                MAKE_TAG();
                STAMP(18);
            } else {
                WAITV(21);
                silu_table(lds, tid);
            }
            WAITV(0);
            BAR();
            float v = mod_gemv(lds, w, l);
            STAMP(19);
            if (isA) {
                const float gv = w < 2 ? rbf(1.f + v) : v;
                if (l < 16) { STF(L0_GS + (w * 16 + l) * 4, gv); *(LAS u16*)((LAS char*)lds + L0_GS + 256 + (w * 16 + l) * 2) = (u16)(__float_as_uint(gv) >> 16); }
                const int q = (l >> 2) & 3, pp = l & 3, jj = w >> 1, j = 2 * jg + jj;
                const unsigned rbase = L0_RW + (16 * (qd >> 1) + 4 * (qd & 1) + q) * 480 + 80 * jj;
                u32x2 ua[4], ub[4];
#pragma unroll
                for (int i = 0; i < 4; ++i) {
                    const unsigned lo = i < 2 ? i * 40 + 8 * pp : 160 + (i - 2) * 40 + 8 * pp;
                    const unsigned hi = i == 0 ? (pp >> 1) * 160 + (pp & 1) * 40 + 32 : i < 3 ? 320 + (i - 1) * 40 + 8 * pp : 320 + (pp & 1) * 40 + 32;
                    const unsigned po = (w & 1) ? hi : lo;
                    ua[i] = tr_read(lds, rbase + po); ub[i] = tr_read(lds, rbase + po + 8 * 480);
                }
                if ((w & 1) && t >= 8) { ua[3] = (u32x2){0u, 0u}; ub[3] = ua[3]; }
                BAR();
                const f32x4 g0 = LDF4(L0_GS + qd * 32), g1 = LDF4(L0_GS + qd * 32 + 16);
                const u32x4 sfr = LD16(L0_GS + 256 + 64 + qd * 16);
#pragma unroll
                for (int i = 0; i < 4; ++i) {
                    const int nt = 4 * (w & 1) + i;
                    const u32x4 o = {pack2(lo_f(ua[i][0]) * g0[0], hi_f(ua[i][0]) * g0[1]), pack2(lo_f(ua[i][1]) * g0[2], hi_f(ua[i][1]) * g0[3]),
                                     pack2(lo_f(ub[i][0]) * g1[0], hi_f(ub[i][0]) * g1[1]), pack2(lo_f(ub[i][1]) * g1[2], hi_f(ub[i][1]) * g1[3])};
                    bstore16<SC1>(o, rs, lo16, WS_WQF + ((j * 20 + sA) * 8 + nt) * 1024);
                }
                {
                    const f32x4 z4 = {0, 0, 0, 0};
                    f32x4 bias = mfma((u32x4){ua[0][0], ua[0][1], ub[0][0], ub[0][1]}, sfr, z4);
#pragma unroll
                    for (int i = 1; i < 4; ++i) { const f32x4 bi4 = mfma((u32x4){ua[i][0], ua[i][1], ub[i][0], ub[i][1]}, sfr, z4); bias = t == i ? bi4 : bias; }
                    bstore16<SC1>(asu4(bias), rs, t < 4 ? t * 64 + qd * 16 : 0x7ff00000u, WS_BP + ((sA * 16 + j) * 128 + (w & 1) * 64) * 4);
                }
                STAMP(20);
                dtask_plan(p, did, l, dp, dout);
            } else {
                const int col = 1280 + 64 * idxB + 16 * w + t;
                if (col >= 2560 && col < 3200) v = rbf(1.f + v);
                bstore4<SC1>(__float_as_uint(v), rs, l < 16 ? l * 4 : 0x7ff00000u, WS_MODV + (1280 + 64 * idxB + 16 * w) * 4);
                const unsigned vn = (unsigned)__builtin_amdgcn_update_dpp(0, (int)__float_as_uint(v), 0xB1, 0xf, 0xf, true);
                bstore4<SC1>((__float_as_uint(v) >> 16) | (vn & 0xffff0000u), rs, (l < 16 && !(l & 1)) ? l * 2 : 0x7ff00000u, WS_MODB + (1280 + 64 * idxB + 16 * w) * 2);
            }
            dtask_go(dp, lds, L0_D, w);
            if (dp.on) WAITV(8); else WAITV(0);
            BAR();
            flag_put(rs, WS_FA + aidx * FS, tag, isA && tid == 0);
        }
        STAMP(1);
    }

    const int g1 = 4 * (xcd >> 1) + (bi >> 3), j1 = 8 * (xcd & 1) + (bi & 7);
    const int P2 = 4 * xcd + (bi >> 3), c2 = bi & 7;
    const int g3 = g1, j3 = j1;
    const int rot2 = ROT2, rot3 = ROT3;
    auto wrap20 = [](int x) { return x >= 20 ? x - 20 : x; };
    const int t3 = j3 < 12 ? 7 * j3 : 84 + 6 * (j3 - 12), cnt3 = j3 < 12 ? 7 : 6;
    u32x4 w2f[14][5];
    const int s4 = __builtin_amdgcn_readfirstlane(w < 2 ? 14 * w : 28 + 13 * (w - 2)), cnt4 = __builtin_amdgcn_readfirstlane(w < 2 ? 14 : 13);
    const unsigned w2b = __builtin_amdgcn_readfirstlane(WS_W2F + (c2 * 54 + s4) * 5 * 1024);
    const int rot4 = __builtin_amdgcn_readfirstlane(ROT4);
    auto step4 = [&](int i) { const int x = (i < 13 ? i : cnt4 - 1) + rot4; return x >= cnt4 ? x - cnt4 : x; };
    auto w2_ld = [&](int f, unsigned lo) { w2f[f / 5][f % 5] = bload16<SC1>(rs, lo, w2b + step4(f / 5) * 5120 + (f % 5) * 1024); };
    constexpr int L2_WO = 61440, L2_X = 46080;
    {
        LANE_IDS();
        const int T0 = 8 * g1 + 2 * w, TH = g1 == 0 ? 0 : 8 * g1 - 1;
        const unsigned xb0 = WS_XNF + T0 * 20480, xbh = WS_XNF + TH * 20480 + (w >> 1) * 1024, wb = WS_WQF + j1 * 163840 + w * 4096;
        const unsigned wob = WS_WOF + c2 * 102400;
        u32x4 xr[10][4], xh[10];
        const int rot1 = ROT1;
        auto gr = [&](int G) { const int x = G + rot1; return x >= 10 ? x - 10 : x; };
        auto w_dma = [&](int G, int k) { dma16s(lds, (G & 3) * 16384 + w * 4096 + k * 1024, rs, lo16, wb + gr(G) * 16384 + k * 1024); };
        auto x_ld = [&](int G, int i) { xr[G][i] = bload16<SC1>(rs, lo16, xb0 + (i & 1) * 20480 + (2 * gr(G) + (i >> 1)) * 1024); };
        if (w == 0) {
            unsigned off[1] = {l == 0 ? WS_FC + TH * FS : WS_FC + (8 * g1 + ((l - 1) & 7)) * FS};
            poll_words<1>(ws, off, l < 9, tag);
        }
        BAR();
#pragma unroll
        for (int G = 0; G < 4; ++G) {
#pragma unroll
            for (int i = 0; i < 4; ++i) x_ld(G, i);
            xh[G] = bload16<SC1>(rs, lo16, xbh + gr(G) * 2048);
        }
        SB();
        if (w == 0) {
            unsigned off[1] = {WS_FA + ((j1 >> 1) * 20 + (l < 20 ? l : 0)) * FS};
            poll_words<1>(ws, off, l < 20, tag);
        }
        DRAIN();
        if (w > 0) dtask_read(dout, lds, L0_D, w, l);
        else {
#pragma unroll
            for (int i = 0; i < 8; ++i) dout.f[i] = (u32x4){0u, 0u, 0u, 0u};
        }
        BAR();
        STAMP(2);
#pragma unroll
        for (int G = 0; G < 3; ++G)
#pragma unroll
            for (int k = 0; k < 4; ++k) w_dma(G, k);
        WAITV(4);
        BAR();
        STAMP(3);
        f32x4 acc[2][8], hacc[3];
#pragma unroll
        for (int i = 0; i < 8; ++i) { acc[0][i] = (f32x4){0, 0, 0, 0}; acc[1][i] = (f32x4){0, 0, 0, 0}; }
#pragma unroll
        for (int i = 0; i < 3; ++i) hacc[i] = (f32x4){0, 0, 0, 0};
        u32x4 wf[2][8];
#pragma unroll
        for (int n = 0; n < 8; ++n) wf[0][n] = LD16(n * 1024 + lo16);
        const unsigned hoff = (w >> 1) * 8192 + ((w & 1) ? 5 : 2) * 1024 + lo16;
        f32x4 bp4[3]; u32x4 fdv;
        constexpr int L1_BPART = 40960;
        SB();
#pragma unroll
        for (int G = 0; G < 10; ++G) {
            u32x4 hw[3];
#pragma unroll
            for (int st = 0; st < 2; ++st) {
                const int s = 2 * G + st;
#pragma unroll
                for (int n = 0; n < 8; ++n) {
                    if (s + 1 < 20) wf[(s + 1) & 1][n] = LD16((((s + 1) >> 1) & 3) * 16384 + ((s + 1) & 1) * 8192 + n * 1024 + lo16);
                    if (st == 0 && n < 3) hw[n] = LD16((G & 3) * 16384 + hoff + n * 1024);
                    if (n < 5) { acc[0][n] = mfma(wf[s & 1][n], xr[G][st * 2], acc[0][n]); acc[1][n] = mfma(wf[s & 1][n], xr[G][st * 2 + 1], acc[1][n]); }
                    else { acc[0][n] = mfma(xr[G][st * 2], wf[s & 1][n], acc[0][n]); acc[1][n] = mfma(xr[G][st * 2 + 1], wf[s & 1][n], acc[1][n]); }
                    if (st == 1 && n >= 5) hacc[n - 5] = mfma(hw[n - 5], xh[G], hacc[n - 5]);
                    if (st == 0) {
                        if (G + 3 < 10 && (n & 1) == 0) w_dma(G + 3, n >> 1);
                        if (G + 4 < 10 && n == 7) xh[G + 4] = bload16<SC1>(rs, lo16, xbh + gr(G + 4) * 2048);
                    } else {
                        if (G + 4 < 10 && (n & 1) == 0) x_ld(G + 4, n >> 1);
                    }
                    if (G == 7 && st == 0 && n < 3) bp4[n] = asf4(bload16<SC1>(rs, (n < 2 || tid < 128) ? ((tid >> 5) + 8 * n) * 8192 + (tid & 31) * 16 : 0x7ff00000u, WS_BP + j1 * 512));
                    if (G == 9 && st == 0 && n == 0) {
                        f32x4 bs = (bp4[0] + bp4[1]) + bp4[2];
#pragma unroll
                        for (int r = 0; r < 4; ++r) { auto sw = __builtin_amdgcn_permlane32_swap(__float_as_uint(bs[r]), __float_as_uint(bs[r]), false, false); bs[r] = __uint_as_float(sw[0]) + __uint_as_float(sw[1]); }
                        STF4(L1_BPART + w * 1024 + lo16, bs);
                    }
                    if (G == 9 && st == 1 && n == 0) fdv = bload16<SC1>(rs, lo16, WS_FD);
                    if (n == 3) {
                        if (s == 0) dtask_frag<0>(dout, rs, l);
                        if (s == 1) dtask_frag<1>(dout, rs, l);
                        if (s == 2) dtask_frag<2>(dout, rs, l);
                        if (s == 3) dtask_frag<3>(dout, rs, l);
                        if (s == 4) dtask_frag<4>(dout, rs, l);
                        if (s == 5) dtask_frag<5>(dout, rs, l);
                        if (s == 6) dtask_frag<6>(dout, rs, l);
                        if (s == 7) dtask_frag<7>(dout, rs, l);
                    }
                    SB();
                }
            }
            if (G == 0) WAITV(11); else if (G <= 3) WAITV(17); else if (G == 4) WAITV(11); else if (G == 5) WAITV(14); else if (G == 6) WAITV(9); else if (G == 7) WAITV(3);
            BAR();
            if (G == 4) flag_put(rs, WS_FD + b * 4, tag, tid == 0);
        }
        STAMP(4);
        constexpr int L1_BIAS = 6144, L1_HP = 0, L1_EX = 8192, L1_VT = 6656;
        if (w == 0) {
            check_all_blocks(ws, rs, WS_FD, fdv, tag);
            if (b == 0 && l == 0) flag_store(ws, WS_EPOCH, tag);
            if (l < 32) STF4(L1_BIAS + l * 16, (LDF4(L1_BPART + l * 16) + LDF4(L1_BPART + 1024 + l * 16)) + (LDF4(L1_BPART + 2048 + l * 16) + LDF4(L1_BPART + 3072 + l * 16)));
        }
        if (w >= 2) {
#pragma unroll
            for (int i = 0; i < 3; ++i) STF4(L1_HP + ((w & 1) * 3 + i) * 1024 + lo16, hacc[i]);
        }
        BAR();
        auto wo_dma = [&](int k) { dma16s(lds, L2_WO + w * 25600 + k * 1024, rs, lo16, wob + w * 25600 + k * 1024); };
        const rsrc_t rx = mk_rsrc(p.x, 2048 * 1280);
        unsigned xvo[3];
#pragma unroll
        for (int k = 0; k < 3; ++k) { const int n = (w * 3 + k) * 64 + l, row = n / 10; xvo[k] = n < 640 ? row * 1280 + (n - 10 * row) * 16 : 0x7ff00000u; }
        const unsigned xlo = L2_X + w * 3072;
        auto x_dma2 = [&](int k) { dma<0>(lds, xlo + k * 1024, rx, xvo[k], P2 * 64 * 1280 + c2 * 160); };
        f32x4 bq[5]; float bv[3];
#pragma unroll
        for (int n = 0; n < 5; ++n) bq[n] = LDF4(L1_BIAS + (n * 16 + 4 * qd) * 4);
#pragma unroll
        for (int n = 0; n < 3; ++n) bv[n] = LDF(L1_BIAS + ((5 + n) * 16 + t) * 4);
        TileKV kv[2]; u32x2 qa[2][2], xq[2];
        auto bg_wo = [&](int k) { SB(); wo_dma(k); SB(); };
#pragma unroll
        for (int tt = 1; tt >= 0; --tt) {
            kv[tt].ka0 = pack4(acc[tt][2] + bq[2]); kv[tt].ka1 = pack4(acc[tt][3] + bq[3]);
            bg_wo(4 * (1 - tt));
            xq[tt] = pack4(acc[tt][4] + bq[4]);
            kv[tt].k2[0] = swap32(xq[tt][0]); kv[tt].k2[1] = swap32(xq[tt][1]);
            bg_wo(4 * (1 - tt) + 1);
            kv[tt].va0 = pack4(acc[tt][5] + bv[0]); kv[tt].va1 = pack4(acc[tt][6] + bv[1]);
            bg_wo(4 * (1 - tt) + 2);
            kv[tt].z = pack4(acc[tt][7] + bv[2]);
            if (tt == 1) {
                const unsigned e = L1_EX + (w + 1) * 3072 + lo16;
                ST16(e, ((u32x4){kv[1].ka0[0], kv[1].ka0[1], kv[1].ka1[0], kv[1].ka1[1]}));
                ST16(e + 1024, ((u32x4){kv[1].k2[0], kv[1].k2[1], kv[1].va0[0], kv[1].va0[1]}));
                ST16(e + 2048, ((u32x4){kv[1].va1[0], kv[1].va1[1], kv[1].z[0], kv[1].z[1]}));
            }
            qa[tt][0] = pack4(acc[tt][0] + bq[0]); qa[tt][1] = pack4(acc[tt][1] + bq[1]);
            bg_wo(4 * (1 - tt) + 3);
        }
        if (w == 0) {
            const u32x2 k0 = pack4(hacc[0] + LDF4(L1_HP + lo16) + bq[2]), k1 = pack4(hacc[1] + LDF4(L1_HP + 1024 + lo16) + bq[3]);
            const u32x2 xx = pack4(hacc[2] + LDF4(L1_HP + 2048 + lo16) + bq[4]);
            ST16(L1_EX + lo16, ((u32x4){k0[0], k0[1], k1[0], k1[1]}));
            ST8(L1_EX + 1024 + lo16, ((u32x2){swap32(xx[0]), swap32(xx[1])}));
        } else if (w == 1) {
#pragma unroll
            for (int i = 0; i < 3; ++i) {
                const f32x4 v = hacc[i] + LDF4(L1_HP + (3 + i) * 1024 + lo16) + LDF4(L1_BIAS + ((5 + i) * 16 + 4 * qd) * 4);
                const u32x2 pk = pack4(v);
                LAS u16* d = (LAS u16*)((LAS char*)lds + L1_VT + i * 512 + (4 * qd) * 32 + t * 2);
                d[0] = (u16)pk[0]; d[16] = (u16)(pk[0] >> 16); d[32] = (u16)pk[1]; d[48] = (u16)(pk[1] >> 16);
            }
        }
        const unsigned ob = WS_OF + T0 * 20480;
        const AttnMask am = attn_mask(t, qd);
        u32x4 om; u32x2 ol;
        SB();
        attn_unit(qa[1], xq[1], kv[1], kv[0], true, am, qd, om, ol, [&](int k) { bg_wo(8 + k); });
        SB();
        bstore16<SC1>(om, rs, lo16, ob + 20480 + j1 * 1024);
        bstore8<SC1>(ol, rs, qd < 2 ? ((j1 & 3) * 16 + t) * 16 + 8 * qd : 0x7ff00000u, ob + 20480 + (16 + (j1 >> 2)) * 1024);
        BAR();
        TileKV pv;
        {
            const unsigned e = L1_EX + w * 3072 + lo16;
            const u32x4 a = LD16(e), bb = LD16(e + 1024), cc = LD16(e + 2048);
            pv.ka0 = (u32x2){a[0], a[1]}; pv.ka1 = (u32x2){a[2], a[3]}; pv.k2 = (u32x2){bb[0], bb[1]}; pv.va0 = (u32x2){bb[2], bb[3]};
            pv.va1 = (u32x2){cc[0], cc[1]}; pv.z = (u32x2){cc[2], cc[3]};
            if (w == 0) { pv.va0 = LD8(L1_VT + t * 32 + qd * 8); pv.va1 = LD8(L1_VT + 512 + t * 32 + qd * 8); pv.z = LD8(L1_VT + 1024 + t * 32 + qd * 8); }
        }
        SB();
        attn_unit(qa[0], xq[0], kv[0], pv, !(g1 == 0 && w == 0), am, qd, om, ol, [&](int k) { if (k < 7) bg_wo(18 + k); else { SB(); x_dma2(k - 7); SB(); } });
        SB();
        bstore16<SC1>(om, rs, lo16, ob + j1 * 1024);
        bstore8<SC1>(ol, rs, qd < 2 ? ((j1 & 3) * 16 + t) * 16 + 8 * qd : 0x7ff00000u, ob + (16 + (j1 >> 2)) * 1024);
        STAMP(5);
        publish(rs, WS_F1 + (g1 * 16 + j1) * FS, tag);
    }
    STAMP(6);

    u32x2 x1b[5], b1v[7];
    u32x4 vf[5];
    {
        LANE_IDS();
        const int T = 4 * P2 + w;
        constexpr int L2_MV = 57344;
        constexpr int L2_TB = 58304, L2_B1 = 60864;
        const u32x4 mvl = bload16<SC1>(rs, (1280 + ((tid / 20) % 3) * 640 + 80 * c2 + (tid % 20) * 4) * 4, WS_MODV);
        if (w < 3) { if (w < 2 || l < 32) dma16s(lds, L2_TB + w * 1024, rs, lo16, WS_MODB + 1920 * 2 + w * 1024); }
        else if (l < 14) dma<0>(lds, L2_B1, mk_rsrc(p.b1, 1728 * 2), lo16, t3 * 32);
#pragma unroll
        for (int f = 0; f < 12; ++f) w2_ld(f, lo16);
        SB();
        if (tid < 60) ST16(L2_MV + tid * 16, mvl);
        WAITV(12);
        BAR();
        u32x2 xv[5], va2[5], vb2[5];
#pragma unroll
        for (int i = 0; i < 5; ++i) {
            const int sr = wrap20(5 * w + i + rot3);
            const int ta = sr < 16 ? 5 * (sr >> 1) + 2 * (sr & 1) : 10 * (sr - 16) + 4, tb = ta + (sr < 16 ? 1 : 5);
            const unsigned sec = L2_TB + (t == 0 ? 1280 : 0) + qd * 8;
            va2[i] = LD8(sec + ta * 32); vb2[i] = LD8(sec + tb * 32);
        }
#pragma unroll
        for (int n = 0; n < 7; ++n) b1v[n] = LD8(L2_B1 + n * 32 + qd * 8);
#pragma unroll
        for (int n = 0; n < 5; ++n) xv[n] = LD8(L2_X + (16 * w + t) * 160 + n * 32 + qd * 8);
        SB();
#pragma unroll
        for (int i = 0; i < 5; ++i) {
            const unsigned cst = t == 2 ? 0x3f803f80u : 0u;
            vf[i] = (u32x4){t < 2 ? va2[i][0] : cst, t < 2 ? va2[i][1] : cst, t < 2 ? vb2[i][0] : cst, t < 2 ? vb2[i][1] : cst};
        }
        asm volatile("" : "+v"(vf[0]), "+v"(vf[1]), "+v"(vf[2]), "+v"(vf[3]), "+v"(vf[4]));
        asm volatile("" : "+v"(b1v[0]), "+v"(b1v[1]), "+v"(b1v[2]), "+v"(b1v[3]), "+v"(b1v[4]), "+v"(b1v[5]), "+v"(b1v[6]), "+v"(xv[0]), "+v"(xv[1]), "+v"(xv[2]), "+v"(xv[3]), "+v"(xv[4]));
        if (w == 0) {
            unsigned off[1] = {WS_F1 + ((P2 >> 1) * 16 + (l & 15)) * FS};
            poll_words<1>(ws, off, l < 16, tag);
        }
        BAR();
        STAMP(7);
        constexpr int PW = 8;
        u32x4 tk[20];
#pragma unroll
        for (int s = 0; s < PW; ++s) tk[s] = bload16<SC1>(rs, lo16, WS_OF + (T * 20 + wrap20(s + rot2)) * 1024);
        f32x4 acc[5];
#pragma unroll
        for (int n = 0; n < 5; ++n) acc[n] = (f32x4){0, 0, 0, 0};
        unsigned wb0 = L2_WO + lo16, wb1 = L2_WO + 51200 + lo16; OPAQUE(wb0); OPAQUE(wb1);
        auto wo_rd = [&](int s, int n) { return s < 10 ? LD16(wb0 + (s * 5 + n) * 1024) : LD16(wb1 + ((s - 10) * 5 + n) * 1024); };
        u32x4 wf[3][5];
#pragma unroll
        for (int n = 0; n < 5; ++n) { wf[0][n] = wo_rd(0, n); wf[1][n] = wo_rd(1, n); }
        SB();
        LSTAMP(40);
#pragma unroll
        for (int s = 0; s < 20; ++s) {
            if (s + PW < 20) tk[s + PW] = bload16<SC1>(rs, lo16, WS_OF + (T * 20 + wrap20(s + PW + rot2)) * 1024);
            if (s + 2 < 20) {
#pragma unroll
                for (int n = 0; n < 5; ++n) wf[(s + 2) % 3][n] = wo_rd(s + 2, n);
            }
#pragma unroll
            for (int n = 0; n < 5; ++n) acc[n] = mfma(wf[s % 3][n], tk[s], acc[n]);
#pragma unroll
            for (int n = 0; n < 5; ++n) { if (s + 2 < 20) SGB(0x100, 1); SGB(0x8, 1); if (n == 1 && s + PW < 20) SGB(0x20, 1); }
            SB();
            if ((s & 3) == 3 || s == 0) LSTEP(41 + (s + 1) / 4);
        }
        STAMP(8);
        BAR();
        const unsigned w1src = WS_W1F + t3 * 20480, w1last = (cnt3 == 7 ? 6 : 0) * 20480;
        auto w1_dma = [&](int base_step, int i) {
            const int n = i % 7;
            dma16s(lds, base_step * 7168 + i * 1024, rs, lo16, w1src + (n == 6 ? w1last : n * 20480) + wrap20(base_step + i / 7 + rot3) * 1024);
        };
        float st[6] = {0, 0, 0, 0, 0, 0};
        u32x2 ub[5];
        const unsigned ufb = WS_UF + T * 20480;
#pragma unroll
        for (int n = 0; n < 5; ++n) {
            const f32x4 ga_n = LDF4(L2_MV + (16 * n + 4 * qd) * 4), s2_n = LDF4(L2_MV + 320 + (16 * n + 4 * qd) * 4), g2_n = LDF4(L2_MV + 640 + (16 * n + 4 * qd) * 4);
            float x1[4] = {lo_f(xv[n][0]), hi_f(xv[n][0]), lo_f(xv[n][1]), hi_f(xv[n][1])};
#pragma unroll
            for (int r = 0; r < 4; ++r) x1[r] = fmaf(ga_n[r], acc[n][r], x1[r]);
            x1b[n] = (u32x2){pack2(x1[0], x1[1]), pack2(x1[2], x1[3])};
            const float xr[4] = {lo_f(x1b[n][0]), hi_f(x1b[n][0]), lo_f(x1b[n][1]), hi_f(x1b[n][1])};
            ub[n] = (u32x2){pack2(xr[0] * g2_n[0], xr[1] * g2_n[1]), pack2(xr[2] * g2_n[2], xr[3] * g2_n[3])};
            const float ur[4] = {lo_f(ub[n][0]), hi_f(ub[n][0]), lo_f(ub[n][1]), hi_f(ub[n][1])};
#pragma unroll
            for (int r = 0; r < 4; ++r) {
                st[0] += xr[r]; st[1] = fmaf(xr[r], xr[r], st[1]); st[2] += ur[r]; st[3] = fmaf(ur[r], g2_n[r], st[3]);
                st[4] = fmaf(ur[r], ur[r], st[4]); st[5] = fmaf(ur[r], s2_n[r], st[5]);
            }
            if (n == 1) bstore16<SC1>((u32x4){ub[0][0], ub[0][1], ub[1][0], ub[1][1]}, rs, lo16, ufb + (2 * c2) * 1024);
            if (n == 3) bstore16<SC1>((u32x4){ub[2][0], ub[2][1], ub[3][0], ub[3][1]}, rs, lo16, ufb + (2 * c2 + 1) * 1024);
            if (n == 4) bstore8<SC1>(ub[4], rs, lo16 + 8 * (c2 & 1), ufb + (16 + (c2 >> 1)) * 1024);
#pragma unroll
            for (int k = 0; k < 3; ++k) if (3 * n + k < 14) w1_dma(2 * w, 3 * n + k);
            SGB(0x20, 1); SGB(0x1, 18); SGB(0x20, 1); SGB(0x1, 18); SGB(0x20, 1); SGB(0x1, 24);
            SB();
        }
#pragma unroll
        for (int i = 0; i < 6; ++i) st[i] = red_add_q(st[i]);
        if (qd < 2) bstore16<SC1>(asu4(qd == 0 ? (f32x4){st[0], st[1], st[2], st[3]} : (f32x4){st[4], st[5], 0.f, 0.f}), rs, qd * 256 + t * 16, WS_ST + (T * 8 + c2) * 512);
#pragma unroll
        for (int n = 0; n < 5; ++n) asm volatile("" : "+v"(x1b[n]));
        STAMP(9);
        publish(rs, WS_F2 + (P2 * 8 + c2) * FS, tag);
#pragma unroll
        for (int i = 0; i < 21; ++i) w1_dma(8 + 3 * w, i);
    }
    STAMP(10);

    {
        LANE_IDS();
        constexpr int L3_GSC = 143360;
        const int T0 = 8 * g3 + 2 * w;
        if (w == 0) {
            unsigned off[1] = {WS_F2 + ((2 * g3 + ((l >> 3) & 1)) * 8 + (l & 7)) * FS};
            poll_words<1>(ws, off, l < 16, tag);
        }
        DRAIN();
        BAR();
        STAMP(11);
        constexpr int PW = 4;
        u32x4 tk[20][2];
        u32x4 spa[2][2]; u32x2 spb[2][2];
        float al[2], be[2], gm[2], de[2], ts[2][6];
        f32x4 red4[4], gr0, gr1;
#pragma unroll
        for (int s = 0; s < PW; ++s) { const unsigned o = WS_UF + (T0 * 20 + wrap20(s + rot3)) * 1024; tk[s][0] = bload16<SC1>(rs, lo16, o); tk[s][1] = bload16<SC1>(rs, lo16, o + 20480); }
        SB();
        LSTAMP(62);
        {
            f32x4 va[7], gram = {0, 0, 0, 0};
            u32x4 wv[2][7];
            unsigned vb = 5 * w * 7168 + lo16; OPAQUE(vb);
#pragma unroll
            for (int n = 0; n < 7; ++n) { va[n] = (f32x4){0, 0, 0, 0}; wv[0][n] = LD16(vb + n * 1024); }
            SB();
#pragma unroll
            for (int i = 0; i < 5; ++i) {
                if (i + 1 < 5) {
#pragma unroll
                    for (int n = 0; n < 7; ++n) wv[(i + 1) & 1][n] = LD16(vb + ((i + 1) * 7 + n) * 1024);
                }
#pragma unroll
                for (int n = 0; n < 7; ++n) va[n] = mfma(wv[i & 1][n], vf[i], va[n]);
                gram = mfma(vf[i], vf[i], gram);
#pragma unroll
                for (int n = 0; n < 7; ++n) { if (i + 1 < 5) SGB(0x100, 1); SGB(0x8, 1); }
                SB();
            }
            if (t < 3) {
#pragma unroll
                for (int n = 0; n < 7; ++n) STF4(L3_GSC + ((w * 8 + n) * 3 + t) * 64 + qd * 16, va[n]);
                if (qd == 0) STF4(L3_GSC + ((w * 8 + 7) * 3 + t) * 64, gram);
            }
        }
        SB();
        f32x4 acc[2][7];
#pragma unroll
        for (int n = 0; n < 7; ++n) { acc[0][n] = (f32x4){0, 0, 0, 0}; acc[1][n] = (f32x4){0, 0, 0, 0}; }
        unsigned wb0 = lo16, wb1 = 57344 + lo16, wb2 = 114688 + lo16; OPAQUE(wb0); OPAQUE(wb1); OPAQUE(wb2);
        auto w1_rd = [&](int s, int n) { return s < 8 ? LD16(wb0 + (s * 7 + n) * 1024) : s < 16 ? LD16(wb1 + ((s - 8) * 7 + n) * 1024) : LD16(wb2 + ((s - 16) * 7 + n) * 1024); };
        u32x4 wf[2][7];
#pragma unroll
        for (int n = 0; n < 7; ++n) wf[0][n] = w1_rd(0, n);
        SB();
        LSTAMP(0);
#pragma unroll
        for (int s = 0; s < 20; ++s) {
            int nv = 0;
            if (s + PW < 20) { const unsigned o = WS_UF + (T0 * 20 + wrap20(s + PW + rot3)) * 1024; tk[s + PW][0] = bload16<SC1>(rs, lo16, o); tk[s + PW][1] = bload16<SC1>(rs, lo16, o + 20480); nv += 2; }
            if (s < 16) { w2_ld(12 + s, lo16); nv += 1; }
            if (s >= 16) {
#pragma unroll
                for (int k = 0; k < 3; ++k) w2_ld(28 + (s - 16) * 3 + k, lo16);
                nv += 3;
            }
            if (s == 10 || s == 11) {
#pragma unroll
                for (int e = 0; e < 2; ++e) {
                    spa[s - 10][e] = bload16<SC1>(rs, (2 * qd + e) * 512 + t * 16, WS_ST + (T0 + s - 10) * 4096);
                    spb[s - 10][e] = bload8<SC1>(rs, (2 * qd + e) * 512 + 256 + t * 16, WS_ST + (T0 + s - 10) * 4096);
                }
                nv += 4;
            }
            int nu = 0;
            if (s == 3) {
                const int r96 = tid < 96 ? tid : 0;
#pragma unroll
                for (int u = 0; u < 4; ++u) red4[u] = LDF4(L3_GSC + (u * 96 + r96) * 16);
            }
            if (s == 4) { STF4(L3_GSC + 6144 + (tid < 96 ? tid : 96 + (tid & 31)) * 16, (red4[0] + red4[1]) + (red4[2] + red4[3])); nu = 1; }
            if (s == 9) { gr0 = LDF4(L3_GSC + 6144 + 84 * 16); gr1 = LDF4(L3_GSC + 6144 + 88 * 16); }
            if (s == 13 || s == 14) {
                const int tt = s - 13;
                const f32x4 a0 = asf4(spa[tt][0]) + asf4(spa[tt][1]);
                const float b00 = __uint_as_float(spb[tt][0][0]) + __uint_as_float(spb[tt][1][0]), b01 = __uint_as_float(spb[tt][0][1]) + __uint_as_float(spb[tt][1][1]);
                ts[tt][0] = red_add_q(a0[0]); ts[tt][1] = red_add_q(a0[1]); ts[tt][2] = red_add_q(a0[2]); ts[tt][3] = red_add_q(a0[3]); ts[tt][4] = red_add_q(b00); ts[tt][5] = red_add_q(b01);
                nu = 4;
            }
            if (s == 15) {
                const float Gg = gr0[0], Ggs = gr0[1], Gs = gr0[2], Sss = gr1[1], Ss = gr1[2], iD = 1.f / 640.f;
#pragma unroll
                for (int tt = 0; tt < 2; ++tt) {
                    const float m1 = ts[tt][0] * iD, v1 = fmaxf(ts[tt][1] * iD - m1 * m1, 0.f), r1v = __builtin_amdgcn_rsqf(v1 + 1e-5f);
                    const float m2 = (r1v * (ts[tt][2] - m1 * Gs) + Ss) * iD;
                    const float e2 = (r1v * r1v * (ts[tt][4] - 2.f * m1 * ts[tt][3] + m1 * m1 * Gg) + 2.f * r1v * (ts[tt][5] - m1 * Ggs) + Sss) * iD;
                    const float r2v = __builtin_amdgcn_rsqf(fmaxf(e2 - m2 * m2, 0.f) + 1e-5f);
                    al[tt] = r1v * r2v; be[tt] = -r1v * r2v * m1; gm[tt] = r2v; de[tt] = -r2v * m2;
                }
                nu = 4;
            }
            if (s + 1 < 20) {
#pragma unroll
                for (int n = 0; n < 7; ++n) wf[(s + 1) & 1][n] = w1_rd(s + 1, n);
            }
#pragma unroll
            for (int n = 0; n < 7; ++n) { acc[0][n] = mfma(wf[s & 1][n], tk[s][0], acc[0][n]); acc[1][n] = mfma(wf[s & 1][n], tk[s][1], acc[1][n]); }
#pragma unroll
            for (int n = 0; n < 7; ++n) {
                if (s + 1 < 20) SGB(0x100, 1);
                SGB(0x8, 1);
                if (nu == 1) SGB(0x402, 1);
                if (nu == 4) SGB(0x402, 4);
                SGB(0x8, 1);
                if (nu == 1) SGB(0x402, 1);
                if (nu == 4) SGB(0x402, 4);
                if (n < nv) SGB(0x20, 1);
            }
            SB();
            if (s == 2 || s == 8) BAR();
            LSTEP(1 + s);
        }
        STAMP(12);
        u32x2 hb[2][7];
        unsigned gsb = L3_GSC + 6144 + qd * 16; OPAQUE(gsb);
        f32x4 gsc[2][3];
#pragma unroll
        for (int k = 0; k < 3; ++k) gsc[0][k] = LDF4(gsb + 64 * k);
#pragma unroll
        for (int n = 0; n < 7; ++n) {
#pragma unroll
            for (int k = 0; k < 2; ++k) w2_ld(40 + n * 2 + k, lo16);
            if (n + 1 < 7) {
#pragma unroll
                for (int k = 0; k < 3; ++k) gsc[(n + 1) & 1][k] = LDF4(gsb + (n + 1) * 192 + 64 * k);
            }
            const f32x4 G = gsc[n & 1][0], Sv = gsc[n & 1][1], C = gsc[n & 1][2];
            const float bb[4] = {lo_f(b1v[n][0]), hi_f(b1v[n][0]), lo_f(b1v[n][1]), hi_f(b1v[n][1])};
#pragma unroll
            for (int tt = 0; tt < 2; ++tt) {
                float hv[4];
#pragma unroll
                for (int r = 0; r < 4; ++r) {
                    float z = fmaf(gm[tt], Sv[r], bb[r]); z = fmaf(be[tt], G[r], z); z = fmaf(de[tt], C[r], z); z = fmaf(al[tt], acc[tt][n][r], z);
                    const float u = z * fmaf(z * z, -0.044715f * 1.5957691216057308f * 1.4426950408889634f, -1.5957691216057308f * 1.4426950408889634f);
                    hv[r] = z * __builtin_amdgcn_rcpf(1.f + __builtin_amdgcn_exp2f(u));
                }
                hb[tt][n] = (u32x2){pack2(hv[0], hv[1]), pack2(hv[2], hv[3])};
            }
            SGB(0x20, 1); SGB(0x1, 40); SGB(0x20, 1); SGB(0x1, 60);
            SB();
#pragma unroll
            for (int tt = 0; tt < 2; ++tt) {
                const unsigned hbase = WS_HF + (T0 + tt) * 54 * 1024;
                if (n & 1) bstore16<SC1>((u32x4){hb[tt][n - 1][0], hb[tt][n - 1][1], hb[tt][n][0], hb[tt][n][1]}, rs, lo16, hbase + (3 * j3 + (n >> 1)) * 1024);
                if (n == 6 && j3 < 12) bstore8<SC1>(hb[tt][6], rs, lo16 + 8 * (j3 & 1), hbase + (48 + (j3 >> 1)) * 1024);
            }
            SB();
        }
        STAMP(13);
        publish(rs, WS_F3 + (g3 * 16 + j3) * FS, tag);
    }
    STAMP(14);

    {
        LANE_IDS();
        f32x4 gb[5]; u32x2 b2v[5];
#pragma unroll
        for (int f = 54; f < 70; ++f) w2_ld(f, lo16);
        SB();
        if (w == 0) {
            unsigned off[1] = {WS_F3 + ((P2 >> 1) * 16 + (l & 15)) * FS};
            poll_words<1>(ws, off, l < 16, tag);
        }
        BAR();
        STAMP(15);
        constexpr int PD = 3;
        u32x4 tk[14][4];
        const unsigned tokb = __builtin_amdgcn_readfirstlane(WS_HF + (4 * P2 * 54 + s4) * 1024);
        auto issue = [&](int i) {
            const int s = step4(i);
#pragma unroll
            for (int a = 0; a < 4; ++a) tk[i][a] = bload16<SC1>(rs, lo16, tokb + (((a + w) & 3) * 54 + s) * 1024);
        };
#pragma unroll
        for (int i = 0; i < PD; ++i) issue(i);
        f32x4 acc[4][5];
#pragma unroll
        for (int a = 0; a < 4; ++a)
#pragma unroll
            for (int n = 0; n < 5; ++n) acc[a][n] = (f32x4){0, 0, 0, 0};
        SB();
        LSTAMP(24);
#pragma unroll
        for (int i = 0; i < 14; ++i) {
            if (i < 13 || w < 2) {
                if (i + PD < 14) issue(i + PD);
                if (i == 11) {
#pragma unroll
                    for (int n = 0; n < 5; ++n) {
                        const int f = 80 * c2 + 16 * n;
                        gb[n] = asf4(bload16<SC1>(rs, qd * 16, WS_MODV + (3200 + f) * 4));
                        b2v[n] = *(const GAS u32x2*)(p.b2 + f + 4 * qd);
                    }
                }
#pragma unroll
                for (int a = 0; a < 4; ++a)
#pragma unroll
                    for (int n = 0; n < 5; ++n) acc[a][n] = mfma(w2f[i][n], tk[i][a], acc[a][n]);
#pragma unroll
                for (int a = 0; a < 4; ++a) { SGB(0x8, 2); if (i == 11) SGB(0x20, 3); else SGB(0x20, 1); SGB(0x8, 3); }
            }
            SB();
            LSTEP(25 + i);
        }
        STAMP(16);
#pragma unroll
        for (int a = 1; a < 4; ++a) {
            unsigned d = ((((a + w) & 3) * 3 + (3 - a)) * 5) * 1024 + lo16; OPAQUE(d);
#pragma unroll
            for (int n = 0; n < 5; ++n) STF4(d + n * 1024, acc[a][n]);
        }
        BAR();
        constexpr int L4_OUT = 61440;
        unsigned rb = w * 15360 + lo16; OPAQUE(rb);
#pragma unroll
        for (int n = 0; n < 5; ++n) {
            f32x4 v = acc[0][n];
#pragma unroll
            for (int u = 0; u < 3; ++u) v += LDF4(rb + (u * 5 + n) * 1024);
            const float x1[4] = {lo_f(x1b[n][0]), hi_f(x1b[n][0]), lo_f(x1b[n][1]), hi_f(x1b[n][1])};
            const float bv[4] = {lo_f(b2v[n][0]), hi_f(b2v[n][0]), lo_f(b2v[n][1]), hi_f(b2v[n][1])};
            float o[4];
#pragma unroll
            for (int r = 0; r < 4; ++r) o[r] = fmaf(gb[n][r], v[r] + bv[r], x1[r]);
            ST8(L4_OUT + (16 * w + t) * 176 + n * 32 + qd * 8, ((u32x2){pack2(o[0], o[1]), pack2(o[2], o[3])}));
        }
        asm volatile("s_waitcnt lgkmcnt(0)" ::: "memory");
        const rsrc_t ro = mk_rsrc(p.out, 2048 * 1280);
#pragma unroll
        for (int k = 0; k < 3; ++k) {
            const int ch = l + 64 * k;
            if (ch < 160) {
                const int row = ch / 10, cc = ch % 10;
                const u32x4 v = LD16(L4_OUT + (16 * w + row) * 176 + cc * 16);
                bstore16<OUT_AUX>(v, ro, row * 1280 + cc * 16, (16 * (4 * P2 + w)) * 1280 + c2 * 160);
            }
        }
    }
    STAMP(17);
    LSTAMP_DUMP;
    asm volatile(".Ladaln_end:" ::: "memory");
}

extern "C" void kernel_launch(void* const* d_in, const int* in_sizes, int n_in, void* d_out, int out_size, void* d_ws, size_t ws_size, hipStream_t stream) {
    Params p;
    p.x = (const u16*)d_in[0]; p.wqkv = (const u16*)d_in[1]; p.wo = (const u16*)d_in[2]; p.w1 = (const u16*)d_in[4]; p.b1 = (const u16*)d_in[5];
    p.w2 = (const u16*)d_in[6]; p.b2 = (const u16*)d_in[7]; p.c = (const u16*)d_in[8]; p.wm = (const u16*)d_in[9];
    p.out = (u16*)d_out; p.ws = (char*)d_ws;
    (void)hipFuncSetAttribute((const void*)adaLN_block_kernel, hipFuncAttributeMaxDynamicSharedMemorySize, LDS_BYTES);
    adaLN_block_kernel<<<256, 256, LDS_BYTES, stream>>>(p);
}
```

```cpp
#include <hip/hip_runtime.h>
#include <stdint.h>

typedef unsigned short u16;
typedef float f32x4 __attribute__((ext_vector_type(4)));
typedef float f32x2 __attribute__((ext_vector_type(2)));
typedef __bf16 bf16x8 __attribute__((ext_vector_type(8)));
typedef __bf16 bf16x2 __attribute__((ext_vector_type(2)));
typedef unsigned u32x4 __attribute__((ext_vector_type(4)));
typedef unsigned u32x2 __attribute__((ext_vector_type(2)));
typedef short s16x4 __attribute__((ext_vector_type(4)));
typedef __amdgpu_buffer_rsrc_t rsrc_t;
#define LAS __attribute__((address_space(3)))
#define GAS __attribute__((address_space(1)))
#define DI __device__ __forceinline__

static constexpr unsigned FS = 64;
static constexpr unsigned WS_EPOCH = 0;
static constexpr unsigned WS_TMO   = 256;
static constexpr unsigned WS_FA    = 1024;
static constexpr unsigned WS_FC    = WS_FA + 160 * FS;
static constexpr unsigned WS_FD    = WS_FC + 128 * FS;
static constexpr unsigned WS_F1    = WS_FD + 1024;
static constexpr unsigned WS_F2    = WS_F1 + 256 * FS;
static constexpr unsigned WS_F3    = WS_F2 + 256 * FS;
static constexpr unsigned WS_FEND  = WS_F3 + 256 * FS;
static constexpr unsigned WS_MODV = 131072;
static constexpr unsigned WS_MODB = WS_MODV + 16384;
static constexpr unsigned WS_BP   = WS_MODB + 8192;
static constexpr unsigned WS_XNF  = WS_BP + 20 * 16 * 128 * 4;
static constexpr unsigned WS_WQF  = WS_XNF + 128 * 20 * 1024;
static constexpr unsigned WS_WOF  = WS_WQF + 16 * 20 * 8 * 1024;
static constexpr unsigned WS_W1F  = WS_WOF + 8 * 20 * 5 * 1024;
static constexpr unsigned WS_W2F  = WS_W1F + 108 * 20 * 1024;
static constexpr unsigned WS_OF   = WS_W2F + 8 * 54 * 5 * 1024;
static constexpr unsigned WS_UF   = WS_OF + 128 * 20 * 1024;
static constexpr unsigned WS_ST   = WS_UF + 128 * 20 * 1024;
static constexpr unsigned WS_HF   = WS_ST + 128 * 8 * 512;
static constexpr unsigned WS_END  = WS_HF + 128 * 54 * 1024;
static_assert(WS_FEND <= WS_MODV, "flags overflow");

#define B_DELAY 0
#define OUT_AUX 0
#define ROT1 0
#define ROT2 0
#define ROT3 0
#define ROT4 0
static constexpr int LDS_BYTES = 163840;
static constexpr int SC1 = 16;
static constexpr unsigned SPIN_CAP = 40000;

#define STAMP(i)
#define LSTAMP(i)
#define LSTEP(i)
#define LSTAMP_DECL
#define LSTAMP_DUMP

struct Params {
    const u16* x; const u16* wqkv; const u16* wo; const u16* w1; const u16* b1; const u16* w2; const u16* b2; const u16* c; const u16* wm;
    u16* out; char* ws;
};

DI float lo_f(unsigned d) { return __uint_as_float(d << 16); }
DI float hi_f(unsigned d) { return __uint_as_float(d & 0xffff0000u); }
DI unsigned pack2(float a, float b) { f32x2 v = {a, b}; return __builtin_bit_cast(unsigned, __builtin_convertvector(v, bf16x2)); }
DI float rbf(float a) { return lo_f(pack2(a, 0.f)); }
DI u32x2 pack4(const f32x4& v) { return (u32x2){pack2(v[0], v[1]), pack2(v[2], v[3])}; }
DI f32x4 mfma(u32x4 a, u32x4 b, f32x4 c) { return __builtin_amdgcn_mfma_f32_16x16x32_bf16(__builtin_bit_cast(bf16x8, a), __builtin_bit_cast(bf16x8, b), c, 0, 0, 0); }
DI float red_add_q(float v) {
    auto a = __builtin_amdgcn_permlane16_swap(__float_as_uint(v), __float_as_uint(v), false, false);
    v = __uint_as_float(a[0]) + __uint_as_float(a[1]);
    auto b = __builtin_amdgcn_permlane32_swap(__float_as_uint(v), __float_as_uint(v), false, false);
    return __uint_as_float(b[0]) + __uint_as_float(b[1]);
}
DI float red_max_q(float v) {
    auto a = __builtin_amdgcn_permlane16_swap(__float_as_uint(v), __float_as_uint(v), false, false);
    v = fmaxf(__uint_as_float(a[0]), __uint_as_float(a[1]));
    auto b = __builtin_amdgcn_permlane32_swap(__float_as_uint(v), __float_as_uint(v), false, false);
    return fmaxf(__uint_as_float(b[0]), __uint_as_float(b[1]));
}
template <int C> DI float dpp_add(float v) { return v + __builtin_bit_cast(float, __builtin_amdgcn_update_dpp(0, __builtin_bit_cast(int, v), C, 0xf, 0xf, true)); }
DI float red16(float v) { v = dpp_add<0xB1>(v); v = dpp_add<0x4E>(v); v = dpp_add<0x141>(v); return dpp_add<0x140>(v); }
DI unsigned swap32(unsigned v) { auto a = __builtin_amdgcn_permlane32_swap(v, v, false, false); return (threadIdx.x & 32) ? a[0] : a[1]; }
DI rsrc_t mk_rsrc(const void* p, unsigned bytes) { return __builtin_amdgcn_make_buffer_rsrc((void*)p, 0, bytes, 0x00020000); }
template <int AUX> DI u32x4 bload16(rsrc_t r, unsigned voff, unsigned soff) { return __builtin_bit_cast(u32x4, __builtin_amdgcn_raw_buffer_load_b128(r, voff, soff, AUX)); }
template <int AUX> DI u32x2 bload8(rsrc_t r, unsigned voff, unsigned soff) { return __builtin_bit_cast(u32x2, __builtin_amdgcn_raw_buffer_load_b64(r, voff, soff, AUX)); }
template <int AUX> DI unsigned bload4(rsrc_t r, unsigned voff, unsigned soff) { return __builtin_amdgcn_raw_buffer_load_b32(r, voff, soff, AUX); }
template <int AUX> DI void bstore16(u32x4 v, rsrc_t r, unsigned voff, unsigned soff) { __builtin_amdgcn_raw_buffer_store_b128(v, r, voff, soff, AUX); }
template <int AUX> DI void bstore8(u32x2 v, rsrc_t r, unsigned voff, unsigned soff) { __builtin_amdgcn_raw_buffer_store_b64(v, r, voff, soff, AUX); }
template <int AUX> DI void bstore4(unsigned v, rsrc_t r, unsigned voff, unsigned soff) { __builtin_amdgcn_raw_buffer_store_b32(v, r, voff, soff, AUX); }
DI f32x4 asf4(u32x4 v) { return __builtin_bit_cast(f32x4, v); }
DI u32x4 asu4(f32x4 v) { return __builtin_bit_cast(u32x4, v); }
#define LANE_IDS() int tz_ = threadIdx.x; asm volatile("" : "+v"(tz_)); const int tid = tz_, l = tid & 63, t = l & 15, qd = l >> 4; const unsigned lo16 = l * 16; (void)t; (void)qd; (void)lo16
#define OPAQUE(x) asm volatile("" : "+v"(x))
#define SB() __builtin_amdgcn_sched_barrier(0)
#define BAR() asm volatile("s_waitcnt lgkmcnt(0)\n\ts_barrier" ::: "memory")
#define DRAIN() asm volatile("s_waitcnt vmcnt(0)" ::: "memory")
#define SGB(mask, n) __builtin_amdgcn_sched_group_barrier(mask, n, 0)
#define LD16(off) (*(const LAS u32x4*)((const LAS char*)lds + (off)))
#define LD8(off) (*(const LAS u32x2*)((const LAS char*)lds + (off)))
#define LDF4(off) (*(const LAS f32x4*)((const LAS char*)lds + (off)))
#define LDF(off) (*(const LAS float*)((const LAS char*)lds + (off)))
#define ST16(off, v) (*(LAS u32x4*)((LAS char*)lds + (off)) = (v))
#define ST8(off, v) (*(LAS u32x2*)((LAS char*)lds + (off)) = (v))
#define STF4(off, v) (*(LAS f32x4*)((LAS char*)lds + (off)) = (v))
#define STF(off, v) (*(LAS float*)((LAS char*)lds + (off)) = (v))
DI u32x2 tr_read(const char* lds, unsigned off) { return __builtin_bit_cast(u32x2, __builtin_amdgcn_ds_read_tr16_b64_v4i16((LAS s16x4*)((LAS char*)lds + off))); }

DI void flag_store(char* ws, unsigned off, unsigned tag) { __hip_atomic_store((GAS unsigned*)(ws + off), tag, __ATOMIC_RELAXED, __HIP_MEMORY_SCOPE_AGENT); }
DI unsigned flag_load(const char* ws, unsigned off) { return __hip_atomic_load((const GAS unsigned*)(ws + off), __ATOMIC_RELAXED, __HIP_MEMORY_SCOPE_AGENT); }
DI void flag_put(rsrc_t rs, unsigned off, unsigned tag, bool me) { bstore4<SC1>(tag, rs, me ? off : 0x7ff00000u, 0); }
DI void publish(rsrc_t rs, unsigned off, unsigned tag) { DRAIN(); BAR(); flag_put(rs, off, tag, threadIdx.x == 0); }
template <int N> DI void poll_words(char* ws, const unsigned (&off)[N], bool active, unsigned tag) {
#pragma nounroll
    for (unsigned spins = 0;; ++spins) {
        bool ok = true;
#pragma unroll
        for (int k = 0; k < N; ++k) ok &= flag_load(ws, off[k]) == tag;
        if (__all(ok || !active)) break;
        if (spins > SPIN_CAP) { if ((threadIdx.x & 63) == 0) flag_store(ws, WS_TMO, tag); break; }
        __builtin_amdgcn_s_sleep(1);
    }
}

DI void check_all_blocks(char* ws, rsrc_t rs, unsigned base, u32x4 v, unsigned tag) {
    asm volatile("" : "+v"(v));
#pragma nounroll
    for (unsigned spins = 0; !__all(v[0] == tag && v[1] == tag && v[2] == tag && v[3] == tag); ++spins) {
        if (spins > SPIN_CAP) { if ((threadIdx.x & 63) == 0) flag_store(ws, WS_TMO, tag); break; }
        __builtin_amdgcn_s_sleep(1);
        v = bload16<SC1>(rs, (threadIdx.x & 63) * 16, base);
        asm volatile("" : "+v"(v) :: "memory");
    }
}

DI void rows_wo(int s, int qd, int& ra, int& rb) { if (s < 16) { ra = 40 * s + 4 * qd; rb = ra + 20; } else { ra = 40 * (4 * (s - 16) + qd) + 16; rb = ra + 20; } }
DI void tiles_p3(int s, int& ta, int& tb) { if (s < 16) { ta = 5 * (s >> 1) + 2 * (s & 1); tb = ta + 1; } else { ta = 10 * (s - 16) + 4; tb = ta + 5; } }
DI void tiles_p4(int s, int& ta, int& tb) { if (s < 48) { int j = s / 3, pr = s % 3; int st = j < 12 ? 7 * j : 84 + 6 * (j - 12); ta = st + 2 * pr; tb = ta + 1; } else { int m = s - 48; ta = 14 * m + 6; tb = ta + 7; } }

static constexpr int L0_SC = 81920, L0_GS = 83200, L0_CRAW = 83968, L0_RW = 86016, L0_D = 101376, L0_XI = 134144, L0_XS = 156160;

template <int AUX> DI void dma(char* lds, unsigned lds_off, rsrc_t r, unsigned voff, unsigned soff) {
    __builtin_amdgcn_raw_ptr_buffer_load_lds(r, (LAS void*)((LAS char*)lds + lds_off), 16, voff, soff, 0, AUX);
}
DI void dma16(char* lds, unsigned lds_off, rsrc_t r, unsigned voff, unsigned soff) {
    const unsigned dst = (unsigned)(size_t)(LAS char*)lds + lds_off;
    asm volatile("s_mov_b32 m0, %0\n\ts_nop 0\n\tbuffer_load_dwordx4 %1, %2, %3 offen lds" :: "s"(dst), "v"(voff), "s"(r), "s"(soff) : "memory");
}
DI void dma16s(char* lds, unsigned lds_off, rsrc_t r, unsigned voff, unsigned soff) { dma<SC1>(lds, lds_off, r, voff, soff); }
#define WAITV(n) asm volatile("s_waitcnt vmcnt(%0)" :: "n"(n) : "memory")

struct DPlan { rsrc_t r; unsigned v0, v1, sa, sb, st; bool on; };
struct DOut { unsigned dst[8]; u32x4 f[8]; };
DI void dtask_plan(const Params& p, int id, int l, DPlan& d, DOut& o) {
    int ra0, rb0, rstep, cg, lim; unsigned pitch, d0, r5, stride, jump;
    if (id < 370) {
        const bool wo = id < 100; const int i2 = wo ? id : id - 100, s = i2 / 5; cg = i2 - 5 * s; pitch = 1280;
        if (wo) { d.r = mk_rsrc(p.wo, 640 * 1280); if (s < 16) { ra0 = 40 * s; rstep = 4; } else { ra0 = 160 * (s - 16) + 16; rstep = 40; } rb0 = ra0 + 20; }
        else { d.r = mk_rsrc(p.w2, 1728 * 1280); int ta, tb; tiles_p4(s, ta, tb); ra0 = 16 * ta; rb0 = 16 * tb; rstep = 4; }
        const int c0 = (8 * cg) / 5;
        r5 = 8 * cg - 5 * c0; d0 = (wo ? WS_WOF + (c0 * 20 + s) * 5120 : WS_W2F + (c0 * 54 + s) * 5120) + r5 * 1024;
        stride = 1024; jump = wo ? 19 * 5120 : 53 * 5120; lim = 8;
    } else {
        const int i2 = id < 650 ? id - 370 : 0, s = i2 / 14; cg = i2 - 14 * s; pitch = 3456; d.r = mk_rsrc(p.w1, 640 * 3456);
        int ta, tb; tiles_p3(s, ta, tb); ra0 = 16 * ta; rb0 = 16 * tb; rstep = 4;
        r5 = 0; d0 = WS_W1F + (8 * cg * 20 + s) * 1024; stride = 20480; jump = 0; lim = id < 650 ? (cg == 13 ? 4 : 8) : 0;
    }
    const int q = l >> 4, cp = l & 15;
    d.v0 = q * pitch + ((cp ^ (q << 1)) * 16); d.v1 = q * pitch + ((cp ^ ((4 + q) << 1)) * 16);
    d.sa = ra0 * pitch + cg * 256; d.sb = rb0 * pitch + cg * 256; d.st = rstep * pitch; d.on = lim > 0;
#pragma unroll
    for (int nt = 0; nt < 8; ++nt) {
        const unsigned v = d0 + nt * stride + ((r5 + nt >= 5 ? 1u : 0u) + (r5 + nt >= 10 ? 1u : 0u)) * jump;
        o.dst[nt] = __builtin_amdgcn_readfirstlane(nt < lim ? v : 0x7ff00000u);
    }
}
DI void dtask_go(const DPlan& d, char* lds, unsigned scr, int w) {
    if (d.on) {
#pragma unroll
        for (int i = 0; i < 8; ++i) dma16(lds, scr + w * 8192 + i * 1024, d.r, (i & 1) ? d.v1 : d.v0, (i < 4 ? d.sa : d.sb) + (i & 3) * d.st);
    }
}
DI void dtask_read(DOut& o, char* lds, unsigned scr, int w, int l) {
    const int qd = l >> 4, q = (l >> 2) & 3, pp = l & 3, r7 = 4 * (qd & 1) + q;
    const unsigned a0 = scr + w * 8192 + (4 * qd + q) * 256 + pp * 8;
#pragma unroll
    for (int nt = 0; nt < 8; ++nt) {
        const u32x2 ua = tr_read(lds, a0 + ((nt ^ r7) * 32)), ub = tr_read(lds, a0 + 4096 + ((nt ^ r7) * 32));
        o.f[nt] = (u32x4){ua[0], ua[1], ub[0], ub[1]};
    }
}
template <int NT> DI void dtask_frag(const DOut& o, rsrc_t rs, int l) { bstore16<SC1>(o.f[NT], rs, l * 16, o.dst[NT]); }

template <int I0, int I1> DI void wm_dma(const Params& p, char* lds, int w, int l, unsigned colA, unsigned colB, int rot) {
    const rsrc_t rw = mk_rsrc(p.wm, 640 * 3840 * 2);
    const int rl = l >> 3, cp = l & 7;
    const unsigned c = cp ^ ((((rl >> 1) & 1) | ((w & 1) << 1)) << 1);
    const unsigned voff = rl * 7680 + (c < 4 ? colA + 8 * c : colB + 8 * (c - 4)) * 2;
    int piece = 4 * I0 + w + rot; piece = piece >= 80 ? piece - 80 : piece;
#pragma unroll
    for (int i = I0; i < I1; ++i) {
        dma16(lds, piece * 1024, rw, voff, piece * 61440);
        piece = piece >= 76 ? piece - 76 : piece + 4;
    }
}
DI void c_dma(const Params& p, char* lds, int w, int l) {
    dma16(lds, L0_CRAW + (w & 1) * 1024, mk_rsrc(p.c, 1280), ((w & 1) * 64 + l) * 16, 0);
}
DI void silu_table(char* lds, int tid) {
    if (tid < 80) {
        const u32x4 creg = LD16(L0_CRAW + tid * 16);
        u32x4 o;
#pragma unroll
        for (int d = 0; d < 4; ++d) { const float a = lo_f(creg[d]), b = hi_f(creg[d]); o[d] = pack2(a * __builtin_amdgcn_rcpf(1.f + __expf(-a)), b * __builtin_amdgcn_rcpf(1.f + __expf(-b))); }
        ST16(L0_SC + tid * 16, o);
    }
}
DI float mod_gemv(char* lds, int w, int l) {
    const int qd = l >> 4, q = (l >> 2) & 3, pp = l & 3;
    f32x4 acc = {0, 0, 0, 0}, acc2 = {0, 0, 0, 0};
    const int r0 = 8 * qd + q;
    const unsigned sg = ((r0 >> 1) & 1) | (((r0 >> 3) & 1) << 1);
    unsigned a0 = r0 * 128 + ((w ^ sg) * 32) + pp * 8, a1 = a0 + 40960, sb = L0_SC + qd * 16;
    OPAQUE(a0); OPAQUE(a1); OPAQUE(sb);
    u32x2 tb[2][4]; u32x4 sc[2][2];
    auto rd = [&](int pr) {
        const unsigned ab = pr < 5 ? a0 : a1; const int po = (pr < 5 ? pr : pr - 5) * 8192;
        tb[pr & 1][0] = tr_read(lds, ab + po); tb[pr & 1][1] = tr_read(lds, ab + po + 512); tb[pr & 1][2] = tr_read(lds, ab + po + 4096); tb[pr & 1][3] = tr_read(lds, ab + po + 4608);
        sc[pr & 1][0] = LD16(sb + pr * 128); sc[pr & 1][1] = LD16(sb + pr * 128 + 64);
    };
    rd(0);
#pragma unroll
    for (int pr = 0; pr < 10; ++pr) {
        if (pr + 1 < 10) rd(pr + 1);
        SB();
        acc = mfma(sc[pr & 1][0], (u32x4){tb[pr & 1][0][0], tb[pr & 1][0][1], tb[pr & 1][1][0], tb[pr & 1][1][1]}, acc);
        acc2 = mfma(sc[pr & 1][1], (u32x4){tb[pr & 1][2][0], tb[pr & 1][2][1], tb[pr & 1][3][0], tb[pr & 1][3][1]}, acc2);
        SB();
    }
    return rbf(acc[0] + acc2[0]);
}
DI void x_dma(const Params& p, char* lds, unsigned img, int T, int w, int l) {
    const rsrc_t rx = mk_rsrc(p.x, 2048 * 1280);
    const int r4 = l >> 4, row = 4 * w + r4;
    const unsigned voff = row * 1280 + (((l & 15) ^ row) * 16);
#pragma unroll
    for (int cb = 0; cb < 5; ++cb) dma16(lds, img + (w * 5 + cb) * 1024, rx, voff, T * 16 * 1280 + cb * 256);
}
DI void x_stats(char* lds, unsigned img, unsigned stat, int w, int l) {
    u32x4 v[5];
#pragma unroll
    for (int cb = 0; cb < 5; ++cb) v[cb] = LD16(img + (w * 5 + cb) * 1024 + l * 16);
    f32x2 s1 = {0.f, 0.f}, s2 = {0.f, 0.f};
#pragma unroll
    for (int i = 0; i < 5; ++i)
#pragma unroll
        for (int d = 0; d < 4; ++d) { const f32x2 x = {lo_f(v[i][d]), hi_f(v[i][d])}; s1 += x; s2 = __builtin_elementwise_fma(x, x, s2); }
    const float mean = red16(s1[0] + s1[1]) * (1.f / 640.f), ex2 = red16(s2[0] + s2[1]) * (1.f / 640.f);
    const float rstd = __builtin_amdgcn_rsqf(fmaxf(ex2 - mean * mean, 0.f) + 1e-5f);
    if ((l & 15) == 0) *(LAS f32x2*)((LAS char*)lds + stat + (4 * w + (l >> 4)) * 8) = (f32x2){rstd, -mean * rstd};
}
DI unsigned ximg_off(int t, int byte_in_row) { return ((t >> 2) * 5 + (byte_in_row >> 8)) * 1024 + (t & 3) * 256 + ((((byte_in_row >> 4) & 15) ^ t) * 16) + (byte_in_row & 15); }
DI void x_frags(char* lds, unsigned img, unsigned stat, rsrc_t rs, int T, int w, int l) {
    const int t = l & 15, qd = l >> 4;
    const f32x2 st = *(const LAS f32x2*)((const LAS char*)lds + stat + t * 8);
    const f32x2 r2 = {st[0], st[0]}, m2 = {st[1], st[1]};
    u32x4 v[5];
#pragma unroll
    for (int i = 0; i < 5; ++i) v[i] = LD16(img + ximg_off(t, (w + 4 * i) * 64 + qd * 16));
#pragma unroll
    for (int i = 0; i < 5; ++i) {
        u32x4 o;
#pragma unroll
        for (int d = 0; d < 4; ++d) { const f32x2 y = __builtin_elementwise_fma((f32x2){lo_f(v[i][d]), hi_f(v[i][d])}, r2, m2); o[d] = pack2(y[0], y[1]); }
        bstore16<SC1>(o, rs, l * 16, WS_XNF + (T * 20 + w + 4 * i) * 1024);
    }
}
struct TileKV { u32x2 ka0, ka1, k2, va0, va1, z; };
struct AttnMask { bool c0, c1, p0, p1, odd; unsigned sel0, sel1; };
DI AttnMask attn_mask(int t, int qd) {
    AttnMask m; const int par = t & 1, dc = t - 4 * qd - par;
    m.odd = par; m.c0 = dc >= 0; m.c1 = dc >= 2; m.p0 = dc + 16 <= 14; m.p1 = dc + 14 <= 14;
    m.sel0 = par ? 0x01000c0cu : 0x0c0c0100u; m.sel1 = par ? 0x03020c0cu : 0x0c0c0302u;
    return m;
}
template <class BG> DI void attn_unit(const u32x2 (&qa)[2], const u32x2& xq, const TileKV& cur, const TileKV& prev, bool prev_ok, const AttnMask& m, int qd, u32x4& omain, u32x2& oleft, BG&& bg) {
    const float cs = 0.22360679774997896f * 1.4426950408889634f, ninf = -__builtin_inff();
    f32x4 oa[2], oz[2];
#pragma unroll
    for (int h = 0; h < 2; ++h) {
        const u32x4 qf = {qa[h][0], qa[h][1], qd == h ? xq[0] : 0u, qd == h ? xq[1] : 0u};
        const u32x2 kcp = h == 0 ? cur.ka0 : cur.ka1, kpp = h == 0 ? prev.ka0 : prev.ka1;
        const f32x4 z4 = {0.f, 0.f, 0.f, 0.f};
        const f32x4 sp = mfma((u32x4){kpp[0], kpp[1], prev.k2[0], prev.k2[1]}, qf, z4);
        const f32x4 sc = mfma((u32x4){kcp[0], kcp[1], cur.k2[0], cur.k2[1]}, qf, z4);
        bg(5 * h);
        float e0 = m.odd ? sp[1] : sp[0], e1 = m.odd ? sp[3] : sp[2], e2 = m.odd ? sc[1] : sc[0], e3 = m.odd ? sc[3] : sc[2];
        e0 = (m.p0 && prev_ok) ? e0 : ninf; e1 = (m.p1 && prev_ok) ? e1 : ninf; e2 = m.c0 ? e2 : ninf; e3 = m.c1 ? e3 : ninf;
        bg(5 * h + 1);
        const float mx = red_max_q(fmaxf(fmaxf(e0, e1), fmaxf(e2, e3))), mc = -mx * cs;
        bg(5 * h + 2);
        e0 = __builtin_amdgcn_exp2f(fmaf(e0, cs, mc)); e1 = __builtin_amdgcn_exp2f(fmaf(e1, cs, mc)); e2 = __builtin_amdgcn_exp2f(fmaf(e2, cs, mc)); e3 = __builtin_amdgcn_exp2f(fmaf(e3, cs, mc));
        bg(5 * h + 3);
        const float inv = __builtin_amdgcn_rcpf(red_add_q((e0 + e1) + (e2 + e3)));
        bg(5 * h + 4);
        const unsigned pp = pack2(e0 * inv, e1 * inv), pc = pack2(e2 * inv, e3 * inv);
        const u32x4 pf = {__builtin_amdgcn_perm(0u, pp, m.sel0), __builtin_amdgcn_perm(0u, pp, m.sel1), __builtin_amdgcn_perm(0u, pc, m.sel0), __builtin_amdgcn_perm(0u, pc, m.sel1)};
        const u32x2 vc2 = h == 0 ? cur.va0 : cur.va1, vp2 = h == 0 ? prev.va0 : prev.va1;
        oa[h] = mfma((u32x4){vp2[0], vp2[1], vc2[0], vc2[1]}, pf, z4);
        oz[h] = mfma((u32x4){prev.z[0], prev.z[1], cur.z[0], cur.z[1]}, pf, z4);
    }
    const u32x2 a0 = pack4(oa[0]), a1 = pack4(oa[1]);
    omain = (u32x4){a0[0], a0[1], a1[0], a1[1]};
    const u32x2 l0 = pack4(oz[0]), l1 = pack4(oz[1]);
    oleft = qd == 0 ? l0 : l1;
}

__global__ __launch_bounds__(256, 1) void adaLN_block_kernel(Params p) {
    extern __shared__ __attribute__((aligned(16))) char lds[];
    const int b = blockIdx.x, w = __builtin_amdgcn_readfirstlane(threadIdx.x >> 6);
    const int xcd = b & 7, bi = b >> 3;
    char* ws = p.ws;
    const rsrc_t rs = mk_rsrc(ws, WS_END);
    STAMP(0);
    LSTAMP_DECL;
    {
        unsigned long long pc; unsigned len;
        asm volatile("s_getpc_b64 %0\n.Ladaln_pc:\n\ts_mov_b32 %1, .Ladaln_end-.Ladaln_pc" : "=s"(pc), "=s"(len));
        unsigned off = (bi * 256 + threadIdx.x) * 64u;
        off = off < len ? off : 0u;
        const unsigned long long a = pc + off;
        const unsigned dst = (unsigned)(size_t)(LAS char*)lds + LDS_BYTES - 1024 + w * 256;
        asm volatile("s_mov_b32 m0, %0\n\ts_nop 0\n\tglobal_load_lds_dword %1, off" :: "s"(dst), "v"(a) : "memory");
    }
    const unsigned epoch = *(const __attribute__((address_space(4))) unsigned*)(size_t)(ws + WS_EPOCH);
    unsigned tag;
#define MAKE_TAG() do { unsigned e_ = epoch; asm volatile("" : "+s"(e_)); tag = e_ + 0x9E3779B9u; } while (0)

    DOut dout;
    {
        LANE_IDS();
        const bool isA = bi < 20, isB = !isA && bi < 25, isC = !isA && !isB;
        const int sA = bi < 16 ? xcd + 8 * (bi >> 3) : 16 + (xcd >> 1), jg = bi < 16 ? (bi & 7) : 4 * (xcd & 1) + (bi - 16), idxB = xcd * 5 + (bi - 20);
        const int aidx = jg * 20 + sA;
        const int idxC = xcd * 7 + (bi - 25);
        const int Ts = idxC < 16 ? 3 * idxC : 48 + 2 * (idxC - 16), nT = idxC < 16 ? 3 : 2;
        const int did = w == 0 ? 1000 : b * 3 + w - 1;
        DPlan dp;
        if (isC) {
#pragma unroll
            for (int k = 0; k < 3; ++k) if (k < nT) x_dma(p, lds, k * 20480, Ts + k, w, l);
            MAKE_TAG();
            dtask_plan(p, did, l, dp, dout);
            STAMP(18);
            if (nT == 3) { WAITV(10); x_stats(lds, 0, L0_XS, w, l); WAITV(5); x_stats(lds, 20480, L0_XS + 128, w, l); WAITV(0); dtask_go(dp, lds, L0_D, w); x_stats(lds, 40960, L0_XS + 256, w, l); }
            else { WAITV(5); x_stats(lds, 0, L0_XS, w, l); WAITV(0); dtask_go(dp, lds, L0_D, w); x_stats(lds, 20480, L0_XS + 128, w, l); }
            BAR();
#pragma unroll
            for (int k = 0; k < 3; ++k) if (k < nT) x_frags(lds, k * 20480, L0_XS + k * 128, rs, Ts + k, w, l);
            DRAIN();
            BAR();
            flag_put(rs, WS_FC + (Ts + (tid & 3)) * FS, tag, tid < nT);
            STAMP(20);
        } else {
            const unsigned colA = isA ? 640 + 32 * sA : 1280 + 64 * idxB, colB = isA ? 32 * sA : 1280 + 64 * idxB + 32;
            const int rot = isA ? (bi < 16 ? 10 * jg : 20 * (bi - 16)) : 0;
            c_dma(p, lds, w, l);
            wm_dma<0, 10>(p, lds, w, l, colA, colB, rot);
            WAITV(10);
            silu_table(lds, tid);
            wm_dma<10, 20>(p, lds, w, l, colA, colB, rot);
            if (isA) {
                const rsrc_t rq = mk_rsrc(p.wqkv, 640 * 3840);
#pragma unroll
                for (int m = 0; m < 4; ++m) {
                    const int n = 64 * (4 * m + w) + l, rho = n / 30, c = n % 30;
                    const int k = (rho & 19) | ((rho & 4) << 1) | ((rho & 8) >> 1);
                    if (4 * m + w < 15) dma16(lds, L0_RW + (4 * m + w) * 1024, rq, k * 3840 + (c / 10) * 1280 + jg * 160 + (c % 10) * 16, sA * 32 * 3840);
                }
            }
            MAKE_TAG();
            dtask_plan(p, did, l, dp, dout);
            STAMP(18);
            WAITV(0);
            dtask_go(dp, lds, L0_D, w);
            BAR();
            float v = mod_gemv(lds, w, l);
            STAMP(19);
            if (isA) {
                const float gv = w < 2 ? rbf(1.f + v) : v;
                if (l < 16) { STF(L0_GS + (w * 16 + l) * 4, gv); *(LAS u16*)((LAS char*)lds + L0_GS + 256 + (w * 16 + l) * 2) = (u16)(__float_as_uint(gv) >> 16); }
                const int q = (l >> 2) & 3, pp = l & 3, jj = w >> 1, j = 2 * jg + jj;
                const unsigned rbase = L0_RW + (16 * (qd >> 1) + 4 * (qd & 1) + q) * 480 + 80 * jj;
                u32x2 ua[4], ub[4];
#pragma unroll
                for (int i = 0; i < 4; ++i) {
                    const unsigned lo = i < 2 ? i * 40 + 8 * pp : 160 + (i - 2) * 40 + 8 * pp;
                    const unsigned hi = i == 0 ? (pp >> 1) * 160 + (pp & 1) * 40 + 32 : i < 3 ? 320 + (i - 1) * 40 + 8 * pp : 320 + (pp & 1) * 40 + 32;
                    const unsigned po = (w & 1) ? hi : lo;
                    ua[i] = tr_read(lds, rbase + po); ub[i] = tr_read(lds, rbase + po + 8 * 480);
                }
                if ((w & 1) && t >= 8) { ua[3] = (u32x2){0u, 0u}; ub[3] = ua[3]; }
                BAR();
                const f32x4 g0 = LDF4(L0_GS + qd * 32), g1 = LDF4(L0_GS + qd * 32 + 16);
                const u32x4 sfr = LD16(L0_GS + 256 + 64 + qd * 16);
#pragma unroll
                for (int i = 0; i < 4; ++i) {
                    const int nt = 4 * (w & 1) + i;
                    const u32x4 o = {pack2(lo_f(ua[i][0]) * g0[0], hi_f(ua[i][0]) * g0[1]), pack2(lo_f(ua[i][1]) * g0[2], hi_f(ua[i][1]) * g0[3]),
                                     pack2(lo_f(ub[i][0]) * g1[0], hi_f(ub[i][0]) * g1[1]), pack2(lo_f(ub[i][1]) * g1[2], hi_f(ub[i][1]) * g1[3])};
                    bstore16<SC1>(o, rs, lo16, WS_WQF + ((j * 20 + sA) * 8 + nt) * 1024);
                }
                {
                    const f32x4 z4 = {0, 0, 0, 0};
                    f32x4 bias = mfma((u32x4){ua[0][0], ua[0][1], ub[0][0], ub[0][1]}, sfr, z4);
#pragma unroll
                    for (int i = 1; i < 4; ++i) { const f32x4 bi4 = mfma((u32x4){ua[i][0], ua[i][1], ub[i][0], ub[i][1]}, sfr, z4); bias = t == i ? bi4 : bias; }
                    bstore16<SC1>(asu4(bias), rs, t < 4 ? t * 64 + qd * 16 : 0x7ff00000u, WS_BP + ((sA * 16 + j) * 128 + (w & 1) * 64) * 4);
                }
                STAMP(20);
            } else {
                const int col = 1280 + 64 * idxB + 16 * w + t;
                if (col >= 2560 && col < 3200) v = rbf(1.f + v);
                bstore4<SC1>(__float_as_uint(v), rs, l < 16 ? l * 4 : 0x7ff00000u, WS_MODV + (1280 + 64 * idxB + 16 * w) * 4);
                const unsigned vn = (unsigned)__builtin_amdgcn_update_dpp(0, (int)__float_as_uint(v), 0xB1, 0xf, 0xf, true);
                bstore4<SC1>((__float_as_uint(v) >> 16) | (vn & 0xffff0000u), rs, (l < 16 && !(l & 1)) ? l * 2 : 0x7ff00000u, WS_MODB + (1280 + 64 * idxB + 16 * w) * 2);
            }
            DRAIN();
            BAR();
            flag_put(rs, WS_FA + aidx * FS, tag, isA && tid == 0);
        }
        STAMP(1);
    }

    const int g1 = 4 * (xcd >> 1) + (bi >> 3), j1 = 8 * (xcd & 1) + (bi & 7);
    const int P2 = 4 * xcd + (bi >> 3), c2 = bi & 7;
    const int g3 = g1, j3 = j1;
    const int rot2 = ROT2, rot3 = ROT3;
    auto wrap20 = [](int x) { return x >= 20 ? x - 20 : x; };
    const int t3 = j3 < 12 ? 7 * j3 : 84 + 6 * (j3 - 12), cnt3 = j3 < 12 ? 7 : 6;
    u32x4 w2f[14][5];
    const int s4 = __builtin_amdgcn_readfirstlane(w < 2 ? 14 * w : 28 + 13 * (w - 2)), cnt4 = __builtin_amdgcn_readfirstlane(w < 2 ? 14 : 13);
    const unsigned w2b = __builtin_amdgcn_readfirstlane(WS_W2F + (c2 * 54 + s4) * 5 * 1024);
    const int rot4 = __builtin_amdgcn_readfirstlane(ROT4);
    auto step4 = [&](int i) { const int x = (i < 13 ? i : cnt4 - 1) + rot4; return x >= cnt4 ? x - cnt4 : x; };
    auto w2_ld = [&](int f, unsigned lo) { w2f[f / 5][f % 5] = bload16<SC1>(rs, lo, w2b + step4(f / 5) * 5120 + (f % 5) * 1024); };
    constexpr int L2_WO = 61440, L2_X = 46080;
    {
        LANE_IDS();
        const int T0 = 8 * g1 + 2 * w, TH = g1 == 0 ? 0 : 8 * g1 - 1;
        const unsigned xb0 = WS_XNF + T0 * 20480, xbh = WS_XNF + TH * 20480 + (w >> 1) * 1024, wb = WS_WQF + j1 * 163840 + w * 4096;
        const unsigned wob = WS_WOF + c2 * 102400;
        u32x4 xr[10][4], xh[10];
        const int rot1 = ROT1;
        auto gr = [&](int G) { const int x = G + rot1; return x >= 10 ? x - 10 : x; };
        auto w_dma = [&](int G, int k) { dma16s(lds, (G & 3) * 16384 + w * 4096 + k * 1024, rs, lo16, wb + gr(G) * 16384 + k * 1024); };
        auto x_ld = [&](int G, int i) { xr[G][i] = bload16<SC1>(rs, lo16, xb0 + (i & 1) * 20480 + (2 * gr(G) + (i >> 1)) * 1024); };
        if (w == 0) {
            unsigned off[1] = {l == 0 ? WS_FC + TH * FS : WS_FC + (8 * g1 + ((l - 1) & 7)) * FS};
            poll_words<1>(ws, off, l < 9, tag);
        }
        BAR();
#pragma unroll
        for (int G = 0; G < 4; ++G) {
#pragma unroll
            for (int i = 0; i < 4; ++i) x_ld(G, i);
            xh[G] = bload16<SC1>(rs, lo16, xbh + gr(G) * 2048);
        }
        SB();
        if (w == 0) {
            unsigned off[1] = {WS_FA + ((j1 >> 1) * 20 + (l < 20 ? l : 0)) * FS};
            poll_words<1>(ws, off, l < 20, tag);
        }
        DRAIN();
        if (w > 0) dtask_read(dout, lds, L0_D, w, l);
        else {
#pragma unroll
            for (int i = 0; i < 8; ++i) dout.f[i] = (u32x4){0u, 0u, 0u, 0u};
        }
        BAR();
        STAMP(2);
#pragma unroll
        for (int G = 0; G < 3; ++G)
#pragma unroll
            for (int k = 0; k < 4; ++k) w_dma(G, k);
        WAITV(4);
        BAR();
        STAMP(3);
        f32x4 acc[2][8], hacc[3];
#pragma unroll
        for (int i = 0; i < 8; ++i) { acc[0][i] = (f32x4){0, 0, 0, 0}; acc[1][i] = (f32x4){0, 0, 0, 0}; }
#pragma unroll
        for (int i = 0; i < 3; ++i) hacc[i] = (f32x4){0, 0, 0, 0};
        u32x4 wf[2][8];
#pragma unroll
        for (int n = 0; n < 8; ++n) wf[0][n] = LD16(n * 1024 + lo16);
        const unsigned hoff = (w >> 1) * 8192 + ((w & 1) ? 5 : 2) * 1024 + lo16;
        f32x4 bp4[3]; u32x4 fdv;
        constexpr int L1_BPART = 40960;
        SB();
#pragma unroll
        for (int G = 0; G < 10; ++G) {
            u32x4 hw[3];
#pragma unroll
            for (int st = 0; st < 2; ++st) {
                const int s = 2 * G + st;
#pragma unroll
                for (int n = 0; n < 8; ++n) {
                    if (s + 1 < 20) wf[(s + 1) & 1][n] = LD16((((s + 1) >> 1) & 3) * 16384 + ((s + 1) & 1) * 8192 + n * 1024 + lo16);
                    if (st == 0 && n < 3) hw[n] = LD16((G & 3) * 16384 + hoff + n * 1024);
                    if (n < 5) { acc[0][n] = mfma(wf[s & 1][n], xr[G][st * 2], acc[0][n]); acc[1][n] = mfma(wf[s & 1][n], xr[G][st * 2 + 1], acc[1][n]); }
                    else { acc[0][n] = mfma(xr[G][st * 2], wf[s & 1][n], acc[0][n]); acc[1][n] = mfma(xr[G][st * 2 + 1], wf[s & 1][n], acc[1][n]); }
                    if (st == 1 && n >= 5) hacc[n - 5] = mfma(hw[n - 5], xh[G], hacc[n - 5]);
                    if (st == 0) {
                        if (G + 3 < 10 && (n & 1) == 0) w_dma(G + 3, n >> 1);
                        if (G + 4 < 10 && n == 7) xh[G + 4] = bload16<SC1>(rs, lo16, xbh + gr(G + 4) * 2048);
                    } else {
                        if (G + 4 < 10 && (n & 1) == 0) x_ld(G + 4, n >> 1);
                    }
                    if (G == 7 && st == 0 && n < 3) bp4[n] = asf4(bload16<SC1>(rs, (n < 2 || tid < 128) ? ((tid >> 5) + 8 * n) * 8192 + (tid & 31) * 16 : 0x7ff00000u, WS_BP + j1 * 512));
                    if (G == 9 && st == 0 && n == 0) {
                        f32x4 bs = (bp4[0] + bp4[1]) + bp4[2];
#pragma unroll
                        for (int r = 0; r < 4; ++r) { auto sw = __builtin_amdgcn_permlane32_swap(__float_as_uint(bs[r]), __float_as_uint(bs[r]), false, false); bs[r] = __uint_as_float(sw[0]) + __uint_as_float(sw[1]); }
                        STF4(L1_BPART + w * 1024 + lo16, bs);
                    }
                    if (G == 9 && st == 1 && n == 0) fdv = bload16<SC1>(rs, lo16, WS_FD);
                    if (n == 3) {
                        if (s == 0) dtask_frag<0>(dout, rs, l);
                        if (s == 1) dtask_frag<1>(dout, rs, l);
                        if (s == 2) dtask_frag<2>(dout, rs, l);
                        if (s == 3) dtask_frag<3>(dout, rs, l);
                        if (s == 4) dtask_frag<4>(dout, rs, l);
                        if (s == 5) dtask_frag<5>(dout, rs, l);
                        if (s == 6) dtask_frag<6>(dout, rs, l);
                        if (s == 7) dtask_frag<7>(dout, rs, l);
                    }
                    SB();
                }
            }
            if (G == 0) WAITV(11); else if (G <= 3) WAITV(17); else if (G == 4) WAITV(11); else if (G == 5) WAITV(14); else if (G == 6) WAITV(9); else if (G == 7) WAITV(3);
            BAR();
            if (G == 4) flag_put(rs, WS_FD + b * 4, tag, tid == 0);
        }
        STAMP(4);
        constexpr int L1_BIAS = 6144, L1_HP = 0, L1_EX = 8192, L1_VT = 6656;
        if (w == 0) {
            check_all_blocks(ws, rs, WS_FD, fdv, tag);
            if (b == 0 && l == 0) flag_store(ws, WS_EPOCH, tag);
            if (l < 32) STF4(L1_BIAS + l * 16, (LDF4(L1_BPART + l * 16) + LDF4(L1_BPART + 1024 + l * 16)) + (LDF4(L1_BPART + 2048 + l * 16) + LDF4(L1_BPART + 3072 + l * 16)));
        }
        if (w >= 2) {
#pragma unroll
            for (int i = 0; i < 3; ++i) STF4(L1_HP + ((w & 1) * 3 + i) * 1024 + lo16, hacc[i]);
        }
        BAR();
        auto wo_dma = [&](int k) { dma16s(lds, L2_WO + w * 25600 + k * 1024, rs, lo16, wob + w * 25600 + k * 1024); };
        const rsrc_t rx = mk_rsrc(p.x, 2048 * 1280);
        unsigned xvo[3];
#pragma unroll
        for (int k = 0; k < 3; ++k) { const int n = (w * 3 + k) * 64 + l, row = n / 10; xvo[k] = n < 640 ? row * 1280 + (n - 10 * row) * 16 : 0x7ff00000u; }
        const unsigned xlo = L2_X + w * 3072;
        auto x_dma2 = [&](int k) { dma<0>(lds, xlo + k * 1024, rx, xvo[k], P2 * 64 * 1280 + c2 * 160); };
        f32x4 bq[5]; float bv[3];
#pragma unroll
        for (int n = 0; n < 5; ++n) bq[n] = LDF4(L1_BIAS + (n * 16 + 4 * qd) * 4);
#pragma unroll
        for (int n = 0; n < 3; ++n) bv[n] = LDF(L1_BIAS + ((5 + n) * 16 + t) * 4);
        TileKV kv[2]; u32x2 qa[2][2], xq[2];
        auto bg_wo = [&](int k) { SB(); wo_dma(k); SB(); };
#pragma unroll
        for (int tt = 1; tt >= 0; --tt) {
            kv[tt].ka0 = pack4(acc[tt][2] + bq[2]); kv[tt].ka1 = pack4(acc[tt][3] + bq[3]);
            bg_wo(4 * (1 - tt));
            xq[tt] = pack4(acc[tt][4] + bq[4]);
            kv[tt].k2[0] = swap32(xq[tt][0]); kv[tt].k2[1] = swap32(xq[tt][1]);
            bg_wo(4 * (1 - tt) + 1);
            kv[tt].va0 = pack4(acc[tt][5] + bv[0]); kv[tt].va1 = pack4(acc[tt][6] + bv[1]);
            bg_wo(4 * (1 - tt) + 2);
            kv[tt].z = pack4(acc[tt][7] + bv[2]);
            if (tt == 1) {
                const unsigned e = L1_EX + (w + 1) * 3072 + lo16;
                ST16(e, ((u32x4){kv[1].ka0[0], kv[1].ka0[1], kv[1].ka1[0], kv[1].ka1[1]}));
                ST16(e + 1024, ((u32x4){kv[1].k2[0], kv[1].k2[1], kv[1].va0[0], kv[1].va0[1]}));
                ST16(e + 2048, ((u32x4){kv[1].va1[0], kv[1].va1[1], kv[1].z[0], kv[1].z[1]}));
            }
            qa[tt][0] = pack4(acc[tt][0] + bq[0]); qa[tt][1] = pack4(acc[tt][1] + bq[1]);
            bg_wo(4 * (1 - tt) + 3);
        }
        if (w == 0) {
            const u32x2 k0 = pack4(hacc[0] + LDF4(L1_HP + lo16) + bq[2]), k1 = pack4(hacc[1] + LDF4(L1_HP + 1024 + lo16) + bq[3]);
            const u32x2 xx = pack4(hacc[2] + LDF4(L1_HP + 2048 + lo16) + bq[4]);
            ST16(L1_EX + lo16, ((u32x4){k0[0], k0[1], k1[0], k1[1]}));
            ST8(L1_EX + 1024 + lo16, ((u32x2){swap32(xx[0]), swap32(xx[1])}));
        } else if (w == 1) {
#pragma unroll
            for (int i = 0; i < 3; ++i) {
                const f32x4 v = hacc[i] + LDF4(L1_HP + (3 + i) * 1024 + lo16) + LDF4(L1_BIAS + ((5 + i) * 16 + 4 * qd) * 4);
                const u32x2 pk = pack4(v);
                LAS u16* d = (LAS u16*)((LAS char*)lds + L1_VT + i * 512 + (4 * qd) * 32 + t * 2);
                d[0] = (u16)pk[0]; d[16] = (u16)(pk[0] >> 16); d[32] = (u16)pk[1]; d[48] = (u16)(pk[1] >> 16);
            }
        }
        const unsigned ob = WS_OF + T0 * 20480;
        const AttnMask am = attn_mask(t, qd);
        u32x4 om; u32x2 ol;
        SB();
        attn_unit(qa[1], xq[1], kv[1], kv[0], true, am, qd, om, ol, [&](int k) { bg_wo(8 + k); });
        SB();
        bstore16<SC1>(om, rs, lo16, ob + 20480 + j1 * 1024);
        bstore8<SC1>(ol, rs, qd < 2 ? ((j1 & 3) * 16 + t) * 16 + 8 * qd : 0x7ff00000u, ob + 20480 + (16 + (j1 >> 2)) * 1024);
        BAR();
        TileKV pv;
        {
            const unsigned e = L1_EX + w * 3072 + lo16;
            const u32x4 a = LD16(e), bb = LD16(e + 1024), cc = LD16(e + 2048);
            pv.ka0 = (u32x2){a[0], a[1]}; pv.ka1 = (u32x2){a[2], a[3]}; pv.k2 = (u32x2){bb[0], bb[1]}; pv.va0 = (u32x2){bb[2], bb[3]};
            pv.va1 = (u32x2){cc[0], cc[1]}; pv.z = (u32x2){cc[2], cc[3]};
            if (w == 0) { pv.va0 = LD8(L1_VT + t * 32 + qd * 8); pv.va1 = LD8(L1_VT + 512 + t * 32 + qd * 8); pv.z = LD8(L1_VT + 1024 + t * 32 + qd * 8); }
        }
        SB();
        attn_unit(qa[0], xq[0], kv[0], pv, !(g1 == 0 && w == 0), am, qd, om, ol, [&](int k) { if (k < 7) bg_wo(18 + k); else { SB(); x_dma2(k - 7); SB(); } });
        SB();
        bstore16<SC1>(om, rs, lo16, ob + j1 * 1024);
        bstore8<SC1>(ol, rs, qd < 2 ? ((j1 & 3) * 16 + t) * 16 + 8 * qd : 0x7ff00000u, ob + (16 + (j1 >> 2)) * 1024);
        STAMP(5);
        publish(rs, WS_F1 + (g1 * 16 + j1) * FS, tag);
    }
    STAMP(6);

    u32x2 x1b[5], b1v[7];
    u32x4 vf[5];
    {
        LANE_IDS();
        const int T = 4 * P2 + w;
        constexpr int L2_MV = 57344;
        constexpr int L2_TB = 58304, L2_B1 = 60864;
        const u32x4 mvl = bload16<SC1>(rs, (1280 + ((tid / 20) % 3) * 640 + 80 * c2 + (tid % 20) * 4) * 4, WS_MODV);
        if (w < 3) { if (w < 2 || l < 32) dma16s(lds, L2_TB + w * 1024, rs, lo16, WS_MODB + 1920 * 2 + w * 1024); }
        else if (l < 14) dma<0>(lds, L2_B1, mk_rsrc(p.b1, 1728 * 2), lo16, t3 * 32);
#pragma unroll
        for (int f = 0; f < 12; ++f) w2_ld(f, lo16);
        SB();
        if (tid < 60) ST16(L2_MV + tid * 16, mvl);
        WAITV(12);
        BAR();
        u32x2 xv[5], va2[5], vb2[5];
#pragma unroll
        for (int i = 0; i < 5; ++i) {
            const int sr = wrap20(5 * w + i + rot3);
            const int ta = sr < 16 ? 5 * (sr >> 1) + 2 * (sr & 1) : 10 * (sr - 16) + 4, tb = ta + (sr < 16 ? 1 : 5);
            const unsigned sec = L2_TB + (t == 0 ? 1280 : 0) + qd * 8;
            va2[i] = LD8(sec + ta * 32); vb2[i] = LD8(sec + tb * 32);
        }
#pragma unroll
        for (int n = 0; n < 7; ++n) b1v[n] = LD8(L2_B1 + n * 32 + qd * 8);
#pragma unroll
        for (int n = 0; n < 5; ++n) xv[n] = LD8(L2_X + (16 * w + t) * 160 + n * 32 + qd * 8);
        SB();
#pragma unroll
        for (int i = 0; i < 5; ++i) {
            const unsigned cst = t == 2 ? 0x3f803f80u : 0u;
            vf[i] = (u32x4){t < 2 ? va2[i][0] : cst, t < 2 ? va2[i][1] : cst, t < 2 ? vb2[i][0] : cst, t < 2 ? vb2[i][1] : cst};
        }
        asm volatile("" : "+v"(vf[0]), "+v"(vf[1]), "+v"(vf[2]), "+v"(vf[3]), "+v"(vf[4]));
        asm volatile("" : "+v"(b1v[0]), "+v"(b1v[1]), "+v"(b1v[2]), "+v"(b1v[3]), "+v"(b1v[4]), "+v"(b1v[5]), "+v"(b1v[6]), "+v"(xv[0]), "+v"(xv[1]), "+v"(xv[2]), "+v"(xv[3]), "+v"(xv[4]));
        if (w == 0) {
            unsigned off[1] = {WS_F1 + ((P2 >> 1) * 16 + (l & 15)) * FS};
            poll_words<1>(ws, off, l < 16, tag);
        }
        BAR();
        STAMP(7);
        constexpr int PW = 8;
        u32x4 tk[20];
#pragma unroll
        for (int s = 0; s < PW; ++s) tk[s] = bload16<SC1>(rs, lo16, WS_OF + (T * 20 + wrap20(s + rot2)) * 1024);
        f32x4 acc[5];
#pragma unroll
        for (int n = 0; n < 5; ++n) acc[n] = (f32x4){0, 0, 0, 0};
        unsigned wb0 = L2_WO + lo16, wb1 = L2_WO + 51200 + lo16; OPAQUE(wb0); OPAQUE(wb1);
        auto wo_rd = [&](int s, int n) { return s < 10 ? LD16(wb0 + (s * 5 + n) * 1024) : LD16(wb1 + ((s - 10) * 5 + n) * 1024); };
        u32x4 wf[3][5];
#pragma unroll
        for (int n = 0; n < 5; ++n) { wf[0][n] = wo_rd(0, n); wf[1][n] = wo_rd(1, n); }
        SB();
        LSTAMP(40);
#pragma unroll
        for (int s = 0; s < 20; ++s) {
            if (s + PW < 20) tk[s + PW] = bload16<SC1>(rs, lo16, WS_OF + (T * 20 + wrap20(s + PW + rot2)) * 1024);
            if (s + 2 < 20) {
#pragma unroll
                for (int n = 0; n < 5; ++n) wf[(s + 2) % 3][n] = wo_rd(s + 2, n);
            }
#pragma unroll
            for (int n = 0; n < 5; ++n) acc[n] = mfma(wf[s % 3][n], tk[s], acc[n]);
#pragma unroll
            for (int n = 0; n < 5; ++n) { if (s + 2 < 20) SGB(0x100, 1); SGB(0x8, 1); if (n == 1 && s + PW < 20) SGB(0x20, 1); }
            SB();
            if ((s & 3) == 3 || s == 0) LSTEP(41 + (s + 1) / 4);
        }
        STAMP(8);
        BAR();
        const unsigned w1src = WS_W1F + t3 * 20480, w1last = (cnt3 == 7 ? 6 : 0) * 20480;
        auto w1_dma = [&](int base_step, int i) {
            const int n = i % 7;
            dma16s(lds, base_step * 7168 + i * 1024, rs, lo16, w1src + (n == 6 ? w1last : n * 20480) + wrap20(base_step + i / 7 + rot3) * 1024);
        };
        float st[6] = {0, 0, 0, 0, 0, 0};
        unsigned mvb = L2_MV + qd * 16; OPAQUE(mvb);
        f32x4 mvr[2][3];
#pragma unroll
        for (int k = 0; k < 3; ++k) mvr[0][k] = LDF4(mvb + k * 320);
        u32x2 ub[5];
        const unsigned ufb = WS_UF + T * 20480;
#pragma unroll
        for (int n = 0; n < 5; ++n) {
            if (n + 1 < 5) {
#pragma unroll
                for (int k = 0; k < 3; ++k) mvr[(n + 1) & 1][k] = LDF4(mvb + k * 320 + (n + 1) * 64);
            }
            const f32x4 ga_n = mvr[n & 1][0], s2_n = mvr[n & 1][1], g2_n = mvr[n & 1][2];
            float x1[4] = {lo_f(xv[n][0]), hi_f(xv[n][0]), lo_f(xv[n][1]), hi_f(xv[n][1])};
#pragma unroll
            for (int r = 0; r < 4; ++r) x1[r] = fmaf(ga_n[r], acc[n][r], x1[r]);
            x1b[n] = (u32x2){pack2(x1[0], x1[1]), pack2(x1[2], x1[3])};
            const float xr[4] = {lo_f(x1b[n][0]), hi_f(x1b[n][0]), lo_f(x1b[n][1]), hi_f(x1b[n][1])};
            ub[n] = (u32x2){pack2(xr[0] * g2_n[0], xr[1] * g2_n[1]), pack2(xr[2] * g2_n[2], xr[3] * g2_n[3])};
            const float ur[4] = {lo_f(ub[n][0]), hi_f(ub[n][0]), lo_f(ub[n][1]), hi_f(ub[n][1])};
#pragma unroll
            for (int r = 0; r < 4; ++r) {
                st[0] += xr[r]; st[1] = fmaf(xr[r], xr[r], st[1]); st[2] += ur[r]; st[3] = fmaf(ur[r], g2_n[r], st[3]);
                st[4] = fmaf(ur[r], ur[r], st[4]); st[5] = fmaf(ur[r], s2_n[r], st[5]);
            }
            if (n == 1) bstore16<SC1>((u32x4){ub[0][0], ub[0][1], ub[1][0], ub[1][1]}, rs, lo16, ufb + (2 * c2) * 1024);
            if (n == 3) bstore16<SC1>((u32x4){ub[2][0], ub[2][1], ub[3][0], ub[3][1]}, rs, lo16, ufb + (2 * c2 + 1) * 1024);
            if (n == 4) bstore8<SC1>(ub[4], rs, lo16 + 8 * (c2 & 1), ufb + (16 + (c2 >> 1)) * 1024);
#pragma unroll
            for (int k = 0; k < 3; ++k) if (3 * n + k < 14) w1_dma(2 * w, 3 * n + k);
            SGB(0x20, 1); SGB(0x1, 18); SGB(0x20, 1); SGB(0x1, 18); SGB(0x20, 1); SGB(0x1, 24);
            SB();
        }
#pragma unroll
        for (int i = 0; i < 6; ++i) st[i] = red_add_q(st[i]);
        if (qd < 2) bstore16<SC1>(asu4(qd == 0 ? (f32x4){st[0], st[1], st[2], st[3]} : (f32x4){st[4], st[5], 0.f, 0.f}), rs, qd * 256 + t * 16, WS_ST + (T * 8 + c2) * 512);
#pragma unroll
        for (int n = 0; n < 5; ++n) asm volatile("" : "+v"(x1b[n]));
        STAMP(9);
        publish(rs, WS_F2 + (P2 * 8 + c2) * FS, tag);
#pragma unroll
        for (int i = 0; i < 21; ++i) w1_dma(8 + 3 * w, i);
    }
    STAMP(10);

    {
        LANE_IDS();
        constexpr int L3_GSC = 143360;
        const int T0 = 8 * g3 + 2 * w;
        if (w == 0) {
            unsigned off[1] = {WS_F2 + ((2 * g3 + ((l >> 3) & 1)) * 8 + (l & 7)) * FS};
            poll_words<1>(ws, off, l < 16, tag);
        }
        DRAIN();
        BAR();
        STAMP(11);
        constexpr int PW = 4;
        u32x4 tk[20][2];
        u32x4 spa[2][2]; u32x2 spb[2][2];
        float al[2], be[2], gm[2], de[2], ts[2][6];
        f32x4 red4[4], gr0, gr1;
#pragma unroll
        for (int s = 0; s < PW; ++s) { const unsigned o = WS_UF + (T0 * 20 + wrap20(s + rot3)) * 1024; tk[s][0] = bload16<SC1>(rs, lo16, o); tk[s][1] = bload16<SC1>(rs, lo16, o + 20480); }
        SB();
        LSTAMP(62);
        {
            f32x4 va[7], gram = {0, 0, 0, 0};
            u32x4 wv[2][7];
            unsigned vb = 5 * w * 7168 + lo16; OPAQUE(vb);
#pragma unroll
            for (int n = 0; n < 7; ++n) { va[n] = (f32x4){0, 0, 0, 0}; wv[0][n] = LD16(vb + n * 1024); }
            SB();
#pragma unroll
            for (int i = 0; i < 5; ++i) {
                if (i + 1 < 5) {
#pragma unroll
                    for (int n = 0; n < 7; ++n) wv[(i + 1) & 1][n] = LD16(vb + ((i + 1) * 7 + n) * 1024);
                }
#pragma unroll
                for (int n = 0; n < 7; ++n) va[n] = mfma(wv[i & 1][n], vf[i], va[n]);
                gram = mfma(vf[i], vf[i], gram);
#pragma unroll
                for (int n = 0; n < 7; ++n) { if (i + 1 < 5) SGB(0x100, 1); SGB(0x8, 1); }
                SB();
            }
            if (t < 3) {
#pragma unroll
                for (int n = 0; n < 7; ++n) STF4(L3_GSC + ((w * 8 + n) * 3 + t) * 64 + qd * 16, va[n]);
                if (qd == 0) STF4(L3_GSC + ((w * 8 + 7) * 3 + t) * 64, gram);
            }
        }
        SB();
        f32x4 acc[2][7];
#pragma unroll
        for (int n = 0; n < 7; ++n) { acc[0][n] = (f32x4){0, 0, 0, 0}; acc[1][n] = (f32x4){0, 0, 0, 0}; }
        unsigned wb0 = lo16, wb1 = 57344 + lo16, wb2 = 114688 + lo16; OPAQUE(wb0); OPAQUE(wb1); OPAQUE(wb2);
        auto w1_rd = [&](int s, int n) { return s < 8 ? LD16(wb0 + (s * 7 + n) * 1024) : s < 16 ? LD16(wb1 + ((s - 8) * 7 + n) * 1024) : LD16(wb2 + ((s - 16) * 7 + n) * 1024); };
        u32x4 wf[2][7];
#pragma unroll
        for (int n = 0; n < 7; ++n) wf[0][n] = w1_rd(0, n);
        SB();
        LSTAMP(0);
#pragma unroll
        for (int s = 0; s < 20; ++s) {
            int nv = 0;
            if (s + PW < 20) { const unsigned o = WS_UF + (T0 * 20 + wrap20(s + PW + rot3)) * 1024; tk[s + PW][0] = bload16<SC1>(rs, lo16, o); tk[s + PW][1] = bload16<SC1>(rs, lo16, o + 20480); nv += 2; }
            if (s < 16) { w2_ld(12 + s, lo16); nv += 1; }
            if (s >= 16) {
#pragma unroll
                for (int k = 0; k < 3; ++k) w2_ld(28 + (s - 16) * 3 + k, lo16);
                nv += 3;
            }
            if (s == 10 || s == 11) {
#pragma unroll
                for (int e = 0; e < 2; ++e) {
                    spa[s - 10][e] = bload16<SC1>(rs, (2 * qd + e) * 512 + t * 16, WS_ST + (T0 + s - 10) * 4096);
                    spb[s - 10][e] = bload8<SC1>(rs, (2 * qd + e) * 512 + 256 + t * 16, WS_ST + (T0 + s - 10) * 4096);
                }
                nv += 4;
            }
            int nu = 0;
            if (s == 3) {
                const int r96 = tid < 96 ? tid : 0;
#pragma unroll
                for (int u = 0; u < 4; ++u) red4[u] = LDF4(L3_GSC + (u * 96 + r96) * 16);
            }
            if (s == 4) { STF4(L3_GSC + 6144 + (tid < 96 ? tid : 96 + (tid & 31)) * 16, (red4[0] + red4[1]) + (red4[2] + red4[3])); nu = 1; }
            if (s == 9) { gr0 = LDF4(L3_GSC + 6144 + 84 * 16); gr1 = LDF4(L3_GSC + 6144 + 88 * 16); }
            if (s == 13 || s == 14) {
                const int tt = s - 13;
                const f32x4 a0 = asf4(spa[tt][0]) + asf4(spa[tt][1]);
                const float b00 = __uint_as_float(spb[tt][0][0]) + __uint_as_float(spb[tt][1][0]), b01 = __uint_as_float(spb[tt][0][1]) + __uint_as_float(spb[tt][1][1]);
                ts[tt][0] = red_add_q(a0[0]); ts[tt][1] = red_add_q(a0[1]); ts[tt][2] = red_add_q(a0[2]); ts[tt][3] = red_add_q(a0[3]); ts[tt][4] = red_add_q(b00); ts[tt][5] = red_add_q(b01);
                nu = 4;
            }
            if (s == 15) {
                const float Gg = gr0[0], Ggs = gr0[1], Gs = gr0[2], Sss = gr1[1], Ss = gr1[2], iD = 1.f / 640.f;
#pragma unroll
                for (int tt = 0; tt < 2; ++tt) {
                    const float m1 = ts[tt][0] * iD, v1 = fmaxf(ts[tt][1] * iD - m1 * m1, 0.f), r1v = __builtin_amdgcn_rsqf(v1 + 1e-5f);
                    const float m2 = (r1v * (ts[tt][2] - m1 * Gs) + Ss) * iD;
                    const float e2 = (r1v * r1v * (ts[tt][4] - 2.f * m1 * ts[tt][3] + m1 * m1 * Gg) + 2.f * r1v * (ts[tt][5] - m1 * Ggs) + Sss) * iD;
                    const float r2v = __builtin_amdgcn_rsqf(fmaxf(e2 - m2 * m2, 0.f) + 1e-5f);
                    al[tt] = r1v * r2v; be[tt] = -r1v * r2v * m1; gm[tt] = r2v; de[tt] = -r2v * m2;
                }
                nu = 4;
            }
            if (s + 1 < 20) {
#pragma unroll
                for (int n = 0; n < 7; ++n) wf[(s + 1) & 1][n] = w1_rd(s + 1, n);
            }
#pragma unroll
            for (int n = 0; n < 7; ++n) { acc[0][n] = mfma(wf[s & 1][n], tk[s][0], acc[0][n]); acc[1][n] = mfma(wf[s & 1][n], tk[s][1], acc[1][n]); }
#pragma unroll
            for (int n = 0; n < 7; ++n) {
                if (s + 1 < 20) SGB(0x100, 1);
                SGB(0x8, 1);
                if (nu == 1) SGB(0x402, 1);
                if (nu == 4) SGB(0x402, 4);
                SGB(0x8, 1);
                if (nu == 1) SGB(0x402, 1);
                if (nu == 4) SGB(0x402, 4);
                if (n < nv) SGB(0x20, 1);
            }
            SB();
            if (s == 2 || s == 8) BAR();
            LSTEP(1 + s);
        }
        STAMP(12);
        u32x2 hb[2][7];
        unsigned gsb = L3_GSC + 6144 + qd * 16; OPAQUE(gsb);
        f32x4 gsc[2][3];
#pragma unroll
        for (int k = 0; k < 3; ++k) gsc[0][k] = LDF4(gsb + 64 * k);
#pragma unroll
        for (int n = 0; n < 7; ++n) {
#pragma unroll
            for (int k = 0; k < 2; ++k) w2_ld(40 + n * 2 + k, lo16);
            if (n + 1 < 7) {
#pragma unroll
                for (int k = 0; k < 3; ++k) gsc[(n + 1) & 1][k] = LDF4(gsb + (n + 1) * 192 + 64 * k);
            }
            const f32x4 G = gsc[n & 1][0], Sv = gsc[n & 1][1], C = gsc[n & 1][2];
            const float bb[4] = {lo_f(b1v[n][0]), hi_f(b1v[n][0]), lo_f(b1v[n][1]), hi_f(b1v[n][1])};
#pragma unroll
            for (int tt = 0; tt < 2; ++tt) {
                float hv[4];
#pragma unroll
                for (int r = 0; r < 4; ++r) {
                    float z = fmaf(gm[tt], Sv[r], bb[r]); z = fmaf(be[tt], G[r], z); z = fmaf(de[tt], C[r], z); z = fmaf(al[tt], acc[tt][n][r], z);
                    const float u = z * fmaf(z * z, -0.044715f * 1.5957691216057308f * 1.4426950408889634f, -1.5957691216057308f * 1.4426950408889634f);
                    hv[r] = z * __builtin_amdgcn_rcpf(1.f + __builtin_amdgcn_exp2f(u));
                }
                hb[tt][n] = (u32x2){pack2(hv[0], hv[1]), pack2(hv[2], hv[3])};
            }
            SGB(0x20, 1); SGB(0x1, 40); SGB(0x20, 1); SGB(0x1, 60);
            SB();
#pragma unroll
            for (int tt = 0; tt < 2; ++tt) {
                const unsigned hbase = WS_HF + (T0 + tt) * 54 * 1024;
                if (n & 1) bstore16<SC1>((u32x4){hb[tt][n - 1][0], hb[tt][n - 1][1], hb[tt][n][0], hb[tt][n][1]}, rs, lo16, hbase + (3 * j3 + (n >> 1)) * 1024);
                if (n == 6 && j3 < 12) bstore8<SC1>(hb[tt][6], rs, lo16 + 8 * (j3 & 1), hbase + (48 + (j3 >> 1)) * 1024);
            }
            SB();
        }
        STAMP(13);
        publish(rs, WS_F3 + (g3 * 16 + j3) * FS, tag);
    }
    STAMP(14);

    {
        LANE_IDS();
        f32x4 gb[5]; u32x2 b2v[5];
#pragma unroll
        for (int f = 54; f < 70; ++f) w2_ld(f, lo16);
        SB();
        if (w == 0) {
            unsigned off[1] = {WS_F3 + ((P2 >> 1) * 16 + (l & 15)) * FS};
            poll_words<1>(ws, off, l < 16, tag);
        }
        BAR();
        STAMP(15);
        constexpr int PD = 3;
        u32x4 tk[14][4];
        const unsigned tokb = __builtin_amdgcn_readfirstlane(WS_HF + (4 * P2 * 54 + s4) * 1024);
        auto issue = [&](int i) {
            const int s = step4(i);
#pragma unroll
            for (int a = 0; a < 4; ++a) tk[i][a] = bload16<SC1>(rs, lo16, tokb + (((a + w) & 3) * 54 + s) * 1024);
        };
#pragma unroll
        for (int i = 0; i < PD; ++i) issue(i);
        f32x4 acc[4][5];
#pragma unroll
        for (int a = 0; a < 4; ++a)
#pragma unroll
            for (int n = 0; n < 5; ++n) acc[a][n] = (f32x4){0, 0, 0, 0};
        SB();
        LSTAMP(24);
#pragma unroll
        for (int i = 0; i < 14; ++i) {
            if (i < 13 || w < 2) {
                if (i + PD < 14) issue(i + PD);
                if (i == 11) {
#pragma unroll
                    for (int n = 0; n < 5; ++n) {
                        const int f = 80 * c2 + 16 * n;
                        gb[n] = asf4(bload16<SC1>(rs, qd * 16, WS_MODV + (3200 + f) * 4));
                        b2v[n] = *(const GAS u32x2*)(p.b2 + f + 4 * qd);
                    }
                }
#pragma unroll
                for (int a = 0; a < 4; ++a)
#pragma unroll
                    for (int n = 0; n < 5; ++n) acc[a][n] = mfma(w2f[i][n], tk[i][a], acc[a][n]);
#pragma unroll
                for (int a = 0; a < 4; ++a) { SGB(0x8, 2); if (i == 11) SGB(0x20, 3); else SGB(0x20, 1); SGB(0x8, 3); }
            }
            SB();
            LSTEP(25 + i);
        }
        STAMP(16);
#pragma unroll
        for (int a = 1; a < 4; ++a) {
            unsigned d = ((((a + w) & 3) * 3 + (3 - a)) * 5) * 1024 + lo16; OPAQUE(d);
#pragma unroll
            for (int n = 0; n < 5; ++n) STF4(d + n * 1024, acc[a][n]);
        }
        BAR();
        constexpr int L4_OUT = 61440;
        unsigned rb = w * 15360 + lo16; OPAQUE(rb);
        f32x4 part[5][3];
#pragma unroll
        for (int n = 0; n < 5; ++n)
#pragma unroll
            for (int u = 0; u < 3; ++u) part[n][u] = LDF4(rb + (u * 5 + n) * 1024);
        SB();
#pragma unroll
        for (int n = 0; n < 5; ++n) {
            const f32x4 v = (acc[0][n] + part[n][0]) + (part[n][1] + part[n][2]);
            const float x1[4] = {lo_f(x1b[n][0]), hi_f(x1b[n][0]), lo_f(x1b[n][1]), hi_f(x1b[n][1])};
            const float bv[4] = {lo_f(b2v[n][0]), hi_f(b2v[n][0]), lo_f(b2v[n][1]), hi_f(b2v[n][1])};
            float o[4];
#pragma unroll
            for (int r = 0; r < 4; ++r) o[r] = fmaf(gb[n][r], v[r] + bv[r], x1[r]);
            ST8(L4_OUT + (16 * w + t) * 176 + n * 32 + qd * 8, ((u32x2){pack2(o[0], o[1]), pack2(o[2], o[3])}));
        }
        asm volatile("s_waitcnt lgkmcnt(0)" ::: "memory");
        const rsrc_t ro = mk_rsrc(p.out, 2048 * 1280);
#pragma unroll
        for (int k = 0; k < 3; ++k) {
            const int ch = l + 64 * k;
            if (ch < 160) {
                const int row = ch / 10, cc = ch % 10;
                const u32x4 v = LD16(L4_OUT + (16 * w + row) * 176 + cc * 16);
                bstore16<OUT_AUX>(v, ro, row * 1280 + cc * 16, (16 * (4 * P2 + w)) * 1280 + c2 * 160);
            }
        }
    }
    STAMP(17);
    LSTAMP_DUMP;
    asm volatile(".Ladaln_end:" ::: "memory");
}

extern "C" void kernel_launch(void* const* d_in, const int* in_sizes, int n_in, void* d_out, int out_size, void* d_ws, size_t ws_size, hipStream_t stream) {
    Params p;
    p.x = (const u16*)d_in[0]; p.wqkv = (const u16*)d_in[1]; p.wo = (const u16*)d_in[2]; p.w1 = (const u16*)d_in[4]; p.b1 = (const u16*)d_in[5];
    p.w2 = (const u16*)d_in[6]; p.b2 = (const u16*)d_in[7]; p.c = (const u16*)d_in[8]; p.wm = (const u16*)d_in[9];
    p.out = (u16*)d_out; p.ws = (char*)d_ws;
    (void)hipFuncSetAttribute((const void*)adaLN_block_kernel, hipFuncAttributeMaxDynamicSharedMemorySize, LDS_BYTES);
    adaLN_block_kernel<<<256, 256, LDS_BYTES, stream>>>(p);
}
```

```cpp
#include <hip/hip_runtime.h>
#include <stdint.h>

typedef unsigned short u16;
typedef float f32x4 __attribute__((ext_vector_type(4)));
typedef float f32x2 __attribute__((ext_vector_type(2)));
typedef __bf16 bf16x8 __attribute__((ext_vector_type(8)));
typedef __bf16 bf16x2 __attribute__((ext_vector_type(2)));
typedef unsigned u32x4 __attribute__((ext_vector_type(4)));
typedef unsigned u32x2 __attribute__((ext_vector_type(2)));
typedef short s16x4 __attribute__((ext_vector_type(4)));
typedef __amdgpu_buffer_rsrc_t rsrc_t;
#define LAS __attribute__((address_space(3)))
#define GAS __attribute__((address_space(1)))
#define DI __device__ __forceinline__

static constexpr unsigned FS = 64;
static constexpr unsigned WS_EPOCH = 0;
static constexpr unsigned WS_TMO   = 256;
static constexpr unsigned WS_FA    = 1024;
static constexpr unsigned WS_FC    = WS_FA + 160 * FS;
static constexpr unsigned WS_FD    = WS_FC + 128 * FS;
static constexpr unsigned WS_F1    = WS_FD + 1024;
static constexpr unsigned WS_F2    = WS_F1 + 256 * FS;
static constexpr unsigned WS_F3    = WS_F2 + 256 * FS;
static constexpr unsigned WS_FEND  = WS_F3 + 256 * FS;
static constexpr unsigned WS_MODV = 131072;
static constexpr unsigned WS_MODB = WS_MODV + 16384;
static constexpr unsigned WS_BP   = WS_MODB + 8192;
static constexpr unsigned WS_XNF  = WS_BP + 20 * 16 * 128 * 4;
static constexpr unsigned WS_WQF  = WS_XNF + 128 * 20 * 1024;
static constexpr unsigned WS_WOF  = WS_WQF + 16 * 20 * 8 * 1024;
static constexpr unsigned WS_W1F  = WS_WOF + 8 * 20 * 5 * 1024;
static constexpr unsigned WS_W2F  = WS_W1F + 108 * 20 * 1024;
static constexpr unsigned WS_OF   = WS_W2F + 8 * 54 * 5 * 1024;
static constexpr unsigned WS_UF   = WS_OF + 128 * 20 * 1024;
static constexpr unsigned WS_ST   = WS_UF + 128 * 20 * 1024;
static constexpr unsigned WS_HF   = WS_ST + 128 * 8 * 512;
static constexpr unsigned WS_END  = WS_HF + 128 * 54 * 1024;
static_assert(WS_FEND <= WS_MODV, "flags overflow");

static constexpr int LDS_BYTES = 163840;
static constexpr int SC1 = 16;
static constexpr unsigned SPIN_CAP = 40000;

struct Params {
    const u16* x; const u16* wqkv; const u16* wo; const u16* w1; const u16* b1; const u16* w2; const u16* b2; const u16* c; const u16* wm;
    u16* out; char* ws;
};

DI float lo_f(unsigned d) { return __uint_as_float(d << 16); }
DI float hi_f(unsigned d) { return __uint_as_float(d & 0xffff0000u); }
DI unsigned pack2(float a, float b) { f32x2 v = {a, b}; return __builtin_bit_cast(unsigned, __builtin_convertvector(v, bf16x2)); }
DI float rbf(float a) { return lo_f(pack2(a, 0.f)); }
DI u32x2 pack4(const f32x4& v) { return (u32x2){pack2(v[0], v[1]), pack2(v[2], v[3])}; }
DI f32x4 mfma(u32x4 a, u32x4 b, f32x4 c) { return __builtin_amdgcn_mfma_f32_16x16x32_bf16(__builtin_bit_cast(bf16x8, a), __builtin_bit_cast(bf16x8, b), c, 0, 0, 0); }
DI float red_add_q(float v) {
    auto a = __builtin_amdgcn_permlane16_swap(__float_as_uint(v), __float_as_uint(v), false, false);
    v = __uint_as_float(a[0]) + __uint_as_float(a[1]);
    auto b = __builtin_amdgcn_permlane32_swap(__float_as_uint(v), __float_as_uint(v), false, false);
    return __uint_as_float(b[0]) + __uint_as_float(b[1]);
}
DI float red_max_q(float v) {
    auto a = __builtin_amdgcn_permlane16_swap(__float_as_uint(v), __float_as_uint(v), false, false);
    v = fmaxf(__uint_as_float(a[0]), __uint_as_float(a[1]));
    auto b = __builtin_amdgcn_permlane32_swap(__float_as_uint(v), __float_as_uint(v), false, false);
    return fmaxf(__uint_as_float(b[0]), __uint_as_float(b[1]));
}
template <int C> DI float dpp_add(float v) { return v + __builtin_bit_cast(float, __builtin_amdgcn_update_dpp(0, __builtin_bit_cast(int, v), C, 0xf, 0xf, true)); }
DI float red16(float v) { v = dpp_add<0xB1>(v); v = dpp_add<0x4E>(v); v = dpp_add<0x141>(v); return dpp_add<0x140>(v); }
DI unsigned swap32(unsigned v) { auto a = __builtin_amdgcn_permlane32_swap(v, v, false, false); return (threadIdx.x & 32) ? a[0] : a[1]; }
DI rsrc_t mk_rsrc(const void* p, unsigned bytes) { return __builtin_amdgcn_make_buffer_rsrc((void*)p, 0, bytes, 0x00020000); }
template <int AUX> DI u32x4 bload16(rsrc_t r, unsigned voff, unsigned soff) { return __builtin_bit_cast(u32x4, __builtin_amdgcn_raw_buffer_load_b128(r, voff, soff, AUX)); }
template <int AUX> DI u32x2 bload8(rsrc_t r, unsigned voff, unsigned soff) { return __builtin_bit_cast(u32x2, __builtin_amdgcn_raw_buffer_load_b64(r, voff, soff, AUX)); }
template <int AUX> DI unsigned bload4(rsrc_t r, unsigned voff, unsigned soff) { return __builtin_amdgcn_raw_buffer_load_b32(r, voff, soff, AUX); }
template <int AUX> DI void bstore16(u32x4 v, rsrc_t r, unsigned voff, unsigned soff) { __builtin_amdgcn_raw_buffer_store_b128(v, r, voff, soff, AUX); }
template <int AUX> DI void bstore8(u32x2 v, rsrc_t r, unsigned voff, unsigned soff) { __builtin_amdgcn_raw_buffer_store_b64(v, r, voff, soff, AUX); }
template <int AUX> DI void bstore4(unsigned v, rsrc_t r, unsigned voff, unsigned soff) { __builtin_amdgcn_raw_buffer_store_b32(v, r, voff, soff, AUX); }
DI f32x4 asf4(u32x4 v) { return __builtin_bit_cast(f32x4, v); }
DI u32x4 asu4(f32x4 v) { return __builtin_bit_cast(u32x4, v); }
#define LANE_IDS() int tz_ = threadIdx.x; asm volatile("" : "+v"(tz_)); const int tid = tz_, l = tid & 63, t = l & 15, qd = l >> 4; const unsigned lo16 = l * 16; (void)t; (void)qd; (void)lo16
#define OPAQUE(x) asm volatile("" : "+v"(x))
#define SB() __builtin_amdgcn_sched_barrier(0)
#define BAR() asm volatile("s_waitcnt lgkmcnt(0)\n\ts_barrier" ::: "memory")
#define DRAIN() asm volatile("s_waitcnt vmcnt(0)" ::: "memory")
#define SGB(mask, n) __builtin_amdgcn_sched_group_barrier(mask, n, 0)
#define LD16(off) (*(const LAS u32x4*)((const LAS char*)lds + (off)))
#define LD8(off) (*(const LAS u32x2*)((const LAS char*)lds + (off)))
#define LDF4(off) (*(const LAS f32x4*)((const LAS char*)lds + (off)))
#define LDF(off) (*(const LAS float*)((const LAS char*)lds + (off)))
#define ST16(off, v) (*(LAS u32x4*)((LAS char*)lds + (off)) = (v))
#define ST8(off, v) (*(LAS u32x2*)((LAS char*)lds + (off)) = (v))
#define STF4(off, v) (*(LAS f32x4*)((LAS char*)lds + (off)) = (v))
#define STF(off, v) (*(LAS float*)((LAS char*)lds + (off)) = (v))
DI u32x2 tr_read(const char* lds, unsigned off) { return __builtin_bit_cast(u32x2, __builtin_amdgcn_ds_read_tr16_b64_v4i16((LAS s16x4*)((LAS char*)lds + off))); }

DI void flag_store(char* ws, unsigned off, unsigned tag) { __hip_atomic_store((GAS unsigned*)(ws + off), tag, __ATOMIC_RELAXED, __HIP_MEMORY_SCOPE_AGENT); }
DI unsigned flag_load(const char* ws, unsigned off) { return __hip_atomic_load((const GAS unsigned*)(ws + off), __ATOMIC_RELAXED, __HIP_MEMORY_SCOPE_AGENT); }
DI void flag_put(rsrc_t rs, unsigned off, unsigned tag, bool me) { bstore4<SC1>(tag, rs, me ? off : 0x7ff00000u, 0); }
DI void publish(rsrc_t rs, unsigned off, unsigned tag) { DRAIN(); BAR(); flag_put(rs, off, tag, threadIdx.x == 0); }
template <int N> DI void poll_words(char* ws, const unsigned (&off)[N], bool active, unsigned tag) {
#pragma nounroll
    for (unsigned spins = 0;; ++spins) {
        bool ok = true;
#pragma unroll
        for (int k = 0; k < N; ++k) ok &= flag_load(ws, off[k]) == tag;
        if (__all(ok || !active)) break;
        if (spins > SPIN_CAP) { if ((threadIdx.x & 63) == 0) flag_store(ws, WS_TMO, tag); break; }
        __builtin_amdgcn_s_sleep(1);
    }
}

DI void check_all_blocks(char* ws, rsrc_t rs, unsigned base, u32x4 v, unsigned tag) {
    asm volatile("" : "+v"(v));
#pragma nounroll
    for (unsigned spins = 0; !__all(v[0] == tag && v[1] == tag && v[2] == tag && v[3] == tag); ++spins) {
        if (spins > SPIN_CAP) { if ((threadIdx.x & 63) == 0) flag_store(ws, WS_TMO, tag); break; }
        __builtin_amdgcn_s_sleep(1);
        v = bload16<SC1>(rs, (threadIdx.x & 63) * 16, base);
        asm volatile("" : "+v"(v) :: "memory");
    }
}

DI void tiles_p3(int s, int& ta, int& tb) { if (s < 16) { ta = 5 * (s >> 1) + 2 * (s & 1); tb = ta + 1; } else { ta = 10 * (s - 16) + 4; tb = ta + 5; } }
DI void tiles_p4(int s, int& ta, int& tb) { if (s < 48) { int j = s / 3, pr = s % 3; int st = j < 12 ? 7 * j : 84 + 6 * (j - 12); ta = st + 2 * pr; tb = ta + 1; } else { int m = s - 48; ta = 14 * m + 6; tb = ta + 7; } }

static constexpr int L0_SC = 81920, L0_GS = 83200, L0_CRAW = 83968, L0_RW = 86016, L0_D = 101376, L0_XS = 156160;

template <int AUX> DI void dma(char* lds, unsigned lds_off, rsrc_t r, unsigned voff, unsigned soff) {
    __builtin_amdgcn_raw_ptr_buffer_load_lds(r, (LAS void*)((LAS char*)lds + lds_off), 16, voff, soff, 0, AUX);
}
DI void dma16(char* lds, unsigned lds_off, rsrc_t r, unsigned voff, unsigned soff) {
    const unsigned dst = (unsigned)(size_t)(LAS char*)lds + lds_off;
    asm volatile("s_mov_b32 m0, %0\n\ts_nop 0\n\tbuffer_load_dwordx4 %1, %2, %3 offen lds" :: "s"(dst), "v"(voff), "s"(r), "s"(soff) : "memory");
}
DI void dma16s(char* lds, unsigned lds_off, rsrc_t r, unsigned voff, unsigned soff) { dma<SC1>(lds, lds_off, r, voff, soff); }
#define WAITV(n) asm volatile("s_waitcnt vmcnt(%0)" :: "n"(n) : "memory")

struct DPlan { rsrc_t r; unsigned v0, v1, sa, sb, st; bool on; };
struct DOut { unsigned dst[8]; u32x4 f[8]; };
DI void dtask_plan(const Params& p, int id, int l, DPlan& d, DOut& o) {
    int ra0, rb0, rstep, cg, lim; unsigned pitch, d0, r5, stride, jump;
    if (id < 370) {
        const bool wo = id < 100; const int i2 = wo ? id : id - 100, s = i2 / 5; cg = i2 - 5 * s; pitch = 1280;
        if (wo) { d.r = mk_rsrc(p.wo, 640 * 1280); if (s < 16) { ra0 = 40 * s; rstep = 4; } else { ra0 = 160 * (s - 16) + 16; rstep = 40; } rb0 = ra0 + 20; }
        else { d.r = mk_rsrc(p.w2, 1728 * 1280); int ta, tb; tiles_p4(s, ta, tb); ra0 = 16 * ta; rb0 = 16 * tb; rstep = 4; }
        const int c0 = (8 * cg) / 5;
        r5 = 8 * cg - 5 * c0; d0 = (wo ? WS_WOF + (c0 * 20 + s) * 5120 : WS_W2F + (c0 * 54 + s) * 5120) + r5 * 1024;
        stride = 1024; jump = wo ? 19 * 5120 : 53 * 5120; lim = 8;
    } else {
        const int i2 = id < 650 ? id - 370 : 0, s = i2 / 14; cg = i2 - 14 * s; pitch = 3456; d.r = mk_rsrc(p.w1, 640 * 3456);
        int ta, tb; tiles_p3(s, ta, tb); ra0 = 16 * ta; rb0 = 16 * tb; rstep = 4;
        r5 = 0; d0 = WS_W1F + (8 * cg * 20 + s) * 1024; stride = 20480; jump = 0; lim = id < 650 ? (cg == 13 ? 4 : 8) : 0;
    }
    const int q = l >> 4, cp = l & 15;
    d.v0 = q * pitch + ((cp ^ (q << 1)) * 16); d.v1 = q * pitch + ((cp ^ ((4 + q) << 1)) * 16);
    d.sa = ra0 * pitch + cg * 256; d.sb = rb0 * pitch + cg * 256; d.st = rstep * pitch; d.on = lim > 0;
#pragma unroll
    for (int nt = 0; nt < 8; ++nt) {
        const unsigned v = d0 + nt * stride + ((r5 + nt >= 5 ? 1u : 0u) + (r5 + nt >= 10 ? 1u : 0u)) * jump;
        o.dst[nt] = __builtin_amdgcn_readfirstlane(nt < lim ? v : 0x7ff00000u);
    }
}
DI void dtask_go(const DPlan& d, char* lds, unsigned scr, int w) {
    if (d.on) {
#pragma unroll
        for (int i = 0; i < 8; ++i) dma16(lds, scr + w * 8192 + i * 1024, d.r, (i & 1) ? d.v1 : d.v0, (i < 4 ? d.sa : d.sb) + (i & 3) * d.st);
    }
}
DI void dtask_read(DOut& o, char* lds, unsigned scr, int w, int l) {
    const int qd = l >> 4, q = (l >> 2) & 3, pp = l & 3, r7 = 4 * (qd & 1) + q;
    const unsigned a0 = scr + w * 8192 + (4 * qd + q) * 256 + pp * 8;
#pragma unroll
    for (int nt = 0; nt < 8; ++nt) {
        const u32x2 ua = tr_read(lds, a0 + ((nt ^ r7) * 32)), ub = tr_read(lds, a0 + 4096 + ((nt ^ r7) * 32));
        o.f[nt] = (u32x4){ua[0], ua[1], ub[0], ub[1]};
    }
}
template <int NT> DI void dtask_frag(const DOut& o, rsrc_t rs, int l) { bstore16<SC1>(o.f[NT], rs, l * 16, o.dst[NT]); }

template <int I0, int I1> DI void wm_dma(const Params& p, char* lds, int w, int l, unsigned colA, unsigned colB, int rot) {
    const rsrc_t rw = mk_rsrc(p.wm, 640 * 3840 * 2);
    const int rl = l >> 3, cp = l & 7;
    const unsigned c = cp ^ ((((rl >> 1) & 1) | ((w & 1) << 1)) << 1);
    const unsigned voff = rl * 7680 + (c < 4 ? colA + 8 * c : colB + 8 * (c - 4)) * 2;
    int piece = 4 * I0 + w + rot; piece = piece >= 80 ? piece - 80 : piece;
#pragma unroll
    for (int i = I0; i < I1; ++i) {
        dma16(lds, piece * 1024, rw, voff, piece * 61440);
        piece = piece >= 76 ? piece - 76 : piece + 4;
    }
}
DI void c_dma(const Params& p, char* lds, int w, int l) {
    dma16(lds, L0_CRAW + (w & 1) * 1024, mk_rsrc(p.c, 1280), ((w & 1) * 64 + l) * 16, 0);
}
DI void silu_table(char* lds, int tid) {
    if (tid < 80) {
        const u32x4 creg = LD16(L0_CRAW + tid * 16);
        u32x4 o;
#pragma unroll
        for (int d = 0; d < 4; ++d) { const float a = lo_f(creg[d]), b = hi_f(creg[d]); o[d] = pack2(a * __builtin_amdgcn_rcpf(1.f + __expf(-a)), b * __builtin_amdgcn_rcpf(1.f + __expf(-b))); }
        ST16(L0_SC + tid * 16, o);
    }
}
DI float mod_gemv(char* lds, int w, int l) {
    const int qd = l >> 4, q = (l >> 2) & 3, pp = l & 3;
    f32x4 acc = {0, 0, 0, 0}, acc2 = {0, 0, 0, 0};
    const int r0 = 8 * qd + q;
    const unsigned sg = ((r0 >> 1) & 1) | (((r0 >> 3) & 1) << 1);
    unsigned a0 = r0 * 128 + ((w ^ sg) * 32) + pp * 8, a1 = a0 + 40960, sb = L0_SC + qd * 16;
    OPAQUE(a0); OPAQUE(a1); OPAQUE(sb);
    u32x2 tb[2][4]; u32x4 sc[2][2];
    auto rd = [&](int pr) {
        const unsigned ab = pr < 5 ? a0 : a1; const int po = (pr < 5 ? pr : pr - 5) * 8192;
        tb[pr & 1][0] = tr_read(lds, ab + po); tb[pr & 1][1] = tr_read(lds, ab + po + 512); tb[pr & 1][2] = tr_read(lds, ab + po + 4096); tb[pr & 1][3] = tr_read(lds, ab + po + 4608);
        sc[pr & 1][0] = LD16(sb + pr * 128); sc[pr & 1][1] = LD16(sb + pr * 128 + 64);
    };
    rd(0);
#pragma unroll
    for (int pr = 0; pr < 10; ++pr) {
        if (pr + 1 < 10) rd(pr + 1);
        SB();
        acc = mfma(sc[pr & 1][0], (u32x4){tb[pr & 1][0][0], tb[pr & 1][0][1], tb[pr & 1][1][0], tb[pr & 1][1][1]}, acc);
        acc2 = mfma(sc[pr & 1][1], (u32x4){tb[pr & 1][2][0], tb[pr & 1][2][1], tb[pr & 1][3][0], tb[pr & 1][3][1]}, acc2);
        SB();
    }
    return rbf(acc[0] + acc2[0]);
}
DI void x_dma(const Params& p, char* lds, unsigned img, int T, int w, int l) {
    const rsrc_t rx = mk_rsrc(p.x, 2048 * 1280);
    const int r4 = l >> 4, row = 4 * w + r4;
    const unsigned voff = row * 1280 + (((l & 15) ^ row) * 16);
#pragma unroll
    for (int cb = 0; cb < 5; ++cb) dma16(lds, img + (w * 5 + cb) * 1024, rx, voff, T * 16 * 1280 + cb * 256);
}
DI void x_stats(char* lds, unsigned img, unsigned stat, int w, int l) {
    u32x4 v[5];
#pragma unroll
    for (int cb = 0; cb < 5; ++cb) v[cb] = LD16(img + (w * 5 + cb) * 1024 + l * 16);
    f32x2 s1 = {0.f, 0.f}, s2 = {0.f, 0.f};
#pragma unroll
    for (int i = 0; i < 5; ++i)
#pragma unroll
        for (int d = 0; d < 4; ++d) { const f32x2 x = {lo_f(v[i][d]), hi_f(v[i][d])}; s1 += x; s2 = __builtin_elementwise_fma(x, x, s2); }
    const float mean = red16(s1[0] + s1[1]) * (1.f / 640.f), ex2 = red16(s2[0] + s2[1]) * (1.f / 640.f);
    const float rstd = __builtin_amdgcn_rsqf(fmaxf(ex2 - mean * mean, 0.f) + 1e-5f);
    if ((l & 15) == 0) *(LAS f32x2*)((LAS char*)lds + stat + (4 * w + (l >> 4)) * 8) = (f32x2){rstd, -mean * rstd};
}
DI unsigned ximg_off(int t, int byte_in_row) { return ((t >> 2) * 5 + (byte_in_row >> 8)) * 1024 + (t & 3) * 256 + ((((byte_in_row >> 4) & 15) ^ t) * 16) + (byte_in_row & 15); }
DI void x_frags(char* lds, unsigned img, unsigned stat, rsrc_t rs, int T, int w, int l) {
    const int t = l & 15, qd = l >> 4;
    const f32x2 st = *(const LAS f32x2*)((const LAS char*)lds + stat + t * 8);
    const f32x2 r2 = {st[0], st[0]}, m2 = {st[1], st[1]};
    u32x4 v[5];
#pragma unroll
    for (int i = 0; i < 5; ++i) v[i] = LD16(img + ximg_off(t, (w + 4 * i) * 64 + qd * 16));
#pragma unroll
    for (int i = 0; i < 5; ++i) {
        u32x4 o;
#pragma unroll
        for (int d = 0; d < 4; ++d) { const f32x2 y = __builtin_elementwise_fma((f32x2){lo_f(v[i][d]), hi_f(v[i][d])}, r2, m2); o[d] = pack2(y[0], y[1]); }
        bstore16<SC1>(o, rs, l * 16, WS_XNF + (T * 20 + w + 4 * i) * 1024);
    }
}
struct TileKV { u32x2 ka0, ka1, k2, va0, va1, z; };
struct AttnMask { bool c0, c1, p0, p1, odd; unsigned sel0, sel1; };
DI AttnMask attn_mask(int t, int qd) {
    AttnMask m; const int par = t & 1, dc = t - 4 * qd - par;
    m.odd = par; m.c0 = dc >= 0; m.c1 = dc >= 2; m.p0 = dc + 16 <= 14; m.p1 = dc + 14 <= 14;
    m.sel0 = par ? 0x01000c0cu : 0x0c0c0100u; m.sel1 = par ? 0x03020c0cu : 0x0c0c0302u;
    return m;
}
template <class BG> DI void attn_unit(const u32x2 (&qa)[2], const u32x2& xq, const TileKV& cur, const TileKV& prev, bool prev_ok, const AttnMask& m, int qd, u32x4& omain, u32x2& oleft, BG&& bg) {
    const float cs = 0.22360679774997896f * 1.4426950408889634f, ninf = -__builtin_inff();
    f32x4 oa[2], oz[2];
#pragma unroll
    for (int h = 0; h < 2; ++h) {
        const u32x4 qf = {qa[h][0], qa[h][1], qd == h ? xq[0] : 0u, qd == h ? xq[1] : 0u};
        const u32x2 kcp = h == 0 ? cur.ka0 : cur.ka1, kpp = h == 0 ? prev.ka0 : prev.ka1;
        const f32x4 z4 = {0.f, 0.f, 0.f, 0.f};
        const f32x4 sp = mfma((u32x4){kpp[0], kpp[1], prev.k2[0], prev.k2[1]}, qf, z4);
        const f32x4 sc = mfma((u32x4){kcp[0], kcp[1], cur.k2[0], cur.k2[1]}, qf, z4);
        bg(5 * h);
        float e0 = m.odd ? sp[1] : sp[0], e1 = m.odd ? sp[3] : sp[2], e2 = m.odd ? sc[1] : sc[0], e3 = m.odd ? sc[3] : sc[2];
        e0 = (m.p0 && prev_ok) ? e0 : ninf; e1 = (m.p1 && prev_ok) ? e1 : ninf; e2 = m.c0 ? e2 : ninf; e3 = m.c1 ? e3 : ninf;
        bg(5 * h + 1);
        const float mx = red_max_q(fmaxf(fmaxf(e0, e1), fmaxf(e2, e3))), mc = -mx * cs;
        bg(5 * h + 2);
        e0 = __builtin_amdgcn_exp2f(fmaf(e0, cs, mc)); e1 = __builtin_amdgcn_exp2f(fmaf(e1, cs, mc)); e2 = __builtin_amdgcn_exp2f(fmaf(e2, cs, mc)); e3 = __builtin_amdgcn_exp2f(fmaf(e3, cs, mc));
        bg(5 * h + 3);
        const float inv = __builtin_amdgcn_rcpf(red_add_q((e0 + e1) + (e2 + e3)));
        bg(5 * h + 4);
        const unsigned pp = pack2(e0 * inv, e1 * inv), pc = pack2(e2 * inv, e3 * inv);
        const u32x4 pf = {__builtin_amdgcn_perm(0u, pp, m.sel0), __builtin_amdgcn_perm(0u, pp, m.sel1), __builtin_amdgcn_perm(0u, pc, m.sel0), __builtin_amdgcn_perm(0u, pc, m.sel1)};
        const u32x2 vc2 = h == 0 ? cur.va0 : cur.va1, vp2 = h == 0 ? prev.va0 : prev.va1;
        oa[h] = mfma((u32x4){vp2[0], vp2[1], vc2[0], vc2[1]}, pf, z4);
        oz[h] = mfma((u32x4){prev.z[0], prev.z[1], cur.z[0], cur.z[1]}, pf, z4);
    }
    const u32x2 a0 = pack4(oa[0]), a1 = pack4(oa[1]);
    omain = (u32x4){a0[0], a0[1], a1[0], a1[1]};
    const u32x2 l0 = pack4(oz[0]), l1 = pack4(oz[1]);
    oleft = qd == 0 ? l0 : l1;
}

__global__ __launch_bounds__(256, 1) void adaLN_block_kernel(Params p) {
    extern __shared__ __attribute__((aligned(16))) char lds[];
    const int b = blockIdx.x, w = __builtin_amdgcn_readfirstlane(threadIdx.x >> 6);
    const int xcd = b & 7, bi = b >> 3;
    char* ws = p.ws;
    const rsrc_t rs = mk_rsrc(ws, WS_END);
    {
        unsigned long long pc; unsigned len;
        asm volatile("s_getpc_b64 %0\n.Ladaln_pc:\n\ts_mov_b32 %1, .Ladaln_end-.Ladaln_pc" : "=s"(pc), "=s"(len));
        unsigned off = (bi * 256 + threadIdx.x) * 64u;
        off = off < len ? off : 0u;
        const unsigned long long a = pc + off;
        const unsigned dst = (unsigned)(size_t)(LAS char*)lds + LDS_BYTES - 1024 + w * 256;
        asm volatile("s_mov_b32 m0, %0\n\ts_nop 0\n\tglobal_load_lds_dword %1, off" :: "s"(dst), "v"(a) : "memory");
    }
    const unsigned epoch = *(const __attribute__((address_space(4))) unsigned*)(size_t)(ws + WS_EPOCH);
    unsigned tag;
#define MAKE_TAG() do { unsigned e_ = epoch; asm volatile("" : "+s"(e_)); tag = e_ + 0x9E3779B9u; } while (0)

    DOut dout;
    {
        LANE_IDS();
        const bool isA = bi < 20, isB = !isA && bi < 25, isC = !isA && !isB;
        const int sA = bi < 16 ? xcd + 8 * (bi >> 3) : 16 + (xcd >> 1), jg = bi < 16 ? (bi & 7) : 4 * (xcd & 1) + (bi - 16), idxB = xcd * 5 + (bi - 20);
        const int aidx = jg * 20 + sA;
        const int idxC = xcd * 7 + (bi - 25);
        const int Ts = idxC < 16 ? 3 * idxC : 48 + 2 * (idxC - 16), nT = idxC < 16 ? 3 : 2;
        const int did = w == 0 ? 1000 : b * 3 + w - 1;
        DPlan dp;
        if (isC) {
#pragma unroll
            for (int k = 0; k < 3; ++k) if (k < nT) x_dma(p, lds, k * 20480, Ts + k, w, l);
            MAKE_TAG();
            dtask_plan(p, did, l, dp, dout);
            if (nT == 3) { WAITV(10); x_stats(lds, 0, L0_XS, w, l); WAITV(5); x_stats(lds, 20480, L0_XS + 128, w, l); WAITV(0); dtask_go(dp, lds, L0_D, w); x_stats(lds, 40960, L0_XS + 256, w, l); }
            else { WAITV(5); x_stats(lds, 0, L0_XS, w, l); WAITV(0); dtask_go(dp, lds, L0_D, w); x_stats(lds, 20480, L0_XS + 128, w, l); }
            BAR();
#pragma unroll
            for (int k = 0; k < 3; ++k) if (k < nT) x_frags(lds, k * 20480, L0_XS + k * 128, rs, Ts + k, w, l);
            DRAIN();
            BAR();
            flag_put(rs, WS_FC + (Ts + (tid & 3)) * FS, tag, tid < nT);
        } else {
            const unsigned colA = isA ? 640 + 32 * sA : 1280 + 64 * idxB, colB = isA ? 32 * sA : 1280 + 64 * idxB + 32;
            const int rot = isA ? (bi < 16 ? 10 * jg : 20 * (bi - 16)) : 0;
            c_dma(p, lds, w, l);
            wm_dma<0, 10>(p, lds, w, l, colA, colB, rot);
            WAITV(10);
            silu_table(lds, tid);
            wm_dma<10, 20>(p, lds, w, l, colA, colB, rot);
            if (isA) {
                const rsrc_t rq = mk_rsrc(p.wqkv, 640 * 3840);
#pragma unroll
                for (int m = 0; m < 4; ++m) {
                    const int n = 64 * (4 * m + w) + l, rho = n / 30, c = n % 30;
                    const int k = (rho & 19) | ((rho & 4) << 1) | ((rho & 8) >> 1);
                    if (4 * m + w < 15) dma16(lds, L0_RW + (4 * m + w) * 1024, rq, k * 3840 + (c / 10) * 1280 + jg * 160 + (c % 10) * 16, sA * 32 * 3840);
                }
            }
            MAKE_TAG();
            dtask_plan(p, did, l, dp, dout);
            WAITV(0);
            dtask_go(dp, lds, L0_D, w);
            BAR();
            float v = mod_gemv(lds, w, l);
            if (isA) {
                const float gv = w < 2 ? rbf(1.f + v) : v;
                if (l < 16) { STF(L0_GS + (w * 16 + l) * 4, gv); *(LAS u16*)((LAS char*)lds + L0_GS + 256 + (w * 16 + l) * 2) = (u16)(__float_as_uint(gv) >> 16); }
                const int q = (l >> 2) & 3, pp = l & 3, jj = w >> 1, j = 2 * jg + jj;
                const unsigned rbase = L0_RW + (16 * (qd >> 1) + 4 * (qd & 1) + q) * 480 + 80 * jj;
                u32x2 ua[4], ub[4];
#pragma unroll
                for (int i = 0; i < 4; ++i) {
                    const unsigned lo = i < 2 ? i * 40 + 8 * pp : 160 + (i - 2) * 40 + 8 * pp;
                    const unsigned hi = i == 0 ? (pp >> 1) * 160 + (pp & 1) * 40 + 32 : i < 3 ? 320 + (i - 1) * 40 + 8 * pp : 320 + (pp & 1) * 40 + 32;
                    const unsigned po = (w & 1) ? hi : lo;
                    ua[i] = tr_read(lds, rbase + po); ub[i] = tr_read(lds, rbase + po + 8 * 480);
                }
                if ((w & 1) && t >= 8) { ua[3] = (u32x2){0u, 0u}; ub[3] = ua[3]; }
                BAR();
                const f32x4 g0 = LDF4(L0_GS + qd * 32), g1 = LDF4(L0_GS + qd * 32 + 16);
                const u32x4 sfr = LD16(L0_GS + 256 + 64 + qd * 16);
#pragma unroll
                for (int i = 0; i < 4; ++i) {
                    const int nt = 4 * (w & 1) + i;
                    const u32x4 o = {pack2(lo_f(ua[i][0]) * g0[0], hi_f(ua[i][0]) * g0[1]), pack2(lo_f(ua[i][1]) * g0[2], hi_f(ua[i][1]) * g0[3]),
                                     pack2(lo_f(ub[i][0]) * g1[0], hi_f(ub[i][0]) * g1[1]), pack2(lo_f(ub[i][1]) * g1[2], hi_f(ub[i][1]) * g1[3])};
                    bstore16<SC1>(o, rs, lo16, WS_WQF + ((j * 20 + sA) * 8 + nt) * 1024);
                }
                {
                    const f32x4 z4 = {0, 0, 0, 0};
                    f32x4 bias = mfma((u32x4){ua[0][0], ua[0][1], ub[0][0], ub[0][1]}, sfr, z4);
#pragma unroll
                    for (int i = 1; i < 4; ++i) { const f32x4 bi4 = mfma((u32x4){ua[i][0], ua[i][1], ub[i][0], ub[i][1]}, sfr, z4); bias = t == i ? bi4 : bias; }
                    bstore16<SC1>(asu4(bias), rs, t < 4 ? t * 64 + qd * 16 : 0x7ff00000u, WS_BP + ((sA * 16 + j) * 128 + (w & 1) * 64) * 4);
                }
            } else {
                const int col = 1280 + 64 * idxB + 16 * w + t;
                if (col >= 2560 && col < 3200) v = rbf(1.f + v);
                bstore4<SC1>(__float_as_uint(v), rs, l < 16 ? l * 4 : 0x7ff00000u, WS_MODV + (1280 + 64 * idxB + 16 * w) * 4);
                const unsigned vn = (unsigned)__builtin_amdgcn_update_dpp(0, (int)__float_as_uint(v), 0xB1, 0xf, 0xf, true);
                bstore4<SC1>((__float_as_uint(v) >> 16) | (vn & 0xffff0000u), rs, (l < 16 && !(l & 1)) ? l * 2 : 0x7ff00000u, WS_MODB + (1280 + 64 * idxB + 16 * w) * 2);
            }
            DRAIN();
            BAR();
            flag_put(rs, WS_FA + aidx * FS, tag, isA && tid == 0);
        }
    }

    const int g1 = 4 * (xcd >> 1) + (bi >> 3), j1 = 8 * (xcd & 1) + (bi & 7);
    const int P2 = 4 * xcd + (bi >> 3), c2 = bi & 7;
    const int g3 = g1, j3 = j1;
    const int t3 = j3 < 12 ? 7 * j3 : 84 + 6 * (j3 - 12), cnt3 = j3 < 12 ? 7 : 6;
    u32x4 w2f[14][5];
    const int s4 = __builtin_amdgcn_readfirstlane(w < 2 ? 14 * w : 28 + 13 * (w - 2)), cnt4 = __builtin_amdgcn_readfirstlane(w < 2 ? 14 : 13);
    const unsigned w2b = __builtin_amdgcn_readfirstlane(WS_W2F + (c2 * 54 + s4) * 5 * 1024);
    auto step4 = [&](int i) { return i < 13 ? i : cnt4 - 1; };
    auto w2_ld = [&](int f, unsigned lo) { w2f[f / 5][f % 5] = bload16<SC1>(rs, lo, w2b + step4(f / 5) * 5120 + (f % 5) * 1024); };
    constexpr int L2_WO = 61440, L2_X = 46080;
    {
        LANE_IDS();
        const int T0 = 8 * g1 + 2 * w, TH = g1 == 0 ? 0 : 8 * g1 - 1;
        const unsigned xb0 = WS_XNF + T0 * 20480, xbh = WS_XNF + TH * 20480 + (w >> 1) * 1024, wb = WS_WQF + j1 * 163840 + w * 4096;
        const unsigned wob = WS_WOF + c2 * 102400;
        u32x4 xr[10][4], xh[10];
        auto w_dma = [&](int G, int k) { dma16s(lds, (G & 3) * 16384 + w * 4096 + k * 1024, rs, lo16, wb + G * 16384 + k * 1024); };
        auto x_ld = [&](int G, int i) { xr[G][i] = bload16<SC1>(rs, lo16, xb0 + (i & 1) * 20480 + (2 * G + (i >> 1)) * 1024); };
        if (w == 0) {
            unsigned off[1] = {l == 0 ? WS_FC + TH * FS : WS_FC + (8 * g1 + ((l - 1) & 7)) * FS};
            poll_words<1>(ws, off, l < 9, tag);
        }
        BAR();
#pragma unroll
        for (int G = 0; G < 4; ++G) {
#pragma unroll
            for (int i = 0; i < 4; ++i) x_ld(G, i);
            xh[G] = bload16<SC1>(rs, lo16, xbh + G * 2048);
        }
        SB();
        if (w == 0) {
            unsigned off[1] = {WS_FA + ((j1 >> 1) * 20 + (l < 20 ? l : 0)) * FS};
            poll_words<1>(ws, off, l < 20, tag);
        }
        DRAIN();
        if (w > 0) dtask_read(dout, lds, L0_D, w, l);
        else {
#pragma unroll
            for (int i = 0; i < 8; ++i) dout.f[i] = (u32x4){0u, 0u, 0u, 0u};
        }
        BAR();
#pragma unroll
        for (int G = 0; G < 3; ++G)
#pragma unroll
            for (int k = 0; k < 4; ++k) w_dma(G, k);
        WAITV(4);
        BAR();
        f32x4 acc[2][8], hacc[3];
#pragma unroll
        for (int i = 0; i < 8; ++i) { acc[0][i] = (f32x4){0, 0, 0, 0}; acc[1][i] = (f32x4){0, 0, 0, 0}; }
#pragma unroll
        for (int i = 0; i < 3; ++i) hacc[i] = (f32x4){0, 0, 0, 0};
        u32x4 wf[2][8];
#pragma unroll
        for (int n = 0; n < 8; ++n) wf[0][n] = LD16(n * 1024 + lo16);
        const unsigned hoff = (w >> 1) * 8192 + ((w & 1) ? 5 : 2) * 1024 + lo16;
        f32x4 bp4[3]; u32x4 fdv;
        constexpr int L1_BPART = 40960;
        SB();
#pragma unroll
        for (int G = 0; G < 10; ++G) {
            u32x4 hw[3];
#pragma unroll
            for (int st = 0; st < 2; ++st) {
                const int s = 2 * G + st;
#pragma unroll
                for (int n = 0; n < 8; ++n) {
                    if (s + 1 < 20) wf[(s + 1) & 1][n] = LD16((((s + 1) >> 1) & 3) * 16384 + ((s + 1) & 1) * 8192 + n * 1024 + lo16);
                    if (st == 0 && n < 3) hw[n] = LD16((G & 3) * 16384 + hoff + n * 1024);
                    if (n < 5) { acc[0][n] = mfma(wf[s & 1][n], xr[G][st * 2], acc[0][n]); acc[1][n] = mfma(wf[s & 1][n], xr[G][st * 2 + 1], acc[1][n]); }
                    else { acc[0][n] = mfma(xr[G][st * 2], wf[s & 1][n], acc[0][n]); acc[1][n] = mfma(xr[G][st * 2 + 1], wf[s & 1][n], acc[1][n]); }
                    if (st == 1 && n >= 5) hacc[n - 5] = mfma(hw[n - 5], xh[G], hacc[n - 5]);
                    if (st == 0) {
                        if (G + 3 < 10 && (n & 1) == 0) w_dma(G + 3, n >> 1);
                        if (G + 4 < 10 && n == 7) xh[G + 4] = bload16<SC1>(rs, lo16, xbh + (G + 4) * 2048);
                    } else {
                        if (G + 4 < 10 && (n & 1) == 0) x_ld(G + 4, n >> 1);
                    }
                    if (G == 7 && st == 0 && n < 3) bp4[n] = asf4(bload16<SC1>(rs, (n < 2 || tid < 128) ? ((tid >> 5) + 8 * n) * 8192 + (tid & 31) * 16 : 0x7ff00000u, WS_BP + j1 * 512));
                    if (G == 9 && st == 0 && n == 0) {
                        f32x4 bs = (bp4[0] + bp4[1]) + bp4[2];
#pragma unroll
                        for (int r = 0; r < 4; ++r) { auto sw = __builtin_amdgcn_permlane32_swap(__float_as_uint(bs[r]), __float_as_uint(bs[r]), false, false); bs[r] = __uint_as_float(sw[0]) + __uint_as_float(sw[1]); }
                        STF4(L1_BPART + w * 1024 + lo16, bs);
                    }
                    if (G == 9 && st == 1 && n == 0) fdv = bload16<SC1>(rs, lo16, WS_FD);
                    if (n == 3) {
                        if (s == 0) dtask_frag<0>(dout, rs, l);
                        if (s == 1) dtask_frag<1>(dout, rs, l);
                        if (s == 2) dtask_frag<2>(dout, rs, l);
                        if (s == 3) dtask_frag<3>(dout, rs, l);
                        if (s == 4) dtask_frag<4>(dout, rs, l);
                        if (s == 5) dtask_frag<5>(dout, rs, l);
                        if (s == 6) dtask_frag<6>(dout, rs, l);
                        if (s == 7) dtask_frag<7>(dout, rs, l);
                    }
                    SB();
                }
            }
            if (G == 0) WAITV(11); else if (G <= 3) WAITV(17); else if (G == 4) WAITV(11); else if (G == 5) WAITV(14); else if (G == 6) WAITV(9); else if (G == 7) WAITV(3);
            BAR();
            if (G == 4) flag_put(rs, WS_FD + b * 4, tag, tid == 0);
        }
        constexpr int L1_BIAS = 6144, L1_HP = 0, L1_EX = 8192, L1_VT = 6656;
        if (w == 0) {
            check_all_blocks(ws, rs, WS_FD, fdv, tag);
            if (b == 0 && l == 0) flag_store(ws, WS_EPOCH, tag);
            if (l < 32) STF4(L1_BIAS + l * 16, (LDF4(L1_BPART + l * 16) + LDF4(L1_BPART + 1024 + l * 16)) + (LDF4(L1_BPART + 2048 + l * 16) + LDF4(L1_BPART + 3072 + l * 16)));
        }
        if (w >= 2) {
#pragma unroll
            for (int i = 0; i < 3; ++i) STF4(L1_HP + ((w & 1) * 3 + i) * 1024 + lo16, hacc[i]);
        }
        BAR();
        auto wo_dma = [&](int k) { dma16s(lds, L2_WO + w * 25600 + k * 1024, rs, lo16, wob + w * 25600 + k * 1024); };
        const rsrc_t rx = mk_rsrc(p.x, 2048 * 1280);
        unsigned xvo[3];
#pragma unroll
        for (int k = 0; k < 3; ++k) { const int n = (w * 3 + k) * 64 + l, row = n / 10; xvo[k] = n < 640 ? row * 1280 + (n - 10 * row) * 16 : 0x7ff00000u; }
        const unsigned xlo = L2_X + w * 3072;
        auto x_dma2 = [&](int k) { dma<0>(lds, xlo + k * 1024, rx, xvo[k], P2 * 64 * 1280 + c2 * 160); };
        f32x4 bq[5]; float bv[3];
#pragma unroll
        for (int n = 0; n < 5; ++n) bq[n] = LDF4(L1_BIAS + (n * 16 + 4 * qd) * 4);
#pragma unroll
        for (int n = 0; n < 3; ++n) bv[n] = LDF(L1_BIAS + ((5 + n) * 16 + t) * 4);
        TileKV kv[2]; u32x2 qa[2][2], xq[2];
        auto bg_wo = [&](int k) { SB(); wo_dma(k); SB(); };
#pragma unroll
        for (int tt = 1; tt >= 0; --tt) {
            kv[tt].ka0 = pack4(acc[tt][2] + bq[2]); kv[tt].ka1 = pack4(acc[tt][3] + bq[3]);
            bg_wo(4 * (1 - tt));
            xq[tt] = pack4(acc[tt][4] + bq[4]);
            kv[tt].k2[0] = swap32(xq[tt][0]); kv[tt].k2[1] = swap32(xq[tt][1]);
            bg_wo(4 * (1 - tt) + 1);
            kv[tt].va0 = pack4(acc[tt][5] + bv[0]); kv[tt].va1 = pack4(acc[tt][6] + bv[1]);
            bg_wo(4 * (1 - tt) + 2);
            kv[tt].z = pack4(acc[tt][7] + bv[2]);
            if (tt == 1) {
                const unsigned e = L1_EX + (w + 1) * 3072 + lo16;
                ST16(e, ((u32x4){kv[1].ka0[0], kv[1].ka0[1], kv[1].ka1[0], kv[1].ka1[1]}));
                ST16(e + 1024, ((u32x4){kv[1].k2[0], kv[1].k2[1], kv[1].va0[0], kv[1].va0[1]}));
                ST16(e + 2048, ((u32x4){kv[1].va1[0], kv[1].va1[1], kv[1].z[0], kv[1].z[1]}));
            }
            qa[tt][0] = pack4(acc[tt][0] + bq[0]); qa[tt][1] = pack4(acc[tt][1] + bq[1]);
            bg_wo(4 * (1 - tt) + 3);
        }
        if (w == 0) {
            const u32x2 k0 = pack4(hacc[0] + LDF4(L1_HP + lo16) + bq[2]), k1 = pack4(hacc[1] + LDF4(L1_HP + 1024 + lo16) + bq[3]);
            const u32x2 xx = pack4(hacc[2] + LDF4(L1_HP + 2048 + lo16) + bq[4]);
            ST16(L1_EX + lo16, ((u32x4){k0[0], k0[1], k1[0], k1[1]}));
            ST8(L1_EX + 1024 + lo16, ((u32x2){swap32(xx[0]), swap32(xx[1])}));
        } else if (w == 1) {
#pragma unroll
            for (int i = 0; i < 3; ++i) {
                const f32x4 v = hacc[i] + LDF4(L1_HP + (3 + i) * 1024 + lo16) + LDF4(L1_BIAS + ((5 + i) * 16 + 4 * qd) * 4);
                const u32x2 pk = pack4(v);
                LAS u16* d = (LAS u16*)((LAS char*)lds + L1_VT + i * 512 + (4 * qd) * 32 + t * 2);
                d[0] = (u16)pk[0]; d[16] = (u16)(pk[0] >> 16); d[32] = (u16)pk[1]; d[48] = (u16)(pk[1] >> 16);
            }
        }
        const unsigned ob = WS_OF + T0 * 20480;
        const AttnMask am = attn_mask(t, qd);
        u32x4 om; u32x2 ol;
        SB();
        attn_unit(qa[1], xq[1], kv[1], kv[0], true, am, qd, om, ol, [&](int k) { bg_wo(8 + k); });
        SB();
        bstore16<SC1>(om, rs, lo16, ob + 20480 + j1 * 1024);
        bstore8<SC1>(ol, rs, qd < 2 ? ((j1 & 3) * 16 + t) * 16 + 8 * qd : 0x7ff00000u, ob + 20480 + (16 + (j1 >> 2)) * 1024);
        BAR();
        TileKV pv;
        {
            const unsigned e = L1_EX + w * 3072 + lo16;
            const u32x4 a = LD16(e), bb = LD16(e + 1024), cc = LD16(e + 2048);
            pv.ka0 = (u32x2){a[0], a[1]}; pv.ka1 = (u32x2){a[2], a[3]}; pv.k2 = (u32x2){bb[0], bb[1]}; pv.va0 = (u32x2){bb[2], bb[3]};
            pv.va1 = (u32x2){cc[0], cc[1]}; pv.z = (u32x2){cc[2], cc[3]};
            if (w == 0) { pv.va0 = LD8(L1_VT + t * 32 + qd * 8); pv.va1 = LD8(L1_VT + 512 + t * 32 + qd * 8); pv.z = LD8(L1_VT + 1024 + t * 32 + qd * 8); }
        }
        SB();
        attn_unit(qa[0], xq[0], kv[0], pv, !(g1 == 0 && w == 0), am, qd, om, ol, [&](int k) { if (k < 7) bg_wo(18 + k); else { SB(); x_dma2(k - 7); SB(); } });
        SB();
        bstore16<SC1>(om, rs, lo16, ob + j1 * 1024);
        bstore8<SC1>(ol, rs, qd < 2 ? ((j1 & 3) * 16 + t) * 16 + 8 * qd : 0x7ff00000u, ob + (16 + (j1 >> 2)) * 1024);
        publish(rs, WS_F1 + (g1 * 16 + j1) * FS, tag);
    }

    u32x2 x1b[5], b1v[7];
    u32x4 vf[5];
    {
        LANE_IDS();
        const int T = 4 * P2 + w;
        constexpr int L2_MV = 57344;
        constexpr int L2_TB = 58304, L2_B1 = 60864;
        const u32x4 mvl = bload16<SC1>(rs, (1280 + ((tid / 20) % 3) * 640 + 80 * c2 + (tid % 20) * 4) * 4, WS_MODV);
        if (w < 3) { if (w < 2 || l < 32) dma16s(lds, L2_TB + w * 1024, rs, lo16, WS_MODB + 1920 * 2 + w * 1024); }
        else if (l < 14) dma<0>(lds, L2_B1, mk_rsrc(p.b1, 1728 * 2), lo16, t3 * 32);
#pragma unroll
        for (int f = 0; f < 12; ++f) w2_ld(f, lo16);
        SB();
        if (tid < 60) ST16(L2_MV + tid * 16, mvl);
        WAITV(12);
        BAR();
        u32x2 xv[5], va2[5], vb2[5];
#pragma unroll
        for (int i = 0; i < 5; ++i) {
            const int sr = 5 * w + i;
            const int ta = sr < 16 ? 5 * (sr >> 1) + 2 * (sr & 1) : 10 * (sr - 16) + 4, tb = ta + (sr < 16 ? 1 : 5);
            const unsigned sec = L2_TB + (t == 0 ? 1280 : 0) + qd * 8;
            va2[i] = LD8(sec + ta * 32); vb2[i] = LD8(sec + tb * 32);
        }
#pragma unroll
        for (int n = 0; n < 7; ++n) b1v[n] = LD8(L2_B1 + n * 32 + qd * 8);
#pragma unroll
        for (int n = 0; n < 5; ++n) xv[n] = LD8(L2_X + (16 * w + t) * 160 + n * 32 + qd * 8);
        SB();
#pragma unroll
        for (int i = 0; i < 5; ++i) {
            const unsigned cst = t == 2 ? 0x3f803f80u : 0u;
            vf[i] = (u32x4){t < 2 ? va2[i][0] : cst, t < 2 ? va2[i][1] : cst, t < 2 ? vb2[i][0] : cst, t < 2 ? vb2[i][1] : cst};
        }
        asm volatile("" : "+v"(vf[0]), "+v"(vf[1]), "+v"(vf[2]), "+v"(vf[3]), "+v"(vf[4]));
        asm volatile("" : "+v"(b1v[0]), "+v"(b1v[1]), "+v"(b1v[2]), "+v"(b1v[3]), "+v"(b1v[4]), "+v"(b1v[5]), "+v"(b1v[6]), "+v"(xv[0]), "+v"(xv[1]), "+v"(xv[2]), "+v"(xv[3]), "+v"(xv[4]));
        if (w == 0) {
            unsigned off[1] = {WS_F1 + ((P2 >> 1) * 16 + (l & 15)) * FS};
            poll_words<1>(ws, off, l < 16, tag);
        }
        BAR();
        constexpr int PW = 8;
        u32x4 tk[20];
#pragma unroll
        for (int s = 0; s < PW; ++s) tk[s] = bload16<SC1>(rs, lo16, WS_OF + (T * 20 + s) * 1024);
        f32x4 acc[5];
#pragma unroll
        for (int n = 0; n < 5; ++n) acc[n] = (f32x4){0, 0, 0, 0};
        unsigned wb0 = L2_WO + lo16, wb1 = L2_WO + 51200 + lo16; OPAQUE(wb0); OPAQUE(wb1);
        auto wo_rd = [&](int s, int n) { return s < 10 ? LD16(wb0 + (s * 5 + n) * 1024) : LD16(wb1 + ((s - 10) * 5 + n) * 1024); };
        u32x4 wf[3][5];
#pragma unroll
        for (int n = 0; n < 5; ++n) { wf[0][n] = wo_rd(0, n); wf[1][n] = wo_rd(1, n); }
        SB();
#pragma unroll
        for (int s = 0; s < 20; ++s) {
            if (s + PW < 20) tk[s + PW] = bload16<SC1>(rs, lo16, WS_OF + (T * 20 + s + PW) * 1024);
            if (s + 2 < 20) {
#pragma unroll
                for (int n = 0; n < 5; ++n) wf[(s + 2) % 3][n] = wo_rd(s + 2, n);
            }
#pragma unroll
            for (int n = 0; n < 5; ++n) acc[n] = mfma(wf[s % 3][n], tk[s], acc[n]);
#pragma unroll
            for (int n = 0; n < 5; ++n) { if (s + 2 < 20) SGB(0x100, 1); SGB(0x8, 1); if (n == 1 && s + PW < 20) SGB(0x20, 1); }
            SB();
        }
        BAR();
        const unsigned w1src = WS_W1F + t3 * 20480, w1last = (cnt3 == 7 ? 6 : 0) * 20480;
        auto w1_dma = [&](int base_step, int i) {
            const int n = i % 7;
            dma16s(lds, base_step * 7168 + i * 1024, rs, lo16, w1src + (n == 6 ? w1last : n * 20480) + (base_step + i / 7) * 1024);
        };
        float st[6] = {0, 0, 0, 0, 0, 0};
        unsigned mvb = L2_MV + qd * 16; OPAQUE(mvb);
        f32x4 mvr[2][3];
#pragma unroll
        for (int k = 0; k < 3; ++k) mvr[0][k] = LDF4(mvb + k * 320);
        u32x2 ub[5];
        const unsigned ufb = WS_UF + T * 20480;
#pragma unroll
        for (int n = 0; n < 5; ++n) {
            if (n + 1 < 5) {
#pragma unroll
                for (int k = 0; k < 3; ++k) mvr[(n + 1) & 1][k] = LDF4(mvb + k * 320 + (n + 1) * 64);
            }
            const f32x4 ga_n = mvr[n & 1][0], s2_n = mvr[n & 1][1], g2_n = mvr[n & 1][2];
            float x1[4] = {lo_f(xv[n][0]), hi_f(xv[n][0]), lo_f(xv[n][1]), hi_f(xv[n][1])};
#pragma unroll
            for (int r = 0; r < 4; ++r) x1[r] = fmaf(ga_n[r], acc[n][r], x1[r]);
            x1b[n] = (u32x2){pack2(x1[0], x1[1]), pack2(x1[2], x1[3])};
            const float xr[4] = {lo_f(x1b[n][0]), hi_f(x1b[n][0]), lo_f(x1b[n][1]), hi_f(x1b[n][1])};
            ub[n] = (u32x2){pack2(xr[0] * g2_n[0], xr[1] * g2_n[1]), pack2(xr[2] * g2_n[2], xr[3] * g2_n[3])};
            const float ur[4] = {lo_f(ub[n][0]), hi_f(ub[n][0]), lo_f(ub[n][1]), hi_f(ub[n][1])};
#pragma unroll
            for (int r = 0; r < 4; ++r) {
                st[0] += xr[r]; st[1] = fmaf(xr[r], xr[r], st[1]); st[2] += ur[r]; st[3] = fmaf(ur[r], g2_n[r], st[3]);
                st[4] = fmaf(ur[r], ur[r], st[4]); st[5] = fmaf(ur[r], s2_n[r], st[5]);
            }
            if (n == 1) bstore16<SC1>((u32x4){ub[0][0], ub[0][1], ub[1][0], ub[1][1]}, rs, lo16, ufb + (2 * c2) * 1024);
            if (n == 3) bstore16<SC1>((u32x4){ub[2][0], ub[2][1], ub[3][0], ub[3][1]}, rs, lo16, ufb + (2 * c2 + 1) * 1024);
            if (n == 4) bstore8<SC1>(ub[4], rs, lo16 + 8 * (c2 & 1), ufb + (16 + (c2 >> 1)) * 1024);
#pragma unroll
            for (int k = 0; k < 3; ++k) if (3 * n + k < 14) w1_dma(2 * w, 3 * n + k);
            SGB(0x20, 1); SGB(0x1, 18); SGB(0x20, 1); SGB(0x1, 18); SGB(0x20, 1); SGB(0x1, 24);
            SB();
        }
#pragma unroll
        for (int i = 0; i < 6; ++i) st[i] = red_add_q(st[i]);
        if (qd < 2) bstore16<SC1>(asu4(qd == 0 ? (f32x4){st[0], st[1], st[2], st[3]} : (f32x4){st[4], st[5], 0.f, 0.f}), rs, qd * 256 + t * 16, WS_ST + (T * 8 + c2) * 512);
#pragma unroll
        for (int n = 0; n < 5; ++n) asm volatile("" : "+v"(x1b[n]));
        publish(rs, WS_F2 + (P2 * 8 + c2) * FS, tag);
#pragma unroll
        for (int i = 0; i < 21; ++i) w1_dma(8 + 3 * w, i);
    }

    {
        LANE_IDS();
        constexpr int L3_GSC = 143360;
        const int T0 = 8 * g3 + 2 * w;
        if (w == 0) {
            unsigned off[1] = {WS_F2 + ((2 * g3 + ((l >> 3) & 1)) * 8 + (l & 7)) * FS};
            poll_words<1>(ws, off, l < 16, tag);
        }
        DRAIN();
        BAR();
        constexpr int PW = 4;
        u32x4 tk[20][2];
        u32x4 spa[2][2]; u32x2 spb[2][2];
        float al[2], be[2], gm[2], de[2], ts[2][6];
        f32x4 red4[4], gr0, gr1;
#pragma unroll
        for (int s = 0; s < PW; ++s) { const unsigned o = WS_UF + (T0 * 20 + s) * 1024; tk[s][0] = bload16<SC1>(rs, lo16, o); tk[s][1] = bload16<SC1>(rs, lo16, o + 20480); }
        SB();
        {
            f32x4 va[7], gram = {0, 0, 0, 0};
            u32x4 wv[2][7];
            unsigned vb = 5 * w * 7168 + lo16; OPAQUE(vb);
#pragma unroll
            for (int n = 0; n < 7; ++n) { va[n] = (f32x4){0, 0, 0, 0}; wv[0][n] = LD16(vb + n * 1024); }
            SB();
#pragma unroll
            for (int i = 0; i < 5; ++i) {
                if (i + 1 < 5) {
#pragma unroll
                    for (int n = 0; n < 7; ++n) wv[(i + 1) & 1][n] = LD16(vb + ((i + 1) * 7 + n) * 1024);
                }
#pragma unroll
                for (int n = 0; n < 7; ++n) va[n] = mfma(wv[i & 1][n], vf[i], va[n]);
                gram = mfma(vf[i], vf[i], gram);
#pragma unroll
                for (int n = 0; n < 7; ++n) { if (i + 1 < 5) SGB(0x100, 1); SGB(0x8, 1); }
                SB();
            }
            if (t < 3) {
#pragma unroll
                for (int n = 0; n < 7; ++n) STF4(L3_GSC + ((w * 8 + n) * 3 + t) * 64 + qd * 16, va[n]);
                if (qd == 0) STF4(L3_GSC + ((w * 8 + 7) * 3 + t) * 64, gram);
            }
        }
        SB();
        f32x4 acc[2][7];
#pragma unroll
        for (int n = 0; n < 7; ++n) { acc[0][n] = (f32x4){0, 0, 0, 0}; acc[1][n] = (f32x4){0, 0, 0, 0}; }
        unsigned wb0 = lo16, wb1 = 57344 + lo16, wb2 = 114688 + lo16; OPAQUE(wb0); OPAQUE(wb1); OPAQUE(wb2);
        auto w1_rd = [&](int s, int n) { return s < 8 ? LD16(wb0 + (s * 7 + n) * 1024) : s < 16 ? LD16(wb1 + ((s - 8) * 7 + n) * 1024) : LD16(wb2 + ((s - 16) * 7 + n) * 1024); };
        u32x4 wf[2][7];
#pragma unroll
        for (int n = 0; n < 7; ++n) wf[0][n] = w1_rd(0, n);
        SB();
#pragma unroll
        for (int s = 0; s < 20; ++s) {
            int nv = 0;
            if (s + PW < 20) { const unsigned o = WS_UF + (T0 * 20 + s + PW) * 1024; tk[s + PW][0] = bload16<SC1>(rs, lo16, o); tk[s + PW][1] = bload16<SC1>(rs, lo16, o + 20480); nv += 2; }
            if (s < 16) { w2_ld(12 + s, lo16); nv += 1; }
            if (s >= 16) {
#pragma unroll
                for (int k = 0; k < 3; ++k) w2_ld(28 + (s - 16) * 3 + k, lo16);
                nv += 3;
            }
            if (s == 10 || s == 11) {
#pragma unroll
                for (int e = 0; e < 2; ++e) {
                    spa[s - 10][e] = bload16<SC1>(rs, (2 * qd + e) * 512 + t * 16, WS_ST + (T0 + s - 10) * 4096);
                    spb[s - 10][e] = bload8<SC1>(rs, (2 * qd + e) * 512 + 256 + t * 16, WS_ST + (T0 + s - 10) * 4096);
                }
                nv += 4;
            }
            int nu = 0;
            if (s == 3) {
                const int r96 = tid < 96 ? tid : 0;
#pragma unroll
                for (int u = 0; u < 4; ++u) red4[u] = LDF4(L3_GSC + (u * 96 + r96) * 16);
            }
            if (s == 4) { STF4(L3_GSC + 6144 + (tid < 96 ? tid : 96 + (tid & 31)) * 16, (red4[0] + red4[1]) + (red4[2] + red4[3])); nu = 1; }
            if (s == 9) { gr0 = LDF4(L3_GSC + 6144 + 84 * 16); gr1 = LDF4(L3_GSC + 6144 + 88 * 16); }
            if (s == 13 || s == 14) {
                const int tt = s - 13;
                const f32x4 a0 = asf4(spa[tt][0]) + asf4(spa[tt][1]);
                const float b00 = __uint_as_float(spb[tt][0][0]) + __uint_as_float(spb[tt][1][0]), b01 = __uint_as_float(spb[tt][0][1]) + __uint_as_float(spb[tt][1][1]);
                ts[tt][0] = red_add_q(a0[0]); ts[tt][1] = red_add_q(a0[1]); ts[tt][2] = red_add_q(a0[2]); ts[tt][3] = red_add_q(a0[3]); ts[tt][4] = red_add_q(b00); ts[tt][5] = red_add_q(b01);
                nu = 4;
            }
            if (s == 15) {
                const float Gg = gr0[0], Ggs = gr0[1], Gs = gr0[2], Sss = gr1[1], Ss = gr1[2], iD = 1.f / 640.f;
#pragma unroll
                for (int tt = 0; tt < 2; ++tt) {
                    const float m1 = ts[tt][0] * iD, v1 = fmaxf(ts[tt][1] * iD - m1 * m1, 0.f), r1v = __builtin_amdgcn_rsqf(v1 + 1e-5f);
                    const float m2 = (r1v * (ts[tt][2] - m1 * Gs) + Ss) * iD;
                    const float e2 = (r1v * r1v * (ts[tt][4] - 2.f * m1 * ts[tt][3] + m1 * m1 * Gg) + 2.f * r1v * (ts[tt][5] - m1 * Ggs) + Sss) * iD;
                    const float r2v = __builtin_amdgcn_rsqf(fmaxf(e2 - m2 * m2, 0.f) + 1e-5f);
                    al[tt] = r1v * r2v; be[tt] = -r1v * r2v * m1; gm[tt] = r2v; de[tt] = -r2v * m2;
                }
                nu = 4;
            }
            if (s + 1 < 20) {
#pragma unroll
                for (int n = 0; n < 7; ++n) wf[(s + 1) & 1][n] = w1_rd(s + 1, n);
            }
#pragma unroll
            for (int n = 0; n < 7; ++n) { acc[0][n] = mfma(wf[s & 1][n], tk[s][0], acc[0][n]); acc[1][n] = mfma(wf[s & 1][n], tk[s][1], acc[1][n]); }
#pragma unroll
            for (int n = 0; n < 7; ++n) {
                if (s + 1 < 20) SGB(0x100, 1);
                SGB(0x8, 1);
                if (nu == 1) SGB(0x402, 1);
                if (nu == 4) SGB(0x402, 4);
                SGB(0x8, 1);
                if (nu == 1) SGB(0x402, 1);
                if (nu == 4) SGB(0x402, 4);
                if (n < nv) SGB(0x20, 1);
            }
            SB();
            if (s == 2 || s == 8) BAR();
        }
        u32x2 hb[2][7];
        unsigned gsb = L3_GSC + 6144 + qd * 16; OPAQUE(gsb);
        f32x4 gsc[2][3];
#pragma unroll
        for (int k = 0; k < 3; ++k) gsc[0][k] = LDF4(gsb + 64 * k);
#pragma unroll
        for (int n = 0; n < 7; ++n) {
#pragma unroll
            for (int k = 0; k < 2; ++k) w2_ld(40 + n * 2 + k, lo16);
            if (n + 1 < 7) {
#pragma unroll
                for (int k = 0; k < 3; ++k) gsc[(n + 1) & 1][k] = LDF4(gsb + (n + 1) * 192 + 64 * k);
            }
            const f32x4 G = gsc[n & 1][0], Sv = gsc[n & 1][1], C = gsc[n & 1][2];
            const float bb[4] = {lo_f(b1v[n][0]), hi_f(b1v[n][0]), lo_f(b1v[n][1]), hi_f(b1v[n][1])};
#pragma unroll
            for (int tt = 0; tt < 2; ++tt) {
                float hv[4];
#pragma unroll
                for (int r = 0; r < 4; ++r) {
                    float z = fmaf(gm[tt], Sv[r], bb[r]); z = fmaf(be[tt], G[r], z); z = fmaf(de[tt], C[r], z); z = fmaf(al[tt], acc[tt][n][r], z);
                    const float u = z * fmaf(z * z, -0.044715f * 1.5957691216057308f * 1.4426950408889634f, -1.5957691216057308f * 1.4426950408889634f);
                    hv[r] = z * __builtin_amdgcn_rcpf(1.f + __builtin_amdgcn_exp2f(u));
                }
                hb[tt][n] = (u32x2){pack2(hv[0], hv[1]), pack2(hv[2], hv[3])};
            }
            SGB(0x20, 1); SGB(0x1, 40); SGB(0x20, 1); SGB(0x1, 60);
            SB();
#pragma unroll
            for (int tt = 0; tt < 2; ++tt) {
                const unsigned hbase = WS_HF + (T0 + tt) * 54 * 1024;
                if (n & 1) bstore16<SC1>((u32x4){hb[tt][n - 1][0], hb[tt][n - 1][1], hb[tt][n][0], hb[tt][n][1]}, rs, lo16, hbase + (3 * j3 + (n >> 1)) * 1024);
                if (n == 6 && j3 < 12) bstore8<SC1>(hb[tt][6], rs, lo16 + 8 * (j3 & 1), hbase + (48 + (j3 >> 1)) * 1024);
            }
            SB();
        }
        publish(rs, WS_F3 + (g3 * 16 + j3) * FS, tag);
    }

    {
        LANE_IDS();
        f32x4 gb[5]; u32x2 b2v[5];
#pragma unroll
        for (int f = 54; f < 70; ++f) w2_ld(f, lo16);
        SB();
        if (w == 0) {
            unsigned off[1] = {WS_F3 + ((P2 >> 1) * 16 + (l & 15)) * FS};
            poll_words<1>(ws, off, l < 16, tag);
        }
        BAR();
        constexpr int PD = 3;
        u32x4 tk[14][4];
        const unsigned tokb = __builtin_amdgcn_readfirstlane(WS_HF + (4 * P2 * 54 + s4) * 1024);
        auto issue = [&](int i) {
            const int s = step4(i);
#pragma unroll
            for (int a = 0; a < 4; ++a) tk[i][a] = bload16<SC1>(rs, lo16, tokb + (((a + w) & 3) * 54 + s) * 1024);
        };
#pragma unroll
        for (int i = 0; i < PD; ++i) issue(i);
        f32x4 acc[4][5];
#pragma unroll
        for (int a = 0; a < 4; ++a)
#pragma unroll
            for (int n = 0; n < 5; ++n) acc[a][n] = (f32x4){0, 0, 0, 0};
        SB();
#pragma unroll
        for (int i = 0; i < 14; ++i) {
            if (i < 13 || w < 2) {
                if (i + PD < 14) issue(i + PD);
                if (i == 11) {
#pragma unroll
                    for (int n = 0; n < 5; ++n) {
                        const int f = 80 * c2 + 16 * n;
                        gb[n] = asf4(bload16<SC1>(rs, qd * 16, WS_MODV + (3200 + f) * 4));
                        b2v[n] = *(const GAS u32x2*)(p.b2 + f + 4 * qd);
                    }
                }
#pragma unroll
                for (int a = 0; a < 4; ++a)
#pragma unroll
                    for (int n = 0; n < 5; ++n) acc[a][n] = mfma(w2f[i][n], tk[i][a], acc[a][n]);
#pragma unroll
                for (int a = 0; a < 4; ++a) { SGB(0x8, 2); if (i == 11) SGB(0x20, 3); else SGB(0x20, 1); SGB(0x8, 3); }
            }
            SB();
        }
#pragma unroll
        for (int a = 1; a < 4; ++a) {
            unsigned d = ((((a + w) & 3) * 3 + (3 - a)) * 5) * 1024 + lo16; OPAQUE(d);
#pragma unroll
            for (int n = 0; n < 5; ++n) STF4(d + n * 1024, acc[a][n]);
        }
        BAR();
        constexpr int L4_OUT = 61440;
        unsigned rb = w * 15360 + lo16; OPAQUE(rb);
        f32x4 part[5][3];
#pragma unroll
        for (int n = 0; n < 5; ++n)
#pragma unroll
            for (int u = 0; u < 3; ++u) part[n][u] = LDF4(rb + (u * 5 + n) * 1024);
        SB();
#pragma unroll
        for (int n = 0; n < 5; ++n) {
            const f32x4 v = (acc[0][n] + part[n][0]) + (part[n][1] + part[n][2]);
            const float x1[4] = {lo_f(x1b[n][0]), hi_f(x1b[n][0]), lo_f(x1b[n][1]), hi_f(x1b[n][1])};
            const float bv[4] = {lo_f(b2v[n][0]), hi_f(b2v[n][0]), lo_f(b2v[n][1]), hi_f(b2v[n][1])};
            float o[4];
#pragma unroll
            for (int r = 0; r < 4; ++r) o[r] = fmaf(gb[n][r], v[r] + bv[r], x1[r]);
            ST8(L4_OUT + (16 * w + t) * 176 + n * 32 + qd * 8, ((u32x2){pack2(o[0], o[1]), pack2(o[2], o[3])}));
        }
        asm volatile("s_waitcnt lgkmcnt(0)" ::: "memory");
        const rsrc_t ro = mk_rsrc(p.out, 2048 * 1280);
#pragma unroll
        for (int k = 0; k < 3; ++k) {
            const int ch = l + 64 * k;
            if (ch < 160) {
                const int row = ch / 10, cc = ch % 10;
                const u32x4 v = LD16(L4_OUT + (16 * w + row) * 176 + cc * 16);
                bstore16<0>(v, ro, row * 1280 + cc * 16, (16 * (4 * P2 + w)) * 1280 + c2 * 160);
            }
        }
    }
    asm volatile(".Ladaln_end:" ::: "memory");
}

extern "C" void kernel_launch(void* const* d_in, const int* in_sizes, int n_in, void* d_out, int out_size, void* d_ws, size_t ws_size, hipStream_t stream) {
    Params p;
    p.x = (const u16*)d_in[0]; p.wqkv = (const u16*)d_in[1]; p.wo = (const u16*)d_in[2]; p.w1 = (const u16*)d_in[4]; p.b1 = (const u16*)d_in[5];
    p.w2 = (const u16*)d_in[6]; p.b2 = (const u16*)d_in[7]; p.c = (const u16*)d_in[8]; p.wm = (const u16*)d_in[9];
    p.out = (u16*)d_out; p.ws = (char*)d_ws;
    (void)hipFuncSetAttribute((const void*)adaLN_block_kernel, hipFuncAttributeMaxDynamicSharedMemorySize, LDS_BYTES);
    adaLN_block_kernel<<<256, 256, LDS_BYTES, stream>>>(p);
}
```

```cpp
#define DIAG 1
#define DIAGB 0
#include <hip/hip_runtime.h>
#include <stdint.h>

typedef unsigned short u16;
typedef float f32x4 __attribute__((ext_vector_type(4)));
typedef float f32x2 __attribute__((ext_vector_type(2)));
typedef __bf16 bf16x8 __attribute__((ext_vector_type(8)));
typedef __bf16 bf16x2 __attribute__((ext_vector_type(2)));
typedef unsigned u32x4 __attribute__((ext_vector_type(4)));
typedef unsigned u32x2 __attribute__((ext_vector_type(2)));
typedef short s16x4 __attribute__((ext_vector_type(4)));
typedef __amdgpu_buffer_rsrc_t rsrc_t;
#define LAS __attribute__((address_space(3)))
#define GAS __attribute__((address_space(1)))
#define DI __device__ __forceinline__

static constexpr unsigned FS = 64;
static constexpr unsigned WS_EPOCH = 0;
static constexpr unsigned WS_TMO   = 256;
static constexpr unsigned WS_FA    = 1024;
static constexpr unsigned WS_FC    = WS_FA + 160 * FS;
static constexpr unsigned WS_FD    = WS_FC + 128 * FS;
static constexpr unsigned WS_F1    = WS_FD + 1024;
static constexpr unsigned WS_F2    = WS_F1 + 256 * FS;
static constexpr unsigned WS_F3    = WS_F2 + 256 * FS;
static constexpr unsigned WS_FEND  = WS_F3 + 256 * FS;
static constexpr unsigned WS_MODV = 131072;
static constexpr unsigned WS_MODB = WS_MODV + 16384;
static constexpr unsigned WS_BP   = WS_MODB + 8192;
static constexpr unsigned WS_XNF  = WS_BP + 20 * 16 * 128 * 4;
static constexpr unsigned WS_WQF  = WS_XNF + 128 * 20 * 1024;
static constexpr unsigned WS_WOF  = WS_WQF + 16 * 20 * 8 * 1024;
static constexpr unsigned WS_W1F  = WS_WOF + 8 * 20 * 5 * 1024;
static constexpr unsigned WS_W2F  = WS_W1F + 108 * 20 * 1024;
static constexpr unsigned WS_OF   = WS_W2F + 8 * 54 * 5 * 1024;
static constexpr unsigned WS_UF   = WS_OF + 128 * 20 * 1024;
static constexpr unsigned WS_ST   = WS_UF + 128 * 20 * 1024;
static constexpr unsigned WS_HF   = WS_ST + 128 * 8 * 512;
static constexpr unsigned WS_END  = WS_HF + 128 * 54 * 1024;
static_assert(WS_FEND <= WS_MODV, "flags overflow");

#define B_DELAY 0
#define OUT_AUX 0
#define ROT1 0
#define ROT2 0
#define ROT3 0
#define ROT4 0
static constexpr int LDS_BYTES = 163840;
static constexpr int SC1 = 16;
static constexpr unsigned SPIN_CAP = 40000;

#define STAMP(i) do { if ((i) == 0) diag_t0 = __builtin_amdgcn_s_memrealtime(); if ((i) == DIAG) diag_t1 = __builtin_amdgcn_s_memrealtime(); } while (0)
#define LSTAMP(i)
#define LSTEP(i)
#define LSTAMP_DECL
#define LSTAMP_DUMP do { if (blockIdx.x == DIAGB) { const unsigned long long te = __builtin_amdgcn_s_memrealtime() + 20 * (diag_t1 - diag_t0); while (__builtin_amdgcn_s_memrealtime() < te) __builtin_amdgcn_s_sleep(4); } } while (0)

struct Params {
    const u16* x; const u16* wqkv; const u16* wo; const u16* w1; const u16* b1; const u16* w2; const u16* b2; const u16* c; const u16* wm;
    u16* out; char* ws;
};

DI float lo_f(unsigned d) { return __uint_as_float(d << 16); }
DI float hi_f(unsigned d) { return __uint_as_float(d & 0xffff0000u); }
DI unsigned pack2(float a, float b) { f32x2 v = {a, b}; return __builtin_bit_cast(unsigned, __builtin_convertvector(v, bf16x2)); }
DI float rbf(float a) { return lo_f(pack2(a, 0.f)); }
DI u32x2 pack4(const f32x4& v) { return (u32x2){pack2(v[0], v[1]), pack2(v[2], v[3])}; }
DI f32x4 mfma(u32x4 a, u32x4 b, f32x4 c) { return __builtin_amdgcn_mfma_f32_16x16x32_bf16(__builtin_bit_cast(bf16x8, a), __builtin_bit_cast(bf16x8, b), c, 0, 0, 0); }
DI float red_add_q(float v) {
    auto a = __builtin_amdgcn_permlane16_swap(__float_as_uint(v), __float_as_uint(v), false, false);
    v = __uint_as_float(a[0]) + __uint_as_float(a[1]);
    auto b = __builtin_amdgcn_permlane32_swap(__float_as_uint(v), __float_as_uint(v), false, false);
    return __uint_as_float(b[0]) + __uint_as_float(b[1]);
}
DI float red_max_q(float v) {
    auto a = __builtin_amdgcn_permlane16_swap(__float_as_uint(v), __float_as_uint(v), false, false);
    v = fmaxf(__uint_as_float(a[0]), __uint_as_float(a[1]));
    auto b = __builtin_amdgcn_permlane32_swap(__float_as_uint(v), __float_as_uint(v), false, false);
    return fmaxf(__uint_as_float(b[0]), __uint_as_float(b[1]));
}
template <int C> DI float dpp_add(float v) { return v + __builtin_bit_cast(float, __builtin_amdgcn_update_dpp(0, __builtin_bit_cast(int, v), C, 0xf, 0xf, true)); }
DI float red16(float v) { v = dpp_add<0xB1>(v); v = dpp_add<0x4E>(v); v = dpp_add<0x141>(v); return dpp_add<0x140>(v); }
DI unsigned swap32(unsigned v) { auto a = __builtin_amdgcn_permlane32_swap(v, v, false, false); return (threadIdx.x & 32) ? a[0] : a[1]; }
DI rsrc_t mk_rsrc(const void* p, unsigned bytes) { return __builtin_amdgcn_make_buffer_rsrc((void*)p, 0, bytes, 0x00020000); }
template <int AUX> DI u32x4 bload16(rsrc_t r, unsigned voff, unsigned soff) { return __builtin_bit_cast(u32x4, __builtin_amdgcn_raw_buffer_load_b128(r, voff, soff, AUX)); }
template <int AUX> DI u32x2 bload8(rsrc_t r, unsigned voff, unsigned soff) { return __builtin_bit_cast(u32x2, __builtin_amdgcn_raw_buffer_load_b64(r, voff, soff, AUX)); }
template <int AUX> DI unsigned bload4(rsrc_t r, unsigned voff, unsigned soff) { return __builtin_amdgcn_raw_buffer_load_b32(r, voff, soff, AUX); }
template <int AUX> DI void bstore16(u32x4 v, rsrc_t r, unsigned voff, unsigned soff) { __builtin_amdgcn_raw_buffer_store_b128(v, r, voff, soff, AUX); }
template <int AUX> DI void bstore8(u32x2 v, rsrc_t r, unsigned voff, unsigned soff) { __builtin_amdgcn_raw_buffer_store_b64(v, r, voff, soff, AUX); }
template <int AUX> DI void bstore4(unsigned v, rsrc_t r, unsigned voff, unsigned soff) { __builtin_amdgcn_raw_buffer_store_b32(v, r, voff, soff, AUX); }
DI f32x4 asf4(u32x4 v) { return __builtin_bit_cast(f32x4, v); }
DI u32x4 asu4(f32x4 v) { return __builtin_bit_cast(u32x4, v); }
#define LANE_IDS() int tz_ = threadIdx.x; asm volatile("" : "+v"(tz_)); const int tid = tz_, l = tid & 63, t = l & 15, qd = l >> 4; const unsigned lo16 = l * 16; (void)t; (void)qd; (void)lo16
#define OPAQUE(x) asm volatile("" : "+v"(x))
#define SB() __builtin_amdgcn_sched_barrier(0)
#define BAR() asm volatile("s_waitcnt lgkmcnt(0)\n\ts_barrier" ::: "memory")
#define DRAIN() asm volatile("s_waitcnt vmcnt(0)" ::: "memory")
#define SGB(mask, n) __builtin_amdgcn_sched_group_barrier(mask, n, 0)
#define LD16(off) (*(const LAS u32x4*)((const LAS char*)lds + (off)))
#define LD8(off) (*(const LAS u32x2*)((const LAS char*)lds + (off)))
#define LDF4(off) (*(const LAS f32x4*)((const LAS char*)lds + (off)))
#define LDF(off) (*(const LAS float*)((const LAS char*)lds + (off)))
#define ST16(off, v) (*(LAS u32x4*)((LAS char*)lds + (off)) = (v))
#define ST8(off, v) (*(LAS u32x2*)((LAS char*)lds + (off)) = (v))
#define STF4(off, v) (*(LAS f32x4*)((LAS char*)lds + (off)) = (v))
#define STF(off, v) (*(LAS float*)((LAS char*)lds + (off)) = (v))
DI u32x2 tr_read(const char* lds, unsigned off) { return __builtin_bit_cast(u32x2, __builtin_amdgcn_ds_read_tr16_b64_v4i16((LAS s16x4*)((LAS char*)lds + off))); }

DI void flag_store(char* ws, unsigned off, unsigned tag) { __hip_atomic_store((GAS unsigned*)(ws + off), tag, __ATOMIC_RELAXED, __HIP_MEMORY_SCOPE_AGENT); }
DI unsigned flag_load(const char* ws, unsigned off) { return __hip_atomic_load((const GAS unsigned*)(ws + off), __ATOMIC_RELAXED, __HIP_MEMORY_SCOPE_AGENT); }
DI void flag_put(rsrc_t rs, unsigned off, unsigned tag, bool me) { bstore4<SC1>(tag, rs, me ? off : 0x7ff00000u, 0); }
DI void publish(rsrc_t rs, unsigned off, unsigned tag) { DRAIN(); BAR(); flag_put(rs, off, tag, threadIdx.x == 0); }
template <int N> DI void poll_words(char* ws, const unsigned (&off)[N], bool active, unsigned tag) {
#pragma nounroll
    for (unsigned spins = 0;; ++spins) {
        bool ok = true;
#pragma unroll
        for (int k = 0; k < N; ++k) ok &= flag_load(ws, off[k]) == tag;
        if (__all(ok || !active)) break;
        if (spins > SPIN_CAP) { if ((threadIdx.x & 63) == 0) flag_store(ws, WS_TMO, tag); break; }
        __builtin_amdgcn_s_sleep(1);
    }
}

DI void check_all_blocks(char* ws, rsrc_t rs, unsigned base, u32x4 v, unsigned tag) {
    asm volatile("" : "+v"(v));
#pragma nounroll
    for (unsigned spins = 0; !__all(v[0] == tag && v[1] == tag && v[2] == tag && v[3] == tag); ++spins) {
        if (spins > SPIN_CAP) { if ((threadIdx.x & 63) == 0) flag_store(ws, WS_TMO, tag); break; }
        __builtin_amdgcn_s_sleep(1);
        v = bload16<SC1>(rs, (threadIdx.x & 63) * 16, base);
        asm volatile("" : "+v"(v) :: "memory");
    }
}

DI void rows_wo(int s, int qd, int& ra, int& rb) { if (s < 16) { ra = 40 * s + 4 * qd; rb = ra + 20; } else { ra = 40 * (4 * (s - 16) + qd) + 16; rb = ra + 20; } }
DI void tiles_p3(int s, int& ta, int& tb) { if (s < 16) { ta = 5 * (s >> 1) + 2 * (s & 1); tb = ta + 1; } else { ta = 10 * (s - 16) + 4; tb = ta + 5; } }
DI void tiles_p4(int s, int& ta, int& tb) { if (s < 48) { int j = s / 3, pr = s % 3; int st = j < 12 ? 7 * j : 84 + 6 * (j - 12); ta = st + 2 * pr; tb = ta + 1; } else { int m = s - 48; ta = 14 * m + 6; tb = ta + 7; } }

static constexpr int L0_SC = 81920, L0_GS = 83200, L0_CRAW = 83968, L0_RW = 86016, L0_D = 101376, L0_XI = 134144, L0_XS = 156160;

template <int AUX> DI void dma(char* lds, unsigned lds_off, rsrc_t r, unsigned voff, unsigned soff) {
    __builtin_amdgcn_raw_ptr_buffer_load_lds(r, (LAS void*)((LAS char*)lds + lds_off), 16, voff, soff, 0, AUX);
}
DI void dma16(char* lds, unsigned lds_off, rsrc_t r, unsigned voff, unsigned soff) {
    const unsigned dst = (unsigned)(size_t)(LAS char*)lds + lds_off;
    asm volatile("s_mov_b32 m0, %0\n\ts_nop 0\n\tbuffer_load_dwordx4 %1, %2, %3 offen lds" :: "s"(dst), "v"(voff), "s"(r), "s"(soff) : "memory");
}
DI void dma16s(char* lds, unsigned lds_off, rsrc_t r, unsigned voff, unsigned soff) { dma<SC1>(lds, lds_off, r, voff, soff); }
#define WAITV(n) asm volatile("s_waitcnt vmcnt(%0)" :: "n"(n) : "memory")

struct DPlan { rsrc_t r; unsigned v0, v1, sa, sb, st; bool on; };
struct DOut { unsigned dst[8]; u32x4 f[8]; };
DI void dtask_plan(const Params& p, int id, int l, DPlan& d, DOut& o) {
    int ra0, rb0, rstep, cg, lim; unsigned pitch, d0, r5, stride, jump;
    if (id < 370) {
        const bool wo = id < 100; const int i2 = wo ? id : id - 100, s = i2 / 5; cg = i2 - 5 * s; pitch = 1280;
        if (wo) { d.r = mk_rsrc(p.wo, 640 * 1280); if (s < 16) { ra0 = 40 * s; rstep = 4; } else { ra0 = 160 * (s - 16) + 16; rstep = 40; } rb0 = ra0 + 20; }
        else { d.r = mk_rsrc(p.w2, 1728 * 1280); int ta, tb; tiles_p4(s, ta, tb); ra0 = 16 * ta; rb0 = 16 * tb; rstep = 4; }
        const int c0 = (8 * cg) / 5;
        r5 = 8 * cg - 5 * c0; d0 = (wo ? WS_WOF + (c0 * 20 + s) * 5120 : WS_W2F + (c0 * 54 + s) * 5120) + r5 * 1024;
        stride = 1024; jump = wo ? 19 * 5120 : 53 * 5120; lim = 8;
    } else {
        const int i2 = id < 650 ? id - 370 : 0, s = i2 / 14; cg = i2 - 14 * s; pitch = 3456; d.r = mk_rsrc(p.w1, 640 * 3456);
        int ta, tb; tiles_p3(s, ta, tb); ra0 = 16 * ta; rb0 = 16 * tb; rstep = 4;
        r5 = 0; d0 = WS_W1F + (8 * cg * 20 + s) * 1024; stride = 20480; jump = 0; lim = id < 650 ? (cg == 13 ? 4 : 8) : 0;
    }
    const int q = l >> 4, cp = l & 15;
    d.v0 = q * pitch + ((cp ^ (q << 1)) * 16); d.v1 = q * pitch + ((cp ^ ((4 + q) << 1)) * 16);
    d.sa = ra0 * pitch + cg * 256; d.sb = rb0 * pitch + cg * 256; d.st = rstep * pitch; d.on = lim > 0;
#pragma unroll
    for (int nt = 0; nt < 8; ++nt) {
        const unsigned v = d0 + nt * stride + ((r5 + nt >= 5 ? 1u : 0u) + (r5 + nt >= 10 ? 1u : 0u)) * jump;
        o.dst[nt] = __builtin_amdgcn_readfirstlane(nt < lim ? v : 0x7ff00000u);
    }
}
DI void dtask_go(const DPlan& d, char* lds, unsigned scr, int w) {
    if (d.on) {
#pragma unroll
        for (int i = 0; i < 8; ++i) dma16(lds, scr + w * 8192 + i * 1024, d.r, (i & 1) ? d.v1 : d.v0, (i < 4 ? d.sa : d.sb) + (i & 3) * d.st);
    }
}
DI void dtask_read(DOut& o, char* lds, unsigned scr, int w, int l) {
    const int qd = l >> 4, q = (l >> 2) & 3, pp = l & 3, r7 = 4 * (qd & 1) + q;
    const unsigned a0 = scr + w * 8192 + (4 * qd + q) * 256 + pp * 8;
#pragma unroll
    for (int nt = 0; nt < 8; ++nt) {
        const u32x2 ua = tr_read(lds, a0 + ((nt ^ r7) * 32)), ub = tr_read(lds, a0 + 4096 + ((nt ^ r7) * 32));
        o.f[nt] = (u32x4){ua[0], ua[1], ub[0], ub[1]};
    }
}
template <int NT> DI void dtask_frag(const DOut& o, rsrc_t rs, int l) { bstore16<SC1>(o.f[NT], rs, l * 16, o.dst[NT]); }

template <int I0, int I1> DI void wm_dma(const Params& p, char* lds, int w, int l, unsigned colA, unsigned colB, int rot) {
    const rsrc_t rw = mk_rsrc(p.wm, 640 * 3840 * 2);
    const int rl = l >> 3, cp = l & 7;
    const unsigned c = cp ^ ((((rl >> 1) & 1) | ((w & 1) << 1)) << 1);
    const unsigned voff = rl * 7680 + (c < 4 ? colA + 8 * c : colB + 8 * (c - 4)) * 2;
    int piece = 4 * I0 + w + rot; piece = piece >= 80 ? piece - 80 : piece;
#pragma unroll
    for (int i = I0; i < I1; ++i) {
        dma16(lds, piece * 1024, rw, voff, piece * 61440);
        piece = piece >= 76 ? piece - 76 : piece + 4;
    }
}
DI void c_dma(const Params& p, char* lds, int w, int l) {
    dma16(lds, L0_CRAW + (w & 1) * 1024, mk_rsrc(p.c, 1280), ((w & 1) * 64 + l) * 16, 0);
}
DI void silu_table(char* lds, int tid) {
    if (tid < 80) {
        const u32x4 creg = LD16(L0_CRAW + tid * 16);
        u32x4 o;
#pragma unroll
        for (int d = 0; d < 4; ++d) { const float a = lo_f(creg[d]), b = hi_f(creg[d]); o[d] = pack2(a * __builtin_amdgcn_rcpf(1.f + __expf(-a)), b * __builtin_amdgcn_rcpf(1.f + __expf(-b))); }
        ST16(L0_SC + tid * 16, o);
    }
}
DI float mod_gemv(char* lds, int w, int l) {
    const int qd = l >> 4, q = (l >> 2) & 3, pp = l & 3;
    f32x4 acc = {0, 0, 0, 0}, acc2 = {0, 0, 0, 0};
    const int r0 = 8 * qd + q;
    const unsigned sg = ((r0 >> 1) & 1) | (((r0 >> 3) & 1) << 1);
    unsigned a0 = r0 * 128 + ((w ^ sg) * 32) + pp * 8, a1 = a0 + 40960, sb = L0_SC + qd * 16;
    OPAQUE(a0); OPAQUE(a1); OPAQUE(sb);
    u32x2 tb[2][4]; u32x4 sc[2][2];
    auto rd = [&](int pr) {
        const unsigned ab = pr < 5 ? a0 : a1; const int po = (pr < 5 ? pr : pr - 5) * 8192;
        tb[pr & 1][0] = tr_read(lds, ab + po); tb[pr & 1][1] = tr_read(lds, ab + po + 512); tb[pr & 1][2] = tr_read(lds, ab + po + 4096); tb[pr & 1][3] = tr_read(lds, ab + po + 4608);
        sc[pr & 1][0] = LD16(sb + pr * 128); sc[pr & 1][1] = LD16(sb + pr * 128 + 64);
    };
    rd(0);
#pragma unroll
    for (int pr = 0; pr < 10; ++pr) {
        if (pr + 1 < 10) rd(pr + 1);
        SB();
        acc = mfma(sc[pr & 1][0], (u32x4){tb[pr & 1][0][0], tb[pr & 1][0][1], tb[pr & 1][1][0], tb[pr & 1][1][1]}, acc);
        acc2 = mfma(sc[pr & 1][1], (u32x4){tb[pr & 1][2][0], tb[pr & 1][2][1], tb[pr & 1][3][0], tb[pr & 1][3][1]}, acc2);
        SB();
    }
    return rbf(acc[0] + acc2[0]);
}
DI void x_dma(const Params& p, char* lds, unsigned img, int T, int w, int l) {
    const rsrc_t rx = mk_rsrc(p.x, 2048 * 1280);
    const int r4 = l >> 4, row = 4 * w + r4;
    const unsigned voff = row * 1280 + (((l & 15) ^ row) * 16);
#pragma unroll
    for (int cb = 0; cb < 5; ++cb) dma16(lds, img + (w * 5 + cb) * 1024, rx, voff, T * 16 * 1280 + cb * 256);
}
DI void x_stats(char* lds, unsigned img, unsigned stat, int w, int l) {
    u32x4 v[5];
#pragma unroll
    for (int cb = 0; cb < 5; ++cb) v[cb] = LD16(img + (w * 5 + cb) * 1024 + l * 16);
    f32x2 s1 = {0.f, 0.f}, s2 = {0.f, 0.f};
#pragma unroll
    for (int i = 0; i < 5; ++i)
#pragma unroll
        for (int d = 0; d < 4; ++d) { const f32x2 x = {lo_f(v[i][d]), hi_f(v[i][d])}; s1 += x; s2 = __builtin_elementwise_fma(x, x, s2); }
    const float mean = red16(s1[0] + s1[1]) * (1.f / 640.f), ex2 = red16(s2[0] + s2[1]) * (1.f / 640.f);
    const float rstd = __builtin_amdgcn_rsqf(fmaxf(ex2 - mean * mean, 0.f) + 1e-5f);
    if ((l & 15) == 0) *(LAS f32x2*)((LAS char*)lds + stat + (4 * w + (l >> 4)) * 8) = (f32x2){rstd, -mean * rstd};
}
DI unsigned ximg_off(int t, int byte_in_row) { return ((t >> 2) * 5 + (byte_in_row >> 8)) * 1024 + (t & 3) * 256 + ((((byte_in_row >> 4) & 15) ^ t) * 16) + (byte_in_row & 15); }
DI void x_frags(char* lds, unsigned img, unsigned stat, rsrc_t rs, int T, int w, int l) {
    const int t = l & 15, qd = l >> 4;
    const f32x2 st = *(const LAS f32x2*)((const LAS char*)lds + stat + t * 8);
    const f32x2 r2 = {st[0], st[0]}, m2 = {st[1], st[1]};
    u32x4 v[5];
#pragma unroll
    for (int i = 0; i < 5; ++i) v[i] = LD16(img + ximg_off(t, (w + 4 * i) * 64 + qd * 16));
#pragma unroll
    for (int i = 0; i < 5; ++i) {
        u32x4 o;
#pragma unroll
        for (int d = 0; d < 4; ++d) { const f32x2 y = __builtin_elementwise_fma((f32x2){lo_f(v[i][d]), hi_f(v[i][d])}, r2, m2); o[d] = pack2(y[0], y[1]); }
        bstore16<SC1>(o, rs, l * 16, WS_XNF + (T * 20 + w + 4 * i) * 1024);
    }
}
struct TileKV { u32x2 ka0, ka1, k2, va0, va1, z; };
struct AttnMask { bool c0, c1, p0, p1, odd; unsigned sel0, sel1; };
DI AttnMask attn_mask(int t, int qd) {
    AttnMask m; const int par = t & 1, dc = t - 4 * qd - par;
    m.odd = par; m.c0 = dc >= 0; m.c1 = dc >= 2; m.p0 = dc + 16 <= 14; m.p1 = dc + 14 <= 14;
    m.sel0 = par ? 0x01000c0cu : 0x0c0c0100u; m.sel1 = par ? 0x03020c0cu : 0x0c0c0302u;
    return m;
}
template <class BG> DI void attn_unit(const u32x2 (&qa)[2], const u32x2& xq, const TileKV& cur, const TileKV& prev, bool prev_ok, const AttnMask& m, int qd, u32x4& omain, u32x2& oleft, BG&& bg) {
    const float cs = 0.22360679774997896f * 1.4426950408889634f, ninf = -__builtin_inff();
    f32x4 oa[2], oz[2];
#pragma unroll
    for (int h = 0; h < 2; ++h) {
        const u32x4 qf = {qa[h][0], qa[h][1], qd == h ? xq[0] : 0u, qd == h ? xq[1] : 0u};
        const u32x2 kcp = h == 0 ? cur.ka0 : cur.ka1, kpp = h == 0 ? prev.ka0 : prev.ka1;
        const f32x4 z4 = {0.f, 0.f, 0.f, 0.f};
        const f32x4 sp = mfma((u32x4){kpp[0], kpp[1], prev.k2[0], prev.k2[1]}, qf, z4);
        const f32x4 sc = mfma((u32x4){kcp[0], kcp[1], cur.k2[0], cur.k2[1]}, qf, z4);
        bg(5 * h);
        float e0 = m.odd ? sp[1] : sp[0], e1 = m.odd ? sp[3] : sp[2], e2 = m.odd ? sc[1] : sc[0], e3 = m.odd ? sc[3] : sc[2];
        e0 = (m.p0 && prev_ok) ? e0 : ninf; e1 = (m.p1 && prev_ok) ? e1 : ninf; e2 = m.c0 ? e2 : ninf; e3 = m.c1 ? e3 : ninf;
        bg(5 * h + 1);
        const float mx = red_max_q(fmaxf(fmaxf(e0, e1), fmaxf(e2, e3))), mc = -mx * cs;
        bg(5 * h + 2);
        e0 = __builtin_amdgcn_exp2f(fmaf(e0, cs, mc)); e1 = __builtin_amdgcn_exp2f(fmaf(e1, cs, mc)); e2 = __builtin_amdgcn_exp2f(fmaf(e2, cs, mc)); e3 = __builtin_amdgcn_exp2f(fmaf(e3, cs, mc));
        bg(5 * h + 3);
        const float inv = __builtin_amdgcn_rcpf(red_add_q((e0 + e1) + (e2 + e3)));
        bg(5 * h + 4);
        const unsigned pp = pack2(e0 * inv, e1 * inv), pc = pack2(e2 * inv, e3 * inv);
        const u32x4 pf = {__builtin_amdgcn_perm(0u, pp, m.sel0), __builtin_amdgcn_perm(0u, pp, m.sel1), __builtin_amdgcn_perm(0u, pc, m.sel0), __builtin_amdgcn_perm(0u, pc, m.sel1)};
        const u32x2 vc2 = h == 0 ? cur.va0 : cur.va1, vp2 = h == 0 ? prev.va0 : prev.va1;
        oa[h] = mfma((u32x4){vp2[0], vp2[1], vc2[0], vc2[1]}, pf, z4);
        oz[h] = mfma((u32x4){prev.z[0], prev.z[1], cur.z[0], cur.z[1]}, pf, z4);
    }
    const u32x2 a0 = pack4(oa[0]), a1 = pack4(oa[1]);
    omain = (u32x4){a0[0], a0[1], a1[0], a1[1]};
    const u32x2 l0 = pack4(oz[0]), l1 = pack4(oz[1]);
    oleft = qd == 0 ? l0 : l1;
}

__global__ __launch_bounds__(256, 1) void adaLN_block_kernel(Params p) {
    extern __shared__ __attribute__((aligned(16))) char lds[];
    const int b = blockIdx.x, w = __builtin_amdgcn_readfirstlane(threadIdx.x >> 6);
    const int xcd = b & 7, bi = b >> 3;
    char* ws = p.ws;
    const rsrc_t rs = mk_rsrc(ws, WS_END);
    unsigned long long diag_t0 = 0, diag_t1 = 0;
    STAMP(0);
    LSTAMP_DECL;
    {
        unsigned long long pc; unsigned len;
        asm volatile("s_getpc_b64 %0\n.Ladaln_pc:\n\ts_mov_b32 %1, .Ladaln_end-.Ladaln_pc" : "=s"(pc), "=s"(len));
        unsigned off = (bi * 256 + threadIdx.x) * 64u;
        off = off < len ? off : 0u;
        const unsigned long long a = pc + off;
        const unsigned dst = (unsigned)(size_t)(LAS char*)lds + LDS_BYTES - 1024 + w * 256;
        asm volatile("s_mov_b32 m0, %0\n\ts_nop 0\n\tglobal_load_lds_dword %1, off" :: "s"(dst), "v"(a) : "memory");
    }
    const unsigned epoch = *(const __attribute__((address_space(4))) unsigned*)(size_t)(ws + WS_EPOCH);
    unsigned tag;
#define MAKE_TAG() do { unsigned e_ = epoch; asm volatile("" : "+s"(e_)); tag = e_ + 0x9E3779B9u; } while (0)

    DOut dout;
    {
        LANE_IDS();
        const bool isA = bi < 20, isB = !isA && bi < 25, isC = !isA && !isB;
        const int sA = bi < 16 ? xcd + 8 * (bi >> 3) : 16 + (xcd >> 1), jg = bi < 16 ? (bi & 7) : 4 * (xcd & 1) + (bi - 16), idxB = xcd * 5 + (bi - 20);
        const int aidx = jg * 20 + sA;
        const int idxC = xcd * 7 + (bi - 25);
        const int Ts = idxC < 16 ? 3 * idxC : 48 + 2 * (idxC - 16), nT = idxC < 16 ? 3 : 2;
        const int did = w == 0 ? 1000 : b * 3 + w - 1;
        DPlan dp;
        if (isC) {
#pragma unroll
            for (int k = 0; k < 3; ++k) if (k < nT) x_dma(p, lds, k * 20480, Ts + k, w, l);
            MAKE_TAG();
            dtask_plan(p, did, l, dp, dout);
            STAMP(18);
            if (nT == 3) { WAITV(10); x_stats(lds, 0, L0_XS, w, l); WAITV(5); x_stats(lds, 20480, L0_XS + 128, w, l); WAITV(0); dtask_go(dp, lds, L0_D, w); x_stats(lds, 40960, L0_XS + 256, w, l); }
            else { WAITV(5); x_stats(lds, 0, L0_XS, w, l); WAITV(0); dtask_go(dp, lds, L0_D, w); x_stats(lds, 20480, L0_XS + 128, w, l); }
            BAR();
#pragma unroll
            for (int k = 0; k < 3; ++k) if (k < nT) x_frags(lds, k * 20480, L0_XS + k * 128, rs, Ts + k, w, l);
            DRAIN();
            BAR();
            flag_put(rs, WS_FC + (Ts + (tid & 3)) * FS, tag, tid < nT);
            STAMP(20);
        } else {
            const unsigned colA = isA ? 640 + 32 * sA : 1280 + 64 * idxB, colB = isA ? 32 * sA : 1280 + 64 * idxB + 32;
            const int rot = isA ? (bi < 16 ? 10 * jg : 20 * (bi - 16)) : 0;
            c_dma(p, lds, w, l);
            wm_dma<0, 10>(p, lds, w, l, colA, colB, rot);
            WAITV(10);
            silu_table(lds, tid);
            wm_dma<10, 20>(p, lds, w, l, colA, colB, rot);
            if (isA) {
                const rsrc_t rq = mk_rsrc(p.wqkv, 640 * 3840);
#pragma unroll
                for (int m = 0; m < 4; ++m) {
                    const int n = 64 * (4 * m + w) + l, rho = n / 30, c = n % 30;
                    const int k = (rho & 19) | ((rho & 4) << 1) | ((rho & 8) >> 1);
                    if (4 * m + w < 15) dma16(lds, L0_RW + (4 * m + w) * 1024, rq, k * 3840 + (c / 10) * 1280 + jg * 160 + (c % 10) * 16, sA * 32 * 3840);
                }
            }
            MAKE_TAG();
            dtask_plan(p, did, l, dp, dout);
            STAMP(18);
            WAITV(0);
            dtask_go(dp, lds, L0_D, w);
            BAR();
            float v = mod_gemv(lds, w, l);
            STAMP(19);
            if (isA) {
                const float gv = w < 2 ? rbf(1.f + v) : v;
                if (l < 16) { STF(L0_GS + (w * 16 + l) * 4, gv); *(LAS u16*)((LAS char*)lds + L0_GS + 256 + (w * 16 + l) * 2) = (u16)(__float_as_uint(gv) >> 16); }
                const int q = (l >> 2) & 3, pp = l & 3, jj = w >> 1, j = 2 * jg + jj;
                const unsigned rbase = L0_RW + (16 * (qd >> 1) + 4 * (qd & 1) + q) * 480 + 80 * jj;
                u32x2 ua[4], ub[4];
#pragma unroll
                for (int i = 0; i < 4; ++i) {
                    const unsigned lo = i < 2 ? i * 40 + 8 * pp : 160 + (i - 2) * 40 + 8 * pp;
                    const unsigned hi = i == 0 ? (pp >> 1) * 160 + (pp & 1) * 40 + 32 : i < 3 ? 320 + (i - 1) * 40 + 8 * pp : 320 + (pp & 1) * 40 + 32;
                    const unsigned po = (w & 1) ? hi : lo;
                    ua[i] = tr_read(lds, rbase + po); ub[i] = tr_read(lds, rbase + po + 8 * 480);
                }
                if ((w & 1) && t >= 8) { ua[3] = (u32x2){0u, 0u}; ub[3] = ua[3]; }
                BAR();
                const f32x4 g0 = LDF4(L0_GS + qd * 32), g1 = LDF4(L0_GS + qd * 32 + 16);
                const u32x4 sfr = LD16(L0_GS + 256 + 64 + qd * 16);
#pragma unroll
                for (int i = 0; i < 4; ++i) {
                    const int nt = 4 * (w & 1) + i;
                    const u32x4 o = {pack2(lo_f(ua[i][0]) * g0[0], hi_f(ua[i][0]) * g0[1]), pack2(lo_f(ua[i][1]) * g0[2], hi_f(ua[i][1]) * g0[3]),
                                     pack2(lo_f(ub[i][0]) * g1[0], hi_f(ub[i][0]) * g1[1]), pack2(lo_f(ub[i][1]) * g1[2], hi_f(ub[i][1]) * g1[3])};
                    bstore16<SC1>(o, rs, lo16, WS_WQF + ((j * 20 + sA) * 8 + nt) * 1024);
                }
                {
                    const f32x4 z4 = {0, 0, 0, 0};
                    f32x4 bias = mfma((u32x4){ua[0][0], ua[0][1], ub[0][0], ub[0][1]}, sfr, z4);
#pragma unroll
                    for (int i = 1; i < 4; ++i) { const f32x4 bi4 = mfma((u32x4){ua[i][0], ua[i][1], ub[i][0], ub[i][1]}, sfr, z4); bias = t == i ? bi4 : bias; }
                    bstore16<SC1>(asu4(bias), rs, t < 4 ? t * 64 + qd * 16 : 0x7ff00000u, WS_BP + ((sA * 16 + j) * 128 + (w & 1) * 64) * 4);
                }
                STAMP(20);
            } else {
                const int col = 1280 + 64 * idxB + 16 * w + t;
                if (col >= 2560 && col < 3200) v = rbf(1.f + v);
                bstore4<SC1>(__float_as_uint(v), rs, l < 16 ? l * 4 : 0x7ff00000u, WS_MODV + (1280 + 64 * idxB + 16 * w) * 4);
                const unsigned vn = (unsigned)__builtin_amdgcn_update_dpp(0, (int)__float_as_uint(v), 0xB1, 0xf, 0xf, true);
                bstore4<SC1>((__float_as_uint(v) >> 16) | (vn & 0xffff0000u), rs, (l < 16 && !(l & 1)) ? l * 2 : 0x7ff00000u, WS_MODB + (1280 + 64 * idxB + 16 * w) * 2);
            }
            DRAIN();
            BAR();
            flag_put(rs, WS_FA + aidx * FS, tag, isA && tid == 0);
        }
        STAMP(1);
    }

    const int g1 = 4 * (xcd >> 1) + (bi >> 3), j1 = 8 * (xcd & 1) + (bi & 7);
    const int P2 = 4 * xcd + (bi >> 3), c2 = bi & 7;
    const int g3 = g1, j3 = j1;
    const int rot2 = ROT2, rot3 = ROT3;
    auto wrap20 = [](int x) { return x >= 20 ? x - 20 : x; };
    const int t3 = j3 < 12 ? 7 * j3 : 84 + 6 * (j3 - 12), cnt3 = j3 < 12 ? 7 : 6;
    u32x4 w2f[14][5];
    const int s4 = __builtin_amdgcn_readfirstlane(w < 2 ? 14 * w : 28 + 13 * (w - 2)), cnt4 = __builtin_amdgcn_readfirstlane(w < 2 ? 14 : 13);
    const unsigned w2b = __builtin_amdgcn_readfirstlane(WS_W2F + (c2 * 54 + s4) * 5 * 1024);
    const int rot4 = __builtin_amdgcn_readfirstlane(ROT4);
    auto step4 = [&](int i) { const int x = (i < 13 ? i : cnt4 - 1) + rot4; return x >= cnt4 ? x - cnt4 : x; };
    auto w2_ld = [&](int f, unsigned lo) { w2f[f / 5][f % 5] = bload16<SC1>(rs, lo, w2b + step4(f / 5) * 5120 + (f % 5) * 1024); };
    constexpr int L2_WO = 61440, L2_X = 46080;
    {
        LANE_IDS();
        const int T0 = 8 * g1 + 2 * w, TH = g1 == 0 ? 0 : 8 * g1 - 1;
        const unsigned xb0 = WS_XNF + T0 * 20480, xbh = WS_XNF + TH * 20480 + (w >> 1) * 1024, wb = WS_WQF + j1 * 163840 + w * 4096;
        const unsigned wob = WS_WOF + c2 * 102400;
        u32x4 xr[10][4], xh[10];
        const int rot1 = ROT1;
        auto gr = [&](int G) { const int x = G + rot1; return x >= 10 ? x - 10 : x; };
        auto w_dma = [&](int G, int k) { dma16s(lds, (G & 3) * 16384 + w * 4096 + k * 1024, rs, lo16, wb + gr(G) * 16384 + k * 1024); };
        auto x_ld = [&](int G, int i) { xr[G][i] = bload16<SC1>(rs, lo16, xb0 + (i & 1) * 20480 + (2 * gr(G) + (i >> 1)) * 1024); };
        if (w == 0) {
            unsigned off[1] = {l == 0 ? WS_FC + TH * FS : WS_FC + (8 * g1 + ((l - 1) & 7)) * FS};
            poll_words<1>(ws, off, l < 9, tag);
        }
        BAR();
#pragma unroll
        for (int G = 0; G < 4; ++G) {
#pragma unroll
            for (int i = 0; i < 4; ++i) x_ld(G, i);
            xh[G] = bload16<SC1>(rs, lo16, xbh + gr(G) * 2048);
        }
        SB();
        if (w == 0) {
            unsigned off[1] = {WS_FA + ((j1 >> 1) * 20 + (l < 20 ? l : 0)) * FS};
            poll_words<1>(ws, off, l < 20, tag);
        }
        DRAIN();
        if (w > 0) dtask_read(dout, lds, L0_D, w, l);
        else {
#pragma unroll
            for (int i = 0; i < 8; ++i) dout.f[i] = (u32x4){0u, 0u, 0u, 0u};
        }
        BAR();
        STAMP(2);
#pragma unroll
        for (int G = 0; G < 3; ++G)
#pragma unroll
            for (int k = 0; k < 4; ++k) w_dma(G, k);
        WAITV(4);
        BAR();
        STAMP(3);
        f32x4 acc[2][8], hacc[3];
#pragma unroll
        for (int i = 0; i < 8; ++i) { acc[0][i] = (f32x4){0, 0, 0, 0}; acc[1][i] = (f32x4){0, 0, 0, 0}; }
#pragma unroll
        for (int i = 0; i < 3; ++i) hacc[i] = (f32x4){0, 0, 0, 0};
        u32x4 wf[2][8];
#pragma unroll
        for (int n = 0; n < 8; ++n) wf[0][n] = LD16(n * 1024 + lo16);
        const unsigned hoff = (w >> 1) * 8192 + ((w & 1) ? 5 : 2) * 1024 + lo16;
        f32x4 bp4[3]; u32x4 fdv;
        constexpr int L1_BPART = 40960;
        SB();
#pragma unroll
        for (int G = 0; G < 10; ++G) {
            u32x4 hw[3];
#pragma unroll
            for (int st = 0; st < 2; ++st) {
                const int s = 2 * G + st;
#pragma unroll
                for (int n = 0; n < 8; ++n) {
                    if (s + 1 < 20) wf[(s + 1) & 1][n] = LD16((((s + 1) >> 1) & 3) * 16384 + ((s + 1) & 1) * 8192 + n * 1024 + lo16);
                    if (st == 0 && n < 3) hw[n] = LD16((G & 3) * 16384 + hoff + n * 1024);
                    if (n < 5) { acc[0][n] = mfma(wf[s & 1][n], xr[G][st * 2], acc[0][n]); acc[1][n] = mfma(wf[s & 1][n], xr[G][st * 2 + 1], acc[1][n]); }
                    else { acc[0][n] = mfma(xr[G][st * 2], wf[s & 1][n], acc[0][n]); acc[1][n] = mfma(xr[G][st * 2 + 1], wf[s & 1][n], acc[1][n]); }
                    if (st == 1 && n >= 5) hacc[n - 5] = mfma(hw[n - 5], xh[G], hacc[n - 5]);
                    if (st == 0) {
                        if (G + 3 < 10 && (n & 1) == 0) w_dma(G + 3, n >> 1);
                        if (G + 4 < 10 && n == 7) xh[G + 4] = bload16<SC1>(rs, lo16, xbh + gr(G + 4) * 2048);
                    } else {
                        if (G + 4 < 10 && (n & 1) == 0) x_ld(G + 4, n >> 1);
                    }
                    if (G == 7 && st == 0 && n < 3) bp4[n] = asf4(bload16<SC1>(rs, (n < 2 || tid < 128) ? ((tid >> 5) + 8 * n) * 8192 + (tid & 31) * 16 : 0x7ff00000u, WS_BP + j1 * 512));
                    if (G == 9 && st == 0 && n == 0) {
                        f32x4 bs = (bp4[0] + bp4[1]) + bp4[2];
#pragma unroll
                        for (int r = 0; r < 4; ++r) { auto sw = __builtin_amdgcn_permlane32_swap(__float_as_uint(bs[r]), __float_as_uint(bs[r]), false, false); bs[r] = __uint_as_float(sw[0]) + __uint_as_float(sw[1]); }
                        STF4(L1_BPART + w * 1024 + lo16, bs);
                    }
                    if (G == 9 && st == 1 && n == 0) fdv = bload16<SC1>(rs, lo16, WS_FD);
                    if (n == 3) {
                        if (s == 0) dtask_frag<0>(dout, rs, l);
                        if (s == 1) dtask_frag<1>(dout, rs, l);
                        if (s == 2) dtask_frag<2>(dout, rs, l);
                        if (s == 3) dtask_frag<3>(dout, rs, l);
                        if (s == 4) dtask_frag<4>(dout, rs, l);
                        if (s == 5) dtask_frag<5>(dout, rs, l);
                        if (s == 6) dtask_frag<6>(dout, rs, l);
                        if (s == 7) dtask_frag<7>(dout, rs, l);
                    }
                    SB();
                }
            }
            if (G == 0) WAITV(11); else if (G <= 3) WAITV(17); else if (G == 4) WAITV(11); else if (G == 5) WAITV(14); else if (G == 6) WAITV(9); else if (G == 7) WAITV(3);
            BAR();
            if (G == 4) flag_put(rs, WS_FD + b * 4, tag, tid == 0);
        }
        STAMP(4);
        constexpr int L1_BIAS = 6144, L1_HP = 0, L1_EX = 8192, L1_VT = 6656;
        if (w == 0) {
            check_all_blocks(ws, rs, WS_FD, fdv, tag);
            if (b == 0 && l == 0) flag_store(ws, WS_EPOCH, tag);
            if (l < 32) STF4(L1_BIAS + l * 16, (LDF4(L1_BPART + l * 16) + LDF4(L1_BPART + 1024 + l * 16)) + (LDF4(L1_BPART + 2048 + l * 16) + LDF4(L1_BPART + 3072 + l * 16)));
        }
        if (w >= 2) {
#pragma unroll
            for (int i = 0; i < 3; ++i) STF4(L1_HP + ((w & 1) * 3 + i) * 1024 + lo16, hacc[i]);
        }
        BAR();
        auto wo_dma = [&](int k) { dma16s(lds, L2_WO + w * 25600 + k * 1024, rs, lo16, wob + w * 25600 + k * 1024); };
        const rsrc_t rx = mk_rsrc(p.x, 2048 * 1280);
        unsigned xvo[3];
#pragma unroll
        for (int k = 0; k < 3; ++k) { const int n = (w * 3 + k) * 64 + l, row = n / 10; xvo[k] = n < 640 ? row * 1280 + (n - 10 * row) * 16 : 0x7ff00000u; }
        const unsigned xlo = L2_X + w * 3072;
        auto x_dma2 = [&](int k) { dma<0>(lds, xlo + k * 1024, rx, xvo[k], P2 * 64 * 1280 + c2 * 160); };
        f32x4 bq[5]; float bv[3];
#pragma unroll
        for (int n = 0; n < 5; ++n) bq[n] = LDF4(L1_BIAS + (n * 16 + 4 * qd) * 4);
#pragma unroll
        for (int n = 0; n < 3; ++n) bv[n] = LDF(L1_BIAS + ((5 + n) * 16 + t) * 4);
        TileKV kv[2]; u32x2 qa[2][2], xq[2];
        auto bg_wo = [&](int k) { SB(); wo_dma(k); SB(); };
#pragma unroll
        for (int tt = 1; tt >= 0; --tt) {
            kv[tt].ka0 = pack4(acc[tt][2] + bq[2]); kv[tt].ka1 = pack4(acc[tt][3] + bq[3]);
            bg_wo(4 * (1 - tt));
            xq[tt] = pack4(acc[tt][4] + bq[4]);
            kv[tt].k2[0] = swap32(xq[tt][0]); kv[tt].k2[1] = swap32(xq[tt][1]);
            bg_wo(4 * (1 - tt) + 1);
            kv[tt].va0 = pack4(acc[tt][5] + bv[0]); kv[tt].va1 = pack4(acc[tt][6] + bv[1]);
            bg_wo(4 * (1 - tt) + 2);
            kv[tt].z = pack4(acc[tt][7] + bv[2]);
            if (tt == 1) {
                const unsigned e = L1_EX + (w + 1) * 3072 + lo16;
                ST16(e, ((u32x4){kv[1].ka0[0], kv[1].ka0[1], kv[1].ka1[0], kv[1].ka1[1]}));
                ST16(e + 1024, ((u32x4){kv[1].k2[0], kv[1].k2[1], kv[1].va0[0], kv[1].va0[1]}));
                ST16(e + 2048, ((u32x4){kv[1].va1[0], kv[1].va1[1], kv[1].z[0], kv[1].z[1]}));
            }
            qa[tt][0] = pack4(acc[tt][0] + bq[0]); qa[tt][1] = pack4(acc[tt][1] + bq[1]);
            bg_wo(4 * (1 - tt) + 3);
        }
        if (w == 0) {
            const u32x2 k0 = pack4(hacc[0] + LDF4(L1_HP + lo16) + bq[2]), k1 = pack4(hacc[1] + LDF4(L1_HP + 1024 + lo16) + bq[3]);
            const u32x2 xx = pack4(hacc[2] + LDF4(L1_HP + 2048 + lo16) + bq[4]);
            ST16(L1_EX + lo16, ((u32x4){k0[0], k0[1], k1[0], k1[1]}));
            ST8(L1_EX + 1024 + lo16, ((u32x2){swap32(xx[0]), swap32(xx[1])}));
        } else if (w == 1) {
#pragma unroll
            for (int i = 0; i < 3; ++i) {
                const f32x4 v = hacc[i] + LDF4(L1_HP + (3 + i) * 1024 + lo16) + LDF4(L1_BIAS + ((5 + i) * 16 + 4 * qd) * 4);
                const u32x2 pk = pack4(v);
                LAS u16* d = (LAS u16*)((LAS char*)lds + L1_VT + i * 512 + (4 * qd) * 32 + t * 2);
                d[0] = (u16)pk[0]; d[16] = (u16)(pk[0] >> 16); d[32] = (u16)pk[1]; d[48] = (u16)(pk[1] >> 16);
            }
        }
        const unsigned ob = WS_OF + T0 * 20480;
        const AttnMask am = attn_mask(t, qd);
        u32x4 om; u32x2 ol;
        SB();
        attn_unit(qa[1], xq[1], kv[1], kv[0], true, am, qd, om, ol, [&](int k) { bg_wo(8 + k); });
        SB();
        bstore16<SC1>(om, rs, lo16, ob + 20480 + j1 * 1024);
        bstore8<SC1>(ol, rs, qd < 2 ? ((j1 & 3) * 16 + t) * 16 + 8 * qd : 0x7ff00000u, ob + 20480 + (16 + (j1 >> 2)) * 1024);
        BAR();
        TileKV pv;
        {
            const unsigned e = L1_EX + w * 3072 + lo16;
            const u32x4 a = LD16(e), bb = LD16(e + 1024), cc = LD16(e + 2048);
            pv.ka0 = (u32x2){a[0], a[1]}; pv.ka1 = (u32x2){a[2], a[3]}; pv.k2 = (u32x2){bb[0], bb[1]}; pv.va0 = (u32x2){bb[2], bb[3]};
            pv.va1 = (u32x2){cc[0], cc[1]}; pv.z = (u32x2){cc[2], cc[3]};
            if (w == 0) { pv.va0 = LD8(L1_VT + t * 32 + qd * 8); pv.va1 = LD8(L1_VT + 512 + t * 32 + qd * 8); pv.z = LD8(L1_VT + 1024 + t * 32 + qd * 8); }
        }
        SB();
        attn_unit(qa[0], xq[0], kv[0], pv, !(g1 == 0 && w == 0), am, qd, om, ol, [&](int k) { if (k < 7) bg_wo(18 + k); else { SB(); x_dma2(k - 7); SB(); } });
        SB();
        bstore16<SC1>(om, rs, lo16, ob + j1 * 1024);
        bstore8<SC1>(ol, rs, qd < 2 ? ((j1 & 3) * 16 + t) * 16 + 8 * qd : 0x7ff00000u, ob + (16 + (j1 >> 2)) * 1024);
        STAMP(5);
        publish(rs, WS_F1 + (g1 * 16 + j1) * FS, tag);
    }
    STAMP(6);

    u32x2 x1b[5], b1v[7];
    u32x4 vf[5];
    {
        LANE_IDS();
        const int T = 4 * P2 + w;
        constexpr int L2_MV = 57344;
        constexpr int L2_TB = 58304, L2_B1 = 60864;
        const u32x4 mvl = bload16<SC1>(rs, (1280 + ((tid / 20) % 3) * 640 + 80 * c2 + (tid % 20) * 4) * 4, WS_MODV);
        if (w < 3) { if (w < 2 || l < 32) dma16s(lds, L2_TB + w * 1024, rs, lo16, WS_MODB + 1920 * 2 + w * 1024); }
        else if (l < 14) dma<0>(lds, L2_B1, mk_rsrc(p.b1, 1728 * 2), lo16, t3 * 32);
#pragma unroll
        for (int f = 0; f < 12; ++f) w2_ld(f, lo16);
        SB();
        if (tid < 60) ST16(L2_MV + tid * 16, mvl);
        WAITV(12);
        BAR();
        u32x2 xv[5], va2[5], vb2[5];
#pragma unroll
        for (int i = 0; i < 5; ++i) {
            const int sr = wrap20(5 * w + i + rot3);
            const int ta = sr < 16 ? 5 * (sr >> 1) + 2 * (sr & 1) : 10 * (sr - 16) + 4, tb = ta + (sr < 16 ? 1 : 5);
            const unsigned sec = L2_TB + (t == 0 ? 1280 : 0) + qd * 8;
            va2[i] = LD8(sec + ta * 32); vb2[i] = LD8(sec + tb * 32);
        }
#pragma unroll
        for (int n = 0; n < 7; ++n) b1v[n] = LD8(L2_B1 + n * 32 + qd * 8);
#pragma unroll
        for (int n = 0; n < 5; ++n) xv[n] = LD8(L2_X + (16 * w + t) * 160 + n * 32 + qd * 8);
        SB();
#pragma unroll
        for (int i = 0; i < 5; ++i) {
            const unsigned cst = t == 2 ? 0x3f803f80u : 0u;
            vf[i] = (u32x4){t < 2 ? va2[i][0] : cst, t < 2 ? va2[i][1] : cst, t < 2 ? vb2[i][0] : cst, t < 2 ? vb2[i][1] : cst};
        }
        asm volatile("" : "+v"(vf[0]), "+v"(vf[1]), "+v"(vf[2]), "+v"(vf[3]), "+v"(vf[4]));
        asm volatile("" : "+v"(b1v[0]), "+v"(b1v[1]), "+v"(b1v[2]), "+v"(b1v[3]), "+v"(b1v[4]), "+v"(b1v[5]), "+v"(b1v[6]), "+v"(xv[0]), "+v"(xv[1]), "+v"(xv[2]), "+v"(xv[3]), "+v"(xv[4]));
        if (w == 0) {
            unsigned off[1] = {WS_F1 + ((P2 >> 1) * 16 + (l & 15)) * FS};
            poll_words<1>(ws, off, l < 16, tag);
        }
        BAR();
        STAMP(7);
        constexpr int PW = 8;
        u32x4 tk[20];
#pragma unroll
        for (int s = 0; s < PW; ++s) tk[s] = bload16<SC1>(rs, lo16, WS_OF + (T * 20 + wrap20(s + rot2)) * 1024);
        f32x4 acc[5];
#pragma unroll
        for (int n = 0; n < 5; ++n) acc[n] = (f32x4){0, 0, 0, 0};
        unsigned wb0 = L2_WO + lo16, wb1 = L2_WO + 51200 + lo16; OPAQUE(wb0); OPAQUE(wb1);
        auto wo_rd = [&](int s, int n) { return s < 10 ? LD16(wb0 + (s * 5 + n) * 1024) : LD16(wb1 + ((s - 10) * 5 + n) * 1024); };
        u32x4 wf[3][5];
#pragma unroll
        for (int n = 0; n < 5; ++n) { wf[0][n] = wo_rd(0, n); wf[1][n] = wo_rd(1, n); }
        SB();
        LSTAMP(40);
#pragma unroll
        for (int s = 0; s < 20; ++s) {
            if (s + PW < 20) tk[s + PW] = bload16<SC1>(rs, lo16, WS_OF + (T * 20 + wrap20(s + PW + rot2)) * 1024);
            if (s + 2 < 20) {
#pragma unroll
                for (int n = 0; n < 5; ++n) wf[(s + 2) % 3][n] = wo_rd(s + 2, n);
            }
#pragma unroll
            for (int n = 0; n < 5; ++n) acc[n] = mfma(wf[s % 3][n], tk[s], acc[n]);
#pragma unroll
            for (int n = 0; n < 5; ++n) { if (s + 2 < 20) SGB(0x100, 1); SGB(0x8, 1); if (n == 1 && s + PW < 20) SGB(0x20, 1); }
            SB();
            if ((s & 3) == 3 || s == 0) LSTEP(41 + (s + 1) / 4);
        }
        STAMP(8);
        BAR();
        const unsigned w1src = WS_W1F + t3 * 20480, w1last = (cnt3 == 7 ? 6 : 0) * 20480;
        auto w1_dma = [&](int base_step, int i) {
            const int n = i % 7;
            dma16s(lds, base_step * 7168 + i * 1024, rs, lo16, w1src + (n == 6 ? w1last : n * 20480) + wrap20(base_step + i / 7 + rot3) * 1024);
        };
        float st[6] = {0, 0, 0, 0, 0, 0};
        unsigned mvb = L2_MV + qd * 16; OPAQUE(mvb);
        f32x4 mvr[2][3];
#pragma unroll
        for (int k = 0; k < 3; ++k) mvr[0][k] = LDF4(mvb + k * 320);
        u32x2 ub[5];
        const unsigned ufb = WS_UF + T * 20480;
#pragma unroll
        for (int n = 0; n < 5; ++n) {
            if (n + 1 < 5) {
#pragma unroll
                for (int k = 0; k < 3; ++k) mvr[(n + 1) & 1][k] = LDF4(mvb + k * 320 + (n + 1) * 64);
            }
            const f32x4 ga_n = mvr[n & 1][0], s2_n = mvr[n & 1][1], g2_n = mvr[n & 1][2];
            float x1[4] = {lo_f(xv[n][0]), hi_f(xv[n][0]), lo_f(xv[n][1]), hi_f(xv[n][1])};
#pragma unroll
            for (int r = 0; r < 4; ++r) x1[r] = fmaf(ga_n[r], acc[n][r], x1[r]);
            x1b[n] = (u32x2){pack2(x1[0], x1[1]), pack2(x1[2], x1[3])};
            const float xr[4] = {lo_f(x1b[n][0]), hi_f(x1b[n][0]), lo_f(x1b[n][1]), hi_f(x1b[n][1])};
            ub[n] = (u32x2){pack2(xr[0] * g2_n[0], xr[1] * g2_n[1]), pack2(xr[2] * g2_n[2], xr[3] * g2_n[3])};
            const float ur[4] = {lo_f(ub[n][0]), hi_f(ub[n][0]), lo_f(ub[n][1]), hi_f(ub[n][1])};
#pragma unroll
            for (int r = 0; r < 4; ++r) {
                st[0] += xr[r]; st[1] = fmaf(xr[r], xr[r], st[1]); st[2] += ur[r]; st[3] = fmaf(ur[r], g2_n[r], st[3]);
                st[4] = fmaf(ur[r], ur[r], st[4]); st[5] = fmaf(ur[r], s2_n[r], st[5]);
            }
            if (n == 1) bstore16<SC1>((u32x4){ub[0][0], ub[0][1], ub[1][0], ub[1][1]}, rs, lo16, ufb + (2 * c2) * 1024);
            if (n == 3) bstore16<SC1>((u32x4){ub[2][0], ub[2][1], ub[3][0], ub[3][1]}, rs, lo16, ufb + (2 * c2 + 1) * 1024);
            if (n == 4) bstore8<SC1>(ub[4], rs, lo16 + 8 * (c2 & 1), ufb + (16 + (c2 >> 1)) * 1024);
#pragma unroll
            for (int k = 0; k < 3; ++k) if (3 * n + k < 14) w1_dma(2 * w, 3 * n + k);
            SGB(0x20, 1); SGB(0x1, 18); SGB(0x20, 1); SGB(0x1, 18); SGB(0x20, 1); SGB(0x1, 24);
            SB();
        }
#pragma unroll
        for (int i = 0; i < 6; ++i) st[i] = red_add_q(st[i]);
        if (qd < 2) bstore16<SC1>(asu4(qd == 0 ? (f32x4){st[0], st[1], st[2], st[3]} : (f32x4){st[4], st[5], 0.f, 0.f}), rs, qd * 256 + t * 16, WS_ST + (T * 8 + c2) * 512);
#pragma unroll
        for (int n = 0; n < 5; ++n) asm volatile("" : "+v"(x1b[n]));
        STAMP(9);
        publish(rs, WS_F2 + (P2 * 8 + c2) * FS, tag);
#pragma unroll
        for (int i = 0; i < 21; ++i) w1_dma(8 + 3 * w, i);
    }
    STAMP(10);

    {
        LANE_IDS();
        constexpr int L3_GSC = 143360;
        const int T0 = 8 * g3 + 2 * w;
        if (w == 0) {
            unsigned off[1] = {WS_F2 + ((2 * g3 + ((l >> 3) & 1)) * 8 + (l & 7)) * FS};
            poll_words<1>(ws, off, l < 16, tag);
        }
        DRAIN();
        BAR();
        STAMP(11);
        constexpr int PW = 4;
        u32x4 tk[20][2];
        u32x4 spa[2][2]; u32x2 spb[2][2];
        float al[2], be[2], gm[2], de[2], ts[2][6];
        f32x4 red4[4], gr0, gr1;
#pragma unroll
        for (int s = 0; s < PW; ++s) { const unsigned o = WS_UF + (T0 * 20 + wrap20(s + rot3)) * 1024; tk[s][0] = bload16<SC1>(rs, lo16, o); tk[s][1] = bload16<SC1>(rs, lo16, o + 20480); }
        SB();
        LSTAMP(62);
        {
            f32x4 va[7], gram = {0, 0, 0, 0};
            u32x4 wv[2][7];
            unsigned vb = 5 * w * 7168 + lo16; OPAQUE(vb);
#pragma unroll
            for (int n = 0; n < 7; ++n) { va[n] = (f32x4){0, 0, 0, 0}; wv[0][n] = LD16(vb + n * 1024); }
            SB();
#pragma unroll
            for (int i = 0; i < 5; ++i) {
                if (i + 1 < 5) {
#pragma unroll
                    for (int n = 0; n < 7; ++n) wv[(i + 1) & 1][n] = LD16(vb + ((i + 1) * 7 + n) * 1024);
                }
#pragma unroll
                for (int n = 0; n < 7; ++n) va[n] = mfma(wv[i & 1][n], vf[i], va[n]);
                gram = mfma(vf[i], vf[i], gram);
#pragma unroll
                for (int n = 0; n < 7; ++n) { if (i + 1 < 5) SGB(0x100, 1); SGB(0x8, 1); }
                SB();
            }
            if (t < 3) {
#pragma unroll
                for (int n = 0; n < 7; ++n) STF4(L3_GSC + ((w * 8 + n) * 3 + t) * 64 + qd * 16, va[n]);
                if (qd == 0) STF4(L3_GSC + ((w * 8 + 7) * 3 + t) * 64, gram);
            }
        }
        SB();
        f32x4 acc[2][7];
#pragma unroll
        for (int n = 0; n < 7; ++n) { acc[0][n] = (f32x4){0, 0, 0, 0}; acc[1][n] = (f32x4){0, 0, 0, 0}; }
        unsigned wb0 = lo16, wb1 = 57344 + lo16, wb2 = 114688 + lo16; OPAQUE(wb0); OPAQUE(wb1); OPAQUE(wb2);
        auto w1_rd = [&](int s, int n) { return s < 8 ? LD16(wb0 + (s * 7 + n) * 1024) : s < 16 ? LD16(wb1 + ((s - 8) * 7 + n) * 1024) : LD16(wb2 + ((s - 16) * 7 + n) * 1024); };
        u32x4 wf[2][7];
#pragma unroll
        for (int n = 0; n < 7; ++n) wf[0][n] = w1_rd(0, n);
        SB();
        LSTAMP(0);
#pragma unroll
        for (int s = 0; s < 20; ++s) {
            int nv = 0;
            if (s + PW < 20) { const unsigned o = WS_UF + (T0 * 20 + wrap20(s + PW + rot3)) * 1024; tk[s + PW][0] = bload16<SC1>(rs, lo16, o); tk[s + PW][1] = bload16<SC1>(rs, lo16, o + 20480); nv += 2; }
            if (s < 16) { w2_ld(12 + s, lo16); nv += 1; }
            if (s >= 16) {
#pragma unroll
                for (int k = 0; k < 3; ++k) w2_ld(28 + (s - 16) * 3 + k, lo16);
                nv += 3;
            }
            if (s == 10 || s == 11) {
#pragma unroll
                for (int e = 0; e < 2; ++e) {
                    spa[s - 10][e] = bload16<SC1>(rs, (2 * qd + e) * 512 + t * 16, WS_ST + (T0 + s - 10) * 4096);
                    spb[s - 10][e] = bload8<SC1>(rs, (2 * qd + e) * 512 + 256 + t * 16, WS_ST + (T0 + s - 10) * 4096);
                }
                nv += 4;
            }
            int nu = 0;
            if (s == 3) {
                const int r96 = tid < 96 ? tid : 0;
#pragma unroll
                for (int u = 0; u < 4; ++u) red4[u] = LDF4(L3_GSC + (u * 96 + r96) * 16);
            }
            if (s == 4) { STF4(L3_GSC + 6144 + (tid < 96 ? tid : 96 + (tid & 31)) * 16, (red4[0] + red4[1]) + (red4[2] + red4[3])); nu = 1; }
            if (s == 9) { gr0 = LDF4(L3_GSC + 6144 + 84 * 16); gr1 = LDF4(L3_GSC + 6144 + 88 * 16); }
            if (s == 13 || s == 14) {
                const int tt = s - 13;
                const f32x4 a0 = asf4(spa[tt][0]) + asf4(spa[tt][1]);
                const float b00 = __uint_as_float(spb[tt][0][0]) + __uint_as_float(spb[tt][1][0]), b01 = __uint_as_float(spb[tt][0][1]) + __uint_as_float(spb[tt][1][1]);
                ts[tt][0] = red_add_q(a0[0]); ts[tt][1] = red_add_q(a0[1]); ts[tt][2] = red_add_q(a0[2]); ts[tt][3] = red_add_q(a0[3]); ts[tt][4] = red_add_q(b00); ts[tt][5] = red_add_q(b01);
                nu = 4;
            }
            if (s == 15) {
                const float Gg = gr0[0], Ggs = gr0[1], Gs = gr0[2], Sss = gr1[1], Ss = gr1[2], iD = 1.f / 640.f;
#pragma unroll
                for (int tt = 0; tt < 2; ++tt) {
                    const float m1 = ts[tt][0] * iD, v1 = fmaxf(ts[tt][1] * iD - m1 * m1, 0.f), r1v = __builtin_amdgcn_rsqf(v1 + 1e-5f);
                    const float m2 = (r1v * (ts[tt][2] - m1 * Gs) + Ss) * iD;
                    const float e2 = (r1v * r1v * (ts[tt][4] - 2.f * m1 * ts[tt][3] + m1 * m1 * Gg) + 2.f * r1v * (ts[tt][5] - m1 * Ggs) + Sss) * iD;
                    const float r2v = __builtin_amdgcn_rsqf(fmaxf(e2 - m2 * m2, 0.f) + 1e-5f);
                    al[tt] = r1v * r2v; be[tt] = -r1v * r2v * m1; gm[tt] = r2v; de[tt] = -r2v * m2;
                }
                nu = 4;
            }
            if (s + 1 < 20) {
#pragma unroll
                for (int n = 0; n < 7; ++n) wf[(s + 1) & 1][n] = w1_rd(s + 1, n);
            }
#pragma unroll
            for (int n = 0; n < 7; ++n) { acc[0][n] = mfma(wf[s & 1][n], tk[s][0], acc[0][n]); acc[1][n] = mfma(wf[s & 1][n], tk[s][1], acc[1][n]); }
#pragma unroll
            for (int n = 0; n < 7; ++n) {
                if (s + 1 < 20) SGB(0x100, 1);
                SGB(0x8, 1);
                if (nu == 1) SGB(0x402, 1);
                if (nu == 4) SGB(0x402, 4);
                SGB(0x8, 1);
                if (nu == 1) SGB(0x402, 1);
                if (nu == 4) SGB(0x402, 4);
                if (n < nv) SGB(0x20, 1);
            }
            SB();
            if (s == 2 || s == 8) BAR();
            LSTEP(1 + s);
        }
        STAMP(12);
        u32x2 hb[2][7];
        unsigned gsb = L3_GSC + 6144 + qd * 16; OPAQUE(gsb);
        f32x4 gsc[2][3];
#pragma unroll
        for (int k = 0; k < 3; ++k) gsc[0][k] = LDF4(gsb + 64 * k);
#pragma unroll
        for (int n = 0; n < 7; ++n) {
#pragma unroll
            for (int k = 0; k < 2; ++k) w2_ld(40 + n * 2 + k, lo16);
            if (n + 1 < 7) {
#pragma unroll
                for (int k = 0; k < 3; ++k) gsc[(n + 1) & 1][k] = LDF4(gsb + (n + 1) * 192 + 64 * k);
            }
            const f32x4 G = gsc[n & 1][0], Sv = gsc[n & 1][1], C = gsc[n & 1][2];
            const float bb[4] = {lo_f(b1v[n][0]), hi_f(b1v[n][0]), lo_f(b1v[n][1]), hi_f(b1v[n][1])};
#pragma unroll
            for (int tt = 0; tt < 2; ++tt) {
                float hv[4];
#pragma unroll
                for (int r = 0; r < 4; ++r) {
                    float z = fmaf(gm[tt], Sv[r], bb[r]); z = fmaf(be[tt], G[r], z); z = fmaf(de[tt], C[r], z); z = fmaf(al[tt], acc[tt][n][r], z);
                    const float u = z * fmaf(z * z, -0.044715f * 1.5957691216057308f * 1.4426950408889634f, -1.5957691216057308f * 1.4426950408889634f);
                    hv[r] = z * __builtin_amdgcn_rcpf(1.f + __builtin_amdgcn_exp2f(u));
                }
                hb[tt][n] = (u32x2){pack2(hv[0], hv[1]), pack2(hv[2], hv[3])};
            }
            SGB(0x20, 1); SGB(0x1, 40); SGB(0x20, 1); SGB(0x1, 60);
            SB();
#pragma unroll
            for (int tt = 0; tt < 2; ++tt) {
                const unsigned hbase = WS_HF + (T0 + tt) * 54 * 1024;
                if (n & 1) bstore16<SC1>((u32x4){hb[tt][n - 1][0], hb[tt][n - 1][1], hb[tt][n][0], hb[tt][n][1]}, rs, lo16, hbase + (3 * j3 + (n >> 1)) * 1024);
                if (n == 6 && j3 < 12) bstore8<SC1>(hb[tt][6], rs, lo16 + 8 * (j3 & 1), hbase + (48 + (j3 >> 1)) * 1024);
            }
            SB();
        }
        STAMP(13);
        publish(rs, WS_F3 + (g3 * 16 + j3) * FS, tag);
    }
    STAMP(14);

    {
        LANE_IDS();
        f32x4 gb[5]; u32x2 b2v[5];
#pragma unroll
        for (int f = 54; f < 70; ++f) w2_ld(f, lo16);
        SB();
        if (w == 0) {
            unsigned off[1] = {WS_F3 + ((P2 >> 1) * 16 + (l & 15)) * FS};
            poll_words<1>(ws, off, l < 16, tag);
        }
        BAR();
        STAMP(15);
        constexpr int PD = 3;
        u32x4 tk[14][4];
        const unsigned tokb = __builtin_amdgcn_readfirstlane(WS_HF + (4 * P2 * 54 + s4) * 1024);
        auto issue = [&](int i) {
            const int s = step4(i);
#pragma unroll
            for (int a = 0; a < 4; ++a) tk[i][a] = bload16<SC1>(rs, lo16, tokb + (((a + w) & 3) * 54 + s) * 1024);
        };
#pragma unroll
        for (int i = 0; i < PD; ++i) issue(i);
        f32x4 acc[4][5];
#pragma unroll
        for (int a = 0; a < 4; ++a)
#pragma unroll
            for (int n = 0; n < 5; ++n) acc[a][n] = (f32x4){0, 0, 0, 0};
        SB();
        LSTAMP(24);
#pragma unroll
        for (int i = 0; i < 14; ++i) {
            if (i < 13 || w < 2) {
                if (i + PD < 14) issue(i + PD);
                if (i == 11) {
#pragma unroll
                    for (int n = 0; n < 5; ++n) {
                        const int f = 80 * c2 + 16 * n;
                        gb[n] = asf4(bload16<SC1>(rs, qd * 16, WS_MODV + (3200 + f) * 4));
                        b2v[n] = *(const GAS u32x2*)(p.b2 + f + 4 * qd);
                    }
                }
#pragma unroll
                for (int a = 0; a < 4; ++a)
#pragma unroll
                    for (int n = 0; n < 5; ++n) acc[a][n] = mfma(w2f[i][n], tk[i][a], acc[a][n]);
#pragma unroll
                for (int a = 0; a < 4; ++a) { SGB(0x8, 2); if (i == 11) SGB(0x20, 3); else SGB(0x20, 1); SGB(0x8, 3); }
            }
            SB();
            LSTEP(25 + i);
        }
        STAMP(16);
#pragma unroll
        for (int a = 1; a < 4; ++a) {
            unsigned d = ((((a + w) & 3) * 3 + (3 - a)) * 5) * 1024 + lo16; OPAQUE(d);
#pragma unroll
            for (int n = 0; n < 5; ++n) STF4(d + n * 1024, acc[a][n]);
        }
        BAR();
        constexpr int L4_OUT = 61440;
        unsigned rb = w * 15360 + lo16; OPAQUE(rb);
        f32x4 part[5][3];
#pragma unroll
        for (int n = 0; n < 5; ++n)
#pragma unroll
            for (int u = 0; u < 3; ++u) part[n][u] = LDF4(rb + (u * 5 + n) * 1024);
        SB();
#pragma unroll
        for (int n = 0; n < 5; ++n) {
            const f32x4 v = (acc[0][n] + part[n][0]) + (part[n][1] + part[n][2]);
            const float x1[4] = {lo_f(x1b[n][0]), hi_f(x1b[n][0]), lo_f(x1b[n][1]), hi_f(x1b[n][1])};
            const float bv[4] = {lo_f(b2v[n][0]), hi_f(b2v[n][0]), lo_f(b2v[n][1]), hi_f(b2v[n][1])};
            float o[4];
#pragma unroll
            for (int r = 0; r < 4; ++r) o[r] = fmaf(gb[n][r], v[r] + bv[r], x1[r]);
            ST8(L4_OUT + (16 * w + t) * 176 + n * 32 + qd * 8, ((u32x2){pack2(o[0], o[1]), pack2(o[2], o[3])}));
        }
        asm volatile("s_waitcnt lgkmcnt(0)" ::: "memory");
        const rsrc_t ro = mk_rsrc(p.out, 2048 * 1280);
#pragma unroll
        for (int k = 0; k < 3; ++k) {
            const int ch = l + 64 * k;
            if (ch < 160) {
                const int row = ch / 10, cc = ch % 10;
                const u32x4 v = LD16(L4_OUT + (16 * w + row) * 176 + cc * 16);
                bstore16<OUT_AUX>(v, ro, row * 1280 + cc * 16, (16 * (4 * P2 + w)) * 1280 + c2 * 160);
            }
        }
    }
    STAMP(17);
    LSTAMP_DUMP;
    asm volatile(".Ladaln_end:" ::: "memory");
}

extern "C" void kernel_launch(void* const* d_in, const int* in_sizes, int n_in, void* d_out, int out_size, void* d_ws, size_t ws_size, hipStream_t stream) {
    Params p;
    p.x = (const u16*)d_in[0]; p.wqkv = (const u16*)d_in[1]; p.wo = (const u16*)d_in[2]; p.w1 = (const u16*)d_in[4]; p.b1 = (const u16*)d_in[5];
    p.w2 = (const u16*)d_in[6]; p.b2 = (const u16*)d_in[7]; p.c = (const u16*)d_in[8]; p.wm = (const u16*)d_in[9];
    p.out = (u16*)d_out; p.ws = (char*)d_ws;
    (void)hipFuncSetAttribute((const void*)adaLN_block_kernel, hipFuncAttributeMaxDynamicSharedMemorySize, LDS_BYTES);
    adaLN_block_kernel<<<256, 256, LDS_BYTES, stream>>>(p);
}
```

```cpp
#include <hip/hip_runtime.h>
#include <stdint.h>

typedef unsigned short u16;
typedef float f32x4 __attribute__((ext_vector_type(4)));
typedef float f32x2 __attribute__((ext_vector_type(2)));
typedef __bf16 bf16x8 __attribute__((ext_vector_type(8)));
typedef __bf16 bf16x2 __attribute__((ext_vector_type(2)));
typedef unsigned u32x4 __attribute__((ext_vector_type(4)));
typedef unsigned u32x2 __attribute__((ext_vector_type(2)));
typedef short s16x4 __attribute__((ext_vector_type(4)));
typedef __amdgpu_buffer_rsrc_t rsrc_t;
#define LAS __attribute__((address_space(3)))
#define GAS __attribute__((address_space(1)))
#define DI __device__ __forceinline__

static constexpr unsigned FS = 64;
static constexpr unsigned WS_EPOCH = 0;
static constexpr unsigned WS_TMO   = 256;
static constexpr unsigned WS_FA    = 1024;
static constexpr unsigned WS_FC    = WS_FA + 160 * FS;
static constexpr unsigned WS_FD    = WS_FC + 128 * FS;
static constexpr unsigned WS_F1    = WS_FD + 1024;
static constexpr unsigned WS_F2    = WS_F1 + 256 * FS;
static constexpr unsigned WS_F3    = WS_F2 + 256 * FS;
static constexpr unsigned WS_FEND  = WS_F3 + 256 * FS;
static constexpr unsigned WS_MODV = 131072;
static constexpr unsigned WS_MODB = WS_MODV + 16384;
static constexpr unsigned WS_BP   = WS_MODB + 8192;
static constexpr unsigned WS_XNF  = WS_BP + 20 * 16 * 128 * 4;
static constexpr unsigned WS_WQF  = WS_XNF + 128 * 20 * 1024;
static constexpr unsigned WS_WOF  = WS_WQF + 16 * 20 * 8 * 1024;
static constexpr unsigned WS_W1F  = WS_WOF + 8 * 20 * 5 * 1024;
static constexpr unsigned WS_W2F  = WS_W1F + 108 * 20 * 1024;
static constexpr unsigned WS_OF   = WS_W2F + 8 * 54 * 5 * 1024;
static constexpr unsigned WS_UF   = WS_OF + 128 * 20 * 1024;
static constexpr unsigned WS_ST   = WS_UF + 128 * 20 * 1024;
static constexpr unsigned WS_HF   = WS_ST + 128 * 8 * 512;
static constexpr unsigned WS_END  = WS_HF + 128 * 54 * 1024;

static constexpr int LDS_BYTES = 163840;
static constexpr int SC1 = 16;
static constexpr int PF_BLOCKS = 1;
static constexpr unsigned SPIN_CAP = 40000;

struct Params {
    const u16* x; const u16* wqkv; const u16* wo; const u16* w1; const u16* b1; const u16* w2; const u16* b2; const u16* c; const u16* wm;
    u16* out; char* ws;
};

DI float lo_f(unsigned d) { return __uint_as_float(d << 16); }
DI float hi_f(unsigned d) { return __uint_as_float(d & 0xffff0000u); }
DI unsigned pack2(float a, float b) { f32x2 v = {a, b}; return __builtin_bit_cast(unsigned, __builtin_convertvector(v, bf16x2)); }
DI float rbf(float a) { return lo_f(pack2(a, 0.f)); }
DI u32x2 pack4(const f32x4& v) { return (u32x2){pack2(v[0], v[1]), pack2(v[2], v[3])}; }
DI f32x4 mfma(u32x4 a, u32x4 b, f32x4 c) { return __builtin_amdgcn_mfma_f32_16x16x32_bf16(__builtin_bit_cast(bf16x8, a), __builtin_bit_cast(bf16x8, b), c, 0, 0, 0); }
DI float red_add_q(float v) {
    auto a = __builtin_amdgcn_permlane16_swap(__float_as_uint(v), __float_as_uint(v), false, false);
    v = __uint_as_float(a[0]) + __uint_as_float(a[1]);
    auto b = __builtin_amdgcn_permlane32_swap(__float_as_uint(v), __float_as_uint(v), false, false);
    return __uint_as_float(b[0]) + __uint_as_float(b[1]);
}
DI float red_max_q(float v) {
    auto a = __builtin_amdgcn_permlane16_swap(__float_as_uint(v), __float_as_uint(v), false, false);
    v = fmaxf(__uint_as_float(a[0]), __uint_as_float(a[1]));
    auto b = __builtin_amdgcn_permlane32_swap(__float_as_uint(v), __float_as_uint(v), false, false);
    return fmaxf(__uint_as_float(b[0]), __uint_as_float(b[1]));
}
template <int C> DI float dpp_add(float v) { return v + __builtin_bit_cast(float, __builtin_amdgcn_update_dpp(0, __builtin_bit_cast(int, v), C, 0xf, 0xf, true)); }
DI float red16(float v) { v = dpp_add<0xB1>(v); v = dpp_add<0x4E>(v); v = dpp_add<0x141>(v); return dpp_add<0x140>(v); }
DI unsigned swap32(unsigned v) { auto a = __builtin_amdgcn_permlane32_swap(v, v, false, false); return (threadIdx.x & 32) ? a[0] : a[1]; }
DI rsrc_t mk_rsrc(const void* p, unsigned bytes) { return __builtin_amdgcn_make_buffer_rsrc((void*)p, 0, bytes, 0x00020000); }
template <int AUX> DI u32x4 bload16(rsrc_t r, unsigned voff, unsigned soff) { return __builtin_bit_cast(u32x4, __builtin_amdgcn_raw_buffer_load_b128(r, voff, soff, AUX)); }
template <int AUX> DI u32x2 bload8(rsrc_t r, unsigned voff, unsigned soff) { return __builtin_bit_cast(u32x2, __builtin_amdgcn_raw_buffer_load_b64(r, voff, soff, AUX)); }
template <int AUX> DI unsigned bload4(rsrc_t r, unsigned voff, unsigned soff) { return __builtin_amdgcn_raw_buffer_load_b32(r, voff, soff, AUX); }
template <int AUX> DI void bstore16(u32x4 v, rsrc_t r, unsigned voff, unsigned soff) { __builtin_amdgcn_raw_buffer_store_b128(v, r, voff, soff, AUX); }
template <int AUX> DI void bstore8(u32x2 v, rsrc_t r, unsigned voff, unsigned soff) { __builtin_amdgcn_raw_buffer_store_b64(v, r, voff, soff, AUX); }
template <int AUX> DI void bstore4(unsigned v, rsrc_t r, unsigned voff, unsigned soff) { __builtin_amdgcn_raw_buffer_store_b32(v, r, voff, soff, AUX); }
DI f32x4 asf4(u32x4 v) { return __builtin_bit_cast(f32x4, v); }
DI u32x4 asu4(f32x4 v) { return __builtin_bit_cast(u32x4, v); }
#define LANE_IDS() int tz_ = threadIdx.x; asm volatile("" : "+v"(tz_)); const int tid = tz_, l = tid & 63, t = l & 15, qd = l >> 4; const unsigned lo16 = l * 16; (void)t; (void)qd; (void)lo16
#define OPAQUE(x) asm volatile("" : "+v"(x))
#define SB() __builtin_amdgcn_sched_barrier(0)
#define BAR() asm volatile("s_waitcnt lgkmcnt(0)\n\ts_barrier" ::: "memory")
#define DRAIN() asm volatile("s_waitcnt vmcnt(0)" ::: "memory")
#define SGB(mask, n) __builtin_amdgcn_sched_group_barrier(mask, n, 0)
#define LD16(off) (*(const LAS u32x4*)((const LAS char*)lds + (off)))
#define LD8(off) (*(const LAS u32x2*)((const LAS char*)lds + (off)))
#define LDF4(off) (*(const LAS f32x4*)((const LAS char*)lds + (off)))
#define LDF(off) (*(const LAS float*)((const LAS char*)lds + (off)))
#define ST16(off, v) (*(LAS u32x4*)((LAS char*)lds + (off)) = (v))
#define ST8(off, v) (*(LAS u32x2*)((LAS char*)lds + (off)) = (v))
#define STF4(off, v) (*(LAS f32x4*)((LAS char*)lds + (off)) = (v))
#define STF(off, v) (*(LAS float*)((LAS char*)lds + (off)) = (v))
DI u32x2 tr_read(const char* lds, unsigned off) { return __builtin_bit_cast(u32x2, __builtin_amdgcn_ds_read_tr16_b64_v4i16((LAS s16x4*)((LAS char*)lds + off))); }

DI void flag_store(char* ws, unsigned off, unsigned tag) { __hip_atomic_store((GAS unsigned*)(ws + off), tag, __ATOMIC_RELAXED, __HIP_MEMORY_SCOPE_AGENT); }
DI unsigned flag_load(const char* ws, unsigned off) { return __hip_atomic_load((const GAS unsigned*)(ws + off), __ATOMIC_RELAXED, __HIP_MEMORY_SCOPE_AGENT); }
DI void flag_put(rsrc_t rs, unsigned off, unsigned tag, bool me) { bstore4<SC1>(tag, rs, me ? off : 0x7ff00000u, 0); }
DI void publish(rsrc_t rs, unsigned off, unsigned tag) { DRAIN(); BAR(); flag_put(rs, off, tag, threadIdx.x == 0); }
template <int N> DI void poll_words(char* ws, const unsigned (&off)[N], bool active, unsigned tag) {
#pragma nounroll
    for (unsigned spins = 0;; ++spins) {
        bool ok = true;
#pragma unroll
        for (int k = 0; k < N; ++k) ok &= flag_load(ws, off[k]) == tag;
        if (__all(ok || !active)) break;
        if (spins > SPIN_CAP) { if ((threadIdx.x & 63) == 0) flag_store(ws, WS_TMO, tag); break; }
        __builtin_amdgcn_s_sleep(1);
    }
}

DI void check_all_blocks(char* ws, rsrc_t rs, unsigned base, u32x4 v, unsigned tag) {
    asm volatile("" : "+v"(v));
#pragma nounroll
    for (unsigned spins = 0; !__all(v[0] == tag && v[1] == tag && v[2] == tag && v[3] == tag); ++spins) {
        if (spins > SPIN_CAP) { if ((threadIdx.x & 63) == 0) flag_store(ws, WS_TMO, tag); break; }
        __builtin_amdgcn_s_sleep(1);
        v = bload16<SC1>(rs, (threadIdx.x & 63) * 16, base);
        asm volatile("" : "+v"(v) :: "memory");
    }
}

DI void tiles_p3(int s, int& ta, int& tb) { if (s < 16) { ta = 5 * (s >> 1) + 2 * (s & 1); tb = ta + 1; } else { ta = 10 * (s - 16) + 4; tb = ta + 5; } }
DI void tiles_p4(int s, int& ta, int& tb) { if (s < 48) { int j = s / 3, pr = s % 3; int st = j < 12 ? 7 * j : 84 + 6 * (j - 12); ta = st + 2 * pr; tb = ta + 1; } else { int m = s - 48; ta = 14 * m + 6; tb = ta + 7; } }

static constexpr int L0_SC = 81920, L0_GS = 83200, L0_CRAW = 83968, L0_RW = 86016, L0_D = 101376, L0_XS = 156160;

template <int AUX> DI void dma(char* lds, unsigned lds_off, rsrc_t r, unsigned voff, unsigned soff) {
    __builtin_amdgcn_raw_ptr_buffer_load_lds(r, (LAS void*)((LAS char*)lds + lds_off), 16, voff, soff, 0, AUX);
}
DI void dma16(char* lds, unsigned lds_off, rsrc_t r, unsigned voff, unsigned soff) {
    const unsigned dst = (unsigned)(size_t)(LAS char*)lds + lds_off;
    asm volatile("s_mov_b32 m0, %0\n\ts_nop 0\n\tbuffer_load_dwordx4 %1, %2, %3 offen lds" :: "s"(dst), "v"(voff), "s"(r), "s"(soff) : "memory");
}
DI void dma16s(char* lds, unsigned lds_off, rsrc_t r, unsigned voff, unsigned soff) { dma<SC1>(lds, lds_off, r, voff, soff); }
#define WAITV(n) asm volatile("s_waitcnt vmcnt(%0)" :: "n"(n) : "memory")

struct DPlan { rsrc_t r; unsigned v0, v1, sa, sb, st; bool on; };
struct DOut { unsigned dst[8]; u32x4 f[8]; };
DI void dtask_plan(const Params& p, int id, int l, DPlan& d, DOut& o) {
    int ra0, rb0, rstep, cg, lim; unsigned pitch, d0, r5, stride, jump;
    if (id < 370) {
        const bool wo = id < 100; const int i2 = wo ? id : id - 100, s = i2 / 5; cg = i2 - 5 * s; pitch = 1280;
        if (wo) { d.r = mk_rsrc(p.wo, 640 * 1280); if (s < 16) { ra0 = 40 * s; rstep = 4; } else { ra0 = 160 * (s - 16) + 16; rstep = 40; } rb0 = ra0 + 20; }
        else { d.r = mk_rsrc(p.w2, 1728 * 1280); int ta, tb; tiles_p4(s, ta, tb); ra0 = 16 * ta; rb0 = 16 * tb; rstep = 4; }
        const int c0 = (8 * cg) / 5;
        r5 = 8 * cg - 5 * c0; d0 = (wo ? WS_WOF + (c0 * 20 + s) * 5120 : WS_W2F + (c0 * 54 + s) * 5120) + r5 * 1024;
        stride = 1024; jump = wo ? 19 * 5120 : 53 * 5120; lim = 8;
    } else {
        const int i2 = id < 650 ? id - 370 : 0, s = i2 / 14; cg = i2 - 14 * s; pitch = 3456; d.r = mk_rsrc(p.w1, 640 * 3456);
        int ta, tb; tiles_p3(s, ta, tb); ra0 = 16 * ta; rb0 = 16 * tb; rstep = 4;
        r5 = 0; d0 = WS_W1F + (8 * cg * 20 + s) * 1024; stride = 20480; jump = 0; lim = id < 650 ? (cg == 13 ? 4 : 8) : 0;
    }
    const int q = l >> 4, cp = l & 15;
    d.v0 = q * pitch + ((cp ^ (q << 1)) * 16); d.v1 = q * pitch + ((cp ^ ((4 + q) << 1)) * 16);
    d.sa = ra0 * pitch + cg * 256; d.sb = rb0 * pitch + cg * 256; d.st = rstep * pitch; d.on = lim > 0;
#pragma unroll
    for (int nt = 0; nt < 8; ++nt) {
        const unsigned v = d0 + nt * stride + ((r5 + nt >= 5 ? 1u : 0u) + (r5 + nt >= 10 ? 1u : 0u)) * jump;
        o.dst[nt] = __builtin_amdgcn_readfirstlane(nt < lim ? v : 0x7ff00000u);
    }
}
DI void dtask_go(const DPlan& d, char* lds, unsigned scr, int w) {
    if (d.on) {
#pragma unroll
        for (int i = 0; i < 8; ++i) dma16(lds, scr + w * 8192 + i * 1024, d.r, (i & 1) ? d.v1 : d.v0, (i < 4 ? d.sa : d.sb) + (i & 3) * d.st);
    }
}
DI void dtask_read(DOut& o, char* lds, unsigned scr, int w, int l) {
    const int qd = l >> 4, q = (l >> 2) & 3, pp = l & 3, r7 = 4 * (qd & 1) + q;
    const unsigned a0 = scr + w * 8192 + (4 * qd + q) * 256 + pp * 8;
#pragma unroll
    for (int nt = 0; nt < 8; ++nt) {
        const u32x2 ua = tr_read(lds, a0 + ((nt ^ r7) * 32)), ub = tr_read(lds, a0 + 4096 + ((nt ^ r7) * 32));
        o.f[nt] = (u32x4){ua[0], ua[1], ub[0], ub[1]};
    }
}
template <int NT> DI void dtask_frag(const DOut& o, rsrc_t rs, int l) { bstore16<SC1>(o.f[NT], rs, l * 16, o.dst[NT]); }

template <int I0, int I1> DI void wm_dma(const Params& p, char* lds, int w, int l, unsigned colA, unsigned colB, int rot) {
    const rsrc_t rw = mk_rsrc(p.wm, 640 * 3840 * 2);
    const int rl = l >> 3, cp = l & 7;
    const unsigned c = cp ^ ((((rl >> 1) & 1) | ((w & 1) << 1)) << 1);
    const unsigned voff = rl * 7680 + (c < 4 ? colA + 8 * c : colB + 8 * (c - 4)) * 2;
    int piece = 4 * I0 + w + rot; piece = piece >= 80 ? piece - 80 : piece;
#pragma unroll
    for (int i = I0; i < I1; ++i) {
        dma16(lds, piece * 1024, rw, voff, piece * 61440);
        piece = piece >= 76 ? piece - 76 : piece + 4;
    }
}
DI void c_dma(const Params& p, char* lds, int w, int l) {
    dma16(lds, L0_CRAW + (w & 1) * 1024, mk_rsrc(p.c, 1280), ((w & 1) * 64 + l) * 16, 0);
}
DI void silu_table(char* lds, int tid) {
    if (tid < 80) {
        const u32x4 creg = LD16(L0_CRAW + tid * 16);
        u32x4 o;
#pragma unroll
        for (int d = 0; d < 4; ++d) { const float a = lo_f(creg[d]), b = hi_f(creg[d]); o[d] = pack2(a * __builtin_amdgcn_rcpf(1.f + __expf(-a)), b * __builtin_amdgcn_rcpf(1.f + __expf(-b))); }
        ST16(L0_SC + tid * 16, o);
    }
}
DI float mod_gemv(char* lds, int w, int l) {
    const int qd = l >> 4, q = (l >> 2) & 3, pp = l & 3;
    f32x4 acc = {0, 0, 0, 0}, acc2 = {0, 0, 0, 0};
    const int r0 = 8 * qd + q;
    const unsigned sg = ((r0 >> 1) & 1) | (((r0 >> 3) & 1) << 1);
    unsigned a0 = r0 * 128 + ((w ^ sg) * 32) + pp * 8, a1 = a0 + 40960, sb = L0_SC + qd * 16;
    OPAQUE(a0); OPAQUE(a1); OPAQUE(sb);
    u32x2 tb[2][4]; u32x4 sc[2][2];
    auto rd = [&](int pr) {
        const unsigned ab = pr < 5 ? a0 : a1; const int po = (pr < 5 ? pr : pr - 5) * 8192;
        tb[pr & 1][0] = tr_read(lds, ab + po); tb[pr & 1][1] = tr_read(lds, ab + po + 512); tb[pr & 1][2] = tr_read(lds, ab + po + 4096); tb[pr & 1][3] = tr_read(lds, ab + po + 4608);
        sc[pr & 1][0] = LD16(sb + pr * 128); sc[pr & 1][1] = LD16(sb + pr * 128 + 64);
    };
    rd(0);
#pragma unroll
    for (int pr = 0; pr < 10; ++pr) {
        if (pr + 1 < 10) rd(pr + 1);
        SB();
        acc = mfma(sc[pr & 1][0], (u32x4){tb[pr & 1][0][0], tb[pr & 1][0][1], tb[pr & 1][1][0], tb[pr & 1][1][1]}, acc);
        acc2 = mfma(sc[pr & 1][1], (u32x4){tb[pr & 1][2][0], tb[pr & 1][2][1], tb[pr & 1][3][0], tb[pr & 1][3][1]}, acc2);
        SB();
    }
    return rbf(acc[0] + acc2[0]);
}
DI void x_dma(const Params& p, char* lds, unsigned img, int T, int w, int l) {
    const rsrc_t rx = mk_rsrc(p.x, 2048 * 1280);
    const int r4 = l >> 4, row = 4 * w + r4;
    const unsigned voff = row * 1280 + (((l & 15) ^ row) * 16);
#pragma unroll
    for (int cb = 0; cb < 5; ++cb) dma16(lds, img + (w * 5 + cb) * 1024, rx, voff, T * 16 * 1280 + cb * 256);
}
DI void x_stats(char* lds, unsigned img, unsigned stat, int w, int l) {
    u32x4 v[5];
#pragma unroll
    for (int cb = 0; cb < 5; ++cb) v[cb] = LD16(img + (w * 5 + cb) * 1024 + l * 16);
    f32x2 s1 = {0.f, 0.f}, s2 = {0.f, 0.f};
#pragma unroll
    for (int i = 0; i < 5; ++i)
#pragma unroll
        for (int d = 0; d < 4; ++d) { const f32x2 x = {lo_f(v[i][d]), hi_f(v[i][d])}; s1 += x; s2 = __builtin_elementwise_fma(x, x, s2); }
    const float mean = red16(s1[0] + s1[1]) * (1.f / 640.f), ex2 = red16(s2[0] + s2[1]) * (1.f / 640.f);
    const float rstd = __builtin_amdgcn_rsqf(fmaxf(ex2 - mean * mean, 0.f) + 1e-5f);
    if ((l & 15) == 0) *(LAS f32x2*)((LAS char*)lds + stat + (4 * w + (l >> 4)) * 8) = (f32x2){rstd, -mean * rstd};
}
DI unsigned ximg_off(int t, int byte_in_row) { return ((t >> 2) * 5 + (byte_in_row >> 8)) * 1024 + (t & 3) * 256 + ((((byte_in_row >> 4) & 15) ^ t) * 16) + (byte_in_row & 15); }
DI void x_frags(char* lds, unsigned img, unsigned stat, rsrc_t rs, int T, int w, int l) {
    const int t = l & 15, qd = l >> 4;
    const f32x2 st = *(const LAS f32x2*)((const LAS char*)lds + stat + t * 8);
    const f32x2 r2 = {st[0], st[0]}, m2 = {st[1], st[1]};
    u32x4 v[5];
#pragma unroll
    for (int i = 0; i < 5; ++i) v[i] = LD16(img + ximg_off(t, (w + 4 * i) * 64 + qd * 16));
#pragma unroll
    for (int i = 0; i < 5; ++i) {
        u32x4 o;
#pragma unroll
        for (int d = 0; d < 4; ++d) { const f32x2 y = __builtin_elementwise_fma((f32x2){lo_f(v[i][d]), hi_f(v[i][d])}, r2, m2); o[d] = pack2(y[0], y[1]); }
        bstore16<SC1>(o, rs, l * 16, WS_XNF + (T * 20 + w + 4 * i) * 1024);
    }
}
struct TileKV { u32x2 ka0, ka1, k2, va0, va1, z; };
struct AttnMask { bool c0, c1, p0, p1, odd; unsigned sel0, sel1; };
DI AttnMask attn_mask(int t, int qd) {
    AttnMask m; const int par = t & 1, dc = t - 4 * qd - par;
    m.odd = par; m.c0 = dc >= 0; m.c1 = dc >= 2; m.p0 = dc + 16 <= 14; m.p1 = dc + 14 <= 14;
    m.sel0 = par ? 0x01000c0cu : 0x0c0c0100u; m.sel1 = par ? 0x03020c0cu : 0x0c0c0302u;
    return m;
}
template <class BG> DI void attn_unit(const u32x2 (&qa)[2], const u32x2& xq, const TileKV& cur, const TileKV& prev, bool prev_ok, const AttnMask& m, int qd, u32x4& omain, u32x2& oleft, BG&& bg) {
    const float cs = 0.22360679774997896f * 1.4426950408889634f, ninf = -__builtin_inff();
    f32x4 oa[2], oz[2];
#pragma unroll
    for (int h = 0; h < 2; ++h) {
        const u32x4 qf = {qa[h][0], qa[h][1], qd == h ? xq[0] : 0u, qd == h ? xq[1] : 0u};
        const u32x2 kcp = h == 0 ? cur.ka0 : cur.ka1, kpp = h == 0 ? prev.ka0 : prev.ka1;
        const f32x4 z4 = {0.f, 0.f, 0.f, 0.f};
        const f32x4 sp = mfma((u32x4){kpp[0], kpp[1], prev.k2[0], prev.k2[1]}, qf, z4);
        const f32x4 sc = mfma((u32x4){kcp[0], kcp[1], cur.k2[0], cur.k2[1]}, qf, z4);
        bg(5 * h);
        float e0 = m.odd ? sp[1] : sp[0], e1 = m.odd ? sp[3] : sp[2], e2 = m.odd ? sc[1] : sc[0], e3 = m.odd ? sc[3] : sc[2];
        e0 = (m.p0 && prev_ok) ? e0 : ninf; e1 = (m.p1 && prev_ok) ? e1 : ninf; e2 = m.c0 ? e2 : ninf; e3 = m.c1 ? e3 : ninf;
        bg(5 * h + 1);
        const float mx = red_max_q(fmaxf(fmaxf(e0, e1), fmaxf(e2, e3))), mc = -mx * cs;
        bg(5 * h + 2);
        e0 = __builtin_amdgcn_exp2f(fmaf(e0, cs, mc)); e1 = __builtin_amdgcn_exp2f(fmaf(e1, cs, mc)); e2 = __builtin_amdgcn_exp2f(fmaf(e2, cs, mc)); e3 = __builtin_amdgcn_exp2f(fmaf(e3, cs, mc));
        bg(5 * h + 3);
        const float inv = __builtin_amdgcn_rcpf(red_add_q((e0 + e1) + (e2 + e3)));
        bg(5 * h + 4);
        const unsigned pp = pack2(e0 * inv, e1 * inv), pc = pack2(e2 * inv, e3 * inv);
        const u32x4 pf = {__builtin_amdgcn_perm(0u, pp, m.sel0), __builtin_amdgcn_perm(0u, pp, m.sel1), __builtin_amdgcn_perm(0u, pc, m.sel0), __builtin_amdgcn_perm(0u, pc, m.sel1)};
        const u32x2 vc2 = h == 0 ? cur.va0 : cur.va1, vp2 = h == 0 ? prev.va0 : prev.va1;
        oa[h] = mfma((u32x4){vp2[0], vp2[1], vc2[0], vc2[1]}, pf, z4);
        oz[h] = mfma((u32x4){prev.z[0], prev.z[1], cur.z[0], cur.z[1]}, pf, z4);
    }
    const u32x2 a0 = pack4(oa[0]), a1 = pack4(oa[1]);
    omain = (u32x4){a0[0], a0[1], a1[0], a1[1]};
    const u32x2 l0 = pack4(oz[0]), l1 = pack4(oz[1]);
    oleft = qd == 0 ? l0 : l1;
}

__global__ __launch_bounds__(256, 1) void adaLN_block_kernel(Params p) {
    extern __shared__ __attribute__((aligned(16))) char lds[];
    const int b = blockIdx.x, w = __builtin_amdgcn_readfirstlane(threadIdx.x >> 6);
    const int xcd = b & 7, bi = b >> 3;
    char* ws = p.ws;
    const rsrc_t rs = mk_rsrc(ws, WS_END);
    {
        unsigned long long pc; unsigned len;
        asm volatile("s_getpc_b64 %0\n.Ladaln_pc:\n\ts_mov_b32 %1, .Ladaln_end-.Ladaln_pc" : "=s"(pc), "=s"(len));
        unsigned off = (bi * 256 + threadIdx.x) * 64u;
        off = off < len ? off : 0u;
        const unsigned long long a = pc + off;
        const unsigned dst = (unsigned)(size_t)(LAS char*)lds + LDS_BYTES - 1024 + w * 256;
        asm volatile("s_mov_b32 m0, %0\n\ts_nop 0\n\tglobal_load_lds_dword %1, off" :: "s"(dst), "v"(a) : "memory");
    }
    const unsigned epoch = *(const __attribute__((address_space(4))) unsigned*)(size_t)(ws + WS_EPOCH);
    unsigned tag;
#define MAKE_TAG() do { unsigned e_ = epoch; asm volatile("" : "+s"(e_)); tag = e_ + 0x9E3779B9u; } while (0)

    DOut dout;
    {
        LANE_IDS();
        const bool isA = bi < 20, isB = !isA && bi < 25, isC = !isA && !isB;
        const int sA = bi < 16 ? xcd + 8 * (bi >> 3) : 16 + (xcd >> 1), jg = bi < 16 ? (bi & 7) : 4 * (xcd & 1) + (bi - 16), idxB = xcd * 5 + (bi - 20);
        const int aidx = jg * 20 + sA;
        const int idxC = xcd * 7 + (bi - 25);
        const int Ts = idxC < 16 ? 3 * idxC : 48 + 2 * (idxC - 16), nT = idxC < 16 ? 3 : 2;
        const int did = w == 0 ? 1000 : b * 3 + w - 1;
        DPlan dp;
        if (isC) {
#pragma unroll
            for (int k = 0; k < 3; ++k) if (k < nT) x_dma(p, lds, k * 20480, Ts + k, w, l);
            MAKE_TAG();
            dtask_plan(p, did, l, dp, dout);
            if (nT == 3) { WAITV(10); x_stats(lds, 0, L0_XS, w, l); WAITV(5); x_stats(lds, 20480, L0_XS + 128, w, l); WAITV(0); dtask_go(dp, lds, L0_D, w); x_stats(lds, 40960, L0_XS + 256, w, l); }
            else { WAITV(5); x_stats(lds, 0, L0_XS, w, l); WAITV(0); dtask_go(dp, lds, L0_D, w); x_stats(lds, 20480, L0_XS + 128, w, l); }
            BAR();
#pragma unroll
            for (int k = 0; k < 3; ++k) if (k < nT) x_frags(lds, k * 20480, L0_XS + k * 128, rs, Ts + k, w, l);
            DRAIN();
            BAR();
            flag_put(rs, WS_FC + (Ts + (tid & 3)) * FS, tag, tid < nT);
        } else {
            const unsigned colA = isA ? 640 + 32 * sA : 1280 + 64 * idxB, colB = isA ? 32 * sA : 1280 + 64 * idxB + 32;
            const int rot = isA ? (bi < 16 ? 10 * jg : 20 * (bi - 16)) : 0;
            c_dma(p, lds, w, l);
            wm_dma<0, 10>(p, lds, w, l, colA, colB, rot);
            WAITV(10);
            silu_table(lds, tid);
            wm_dma<10, 20>(p, lds, w, l, colA, colB, rot);
            if (isA) {
                const rsrc_t rq = mk_rsrc(p.wqkv, 640 * 3840);
#pragma unroll
                for (int m = 0; m < 4; ++m) {
                    const int n = 64 * (4 * m + w) + l, rho = n / 30, c = n % 30;
                    const int k = (rho & 19) | ((rho & 4) << 1) | ((rho & 8) >> 1);
                    if (4 * m + w < 15) dma16(lds, L0_RW + (4 * m + w) * 1024, rq, k * 3840 + (c / 10) * 1280 + jg * 160 + (c % 10) * 16, sA * 32 * 3840);
                }
            }
            MAKE_TAG();
            dtask_plan(p, did, l, dp, dout);
            WAITV(0);
            dtask_go(dp, lds, L0_D, w);
            BAR();
            float v = mod_gemv(lds, w, l);
            if (isA) {
                const float gv = w < 2 ? rbf(1.f + v) : v;
                if (l < 16) { STF(L0_GS + (w * 16 + l) * 4, gv); *(LAS u16*)((LAS char*)lds + L0_GS + 256 + (w * 16 + l) * 2) = (u16)(__float_as_uint(gv) >> 16); }
                const int q = (l >> 2) & 3, pp = l & 3, jj = w >> 1, j = 2 * jg + jj;
                const unsigned rbase = L0_RW + (16 * (qd >> 1) + 4 * (qd & 1) + q) * 480 + 80 * jj;
                u32x2 ua[4], ub[4];
#pragma unroll
                for (int i = 0; i < 4; ++i) {
                    const unsigned lo = i < 2 ? i * 40 + 8 * pp : 160 + (i - 2) * 40 + 8 * pp;
                    const unsigned hi = i == 0 ? (pp >> 1) * 160 + (pp & 1) * 40 + 32 : i < 3 ? 320 + (i - 1) * 40 + 8 * pp : 320 + (pp & 1) * 40 + 32;
                    const unsigned po = (w & 1) ? hi : lo;
                    ua[i] = tr_read(lds, rbase + po); ub[i] = tr_read(lds, rbase + po + 8 * 480);
                }
                if ((w & 1) && t >= 8) { ua[3] = (u32x2){0u, 0u}; ub[3] = ua[3]; }
                BAR();
                const f32x4 g0 = LDF4(L0_GS + qd * 32), g1 = LDF4(L0_GS + qd * 32 + 16);
                const u32x4 sfr = LD16(L0_GS + 256 + 64 + qd * 16);
#pragma unroll
                for (int i = 0; i < 4; ++i) {
                    const int nt = 4 * (w & 1) + i;
                    const u32x4 o = {pack2(lo_f(ua[i][0]) * g0[0], hi_f(ua[i][0]) * g0[1]), pack2(lo_f(ua[i][1]) * g0[2], hi_f(ua[i][1]) * g0[3]),
                                     pack2(lo_f(ub[i][0]) * g1[0], hi_f(ub[i][0]) * g1[1]), pack2(lo_f(ub[i][1]) * g1[2], hi_f(ub[i][1]) * g1[3])};
                    bstore16<SC1>(o, rs, lo16, WS_WQF + ((j * 20 + sA) * 8 + nt) * 1024);
                }
                {
                    const f32x4 z4 = {0, 0, 0, 0};
                    f32x4 bias = mfma((u32x4){ua[0][0], ua[0][1], ub[0][0], ub[0][1]}, sfr, z4);
#pragma unroll
                    for (int i = 1; i < 4; ++i) { const f32x4 bi4 = mfma((u32x4){ua[i][0], ua[i][1], ub[i][0], ub[i][1]}, sfr, z4); bias = t == i ? bi4 : bias; }
                    bstore16<SC1>(asu4(bias), rs, t < 4 ? t * 64 + qd * 16 : 0x7ff00000u, WS_BP + ((sA * 16 + j) * 128 + (w & 1) * 64) * 4);
                }
            } else {
                const int col = 1280 + 64 * idxB + 16 * w + t;
                if (col >= 2560 && col < 3200) v = rbf(1.f + v);
                bstore4<SC1>(__float_as_uint(v), rs, l < 16 ? l * 4 : 0x7ff00000u, WS_MODV + (1280 + 64 * idxB + 16 * w) * 4);
                const unsigned vn = (unsigned)__builtin_amdgcn_update_dpp(0, (int)__float_as_uint(v), 0xB1, 0xf, 0xf, true);
                bstore4<SC1>((__float_as_uint(v) >> 16) | (vn & 0xffff0000u), rs, (l < 16 && !(l & 1)) ? l * 2 : 0x7ff00000u, WS_MODB + (1280 + 64 * idxB + 16 * w) * 2);
            }
            DRAIN();
            BAR();
            flag_put(rs, WS_FA + aidx * FS, tag, isA && tid == 0);
        }
    }

    const int g1 = 4 * (xcd >> 1) + (bi >> 3), j1 = 8 * (xcd & 1) + (bi & 7);
    const int P2 = 4 * xcd + (bi >> 3), c2 = bi & 7;
    const int g3 = g1, j3 = j1;
    const int t3 = j3 < 12 ? 7 * j3 : 84 + 6 * (j3 - 12), cnt3 = j3 < 12 ? 7 : 6;
    u32x4 w2f[14][5];
    const int s4 = __builtin_amdgcn_readfirstlane(w < 2 ? 14 * w : 28 + 13 * (w - 2)), cnt4 = __builtin_amdgcn_readfirstlane(w < 2 ? 14 : 13);
    const unsigned w2b = __builtin_amdgcn_readfirstlane(WS_W2F + (c2 * 54 + s4) * 5 * 1024);
    auto step4 = [&](int i) { return i < 13 ? i : cnt4 - 1; };
    auto w2_ld = [&](int f, unsigned lo) { w2f[f / 5][f % 5] = bload16<SC1>(rs, lo, w2b + step4(f / 5) * 5120 + (f % 5) * 1024); };
    constexpr int L2_WO = 61440, L2_X = 46080;
    {
        LANE_IDS();
        const int T0 = 8 * g1 + 2 * w, TH = g1 == 0 ? 0 : 8 * g1 - 1;
        const unsigned xb0 = WS_XNF + T0 * 20480, xbh = WS_XNF + TH * 20480 + (w >> 1) * 1024, wb = WS_WQF + j1 * 163840 + w * 4096;
        const unsigned wob = WS_WOF + c2 * 102400;
        u32x4 xr[10][4], xh[10];
        auto w_dma = [&](int G, int k) { dma16s(lds, (G & 3) * 16384 + w * 4096 + k * 1024, rs, lo16, wb + G * 16384 + k * 1024); };
        auto x_ld = [&](int G, int i) { xr[G][i] = bload16<SC1>(rs, lo16, xb0 + (i & 1) * 20480 + (2 * G + (i >> 1)) * 1024); };
        if (w == 0) {
            unsigned off[1] = {l == 0 ? WS_FC + TH * FS : WS_FC + (8 * g1 + ((l - 1) & 7)) * FS};
            poll_words<1>(ws, off, l < 9, tag);
        }
        BAR();
#pragma unroll
        for (int G = 0; G < 4; ++G) {
#pragma unroll
            for (int i = 0; i < 4; ++i) x_ld(G, i);
            xh[G] = bload16<SC1>(rs, lo16, xbh + G * 2048);
        }
        SB();
        if (w == 0) {
            unsigned off[1] = {WS_FA + ((j1 >> 1) * 20 + (l < 20 ? l : 0)) * FS};
            poll_words<1>(ws, off, l < 20, tag);
        }
        DRAIN();
        if (w > 0) dtask_read(dout, lds, L0_D, w, l);
        else {
#pragma unroll
            for (int i = 0; i < 8; ++i) dout.f[i] = (u32x4){0u, 0u, 0u, 0u};
        }
        BAR();
#pragma unroll
        for (int G = 0; G < 3; ++G)
#pragma unroll
            for (int k = 0; k < 4; ++k) w_dma(G, k);
        WAITV(4);
        BAR();
        f32x4 acc[2][8], hacc[3];
#pragma unroll
        for (int i = 0; i < 8; ++i) { acc[0][i] = (f32x4){0, 0, 0, 0}; acc[1][i] = (f32x4){0, 0, 0, 0}; }
#pragma unroll
        for (int i = 0; i < 3; ++i) hacc[i] = (f32x4){0, 0, 0, 0};
        u32x4 wf[2][8];
#pragma unroll
        for (int n = 0; n < 8; ++n) wf[0][n] = LD16(n * 1024 + lo16);
        const unsigned hoff = (w >> 1) * 8192 + ((w & 1) ? 5 : 2) * 1024 + lo16;
        f32x4 bp4[3]; u32x4 fdv;
        constexpr int L1_BPART = 40960;
        SB();
#pragma unroll
        for (int G = 0; G < 10; ++G) {
            u32x4 hw[3];
#pragma unroll
            for (int st = 0; st < 2; ++st) {
                const int s = 2 * G + st;
#pragma unroll
                for (int n = 0; n < 8; ++n) {
                    if (s + 1 < 20) wf[(s + 1) & 1][n] = LD16((((s + 1) >> 1) & 3) * 16384 + ((s + 1) & 1) * 8192 + n * 1024 + lo16);
                    if (st == 0 && n < 3) hw[n] = LD16((G & 3) * 16384 + hoff + n * 1024);
                    if (n < 5) { acc[0][n] = mfma(wf[s & 1][n], xr[G][st * 2], acc[0][n]); acc[1][n] = mfma(wf[s & 1][n], xr[G][st * 2 + 1], acc[1][n]); }
                    else { acc[0][n] = mfma(xr[G][st * 2], wf[s & 1][n], acc[0][n]); acc[1][n] = mfma(xr[G][st * 2 + 1], wf[s & 1][n], acc[1][n]); }
                    if (st == 1 && n >= 5) hacc[n - 5] = mfma(hw[n - 5], xh[G], hacc[n - 5]);
                    if (st == 0) {
                        if (G + 3 < 10 && (n & 1) == 0) w_dma(G + 3, n >> 1);
                        if (G + 4 < 10 && n == 7) xh[G + 4] = bload16<SC1>(rs, lo16, xbh + (G + 4) * 2048);
                    } else {
                        if (G + 4 < 10 && (n & 1) == 0) x_ld(G + 4, n >> 1);
                    }
                    if (G == 7 && st == 0 && n < 3) bp4[n] = asf4(bload16<SC1>(rs, (n < 2 || tid < 128) ? ((tid >> 5) + 8 * n) * 8192 + (tid & 31) * 16 : 0x7ff00000u, WS_BP + j1 * 512));
                    if (G == 9 && st == 0 && n == 0) {
                        f32x4 bs = (bp4[0] + bp4[1]) + bp4[2];
#pragma unroll
                        for (int r = 0; r < 4; ++r) { auto sw = __builtin_amdgcn_permlane32_swap(__float_as_uint(bs[r]), __float_as_uint(bs[r]), false, false); bs[r] = __uint_as_float(sw[0]) + __uint_as_float(sw[1]); }
                        STF4(L1_BPART + w * 1024 + lo16, bs);
                    }
                    if (G == 9 && st == 1 && n == 0) fdv = bload16<SC1>(rs, lo16, WS_FD);
                    if (n == 3) {
                        if (s == 0) dtask_frag<0>(dout, rs, l);
                        if (s == 1) dtask_frag<1>(dout, rs, l);
                        if (s == 2) dtask_frag<2>(dout, rs, l);
                        if (s == 3) dtask_frag<3>(dout, rs, l);
                        if (s == 4) dtask_frag<4>(dout, rs, l);
                        if (s == 5) dtask_frag<5>(dout, rs, l);
                        if (s == 6) dtask_frag<6>(dout, rs, l);
                        if (s == 7) dtask_frag<7>(dout, rs, l);
                    }
                    SB();
                }
            }
            if (G == 0) WAITV(11); else if (G <= 3) WAITV(17); else if (G == 4) WAITV(11); else if (G == 5) WAITV(14); else if (G == 6) WAITV(9); else if (G == 7) WAITV(3);
            BAR();
            if (G == 4) flag_put(rs, WS_FD + b * 4, tag, tid == 0);
        }
        constexpr int L1_BIAS = 6144, L1_HP = 0, L1_EX = 8192, L1_VT = 6656;
        if (w == 0) {
            check_all_blocks(ws, rs, WS_FD, fdv, tag);
            if (b == 0 && l == 0) flag_store(ws, WS_EPOCH, tag);
            if (l < 32) STF4(L1_BIAS + l * 16, (LDF4(L1_BPART + l * 16) + LDF4(L1_BPART + 1024 + l * 16)) + (LDF4(L1_BPART + 2048 + l * 16) + LDF4(L1_BPART + 3072 + l * 16)));
        }
        if (w >= 2) {
#pragma unroll
            for (int i = 0; i < 3; ++i) STF4(L1_HP + ((w & 1) * 3 + i) * 1024 + lo16, hacc[i]);
        }
        BAR();
        auto wo_dma = [&](int k) { dma16s(lds, L2_WO + w * 25600 + k * 1024, rs, lo16, wob + w * 25600 + k * 1024); };
        const rsrc_t rx = mk_rsrc(p.x, 2048 * 1280);
        unsigned xvo[3];
#pragma unroll
        for (int k = 0; k < 3; ++k) { const int n = (w * 3 + k) * 64 + l, row = n / 10; xvo[k] = n < 640 ? row * 1280 + (n - 10 * row) * 16 : 0x7ff00000u; }
        const unsigned xlo = L2_X + w * 3072;
        auto x_dma2 = [&](int k) { dma<0>(lds, xlo + k * 1024, rx, xvo[k], P2 * 64 * 1280 + c2 * 160); };
        f32x4 bq[5]; float bv[3];
#pragma unroll
        for (int n = 0; n < 5; ++n) bq[n] = LDF4(L1_BIAS + (n * 16 + 4 * qd) * 4);
#pragma unroll
        for (int n = 0; n < 3; ++n) bv[n] = LDF(L1_BIAS + ((5 + n) * 16 + t) * 4);
        TileKV kv[2]; u32x2 qa[2][2], xq[2];
        auto bg_wo = [&](int k) { SB(); wo_dma(k); SB(); };
#pragma unroll
        for (int tt = 1; tt >= 0; --tt) {
            kv[tt].ka0 = pack4(acc[tt][2] + bq[2]); kv[tt].ka1 = pack4(acc[tt][3] + bq[3]);
            bg_wo(4 * (1 - tt));
            xq[tt] = pack4(acc[tt][4] + bq[4]);
            kv[tt].k2[0] = swap32(xq[tt][0]); kv[tt].k2[1] = swap32(xq[tt][1]);
            bg_wo(4 * (1 - tt) + 1);
            kv[tt].va0 = pack4(acc[tt][5] + bv[0]); kv[tt].va1 = pack4(acc[tt][6] + bv[1]);
            bg_wo(4 * (1 - tt) + 2);
            kv[tt].z = pack4(acc[tt][7] + bv[2]);
            if (tt == 1) {
                const unsigned e = L1_EX + (w + 1) * 3072 + lo16;
                ST16(e, ((u32x4){kv[1].ka0[0], kv[1].ka0[1], kv[1].ka1[0], kv[1].ka1[1]}));
                ST16(e + 1024, ((u32x4){kv[1].k2[0], kv[1].k2[1], kv[1].va0[0], kv[1].va0[1]}));
                ST16(e + 2048, ((u32x4){kv[1].va1[0], kv[1].va1[1], kv[1].z[0], kv[1].z[1]}));
            }
            qa[tt][0] = pack4(acc[tt][0] + bq[0]); qa[tt][1] = pack4(acc[tt][1] + bq[1]);
            bg_wo(4 * (1 - tt) + 3);
        }
        if (w == 0) {
            const u32x2 k0 = pack4(hacc[0] + LDF4(L1_HP + lo16) + bq[2]), k1 = pack4(hacc[1] + LDF4(L1_HP + 1024 + lo16) + bq[3]);
            const u32x2 xx = pack4(hacc[2] + LDF4(L1_HP + 2048 + lo16) + bq[4]);
            ST16(L1_EX + lo16, ((u32x4){k0[0], k0[1], k1[0], k1[1]}));
            ST8(L1_EX + 1024 + lo16, ((u32x2){swap32(xx[0]), swap32(xx[1])}));
        } else if (w == 1) {
#pragma unroll
            for (int i = 0; i < 3; ++i) {
                const f32x4 v = hacc[i] + LDF4(L1_HP + (3 + i) * 1024 + lo16) + LDF4(L1_BIAS + ((5 + i) * 16 + 4 * qd) * 4);
                const u32x2 pk = pack4(v);
                LAS u16* d = (LAS u16*)((LAS char*)lds + L1_VT + i * 512 + (4 * qd) * 32 + t * 2);
                d[0] = (u16)pk[0]; d[16] = (u16)(pk[0] >> 16); d[32] = (u16)pk[1]; d[48] = (u16)(pk[1] >> 16);
            }
        }
        const unsigned ob = WS_OF + T0 * 20480;
        const AttnMask am = attn_mask(t, qd);
        u32x4 om; u32x2 ol;
        SB();
        attn_unit(qa[1], xq[1], kv[1], kv[0], true, am, qd, om, ol, [&](int k) { bg_wo(8 + k); });
        SB();
        bstore16<SC1>(om, rs, lo16, ob + 20480 + j1 * 1024);
        bstore8<SC1>(ol, rs, qd < 2 ? ((j1 & 3) * 16 + t) * 16 + 8 * qd : 0x7ff00000u, ob + 20480 + (16 + (j1 >> 2)) * 1024);
        BAR();
        TileKV pv;
        {
            const unsigned e = L1_EX + w * 3072 + lo16;
            const u32x4 a = LD16(e), bb = LD16(e + 1024), cc = LD16(e + 2048);
            pv.ka0 = (u32x2){a[0], a[1]}; pv.ka1 = (u32x2){a[2], a[3]}; pv.k2 = (u32x2){bb[0], bb[1]}; pv.va0 = (u32x2){bb[2], bb[3]};
            pv.va1 = (u32x2){cc[0], cc[1]}; pv.z = (u32x2){cc[2], cc[3]};
            if (w == 0) { pv.va0 = LD8(L1_VT + t * 32 + qd * 8); pv.va1 = LD8(L1_VT + 512 + t * 32 + qd * 8); pv.z = LD8(L1_VT + 1024 + t * 32 + qd * 8); }
        }
        SB();
        attn_unit(qa[0], xq[0], kv[0], pv, !(g1 == 0 && w == 0), am, qd, om, ol, [&](int k) { if (k < 7) bg_wo(18 + k); else { SB(); x_dma2(k - 7); SB(); } });
        SB();
        bstore16<SC1>(om, rs, lo16, ob + j1 * 1024);
        bstore8<SC1>(ol, rs, qd < 2 ? ((j1 & 3) * 16 + t) * 16 + 8 * qd : 0x7ff00000u, ob + (16 + (j1 >> 2)) * 1024);
        publish(rs, WS_F1 + (g1 * 16 + j1) * FS, tag);
    }

    u32x2 x1b[5], b1v[7];
    u32x4 vf[5];
    {
        LANE_IDS();
        const int T = 4 * P2 + w;
        constexpr int L2_MV = 57344;
        constexpr int L2_TB = 58304, L2_B1 = 60864;
        const u32x4 mvl = bload16<SC1>(rs, (1280 + ((tid / 20) % 3) * 640 + 80 * c2 + (tid % 20) * 4) * 4, WS_MODV);
        if (w < 3) { if (w < 2 || l < 32) dma16s(lds, L2_TB + w * 1024, rs, lo16, WS_MODB + 1920 * 2 + w * 1024); }
        else if (l < 14) dma<0>(lds, L2_B1, mk_rsrc(p.b1, 1728 * 2), lo16, t3 * 32);
#pragma unroll
        for (int f = 0; f < 12; ++f) w2_ld(f, lo16);
        SB();
        if (tid < 60) ST16(L2_MV + tid * 16, mvl);
        WAITV(12);
        BAR();
        u32x2 xv[5], va2[5], vb2[5];
#pragma unroll
        for (int i = 0; i < 5; ++i) {
            const int sr = 5 * w + i;
            const int ta = sr < 16 ? 5 * (sr >> 1) + 2 * (sr & 1) : 10 * (sr - 16) + 4, tb = ta + (sr < 16 ? 1 : 5);
            const unsigned sec = L2_TB + (t == 0 ? 1280 : 0) + qd * 8;
            va2[i] = LD8(sec + ta * 32); vb2[i] = LD8(sec + tb * 32);
        }
#pragma unroll
        for (int n = 0; n < 7; ++n) b1v[n] = LD8(L2_B1 + n * 32 + qd * 8);
#pragma unroll
        for (int n = 0; n < 5; ++n) xv[n] = LD8(L2_X + (16 * w + t) * 160 + n * 32 + qd * 8);
        SB();
#pragma unroll
        for (int i = 0; i < 5; ++i) {
            const unsigned cst = t == 2 ? 0x3f803f80u : 0u;
            vf[i] = (u32x4){t < 2 ? va2[i][0] : cst, t < 2 ? va2[i][1] : cst, t < 2 ? vb2[i][0] : cst, t < 2 ? vb2[i][1] : cst};
        }
        asm volatile("" : "+v"(vf[0]), "+v"(vf[1]), "+v"(vf[2]), "+v"(vf[3]), "+v"(vf[4]));
        asm volatile("" : "+v"(b1v[0]), "+v"(b1v[1]), "+v"(b1v[2]), "+v"(b1v[3]), "+v"(b1v[4]), "+v"(b1v[5]), "+v"(b1v[6]), "+v"(xv[0]), "+v"(xv[1]), "+v"(xv[2]), "+v"(xv[3]), "+v"(xv[4]));
        if (w == 0) {
            unsigned off[1] = {WS_F1 + ((P2 >> 1) * 16 + (l & 15)) * FS};
            poll_words<1>(ws, off, l < 16, tag);
        }
        BAR();
        constexpr int PW = 8;
        u32x4 tk[20];
#pragma unroll
        for (int s = 0; s < PW; ++s) tk[s] = bload16<SC1>(rs, lo16, WS_OF + (T * 20 + s) * 1024);
        f32x4 acc[5];
#pragma unroll
        for (int n = 0; n < 5; ++n) acc[n] = (f32x4){0, 0, 0, 0};
        unsigned wb0 = L2_WO + lo16, wb1 = L2_WO + 51200 + lo16; OPAQUE(wb0); OPAQUE(wb1);
        auto wo_rd = [&](int s, int n) { return s < 10 ? LD16(wb0 + (s * 5 + n) * 1024) : LD16(wb1 + ((s - 10) * 5 + n) * 1024); };
        u32x4 wf[3][5];
#pragma unroll
        for (int n = 0; n < 5; ++n) { wf[0][n] = wo_rd(0, n); wf[1][n] = wo_rd(1, n); }
        SB();
#pragma unroll
        for (int s = 0; s < 20; ++s) {
            if (s + PW < 20) tk[s + PW] = bload16<SC1>(rs, lo16, WS_OF + (T * 20 + s + PW) * 1024);
            if (s + 2 < 20) {
#pragma unroll
                for (int n = 0; n < 5; ++n) wf[(s + 2) % 3][n] = wo_rd(s + 2, n);
            }
#pragma unroll
            for (int n = 0; n < 5; ++n) acc[n] = mfma(wf[s % 3][n], tk[s], acc[n]);
#pragma unroll
            for (int n = 0; n < 5; ++n) { if (s + 2 < 20) SGB(0x100, 1); SGB(0x8, 1); if (n == 1 && s + PW < 20) SGB(0x20, 1); }
            SB();
        }
        BAR();
        const unsigned w1src = WS_W1F + t3 * 20480, w1last = (cnt3 == 7 ? 6 : 0) * 20480;
        auto w1_dma = [&](int base_step, int i) {
            const int n = i % 7;
            dma16s(lds, base_step * 7168 + i * 1024, rs, lo16, w1src + (n == 6 ? w1last : n * 20480) + (base_step + i / 7) * 1024);
        };
        float st[6] = {0, 0, 0, 0, 0, 0};
        unsigned mvb = L2_MV + qd * 16; OPAQUE(mvb);
        f32x4 mvr[2][3];
#pragma unroll
        for (int k = 0; k < 3; ++k) mvr[0][k] = LDF4(mvb + k * 320);
        u32x2 ub[5];
        const unsigned ufb = WS_UF + T * 20480;
#pragma unroll
        for (int n = 0; n < 5; ++n) {
            if (n + 1 < 5) {
#pragma unroll
                for (int k = 0; k < 3; ++k) mvr[(n + 1) & 1][k] = LDF4(mvb + k * 320 + (n + 1) * 64);
            }
            const f32x4 ga_n = mvr[n & 1][0], s2_n = mvr[n & 1][1], g2_n = mvr[n & 1][2];
            float x1[4] = {lo_f(xv[n][0]), hi_f(xv[n][0]), lo_f(xv[n][1]), hi_f(xv[n][1])};
#pragma unroll
            for (int r = 0; r < 4; ++r) x1[r] = fmaf(ga_n[r], acc[n][r], x1[r]);
            x1b[n] = (u32x2){pack2(x1[0], x1[1]), pack2(x1[2], x1[3])};
            const float xr[4] = {lo_f(x1b[n][0]), hi_f(x1b[n][0]), lo_f(x1b[n][1]), hi_f(x1b[n][1])};
            ub[n] = (u32x2){pack2(xr[0] * g2_n[0], xr[1] * g2_n[1]), pack2(xr[2] * g2_n[2], xr[3] * g2_n[3])};
            const float ur[4] = {lo_f(ub[n][0]), hi_f(ub[n][0]), lo_f(ub[n][1]), hi_f(ub[n][1])};
#pragma unroll
            for (int r = 0; r < 4; ++r) {
                st[0] += xr[r]; st[1] = fmaf(xr[r], xr[r], st[1]); st[2] += ur[r]; st[3] = fmaf(ur[r], g2_n[r], st[3]);
                st[4] = fmaf(ur[r], ur[r], st[4]); st[5] = fmaf(ur[r], s2_n[r], st[5]);
            }
            if (n == 1) bstore16<SC1>((u32x4){ub[0][0], ub[0][1], ub[1][0], ub[1][1]}, rs, lo16, ufb + (2 * c2) * 1024);
            if (n == 3) bstore16<SC1>((u32x4){ub[2][0], ub[2][1], ub[3][0], ub[3][1]}, rs, lo16, ufb + (2 * c2 + 1) * 1024);
            if (n == 4) bstore8<SC1>(ub[4], rs, lo16 + 8 * (c2 & 1), ufb + (16 + (c2 >> 1)) * 1024);
#pragma unroll
            for (int k = 0; k < 3; ++k) if (3 * n + k < 14) w1_dma(2 * w, 3 * n + k);
            SGB(0x20, 1); SGB(0x1, 18); SGB(0x20, 1); SGB(0x1, 18); SGB(0x20, 1); SGB(0x1, 24);
            SB();
        }
#pragma unroll
        for (int i = 0; i < 6; ++i) st[i] = red_add_q(st[i]);
        if (qd < 2) bstore16<SC1>(asu4(qd == 0 ? (f32x4){st[0], st[1], st[2], st[3]} : (f32x4){st[4], st[5], 0.f, 0.f}), rs, qd * 256 + t * 16, WS_ST + (T * 8 + c2) * 512);
#pragma unroll
        for (int n = 0; n < 5; ++n) asm volatile("" : "+v"(x1b[n]));
        publish(rs, WS_F2 + (P2 * 8 + c2) * FS, tag);
        if (bi < PF_BLOCKS) {
            unsigned long long pc; unsigned len, dist;
            asm volatile("s_getpc_b64 %0\n.Ladaln_pc2:\n\ts_mov_b32 %1, .Ladaln_end-.Ladaln_p3\n\ts_mov_b32 %2, .Ladaln_p3-.Ladaln_pc2" : "=s"(pc), "=s"(len), "=s"(dist));
            unsigned off = (bi * 256 + tid) * 128u;
            off = off < len ? off : 0u;
            const unsigned long long a = pc + dist + off;
            const unsigned dst = (unsigned)(size_t)(LAS char*)lds + LDS_BYTES - 1024 + w * 256;
            asm volatile("s_mov_b32 m0, %0\n\ts_nop 0\n\tglobal_load_lds_dword %1, off" :: "s"(dst), "v"(a) : "memory");
        }
#pragma unroll
        for (int i = 0; i < 21; ++i) w1_dma(8 + 3 * w, i);
    }

    asm volatile(".Ladaln_p3:" ::: "memory");
    {
        LANE_IDS();
        constexpr int L3_GSC = 143360;
        const int T0 = 8 * g3 + 2 * w;
        if (w == 0) {
            unsigned off[1] = {WS_F2 + ((2 * g3 + ((l >> 3) & 1)) * 8 + (l & 7)) * FS};
            poll_words<1>(ws, off, l < 16, tag);
        }
        DRAIN();
        BAR();
        constexpr int PW = 4;
        u32x4 tk[20][2];
        u32x4 spa[2][2]; u32x2 spb[2][2];
        float al[2], be[2], gm[2], de[2], ts[2][6];
        f32x4 red4[4], gr0, gr1;
#pragma unroll
        for (int s = 0; s < PW; ++s) { const unsigned o = WS_UF + (T0 * 20 + s) * 1024; tk[s][0] = bload16<SC1>(rs, lo16, o); tk[s][1] = bload16<SC1>(rs, lo16, o + 20480); }
        SB();
        {
            f32x4 va[7], gram = {0, 0, 0, 0};
            u32x4 wv[2][7];
            unsigned vb = 5 * w * 7168 + lo16; OPAQUE(vb);
#pragma unroll
            for (int n = 0; n < 7; ++n) { va[n] = (f32x4){0, 0, 0, 0}; wv[0][n] = LD16(vb + n * 1024); }
            SB();
#pragma unroll
            for (int i = 0; i < 5; ++i) {
                if (i + 1 < 5) {
#pragma unroll
                    for (int n = 0; n < 7; ++n) wv[(i + 1) & 1][n] = LD16(vb + ((i + 1) * 7 + n) * 1024);
                }
#pragma unroll
                for (int n = 0; n < 7; ++n) va[n] = mfma(wv[i & 1][n], vf[i], va[n]);
                gram = mfma(vf[i], vf[i], gram);
#pragma unroll
                for (int n = 0; n < 7; ++n) { if (i + 1 < 5) SGB(0x100, 1); SGB(0x8, 1); }
                SB();
            }
            if (t < 3) {
#pragma unroll
                for (int n = 0; n < 7; ++n) STF4(L3_GSC + ((w * 8 + n) * 3 + t) * 64 + qd * 16, va[n]);
                if (qd == 0) STF4(L3_GSC + ((w * 8 + 7) * 3 + t) * 64, gram);
            }
        }
        SB();
        f32x4 acc[2][7];
#pragma unroll
        for (int n = 0; n < 7; ++n) { acc[0][n] = (f32x4){0, 0, 0, 0}; acc[1][n] = (f32x4){0, 0, 0, 0}; }
        unsigned wb0 = lo16, wb1 = 57344 + lo16, wb2 = 114688 + lo16; OPAQUE(wb0); OPAQUE(wb1); OPAQUE(wb2);
        auto w1_rd = [&](int s, int n) { return s < 8 ? LD16(wb0 + (s * 7 + n) * 1024) : s < 16 ? LD16(wb1 + ((s - 8) * 7 + n) * 1024) : LD16(wb2 + ((s - 16) * 7 + n) * 1024); };
        u32x4 wf[2][7];
#pragma unroll
        for (int n = 0; n < 7; ++n) wf[0][n] = w1_rd(0, n);
        SB();
#pragma unroll
        for (int s = 0; s < 20; ++s) {
            int nv = 0;
            if (s + PW < 20) { const unsigned o = WS_UF + (T0 * 20 + s + PW) * 1024; tk[s + PW][0] = bload16<SC1>(rs, lo16, o); tk[s + PW][1] = bload16<SC1>(rs, lo16, o + 20480); nv += 2; }
            if (s < 16) { w2_ld(12 + s, lo16); nv += 1; }
            if (s >= 16) {
#pragma unroll
                for (int k = 0; k < 3; ++k) w2_ld(28 + (s - 16) * 3 + k, lo16);
                nv += 3;
            }
            if (s == 10 || s == 11) {
#pragma unroll
                for (int e = 0; e < 2; ++e) {
                    spa[s - 10][e] = bload16<SC1>(rs, (2 * qd + e) * 512 + t * 16, WS_ST + (T0 + s - 10) * 4096);
                    spb[s - 10][e] = bload8<SC1>(rs, (2 * qd + e) * 512 + 256 + t * 16, WS_ST + (T0 + s - 10) * 4096);
                }
                nv += 4;
            }
            int nu = 0;
            if (s == 3) {
                const int r96 = tid < 96 ? tid : 0;
#pragma unroll
                for (int u = 0; u < 4; ++u) red4[u] = LDF4(L3_GSC + (u * 96 + r96) * 16);
            }
            if (s == 4) { STF4(L3_GSC + 6144 + (tid < 96 ? tid : 96 + (tid & 31)) * 16, (red4[0] + red4[1]) + (red4[2] + red4[3])); nu = 1; }
            if (s == 9) { gr0 = LDF4(L3_GSC + 6144 + 84 * 16); gr1 = LDF4(L3_GSC + 6144 + 88 * 16); }
            if (s == 13 || s == 14) {
                const int tt = s - 13;
                const f32x4 a0 = asf4(spa[tt][0]) + asf4(spa[tt][1]);
                const float b00 = __uint_as_float(spb[tt][0][0]) + __uint_as_float(spb[tt][1][0]), b01 = __uint_as_float(spb[tt][0][1]) + __uint_as_float(spb[tt][1][1]);
                ts[tt][0] = red_add_q(a0[0]); ts[tt][1] = red_add_q(a0[1]); ts[tt][2] = red_add_q(a0[2]); ts[tt][3] = red_add_q(a0[3]); ts[tt][4] = red_add_q(b00); ts[tt][5] = red_add_q(b01);
                nu = 4;
            }
            if (s == 15) {
                const float Gg = gr0[0], Ggs = gr0[1], Gs = gr0[2], Sss = gr1[1], Ss = gr1[2], iD = 1.f / 640.f;
#pragma unroll
                for (int tt = 0; tt < 2; ++tt) {
                    const float m1 = ts[tt][0] * iD, v1 = fmaxf(ts[tt][1] * iD - m1 * m1, 0.f), r1v = __builtin_amdgcn_rsqf(v1 + 1e-5f);
                    const float m2 = (r1v * (ts[tt][2] - m1 * Gs) + Ss) * iD;
                    const float e2 = (r1v * r1v * (ts[tt][4] - 2.f * m1 * ts[tt][3] + m1 * m1 * Gg) + 2.f * r1v * (ts[tt][5] - m1 * Ggs) + Sss) * iD;
                    const float r2v = __builtin_amdgcn_rsqf(fmaxf(e2 - m2 * m2, 0.f) + 1e-5f);
                    al[tt] = r1v * r2v; be[tt] = -r1v * r2v * m1; gm[tt] = r2v; de[tt] = -r2v * m2;
                }
                nu = 4;
            }
            if (s + 1 < 20) {
#pragma unroll
                for (int n = 0; n < 7; ++n) wf[(s + 1) & 1][n] = w1_rd(s + 1, n);
            }
#pragma unroll
            for (int n = 0; n < 7; ++n) { acc[0][n] = mfma(wf[s & 1][n], tk[s][0], acc[0][n]); acc[1][n] = mfma(wf[s & 1][n], tk[s][1], acc[1][n]); }
#pragma unroll
            for (int n = 0; n < 7; ++n) {
                if (s + 1 < 20) SGB(0x100, 1);
                SGB(0x8, 1);
                if (nu == 1) SGB(0x402, 1);
                if (nu == 4) SGB(0x402, 4);
                SGB(0x8, 1);
                if (nu == 1) SGB(0x402, 1);
                if (nu == 4) SGB(0x402, 4);
                if (n < nv) SGB(0x20, 1);
            }
            SB();
            if (s == 2 || s == 8) BAR();
        }
        u32x2 hb[2][7];
        unsigned gsb = L3_GSC + 6144 + qd * 16; OPAQUE(gsb);
        f32x4 gsc[2][3];
#pragma unroll
        for (int k = 0; k < 3; ++k) gsc[0][k] = LDF4(gsb + 64 * k);
#pragma unroll
        for (int n = 0; n < 7; ++n) {
#pragma unroll
            for (int k = 0; k < 2; ++k) w2_ld(40 + n * 2 + k, lo16);
            if (n + 1 < 7) {
#pragma unroll
                for (int k = 0; k < 3; ++k) gsc[(n + 1) & 1][k] = LDF4(gsb + (n + 1) * 192 + 64 * k);
            }
            const f32x4 G = gsc[n & 1][0], Sv = gsc[n & 1][1], C = gsc[n & 1][2];
            const float bb[4] = {lo_f(b1v[n][0]), hi_f(b1v[n][0]), lo_f(b1v[n][1]), hi_f(b1v[n][1])};
#pragma unroll
            for (int tt = 0; tt < 2; ++tt) {
                float hv[4];
#pragma unroll
                for (int r = 0; r < 4; ++r) {
                    float z = fmaf(gm[tt], Sv[r], bb[r]); z = fmaf(be[tt], G[r], z); z = fmaf(de[tt], C[r], z); z = fmaf(al[tt], acc[tt][n][r], z);
                    const float u = z * fmaf(z * z, -0.044715f * 1.5957691216057308f * 1.4426950408889634f, -1.5957691216057308f * 1.4426950408889634f);
                    hv[r] = z * __builtin_amdgcn_rcpf(1.f + __builtin_amdgcn_exp2f(u));
                }
                hb[tt][n] = (u32x2){pack2(hv[0], hv[1]), pack2(hv[2], hv[3])};
            }
            SGB(0x20, 1); SGB(0x1, 40); SGB(0x20, 1); SGB(0x1, 60);
            SB();
#pragma unroll
            for (int tt = 0; tt < 2; ++tt) {
                const unsigned hbase = WS_HF + (T0 + tt) * 54 * 1024;
                if (n & 1) bstore16<SC1>((u32x4){hb[tt][n - 1][0], hb[tt][n - 1][1], hb[tt][n][0], hb[tt][n][1]}, rs, lo16, hbase + (3 * j3 + (n >> 1)) * 1024);
                if (n == 6 && j3 < 12) bstore8<SC1>(hb[tt][6], rs, lo16 + 8 * (j3 & 1), hbase + (48 + (j3 >> 1)) * 1024);
            }
            SB();
        }
        publish(rs, WS_F3 + (g3 * 16 + j3) * FS, tag);
    }

    {
        LANE_IDS();
        f32x4 gb[5]; u32x2 b2v[5];
#pragma unroll
        for (int f = 54; f < 70; ++f) w2_ld(f, lo16);
        SB();
        if (w == 0) {
            unsigned off[1] = {WS_F3 + ((P2 >> 1) * 16 + (l & 15)) * FS};
            poll_words<1>(ws, off, l < 16, tag);
        }
        BAR();
        constexpr int PD = 3;
        u32x4 tk[14][4];
        const unsigned tokb = __builtin_amdgcn_readfirstlane(WS_HF + (4 * P2 * 54 + s4) * 1024);
        auto issue = [&](int i) {
            const int s = step4(i);
#pragma unroll
            for (int a = 0; a < 4; ++a) tk[i][a] = bload16<SC1>(rs, lo16, tokb + (((a + w) & 3) * 54 + s) * 1024);
        };
#pragma unroll
        for (int i = 0; i < PD; ++i) issue(i);
        f32x4 acc[4][5];
#pragma unroll
        for (int a = 0; a < 4; ++a)
#pragma unroll
            for (int n = 0; n < 5; ++n) acc[a][n] = (f32x4){0, 0, 0, 0};
        SB();
#pragma unroll
        for (int i = 0; i < 14; ++i) {
            if (i < 13 || w < 2) {
                if (i + PD < 14) issue(i + PD);
                if (i == 11) {
#pragma unroll
                    for (int n = 0; n < 5; ++n) {
                        const int f = 80 * c2 + 16 * n;
                        gb[n] = asf4(bload16<SC1>(rs, qd * 16, WS_MODV + (3200 + f) * 4));
                        b2v[n] = *(const GAS u32x2*)(p.b2 + f + 4 * qd);
                    }
                }
#pragma unroll
                for (int a = 0; a < 4; ++a)
#pragma unroll
                    for (int n = 0; n < 5; ++n) acc[a][n] = mfma(w2f[i][n], tk[i][a], acc[a][n]);
#pragma unroll
                for (int a = 0; a < 4; ++a) { SGB(0x8, 2); if (i == 11) SGB(0x20, 3); else SGB(0x20, 1); SGB(0x8, 3); }
            }
            SB();
        }
#pragma unroll
        for (int a = 1; a < 4; ++a) {
            unsigned d = ((((a + w) & 3) * 3 + (3 - a)) * 5) * 1024 + lo16; OPAQUE(d);
#pragma unroll
            for (int n = 0; n < 5; ++n) STF4(d + n * 1024, acc[a][n]);
        }
        BAR();
        constexpr int L4_OUT = 61440;
        unsigned rb = w * 15360 + lo16; OPAQUE(rb);
        f32x4 part[5][3];
#pragma unroll
        for (int n = 0; n < 5; ++n)
#pragma unroll
            for (int u = 0; u < 3; ++u) part[n][u] = LDF4(rb + (u * 5 + n) * 1024);
        SB();
#pragma unroll
        for (int n = 0; n < 5; ++n) {
            const f32x4 v = (acc[0][n] + part[n][0]) + (part[n][1] + part[n][2]);
            const float x1[4] = {lo_f(x1b[n][0]), hi_f(x1b[n][0]), lo_f(x1b[n][1]), hi_f(x1b[n][1])};
            const float bv[4] = {lo_f(b2v[n][0]), hi_f(b2v[n][0]), lo_f(b2v[n][1]), hi_f(b2v[n][1])};
            float o[4];
#pragma unroll
            for (int r = 0; r < 4; ++r) o[r] = fmaf(gb[n][r], v[r] + bv[r], x1[r]);
            ST8(L4_OUT + (16 * w + t) * 176 + n * 32 + qd * 8, ((u32x2){pack2(o[0], o[1]), pack2(o[2], o[3])}));
        }
        asm volatile("s_waitcnt lgkmcnt(0)" ::: "memory");
        const rsrc_t ro = mk_rsrc(p.out, 2048 * 1280);
#pragma unroll
        for (int k = 0; k < 3; ++k) {
            const int ch = l + 64 * k;
            if (ch < 160) {
                const int row = ch / 10, cc = ch % 10;
                const u32x4 v = LD16(L4_OUT + (16 * w + row) * 176 + cc * 16);
                bstore16<0>(v, ro, row * 1280 + cc * 16, (16 * (4 * P2 + w)) * 1280 + c2 * 160);
            }
        }
    }
    asm volatile(".Ladaln_end:" ::: "memory");
}

extern "C" void kernel_launch(void* const* d_in, const int* in_sizes, int n_in, void* d_out, int out_size, void* d_ws, size_t ws_size, hipStream_t stream) {
    Params p;
    p.x = (const u16*)d_in[0]; p.wqkv = (const u16*)d_in[1]; p.wo = (const u16*)d_in[2]; p.w1 = (const u16*)d_in[4]; p.b1 = (const u16*)d_in[5];
    p.w2 = (const u16*)d_in[6]; p.b2 = (const u16*)d_in[7]; p.c = (const u16*)d_in[8]; p.wm = (const u16*)d_in[9];
    p.out = (u16*)d_out; p.ws = (char*)d_ws;
    (void)hipFuncSetAttribute((const void*)adaLN_block_kernel, hipFuncAttributeMaxDynamicSharedMemorySize, LDS_BYTES);
    adaLN_block_kernel<<<256, 256, LDS_BYTES, stream>>>(p);
}
```

```cpp
#include <hip/hip_runtime.h>
#include <stdint.h>

typedef unsigned short u16;
typedef float f32x4 __attribute__((ext_vector_type(4)));
typedef float f32x2 __attribute__((ext_vector_type(2)));
typedef __bf16 bf16x8 __attribute__((ext_vector_type(8)));
typedef __bf16 bf16x2 __attribute__((ext_vector_type(2)));
typedef unsigned u32x4 __attribute__((ext_vector_type(4)));
typedef unsigned u32x2 __attribute__((ext_vector_type(2)));
typedef short s16x4 __attribute__((ext_vector_type(4)));
typedef __amdgpu_buffer_rsrc_t rsrc_t;
#define LAS __attribute__((address_space(3)))
#define GAS __attribute__((address_space(1)))
#define DI __device__ __forceinline__

static constexpr unsigned FS = 64;
static constexpr unsigned WS_EPOCH = 0;
static constexpr unsigned WS_TMO   = 256;
static constexpr unsigned WS_FA    = 1024;
static constexpr unsigned WS_FC    = WS_FA + 160 * FS;
static constexpr unsigned WS_FD    = WS_FC + 128 * FS;
static constexpr unsigned WS_F1    = WS_FD + 1024;
static constexpr unsigned WS_F2    = WS_F1 + 256 * FS;
static constexpr unsigned WS_F3    = WS_F2 + 256 * FS;
static constexpr unsigned WS_FEND  = WS_F3 + 256 * FS;
static constexpr unsigned WS_MODV = 131072;
static constexpr unsigned WS_MODB = WS_MODV + 16384;
static constexpr unsigned WS_BP   = WS_MODB + 8192;
static constexpr unsigned WS_XNF  = WS_BP + 20 * 16 * 128 * 4;
static constexpr unsigned WS_WQF  = WS_XNF + 128 * 20 * 1024;
static constexpr unsigned WS_WOF  = WS_WQF + 16 * 20 * 8 * 1024;
static constexpr unsigned WS_W1F  = WS_WOF + 8 * 20 * 5 * 1024;
static constexpr unsigned WS_W2F  = WS_W1F + 108 * 20 * 1024;
static constexpr unsigned WS_OF   = WS_W2F + 8 * 54 * 5 * 1024;
static constexpr unsigned WS_UF   = WS_OF + 128 * 20 * 1024;
static constexpr unsigned WS_ST   = WS_UF + 128 * 20 * 1024;
static constexpr unsigned WS_HF   = WS_ST + 128 * 8 * 512;
static constexpr unsigned WS_END  = WS_HF + 128 * 54 * 1024;

static constexpr int LDS_BYTES = 163840;
static constexpr int SC1 = 16;
static constexpr unsigned SPIN_CAP = 40000;

struct Params {
    const u16* x; const u16* wqkv; const u16* wo; const u16* w1; const u16* b1; const u16* w2; const u16* b2; const u16* c; const u16* wm;
    u16* out; char* ws;
};

DI float lo_f(unsigned d) { return __uint_as_float(d << 16); }
DI float hi_f(unsigned d) { return __uint_as_float(d & 0xffff0000u); }
DI unsigned pack2(float a, float b) { f32x2 v = {a, b}; return __builtin_bit_cast(unsigned, __builtin_convertvector(v, bf16x2)); }
DI float rbf(float a) { return lo_f(pack2(a, 0.f)); }
DI u32x2 pack4(const f32x4& v) { return (u32x2){pack2(v[0], v[1]), pack2(v[2], v[3])}; }
DI f32x4 mfma(u32x4 a, u32x4 b, f32x4 c) { return __builtin_amdgcn_mfma_f32_16x16x32_bf16(__builtin_bit_cast(bf16x8, a), __builtin_bit_cast(bf16x8, b), c, 0, 0, 0); }
DI float red_add_q(float v) {
    auto a = __builtin_amdgcn_permlane16_swap(__float_as_uint(v), __float_as_uint(v), false, false);
    v = __uint_as_float(a[0]) + __uint_as_float(a[1]);
    auto b = __builtin_amdgcn_permlane32_swap(__float_as_uint(v), __float_as_uint(v), false, false);
    return __uint_as_float(b[0]) + __uint_as_float(b[1]);
}
DI float red_max_q(float v) {
    auto a = __builtin_amdgcn_permlane16_swap(__float_as_uint(v), __float_as_uint(v), false, false);
    v = fmaxf(__uint_as_float(a[0]), __uint_as_float(a[1]));
    auto b = __builtin_amdgcn_permlane32_swap(__float_as_uint(v), __float_as_uint(v), false, false);
    return fmaxf(__uint_as_float(b[0]), __uint_as_float(b[1]));
}
template <int C> DI float dpp_add(float v) { return v + __builtin_bit_cast(float, __builtin_amdgcn_update_dpp(0, __builtin_bit_cast(int, v), C, 0xf, 0xf, true)); }
DI float red16(float v) { v = dpp_add<0xB1>(v); v = dpp_add<0x4E>(v); v = dpp_add<0x141>(v); return dpp_add<0x140>(v); }
DI unsigned swap32(unsigned v) { auto a = __builtin_amdgcn_permlane32_swap(v, v, false, false); return (threadIdx.x & 32) ? a[0] : a[1]; }
DI rsrc_t mk_rsrc(const void* p, unsigned bytes) { return __builtin_amdgcn_make_buffer_rsrc((void*)p, 0, bytes, 0x00020000); }
template <int AUX> DI u32x4 bload16(rsrc_t r, unsigned voff, unsigned soff) { return __builtin_bit_cast(u32x4, __builtin_amdgcn_raw_buffer_load_b128(r, voff, soff, AUX)); }
template <int AUX> DI u32x2 bload8(rsrc_t r, unsigned voff, unsigned soff) { return __builtin_bit_cast(u32x2, __builtin_amdgcn_raw_buffer_load_b64(r, voff, soff, AUX)); }
template <int AUX> DI unsigned bload4(rsrc_t r, unsigned voff, unsigned soff) { return __builtin_amdgcn_raw_buffer_load_b32(r, voff, soff, AUX); }
template <int AUX> DI void bstore16(u32x4 v, rsrc_t r, unsigned voff, unsigned soff) { __builtin_amdgcn_raw_buffer_store_b128(v, r, voff, soff, AUX); }
template <int AUX> DI void bstore8(u32x2 v, rsrc_t r, unsigned voff, unsigned soff) { __builtin_amdgcn_raw_buffer_store_b64(v, r, voff, soff, AUX); }
template <int AUX> DI void bstore4(unsigned v, rsrc_t r, unsigned voff, unsigned soff) { __builtin_amdgcn_raw_buffer_store_b32(v, r, voff, soff, AUX); }
DI f32x4 asf4(u32x4 v) { return __builtin_bit_cast(f32x4, v); }
DI u32x4 asu4(f32x4 v) { return __builtin_bit_cast(u32x4, v); }
#define LANE_IDS() int tz_ = threadIdx.x; asm volatile("" : "+v"(tz_)); const int tid = tz_, l = tid & 63, t = l & 15, qd = l >> 4; const unsigned lo16 = l * 16; (void)t; (void)qd; (void)lo16
#define OPAQUE(x) asm volatile("" : "+v"(x))
#define SB() __builtin_amdgcn_sched_barrier(0)
#define BAR() asm volatile("s_waitcnt lgkmcnt(0)\n\ts_barrier" ::: "memory")
#define DRAIN() asm volatile("s_waitcnt vmcnt(0)" ::: "memory")
#define SGB(mask, n) __builtin_amdgcn_sched_group_barrier(mask, n, 0)
#define LD16(off) (*(const LAS u32x4*)((const LAS char*)lds + (off)))
#define LD8(off) (*(const LAS u32x2*)((const LAS char*)lds + (off)))
#define LDF4(off) (*(const LAS f32x4*)((const LAS char*)lds + (off)))
#define LDF(off) (*(const LAS float*)((const LAS char*)lds + (off)))
#define ST16(off, v) (*(LAS u32x4*)((LAS char*)lds + (off)) = (v))
#define ST8(off, v) (*(LAS u32x2*)((LAS char*)lds + (off)) = (v))
#define STF4(off, v) (*(LAS f32x4*)((LAS char*)lds + (off)) = (v))
#define STF(off, v) (*(LAS float*)((LAS char*)lds + (off)) = (v))
DI u32x2 tr_read(const char* lds, unsigned off) { return __builtin_bit_cast(u32x2, __builtin_amdgcn_ds_read_tr16_b64_v4i16((LAS s16x4*)((LAS char*)lds + off))); }

DI void flag_store(char* ws, unsigned off, unsigned tag) { __hip_atomic_store((GAS unsigned*)(ws + off), tag, __ATOMIC_RELAXED, __HIP_MEMORY_SCOPE_AGENT); }
DI unsigned flag_load(const char* ws, unsigned off) { return __hip_atomic_load((const GAS unsigned*)(ws + off), __ATOMIC_RELAXED, __HIP_MEMORY_SCOPE_AGENT); }
DI void flag_put(rsrc_t rs, unsigned off, unsigned tag, bool me) { bstore4<SC1>(tag, rs, me ? off : 0x7ff00000u, 0); }
DI void publish(rsrc_t rs, unsigned off, unsigned tag) { DRAIN(); BAR(); flag_put(rs, off, tag, threadIdx.x == 0); }
template <int N> DI void poll_words(char* ws, const unsigned (&off)[N], bool active, unsigned tag) {
#pragma nounroll
    for (unsigned spins = 0;; ++spins) {
        bool ok = true;
#pragma unroll
        for (int k = 0; k < N; ++k) ok &= flag_load(ws, off[k]) == tag;
        if (__all(ok || !active)) break;
        if (spins > SPIN_CAP) { if ((threadIdx.x & 63) == 0) flag_store(ws, WS_TMO, tag); break; }
        __builtin_amdgcn_s_sleep(1);
    }
}

DI void check_all_blocks(char* ws, rsrc_t rs, unsigned base, u32x4 v, unsigned tag) {
    asm volatile("" : "+v"(v));
#pragma nounroll
    for (unsigned spins = 0; !__all(v[0] == tag && v[1] == tag && v[2] == tag && v[3] == tag); ++spins) {
        if (spins > SPIN_CAP) { if ((threadIdx.x & 63) == 0) flag_store(ws, WS_TMO, tag); break; }
        __builtin_amdgcn_s_sleep(1);
        v = bload16<SC1>(rs, (threadIdx.x & 63) * 16, base);
        asm volatile("" : "+v"(v) :: "memory");
    }
}

DI void tiles_p3(int s, int& ta, int& tb) { if (s < 16) { ta = 5 * (s >> 1) + 2 * (s & 1); tb = ta + 1; } else { ta = 10 * (s - 16) + 4; tb = ta + 5; } }
DI void tiles_p4(int s, int& ta, int& tb) { if (s < 48) { int j = s / 3, pr = s % 3; int st = j < 12 ? 7 * j : 84 + 6 * (j - 12); ta = st + 2 * pr; tb = ta + 1; } else { int m = s - 48; ta = 14 * m + 6; tb = ta + 7; } }

static constexpr int L0_SC = 81920, L0_GS = 83200, L0_CRAW = 83968, L0_RW = 86016, L0_D = 101376, L0_XS = 156160;

template <int AUX> DI void dma(char* lds, unsigned lds_off, rsrc_t r, unsigned voff, unsigned soff) {
    __builtin_amdgcn_raw_ptr_buffer_load_lds(r, (LAS void*)((LAS char*)lds + lds_off), 16, voff, soff, 0, AUX);
}
DI void dma16(char* lds, unsigned lds_off, rsrc_t r, unsigned voff, unsigned soff) {
    const unsigned dst = (unsigned)(size_t)(LAS char*)lds + lds_off;
    asm volatile("s_mov_b32 m0, %0\n\ts_nop 0\n\tbuffer_load_dwordx4 %1, %2, %3 offen lds" :: "s"(dst), "v"(voff), "s"(r), "s"(soff) : "memory");
}
DI void dma16s(char* lds, unsigned lds_off, rsrc_t r, unsigned voff, unsigned soff) { dma<SC1>(lds, lds_off, r, voff, soff); }
#define WAITV(n) asm volatile("s_waitcnt vmcnt(%0)" :: "n"(n) : "memory")

struct DPlan { rsrc_t r; unsigned v0, v1, sa, sb, st; bool on; };
struct DOut { unsigned dst[8]; u32x4 f[8]; };
DI void dtask_plan(const Params& p, int id, int l, DPlan& d, DOut& o) {
    int ra0, rb0, rstep, cg, lim; unsigned pitch, d0, r5, stride, jump;
    if (id < 370) {
        const bool wo = id < 100; const int i2 = wo ? id : id - 100, s = i2 / 5; cg = i2 - 5 * s; pitch = 1280;
        if (wo) { d.r = mk_rsrc(p.wo, 640 * 1280); if (s < 16) { ra0 = 40 * s; rstep = 4; } else { ra0 = 160 * (s - 16) + 16; rstep = 40; } rb0 = ra0 + 20; }
        else { d.r = mk_rsrc(p.w2, 1728 * 1280); int ta, tb; tiles_p4(s, ta, tb); ra0 = 16 * ta; rb0 = 16 * tb; rstep = 4; }
        const int c0 = (8 * cg) / 5;
        r5 = 8 * cg - 5 * c0; d0 = (wo ? WS_WOF + (c0 * 20 + s) * 5120 : WS_W2F + (c0 * 54 + s) * 5120) + r5 * 1024;
        stride = 1024; jump = wo ? 19 * 5120 : 53 * 5120; lim = 8;
    } else {
        const int i2 = id < 650 ? id - 370 : 0, s = i2 / 14; cg = i2 - 14 * s; pitch = 3456; d.r = mk_rsrc(p.w1, 640 * 3456);
        int ta, tb; tiles_p3(s, ta, tb); ra0 = 16 * ta; rb0 = 16 * tb; rstep = 4;
        r5 = 0; d0 = WS_W1F + (8 * cg * 20 + s) * 1024; stride = 20480; jump = 0; lim = id < 650 ? (cg == 13 ? 4 : 8) : 0;
    }
    const int q = l >> 4, cp = l & 15;
    d.v0 = q * pitch + ((cp ^ (q << 1)) * 16); d.v1 = q * pitch + ((cp ^ ((4 + q) << 1)) * 16);
    d.sa = ra0 * pitch + cg * 256; d.sb = rb0 * pitch + cg * 256; d.st = rstep * pitch; d.on = lim > 0;
#pragma unroll
    for (int nt = 0; nt < 8; ++nt) {
        const unsigned v = d0 + nt * stride + ((r5 + nt >= 5 ? 1u : 0u) + (r5 + nt >= 10 ? 1u : 0u)) * jump;
        o.dst[nt] = __builtin_amdgcn_readfirstlane(nt < lim ? v : 0x7ff00000u);
    }
}
DI void dtask_go(const DPlan& d, char* lds, unsigned scr, int w) {
    if (d.on) {
#pragma unroll
        for (int i = 0; i < 8; ++i) dma16(lds, scr + w * 8192 + i * 1024, d.r, (i & 1) ? d.v1 : d.v0, (i < 4 ? d.sa : d.sb) + (i & 3) * d.st);
    }
}
DI void dtask_read(DOut& o, char* lds, unsigned scr, int w, int l) {
    const int qd = l >> 4, q = (l >> 2) & 3, pp = l & 3, r7 = 4 * (qd & 1) + q;
    const unsigned a0 = scr + w * 8192 + (4 * qd + q) * 256 + pp * 8;
#pragma unroll
    for (int nt = 0; nt < 8; ++nt) {
        const u32x2 ua = tr_read(lds, a0 + ((nt ^ r7) * 32)), ub = tr_read(lds, a0 + 4096 + ((nt ^ r7) * 32));
        o.f[nt] = (u32x4){ua[0], ua[1], ub[0], ub[1]};
    }
}
template <int NT> DI void dtask_frag(const DOut& o, rsrc_t rs, int l) { bstore16<SC1>(o.f[NT], rs, l * 16, o.dst[NT]); }

template <int I0, int I1> DI void wm_dma(const Params& p, char* lds, int w, int l, unsigned colA, unsigned colB, int rot) {
    const rsrc_t rw = mk_rsrc(p.wm, 640 * 3840 * 2);
    const int rl = l >> 3, cp = l & 7;
    const unsigned c = cp ^ ((((rl >> 1) & 1) | ((w & 1) << 1)) << 1);
    const unsigned voff = rl * 7680 + (c < 4 ? colA + 8 * c : colB + 8 * (c - 4)) * 2;
    int piece = 4 * I0 + w + rot; piece = piece >= 80 ? piece - 80 : piece;
#pragma unroll
    for (int i = I0; i < I1; ++i) {
        dma16(lds, piece * 1024, rw, voff, piece * 61440);
        piece = piece >= 76 ? piece - 76 : piece + 4;
    }
}
DI void c_dma(const Params& p, char* lds, int w, int l) {
    dma16(lds, L0_CRAW + (w & 1) * 1024, mk_rsrc(p.c, 1280), ((w & 1) * 64 + l) * 16, 0);
}
DI void silu_table(char* lds, int tid) {
    if (tid < 80) {
        const u32x4 creg = LD16(L0_CRAW + tid * 16);
        u32x4 o;
#pragma unroll
        for (int d = 0; d < 4; ++d) { const float a = lo_f(creg[d]), b = hi_f(creg[d]); o[d] = pack2(a * __builtin_amdgcn_rcpf(1.f + __expf(-a)), b * __builtin_amdgcn_rcpf(1.f + __expf(-b))); }
        ST16(L0_SC + tid * 16, o);
    }
}
DI float mod_gemv(char* lds, int w, int l) {
    const int qd = l >> 4, q = (l >> 2) & 3, pp = l & 3;
    f32x4 acc = {0, 0, 0, 0}, acc2 = {0, 0, 0, 0};
    const int r0 = 8 * qd + q;
    const unsigned sg = ((r0 >> 1) & 1) | (((r0 >> 3) & 1) << 1);
    unsigned a0 = r0 * 128 + ((w ^ sg) * 32) + pp * 8, a1 = a0 + 40960, sb = L0_SC + qd * 16;
    OPAQUE(a0); OPAQUE(a1); OPAQUE(sb);
    u32x2 tb[2][4]; u32x4 sc[2][2];
    auto rd = [&](int pr) {
        const unsigned ab = pr < 5 ? a0 : a1; const int po = (pr < 5 ? pr : pr - 5) * 8192;
        tb[pr & 1][0] = tr_read(lds, ab + po); tb[pr & 1][1] = tr_read(lds, ab + po + 512); tb[pr & 1][2] = tr_read(lds, ab + po + 4096); tb[pr & 1][3] = tr_read(lds, ab + po + 4608);
        sc[pr & 1][0] = LD16(sb + pr * 128); sc[pr & 1][1] = LD16(sb + pr * 128 + 64);
    };
    rd(0);
#pragma unroll
    for (int pr = 0; pr < 10; ++pr) {
        if (pr + 1 < 10) rd(pr + 1);
        SB();
        acc = mfma(sc[pr & 1][0], (u32x4){tb[pr & 1][0][0], tb[pr & 1][0][1], tb[pr & 1][1][0], tb[pr & 1][1][1]}, acc);
        acc2 = mfma(sc[pr & 1][1], (u32x4){tb[pr & 1][2][0], tb[pr & 1][2][1], tb[pr & 1][3][0], tb[pr & 1][3][1]}, acc2);
        SB();
    }
    return rbf(acc[0] + acc2[0]);
}
DI void x_dma(const Params& p, char* lds, unsigned img, int T, int w, int l) {
    const rsrc_t rx = mk_rsrc(p.x, 2048 * 1280);
    const int r4 = l >> 4, row = 4 * w + r4;
    const unsigned voff = row * 1280 + (((l & 15) ^ row) * 16);
#pragma unroll
    for (int cb = 0; cb < 5; ++cb) dma16(lds, img + (w * 5 + cb) * 1024, rx, voff, T * 16 * 1280 + cb * 256);
}
DI void x_stats(char* lds, unsigned img, unsigned stat, int w, int l) {
    u32x4 v[5];
#pragma unroll
    for (int cb = 0; cb < 5; ++cb) v[cb] = LD16(img + (w * 5 + cb) * 1024 + l * 16);
    f32x2 s1 = {0.f, 0.f}, s2 = {0.f, 0.f};
#pragma unroll
    for (int i = 0; i < 5; ++i)
#pragma unroll
        for (int d = 0; d < 4; ++d) { const f32x2 x = {lo_f(v[i][d]), hi_f(v[i][d])}; s1 += x; s2 = __builtin_elementwise_fma(x, x, s2); }
    const float mean = red16(s1[0] + s1[1]) * (1.f / 640.f), ex2 = red16(s2[0] + s2[1]) * (1.f / 640.f);
    const float rstd = __builtin_amdgcn_rsqf(fmaxf(ex2 - mean * mean, 0.f) + 1e-5f);
    if ((l & 15) == 0) *(LAS f32x2*)((LAS char*)lds + stat + (4 * w + (l >> 4)) * 8) = (f32x2){rstd, -mean * rstd};
}
DI unsigned ximg_off(int t, int byte_in_row) { return ((t >> 2) * 5 + (byte_in_row >> 8)) * 1024 + (t & 3) * 256 + ((((byte_in_row >> 4) & 15) ^ t) * 16) + (byte_in_row & 15); }
DI void x_frags(char* lds, unsigned img, unsigned stat, rsrc_t rs, int T, int w, int l) {
    const int t = l & 15, qd = l >> 4;
    const f32x2 st = *(const LAS f32x2*)((const LAS char*)lds + stat + t * 8);
    const f32x2 r2 = {st[0], st[0]}, m2 = {st[1], st[1]};
    u32x4 v[5];
#pragma unroll
    for (int i = 0; i < 5; ++i) v[i] = LD16(img + ximg_off(t, (w + 4 * i) * 64 + qd * 16));
#pragma unroll
    for (int i = 0; i < 5; ++i) {
        u32x4 o;
#pragma unroll
        for (int d = 0; d < 4; ++d) { const f32x2 y = __builtin_elementwise_fma((f32x2){lo_f(v[i][d]), hi_f(v[i][d])}, r2, m2); o[d] = pack2(y[0], y[1]); }
        bstore16<SC1>(o, rs, l * 16, WS_XNF + (T * 20 + w + 4 * i) * 1024);
    }
}
struct TileKV { u32x2 ka0, ka1, k2, va0, va1, z; };
struct AttnMask { bool c0, c1, p0, p1, odd; unsigned sel0, sel1; };
DI AttnMask attn_mask(int t, int qd) {
    AttnMask m; const int par = t & 1, dc = t - 4 * qd - par;
    m.odd = par; m.c0 = dc >= 0; m.c1 = dc >= 2; m.p0 = dc + 16 <= 14; m.p1 = dc + 14 <= 14;
    m.sel0 = par ? 0x01000c0cu : 0x0c0c0100u; m.sel1 = par ? 0x03020c0cu : 0x0c0c0302u;
    return m;
}
template <class BG> DI void attn_unit(const u32x2 (&qa)[2], const u32x2& xq, const TileKV& cur, const TileKV& prev, bool prev_ok, const AttnMask& m, int qd, u32x4& omain, u32x2& oleft, BG&& bg) {
    const float cs = 0.22360679774997896f * 1.4426950408889634f, ninf = -__builtin_inff();
    f32x4 oa[2], oz[2];
#pragma unroll
    for (int h = 0; h < 2; ++h) {
        const u32x4 qf = {qa[h][0], qa[h][1], qd == h ? xq[0] : 0u, qd == h ? xq[1] : 0u};
        const u32x2 kcp = h == 0 ? cur.ka0 : cur.ka1, kpp = h == 0 ? prev.ka0 : prev.ka1;
        const f32x4 z4 = {0.f, 0.f, 0.f, 0.f};
        const f32x4 sp = mfma((u32x4){kpp[0], kpp[1], prev.k2[0], prev.k2[1]}, qf, z4);
        const f32x4 sc = mfma((u32x4){kcp[0], kcp[1], cur.k2[0], cur.k2[1]}, qf, z4);
        bg(5 * h);
        float e0 = m.odd ? sp[1] : sp[0], e1 = m.odd ? sp[3] : sp[2], e2 = m.odd ? sc[1] : sc[0], e3 = m.odd ? sc[3] : sc[2];
        e0 = (m.p0 && prev_ok) ? e0 : ninf; e1 = (m.p1 && prev_ok) ? e1 : ninf; e2 = m.c0 ? e2 : ninf; e3 = m.c1 ? e3 : ninf;
        bg(5 * h + 1);
        const float mx = red_max_q(fmaxf(fmaxf(e0, e1), fmaxf(e2, e3))), mc = -mx * cs;
        bg(5 * h + 2);
        e0 = __builtin_amdgcn_exp2f(fmaf(e0, cs, mc)); e1 = __builtin_amdgcn_exp2f(fmaf(e1, cs, mc)); e2 = __builtin_amdgcn_exp2f(fmaf(e2, cs, mc)); e3 = __builtin_amdgcn_exp2f(fmaf(e3, cs, mc));
        bg(5 * h + 3);
        const float inv = __builtin_amdgcn_rcpf(red_add_q((e0 + e1) + (e2 + e3)));
        bg(5 * h + 4);
        const unsigned pp = pack2(e0 * inv, e1 * inv), pc = pack2(e2 * inv, e3 * inv);
        const u32x4 pf = {__builtin_amdgcn_perm(0u, pp, m.sel0), __builtin_amdgcn_perm(0u, pp, m.sel1), __builtin_amdgcn_perm(0u, pc, m.sel0), __builtin_amdgcn_perm(0u, pc, m.sel1)};
        const u32x2 vc2 = h == 0 ? cur.va0 : cur.va1, vp2 = h == 0 ? prev.va0 : prev.va1;
        oa[h] = mfma((u32x4){vp2[0], vp2[1], vc2[0], vc2[1]}, pf, z4);
        oz[h] = mfma((u32x4){prev.z[0], prev.z[1], cur.z[0], cur.z[1]}, pf, z4);
    }
    const u32x2 a0 = pack4(oa[0]), a1 = pack4(oa[1]);
    omain = (u32x4){a0[0], a0[1], a1[0], a1[1]};
    const u32x2 l0 = pack4(oz[0]), l1 = pack4(oz[1]);
    oleft = qd == 0 ? l0 : l1;
}

__global__ __launch_bounds__(256, 1) void adaLN_block_kernel(Params p) {
    extern __shared__ __attribute__((aligned(16))) char lds[];
    const int b = blockIdx.x, w = __builtin_amdgcn_readfirstlane(threadIdx.x >> 6);
    const int xcd = b & 7, bi = b >> 3;
    char* ws = p.ws;
    const rsrc_t rs = mk_rsrc(ws, WS_END);
    {
        unsigned long long pc; unsigned len;
        asm volatile("s_getpc_b64 %0\n.Ladaln_pc:\n\ts_mov_b32 %1, .Ladaln_end-.Ladaln_pc" : "=s"(pc), "=s"(len));
        unsigned off = (bi * 256 + threadIdx.x) * 64u;
        off = off < len ? off : 0u;
        const unsigned long long a = pc + off;
        const unsigned dst = (unsigned)(size_t)(LAS char*)lds + LDS_BYTES - 1024 + w * 256;
        asm volatile("s_mov_b32 m0, %0\n\ts_nop 0\n\tglobal_load_lds_dword %1, off" :: "s"(dst), "v"(a) : "memory");
    }
    const unsigned epoch = *(const __attribute__((address_space(4))) unsigned*)(size_t)(ws + WS_EPOCH);
    unsigned tag;
#define MAKE_TAG() do { unsigned e_ = epoch; asm volatile("" : "+s"(e_)); tag = e_ + 0x9E3779B9u; } while (0)

    DOut dout;
    {
        LANE_IDS();
        const bool isA = bi < 20, isB = !isA && bi < 25, isC = !isA && !isB;
        const int sA = bi < 16 ? xcd + 8 * (bi >> 3) : 16 + (xcd >> 1), jg = bi < 16 ? (bi & 7) : 4 * (xcd & 1) + (bi - 16), idxB = xcd * 5 + (bi - 20);
        const int aidx = jg * 20 + sA;
        const int idxC = xcd * 7 + (bi - 25);
        const int Ts = idxC < 16 ? 3 * idxC : 48 + 2 * (idxC - 16), nT = idxC < 16 ? 3 : 2;
        const int did = w == 0 ? 1000 : b * 3 + w - 1;
        DPlan dp;
        if (isC) {
#pragma unroll
            for (int k = 0; k < 3; ++k) if (k < nT) x_dma(p, lds, k * 20480, Ts + k, w, l);
            MAKE_TAG();
            dtask_plan(p, did, l, dp, dout);
            if (nT == 3) { WAITV(10); x_stats(lds, 0, L0_XS, w, l); WAITV(5); x_stats(lds, 20480, L0_XS + 128, w, l); WAITV(0); dtask_go(dp, lds, L0_D, w); x_stats(lds, 40960, L0_XS + 256, w, l); }
            else { WAITV(5); x_stats(lds, 0, L0_XS, w, l); WAITV(0); dtask_go(dp, lds, L0_D, w); x_stats(lds, 20480, L0_XS + 128, w, l); }
            BAR();
#pragma unroll
            for (int k = 0; k < 3; ++k) if (k < nT) x_frags(lds, k * 20480, L0_XS + k * 128, rs, Ts + k, w, l);
            DRAIN();
            BAR();
            flag_put(rs, WS_FC + (Ts + (tid & 3)) * FS, tag, tid < nT);
        } else {
            const unsigned colA = isA ? 640 + 32 * sA : 1280 + 64 * idxB, colB = isA ? 32 * sA : 1280 + 64 * idxB + 32;
            const int rot = isA ? (bi < 16 ? 10 * jg : 20 * (bi - 16)) : 0;
            c_dma(p, lds, w, l);
            wm_dma<0, 10>(p, lds, w, l, colA, colB, rot);
            WAITV(10);
            silu_table(lds, tid);
            wm_dma<10, 20>(p, lds, w, l, colA, colB, rot);
            if (isA) {
                const rsrc_t rq = mk_rsrc(p.wqkv, 640 * 3840);
#pragma unroll
                for (int m = 0; m < 4; ++m) {
                    const int n = 64 * (4 * m + w) + l, rho = n / 30, c = n % 30;
                    const int k = (rho & 19) | ((rho & 4) << 1) | ((rho & 8) >> 1);
                    if (4 * m + w < 15) dma16(lds, L0_RW + (4 * m + w) * 1024, rq, k * 3840 + (c / 10) * 1280 + jg * 160 + (c % 10) * 16, sA * 32 * 3840);
                }
            }
            MAKE_TAG();
            dtask_plan(p, did, l, dp, dout);
            WAITV(0);
            dtask_go(dp, lds, L0_D, w);
            BAR();
            float v = mod_gemv(lds, w, l);
            if (isA) {
                const float gv = w < 2 ? rbf(1.f + v) : v;
                if (l < 16) { STF(L0_GS + (w * 16 + l) * 4, gv); *(LAS u16*)((LAS char*)lds + L0_GS + 256 + (w * 16 + l) * 2) = (u16)(__float_as_uint(gv) >> 16); }
                const int q = (l >> 2) & 3, pp = l & 3, jj = w >> 1, j = 2 * jg + jj;
                const unsigned rbase = L0_RW + (16 * (qd >> 1) + 4 * (qd & 1) + q) * 480 + 80 * jj;
                u32x2 ua[4], ub[4];
#pragma unroll
                for (int i = 0; i < 4; ++i) {
                    const unsigned lo = i < 2 ? i * 40 + 8 * pp : 160 + (i - 2) * 40 + 8 * pp;
                    const unsigned hi = i == 0 ? (pp >> 1) * 160 + (pp & 1) * 40 + 32 : i < 3 ? 320 + (i - 1) * 40 + 8 * pp : 320 + (pp & 1) * 40 + 32;
                    const unsigned po = (w & 1) ? hi : lo;
                    ua[i] = tr_read(lds, rbase + po); ub[i] = tr_read(lds, rbase + po + 8 * 480);
                }
                if ((w & 1) && t >= 8) { ua[3] = (u32x2){0u, 0u}; ub[3] = ua[3]; }
                BAR();
                const f32x4 g0 = LDF4(L0_GS + qd * 32), g1 = LDF4(L0_GS + qd * 32 + 16);
                const u32x4 sfr = LD16(L0_GS + 256 + 64 + qd * 16);
#pragma unroll
                for (int i = 0; i < 4; ++i) {
                    const int nt = 4 * (w & 1) + i;
                    const u32x4 o = {pack2(lo_f(ua[i][0]) * g0[0], hi_f(ua[i][0]) * g0[1]), pack2(lo_f(ua[i][1]) * g0[2], hi_f(ua[i][1]) * g0[3]),
                                     pack2(lo_f(ub[i][0]) * g1[0], hi_f(ub[i][0]) * g1[1]), pack2(lo_f(ub[i][1]) * g1[2], hi_f(ub[i][1]) * g1[3])};
                    bstore16<SC1>(o, rs, lo16, WS_WQF + ((j * 20 + sA) * 8 + nt) * 1024);
                }
                {
                    const f32x4 z4 = {0, 0, 0, 0};
                    f32x4 bias = mfma((u32x4){ua[0][0], ua[0][1], ub[0][0], ub[0][1]}, sfr, z4);
#pragma unroll
                    for (int i = 1; i < 4; ++i) { const f32x4 bi4 = mfma((u32x4){ua[i][0], ua[i][1], ub[i][0], ub[i][1]}, sfr, z4); bias = t == i ? bi4 : bias; }
                    bstore16<SC1>(asu4(bias), rs, t < 4 ? t * 64 + qd * 16 : 0x7ff00000u, WS_BP + ((sA * 16 + j) * 128 + (w & 1) * 64) * 4);
                }
            } else {
                const int col = 1280 + 64 * idxB + 16 * w + t;
                if (col >= 2560 && col < 3200) v = rbf(1.f + v);
                bstore4<SC1>(__float_as_uint(v), rs, l < 16 ? l * 4 : 0x7ff00000u, WS_MODV + (1280 + 64 * idxB + 16 * w) * 4);
                const unsigned vn = (unsigned)__builtin_amdgcn_update_dpp(0, (int)__float_as_uint(v), 0xB1, 0xf, 0xf, true);
                bstore4<SC1>((__float_as_uint(v) >> 16) | (vn & 0xffff0000u), rs, (l < 16 && !(l & 1)) ? l * 2 : 0x7ff00000u, WS_MODB + (1280 + 64 * idxB + 16 * w) * 2);
            }
            DRAIN();
            BAR();
            flag_put(rs, WS_FA + aidx * FS, tag, isA && tid == 0);
        }
    }

    const int g1 = 4 * (xcd >> 1) + (bi >> 3), j1 = 8 * (xcd & 1) + (bi & 7);
    const int P2 = 4 * xcd + (bi >> 3), c2 = bi & 7;
    const int g3 = g1, j3 = j1;
    const int t3 = j3 < 12 ? 7 * j3 : 84 + 6 * (j3 - 12), cnt3 = j3 < 12 ? 7 : 6;
    u32x4 w2f[14][5];
    const int s4 = __builtin_amdgcn_readfirstlane(w < 2 ? 14 * w : 28 + 13 * (w - 2)), cnt4 = __builtin_amdgcn_readfirstlane(w < 2 ? 14 : 13);
    const unsigned w2b = __builtin_amdgcn_readfirstlane(WS_W2F + (c2 * 54 + s4) * 5 * 1024);
    auto step4 = [&](int i) { return i < 13 ? i : cnt4 - 1; };
    auto w2_ld = [&](int f, unsigned lo) { w2f[f / 5][f % 5] = bload16<SC1>(rs, lo, w2b + step4(f / 5) * 5120 + (f % 5) * 1024); };
    constexpr int L2_WO = 61440, L2_X = 46080;
    {
        LANE_IDS();
        const int T0 = 8 * g1 + 2 * w, TH = g1 == 0 ? 0 : 8 * g1 - 1;
        const unsigned xb0 = WS_XNF + T0 * 20480, xbh = WS_XNF + TH * 20480 + (w >> 1) * 1024, wb = WS_WQF + j1 * 163840 + w * 4096;
        const unsigned wob = WS_WOF + c2 * 102400;
        u32x4 xr[10][4], xh[10];
        auto w_dma = [&](int G, int k) { dma16s(lds, (G & 3) * 16384 + w * 4096 + k * 1024, rs, lo16, wb + G * 16384 + k * 1024); };
        auto x_ld = [&](int G, int i) { xr[G][i] = bload16<SC1>(rs, lo16, xb0 + (i & 1) * 20480 + (2 * G + (i >> 1)) * 1024); };
        if (w == 0) {
            unsigned off[1] = {l == 0 ? WS_FC + TH * FS : WS_FC + (8 * g1 + ((l - 1) & 7)) * FS};
            poll_words<1>(ws, off, l < 9, tag);
        }
        BAR();
#pragma unroll
        for (int G = 0; G < 4; ++G) {
#pragma unroll
            for (int i = 0; i < 4; ++i) x_ld(G, i);
            xh[G] = bload16<SC1>(rs, lo16, xbh + G * 2048);
        }
        SB();
        if (w == 0) {
            unsigned off[1] = {WS_FA + ((j1 >> 1) * 20 + (l < 20 ? l : 0)) * FS};
            poll_words<1>(ws, off, l < 20, tag);
        }
        DRAIN();
        if (w > 0) dtask_read(dout, lds, L0_D, w, l);
        else {
#pragma unroll
            for (int i = 0; i < 8; ++i) dout.f[i] = (u32x4){0u, 0u, 0u, 0u};
        }
        BAR();
#pragma unroll
        for (int G = 0; G < 3; ++G)
#pragma unroll
            for (int k = 0; k < 4; ++k) w_dma(G, k);
        WAITV(4);
        BAR();
        f32x4 acc[2][8], hacc[3];
#pragma unroll
        for (int i = 0; i < 8; ++i) { acc[0][i] = (f32x4){0, 0, 0, 0}; acc[1][i] = (f32x4){0, 0, 0, 0}; }
#pragma unroll
        for (int i = 0; i < 3; ++i) hacc[i] = (f32x4){0, 0, 0, 0};
        u32x4 wf[2][8];
#pragma unroll
        for (int n = 0; n < 8; ++n) wf[0][n] = LD16(n * 1024 + lo16);
        const unsigned hoff = (w >> 1) * 8192 + ((w & 1) ? 5 : 2) * 1024 + lo16;
        f32x4 bp4[3]; u32x4 fdv;
        constexpr int L1_BPART = 40960;
        SB();
#pragma unroll
        for (int G = 0; G < 10; ++G) {
            u32x4 hw[3];
#pragma unroll
            for (int st = 0; st < 2; ++st) {
                const int s = 2 * G + st;
#pragma unroll
                for (int n = 0; n < 8; ++n) {
                    if (s + 1 < 20) wf[(s + 1) & 1][n] = LD16((((s + 1) >> 1) & 3) * 16384 + ((s + 1) & 1) * 8192 + n * 1024 + lo16);
                    if (st == 0 && n < 3) hw[n] = LD16((G & 3) * 16384 + hoff + n * 1024);
                    if (n < 5) { acc[0][n] = mfma(wf[s & 1][n], xr[G][st * 2], acc[0][n]); acc[1][n] = mfma(wf[s & 1][n], xr[G][st * 2 + 1], acc[1][n]); }
                    else { acc[0][n] = mfma(xr[G][st * 2], wf[s & 1][n], acc[0][n]); acc[1][n] = mfma(xr[G][st * 2 + 1], wf[s & 1][n], acc[1][n]); }
                    if (st == 1 && n >= 5) hacc[n - 5] = mfma(hw[n - 5], xh[G], hacc[n - 5]);
                    if (st == 0) {
                        if (G + 3 < 10 && (n & 1) == 0) w_dma(G + 3, n >> 1);
                        if (G + 4 < 10 && n == 7) xh[G + 4] = bload16<SC1>(rs, lo16, xbh + (G + 4) * 2048);
                    } else {
                        if (G + 4 < 10 && (n & 1) == 0) x_ld(G + 4, n >> 1);
                    }
                    if (G == 7 && st == 0 && n < 3) bp4[n] = asf4(bload16<SC1>(rs, (n < 2 || tid < 128) ? ((tid >> 5) + 8 * n) * 8192 + (tid & 31) * 16 : 0x7ff00000u, WS_BP + j1 * 512));
                    if (G == 9 && st == 0 && n == 0) {
                        f32x4 bs = (bp4[0] + bp4[1]) + bp4[2];
#pragma unroll
                        for (int r = 0; r < 4; ++r) { auto sw = __builtin_amdgcn_permlane32_swap(__float_as_uint(bs[r]), __float_as_uint(bs[r]), false, false); bs[r] = __uint_as_float(sw[0]) + __uint_as_float(sw[1]); }
                        STF4(L1_BPART + w * 1024 + lo16, bs);
                    }
                    if (G == 9 && st == 1 && n == 0) fdv = bload16<SC1>(rs, lo16, WS_FD);
                    if (n == 3) {
                        if (s == 0) dtask_frag<0>(dout, rs, l);
                        if (s == 1) dtask_frag<1>(dout, rs, l);
                        if (s == 2) dtask_frag<2>(dout, rs, l);
                        if (s == 3) dtask_frag<3>(dout, rs, l);
                        if (s == 4) dtask_frag<4>(dout, rs, l);
                        if (s == 5) dtask_frag<5>(dout, rs, l);
                        if (s == 6) dtask_frag<6>(dout, rs, l);
                        if (s == 7) dtask_frag<7>(dout, rs, l);
                    }
                    SB();
                }
            }
            if (G == 0) WAITV(11); else if (G <= 3) WAITV(17); else if (G == 4) WAITV(11); else if (G == 5) WAITV(14); else if (G == 6) WAITV(9); else if (G == 7) WAITV(3);
            BAR();
            if (G == 4) flag_put(rs, WS_FD + b * 4, tag, tid == 0);
        }
        constexpr int L1_BIAS = 6144, L1_HP = 0, L1_EX = 8192, L1_VT = 6656;
        if (w == 0) {
            check_all_blocks(ws, rs, WS_FD, fdv, tag);
            if (b == 0 && l == 0) flag_store(ws, WS_EPOCH, tag);
            if (l < 32) STF4(L1_BIAS + l * 16, (LDF4(L1_BPART + l * 16) + LDF4(L1_BPART + 1024 + l * 16)) + (LDF4(L1_BPART + 2048 + l * 16) + LDF4(L1_BPART + 3072 + l * 16)));
        }
        if (w >= 2) {
#pragma unroll
            for (int i = 0; i < 3; ++i) STF4(L1_HP + ((w & 1) * 3 + i) * 1024 + lo16, hacc[i]);
        }
        BAR();
        auto wo_dma = [&](int k) { dma16s(lds, L2_WO + w * 25600 + k * 1024, rs, lo16, wob + w * 25600 + k * 1024); };
        const rsrc_t rx = mk_rsrc(p.x, 2048 * 1280);
        unsigned xvo[3];
#pragma unroll
        for (int k = 0; k < 3; ++k) { const int n = (w * 3 + k) * 64 + l, row = n / 10; xvo[k] = n < 640 ? row * 1280 + (n - 10 * row) * 16 : 0x7ff00000u; }
        const unsigned xlo = L2_X + w * 3072;
        auto x_dma2 = [&](int k) { dma<0>(lds, xlo + k * 1024, rx, xvo[k], P2 * 64 * 1280 + c2 * 160); };
        f32x4 bq[5]; float bv[3];
#pragma unroll
        for (int n = 0; n < 5; ++n) bq[n] = LDF4(L1_BIAS + (n * 16 + 4 * qd) * 4);
#pragma unroll
        for (int n = 0; n < 3; ++n) bv[n] = LDF(L1_BIAS + ((5 + n) * 16 + t) * 4);
        TileKV kv[2]; u32x2 qa[2][2], xq[2];
        auto bg_wo = [&](int k) { SB(); wo_dma(k); SB(); };
#pragma unroll
        for (int tt = 1; tt >= 0; --tt) {
            kv[tt].ka0 = pack4(acc[tt][2] + bq[2]); kv[tt].ka1 = pack4(acc[tt][3] + bq[3]);
            bg_wo(4 * (1 - tt));
            xq[tt] = pack4(acc[tt][4] + bq[4]);
            kv[tt].k2[0] = swap32(xq[tt][0]); kv[tt].k2[1] = swap32(xq[tt][1]);
            bg_wo(4 * (1 - tt) + 1);
            kv[tt].va0 = pack4(acc[tt][5] + bv[0]); kv[tt].va1 = pack4(acc[tt][6] + bv[1]);
            bg_wo(4 * (1 - tt) + 2);
            kv[tt].z = pack4(acc[tt][7] + bv[2]);
            if (tt == 1) {
                const unsigned e = L1_EX + (w + 1) * 3072 + lo16;
                ST16(e, ((u32x4){kv[1].ka0[0], kv[1].ka0[1], kv[1].ka1[0], kv[1].ka1[1]}));
                ST16(e + 1024, ((u32x4){kv[1].k2[0], kv[1].k2[1], kv[1].va0[0], kv[1].va0[1]}));
                ST16(e + 2048, ((u32x4){kv[1].va1[0], kv[1].va1[1], kv[1].z[0], kv[1].z[1]}));
            }
            qa[tt][0] = pack4(acc[tt][0] + bq[0]); qa[tt][1] = pack4(acc[tt][1] + bq[1]);
            bg_wo(4 * (1 - tt) + 3);
        }
        if (w == 0) {
            const u32x2 k0 = pack4(hacc[0] + LDF4(L1_HP + lo16) + bq[2]), k1 = pack4(hacc[1] + LDF4(L1_HP + 1024 + lo16) + bq[3]);
            const u32x2 xx = pack4(hacc[2] + LDF4(L1_HP + 2048 + lo16) + bq[4]);
            ST16(L1_EX + lo16, ((u32x4){k0[0], k0[1], k1[0], k1[1]}));
            ST8(L1_EX + 1024 + lo16, ((u32x2){swap32(xx[0]), swap32(xx[1])}));
        } else if (w == 1) {
#pragma unroll
            for (int i = 0; i < 3; ++i) {
                const f32x4 v = hacc[i] + LDF4(L1_HP + (3 + i) * 1024 + lo16) + LDF4(L1_BIAS + ((5 + i) * 16 + 4 * qd) * 4);
                const u32x2 pk = pack4(v);
                LAS u16* d = (LAS u16*)((LAS char*)lds + L1_VT + i * 512 + (4 * qd) * 32 + t * 2);
                d[0] = (u16)pk[0]; d[16] = (u16)(pk[0] >> 16); d[32] = (u16)pk[1]; d[48] = (u16)(pk[1] >> 16);
            }
        }
        const unsigned ob = WS_OF + T0 * 20480;
        const AttnMask am = attn_mask(t, qd);
        u32x4 om; u32x2 ol;
        SB();
        attn_unit(qa[1], xq[1], kv[1], kv[0], true, am, qd, om, ol, [&](int k) { bg_wo(8 + k); });
        SB();
        bstore16<SC1>(om, rs, lo16, ob + 20480 + j1 * 1024);
        bstore8<SC1>(ol, rs, qd < 2 ? ((j1 & 3) * 16 + t) * 16 + 8 * qd : 0x7ff00000u, ob + 20480 + (16 + (j1 >> 2)) * 1024);
        BAR();
        TileKV pv;
        {
            const unsigned e = L1_EX + w * 3072 + lo16;
            const u32x4 a = LD16(e), bb = LD16(e + 1024), cc = LD16(e + 2048);
            pv.ka0 = (u32x2){a[0], a[1]}; pv.ka1 = (u32x2){a[2], a[3]}; pv.k2 = (u32x2){bb[0], bb[1]}; pv.va0 = (u32x2){bb[2], bb[3]};
            pv.va1 = (u32x2){cc[0], cc[1]}; pv.z = (u32x2){cc[2], cc[3]};
            if (w == 0) { pv.va0 = LD8(L1_VT + t * 32 + qd * 8); pv.va1 = LD8(L1_VT + 512 + t * 32 + qd * 8); pv.z = LD8(L1_VT + 1024 + t * 32 + qd * 8); }
        }
        SB();
        attn_unit(qa[0], xq[0], kv[0], pv, !(g1 == 0 && w == 0), am, qd, om, ol, [&](int k) { if (k < 7) bg_wo(18 + k); else { SB(); x_dma2(k - 7); SB(); } });
        SB();
        bstore16<SC1>(om, rs, lo16, ob + j1 * 1024);
        bstore8<SC1>(ol, rs, qd < 2 ? ((j1 & 3) * 16 + t) * 16 + 8 * qd : 0x7ff00000u, ob + (16 + (j1 >> 2)) * 1024);
        publish(rs, WS_F1 + (g1 * 16 + j1) * FS, tag);
    }

    u32x2 x1b[5], b1v[7];
    u32x4 vf[5];
    {
        LANE_IDS();
        const int T = 4 * P2 + w;
        constexpr int L2_MV = 57344;
        constexpr int L2_TB = 58304, L2_B1 = 60864;
        const u32x4 mvl = bload16<SC1>(rs, (1280 + ((tid / 20) % 3) * 640 + 80 * c2 + (tid % 20) * 4) * 4, WS_MODV);
        if (w < 3) { if (w < 2 || l < 32) dma16s(lds, L2_TB + w * 1024, rs, lo16, WS_MODB + 1920 * 2 + w * 1024); }
        else if (l < 14) dma<0>(lds, L2_B1, mk_rsrc(p.b1, 1728 * 2), lo16, t3 * 32);
#pragma unroll
        for (int f = 0; f < 12; ++f) w2_ld(f, lo16);
        SB();
        if (tid < 60) ST16(L2_MV + tid * 16, mvl);
        WAITV(12);
        BAR();
        u32x2 xv[5], va2[5], vb2[5];
#pragma unroll
        for (int i = 0; i < 5; ++i) {
            const int sr = 5 * w + i;
            const int ta = sr < 16 ? 5 * (sr >> 1) + 2 * (sr & 1) : 10 * (sr - 16) + 4, tb = ta + (sr < 16 ? 1 : 5);
            const unsigned sec = L2_TB + (t == 0 ? 1280 : 0) + qd * 8;
            va2[i] = LD8(sec + ta * 32); vb2[i] = LD8(sec + tb * 32);
        }
#pragma unroll
        for (int n = 0; n < 7; ++n) b1v[n] = LD8(L2_B1 + n * 32 + qd * 8);
#pragma unroll
        for (int n = 0; n < 5; ++n) xv[n] = LD8(L2_X + (16 * w + t) * 160 + n * 32 + qd * 8);
        SB();
#pragma unroll
        for (int i = 0; i < 5; ++i) {
            const unsigned cst = t == 2 ? 0x3f803f80u : 0u;
            vf[i] = (u32x4){t < 2 ? va2[i][0] : cst, t < 2 ? va2[i][1] : cst, t < 2 ? vb2[i][0] : cst, t < 2 ? vb2[i][1] : cst};
        }
        asm volatile("" : "+v"(vf[0]), "+v"(vf[1]), "+v"(vf[2]), "+v"(vf[3]), "+v"(vf[4]));
        asm volatile("" : "+v"(b1v[0]), "+v"(b1v[1]), "+v"(b1v[2]), "+v"(b1v[3]), "+v"(b1v[4]), "+v"(b1v[5]), "+v"(b1v[6]), "+v"(xv[0]), "+v"(xv[1]), "+v"(xv[2]), "+v"(xv[3]), "+v"(xv[4]));
        if (w == 0) {
            unsigned off[1] = {WS_F1 + ((P2 >> 1) * 16 + (l & 15)) * FS};
            poll_words<1>(ws, off, l < 16, tag);
        }
        BAR();
        constexpr int PW = 8;
        u32x4 tk[20];
#pragma unroll
        for (int s = 0; s < PW; ++s) tk[s] = bload16<SC1>(rs, lo16, WS_OF + (T * 20 + s) * 1024);
        f32x4 acc[5];
#pragma unroll
        for (int n = 0; n < 5; ++n) acc[n] = (f32x4){0, 0, 0, 0};
        unsigned wb0 = L2_WO + lo16, wb1 = L2_WO + 51200 + lo16; OPAQUE(wb0); OPAQUE(wb1);
        auto wo_rd = [&](int s, int n) { return s < 10 ? LD16(wb0 + (s * 5 + n) * 1024) : LD16(wb1 + ((s - 10) * 5 + n) * 1024); };
        u32x4 wf[3][5];
#pragma unroll
        for (int n = 0; n < 5; ++n) { wf[0][n] = wo_rd(0, n); wf[1][n] = wo_rd(1, n); }
        SB();
#pragma unroll
        for (int s = 0; s < 20; ++s) {
            if (s + PW < 20) tk[s + PW] = bload16<SC1>(rs, lo16, WS_OF + (T * 20 + s + PW) * 1024);
            if (s + 2 < 20) {
#pragma unroll
                for (int n = 0; n < 5; ++n) wf[(s + 2) % 3][n] = wo_rd(s + 2, n);
            }
#pragma unroll
            for (int n = 0; n < 5; ++n) acc[n] = mfma(wf[s % 3][n], tk[s], acc[n]);
#pragma unroll
            for (int n = 0; n < 5; ++n) { if (s + 2 < 20) SGB(0x100, 1); SGB(0x8, 1); if (n == 1 && s + PW < 20) SGB(0x20, 1); }
            SB();
        }
        BAR();
        const unsigned w1src = WS_W1F + t3 * 20480, w1last = (cnt3 == 7 ? 6 : 0) * 20480;
        auto w1_dma = [&](int base_step, int i) {
            const int n = i % 7;
            dma16s(lds, base_step * 7168 + i * 1024, rs, lo16, w1src + (n == 6 ? w1last : n * 20480) + (base_step + i / 7) * 1024);
        };
        float st[6] = {0, 0, 0, 0, 0, 0};
        unsigned mvb = L2_MV + qd * 16; OPAQUE(mvb);
        f32x4 mvr[2][3];
#pragma unroll
        for (int k = 0; k < 3; ++k) mvr[0][k] = LDF4(mvb + k * 320);
        u32x2 ub[5];
        const unsigned ufb = WS_UF + T * 20480;
#pragma unroll
        for (int n = 0; n < 5; ++n) {
            if (n + 1 < 5) {
#pragma unroll
                for (int k = 0; k < 3; ++k) mvr[(n + 1) & 1][k] = LDF4(mvb + k * 320 + (n + 1) * 64);
            }
            const f32x4 ga_n = mvr[n & 1][0], s2_n = mvr[n & 1][1], g2_n = mvr[n & 1][2];
            float x1[4] = {lo_f(xv[n][0]), hi_f(xv[n][0]), lo_f(xv[n][1]), hi_f(xv[n][1])};
#pragma unroll
            for (int r = 0; r < 4; ++r) x1[r] = fmaf(ga_n[r], acc[n][r], x1[r]);
            x1b[n] = (u32x2){pack2(x1[0], x1[1]), pack2(x1[2], x1[3])};
            const float xr[4] = {lo_f(x1b[n][0]), hi_f(x1b[n][0]), lo_f(x1b[n][1]), hi_f(x1b[n][1])};
            ub[n] = (u32x2){pack2(xr[0] * g2_n[0], xr[1] * g2_n[1]), pack2(xr[2] * g2_n[2], xr[3] * g2_n[3])};
            const float ur[4] = {lo_f(ub[n][0]), hi_f(ub[n][0]), lo_f(ub[n][1]), hi_f(ub[n][1])};
#pragma unroll
            for (int r = 0; r < 4; ++r) {
                st[0] += xr[r]; st[1] = fmaf(xr[r], xr[r], st[1]); st[2] += ur[r]; st[3] = fmaf(ur[r], g2_n[r], st[3]);
                st[4] = fmaf(ur[r], ur[r], st[4]); st[5] = fmaf(ur[r], s2_n[r], st[5]);
            }
            if (n == 1) bstore16<SC1>((u32x4){ub[0][0], ub[0][1], ub[1][0], ub[1][1]}, rs, lo16, ufb + (2 * c2) * 1024);
            if (n == 3) bstore16<SC1>((u32x4){ub[2][0], ub[2][1], ub[3][0], ub[3][1]}, rs, lo16, ufb + (2 * c2 + 1) * 1024);
            if (n == 4) bstore8<SC1>(ub[4], rs, lo16 + 8 * (c2 & 1), ufb + (16 + (c2 >> 1)) * 1024);
#pragma unroll
            for (int k = 0; k < 3; ++k) if (3 * n + k < 14) w1_dma(2 * w, 3 * n + k);
            SGB(0x20, 1); SGB(0x1, 18); SGB(0x20, 1); SGB(0x1, 18); SGB(0x20, 1); SGB(0x1, 24);
            SB();
        }
#pragma unroll
        for (int i = 0; i < 6; ++i) st[i] = red_add_q(st[i]);
        if (qd < 2) bstore16<SC1>(asu4(qd == 0 ? (f32x4){st[0], st[1], st[2], st[3]} : (f32x4){st[4], st[5], 0.f, 0.f}), rs, qd * 256 + t * 16, WS_ST + (T * 8 + c2) * 512);
#pragma unroll
        for (int n = 0; n < 5; ++n) asm volatile("" : "+v"(x1b[n]));
        publish(rs, WS_F2 + (P2 * 8 + c2) * FS, tag);
#pragma unroll
        for (int i = 0; i < 21; ++i) w1_dma(8 + 3 * w, i);
    }

    {
        LANE_IDS();
        constexpr int L3_GSC = 143360;
        const int T0 = 8 * g3 + 2 * w;
        if (w == 0) {
            unsigned off[1] = {WS_F2 + ((2 * g3 + ((l >> 3) & 1)) * 8 + (l & 7)) * FS};
            poll_words<1>(ws, off, l < 16, tag);
        }
        DRAIN();
        BAR();
        constexpr int PW = 4;
        u32x4 tk[20][2];
        u32x4 spa[2][2]; u32x2 spb[2][2];
        float al[2], be[2], gm[2], de[2], ts[2][6];
        f32x4 red4[4], gr0, gr1;
#pragma unroll
        for (int s = 0; s < PW; ++s) { const unsigned o = WS_UF + (T0 * 20 + s) * 1024; tk[s][0] = bload16<SC1>(rs, lo16, o); tk[s][1] = bload16<SC1>(rs, lo16, o + 20480); }
        SB();
        {
            f32x4 va[7], gram = {0, 0, 0, 0};
            u32x4 wv[2][7];
            unsigned vb = 5 * w * 7168 + lo16; OPAQUE(vb);
#pragma unroll
            for (int n = 0; n < 7; ++n) { va[n] = (f32x4){0, 0, 0, 0}; wv[0][n] = LD16(vb + n * 1024); }
            SB();
#pragma unroll
            for (int i = 0; i < 5; ++i) {
                if (i + 1 < 5) {
#pragma unroll
                    for (int n = 0; n < 7; ++n) wv[(i + 1) & 1][n] = LD16(vb + ((i + 1) * 7 + n) * 1024);
                }
#pragma unroll
                for (int n = 0; n < 7; ++n) va[n] = mfma(wv[i & 1][n], vf[i], va[n]);
                gram = mfma(vf[i], vf[i], gram);
#pragma unroll
                for (int n = 0; n < 7; ++n) { if (i + 1 < 5) SGB(0x100, 1); SGB(0x8, 1); }
                SB();
            }
            if (t < 3) {
#pragma unroll
                for (int n = 0; n < 7; ++n) STF4(L3_GSC + ((w * 8 + n) * 3 + t) * 64 + qd * 16, va[n]);
                if (qd == 0) STF4(L3_GSC + ((w * 8 + 7) * 3 + t) * 64, gram);
            }
        }
        SB();
        f32x4 acc[2][7];
#pragma unroll
        for (int n = 0; n < 7; ++n) { acc[0][n] = (f32x4){0, 0, 0, 0}; acc[1][n] = (f32x4){0, 0, 0, 0}; }
        unsigned wb0 = lo16, wb1 = 57344 + lo16, wb2 = 114688 + lo16; OPAQUE(wb0); OPAQUE(wb1); OPAQUE(wb2);
        auto w1_rd = [&](int s, int n) { return s < 8 ? LD16(wb0 + (s * 7 + n) * 1024) : s < 16 ? LD16(wb1 + ((s - 8) * 7 + n) * 1024) : LD16(wb2 + ((s - 16) * 7 + n) * 1024); };
        u32x4 wf[2][7];
#pragma unroll
        for (int n = 0; n < 7; ++n) wf[0][n] = w1_rd(0, n);
        SB();
#pragma unroll
        for (int s = 0; s < 20; ++s) {
            int nv = 0;
            if (s + PW < 20) { const unsigned o = WS_UF + (T0 * 20 + s + PW) * 1024; tk[s + PW][0] = bload16<SC1>(rs, lo16, o); tk[s + PW][1] = bload16<SC1>(rs, lo16, o + 20480); nv += 2; }
            if (s < 16) { w2_ld(12 + s, lo16); nv += 1; }
            if (s >= 16) {
#pragma unroll
                for (int k = 0; k < 3; ++k) w2_ld(28 + (s - 16) * 3 + k, lo16);
                nv += 3;
            }
            if (s == 10 || s == 11) {
#pragma unroll
                for (int e = 0; e < 2; ++e) {
                    spa[s - 10][e] = bload16<SC1>(rs, (2 * qd + e) * 512 + t * 16, WS_ST + (T0 + s - 10) * 4096);
                    spb[s - 10][e] = bload8<SC1>(rs, (2 * qd + e) * 512 + 256 + t * 16, WS_ST + (T0 + s - 10) * 4096);
                }
                nv += 4;
            }
            int nu = 0;
            if (s == 3) {
                const int r96 = tid < 96 ? tid : 0;
#pragma unroll
                for (int u = 0; u < 4; ++u) red4[u] = LDF4(L3_GSC + (u * 96 + r96) * 16);
            }
            if (s == 4) { STF4(L3_GSC + 6144 + (tid < 96 ? tid : 96 + (tid & 31)) * 16, (red4[0] + red4[1]) + (red4[2] + red4[3])); nu = 1; }
            if (s == 9) { gr0 = LDF4(L3_GSC + 6144 + 84 * 16); gr1 = LDF4(L3_GSC + 6144 + 88 * 16); }
            if (s == 13 || s == 14) {
                const int tt = s - 13;
                const f32x4 a0 = asf4(spa[tt][0]) + asf4(spa[tt][1]);
                const float b00 = __uint_as_float(spb[tt][0][0]) + __uint_as_float(spb[tt][1][0]), b01 = __uint_as_float(spb[tt][0][1]) + __uint_as_float(spb[tt][1][1]);
                ts[tt][0] = red_add_q(a0[0]); ts[tt][1] = red_add_q(a0[1]); ts[tt][2] = red_add_q(a0[2]); ts[tt][3] = red_add_q(a0[3]); ts[tt][4] = red_add_q(b00); ts[tt][5] = red_add_q(b01);
                nu = 4;
            }
            if (s == 15) {
                const float Gg = gr0[0], Ggs = gr0[1], Gs = gr0[2], Sss = gr1[1], Ss = gr1[2], iD = 1.f / 640.f;
#pragma unroll
                for (int tt = 0; tt < 2; ++tt) {
                    const float m1 = ts[tt][0] * iD, v1 = fmaxf(ts[tt][1] * iD - m1 * m1, 0.f), r1v = __builtin_amdgcn_rsqf(v1 + 1e-5f);
                    const float m2 = (r1v * (ts[tt][2] - m1 * Gs) + Ss) * iD;
                    const float e2 = (r1v * r1v * (ts[tt][4] - 2.f * m1 * ts[tt][3] + m1 * m1 * Gg) + 2.f * r1v * (ts[tt][5] - m1 * Ggs) + Sss) * iD;
                    const float r2v = __builtin_amdgcn_rsqf(fmaxf(e2 - m2 * m2, 0.f) + 1e-5f);
                    al[tt] = r1v * r2v; be[tt] = -r1v * r2v * m1; gm[tt] = r2v; de[tt] = -r2v * m2;
                }
                nu = 4;
            }
            if (s + 1 < 20) {
#pragma unroll
                for (int n = 0; n < 7; ++n) wf[(s + 1) & 1][n] = w1_rd(s + 1, n);
            }
#pragma unroll
            for (int n = 0; n < 7; ++n) { acc[0][n] = mfma(wf[s & 1][n], tk[s][0], acc[0][n]); acc[1][n] = mfma(wf[s & 1][n], tk[s][1], acc[1][n]); }
#pragma unroll
            for (int n = 0; n < 7; ++n) {
                if (s + 1 < 20) SGB(0x100, 1);
                SGB(0x8, 1);
                if (nu == 1) SGB(0x402, 1);
                if (nu == 4) SGB(0x402, 4);
                SGB(0x8, 1);
                if (nu == 1) SGB(0x402, 1);
                if (nu == 4) SGB(0x402, 4);
                if (n < nv) SGB(0x20, 1);
            }
            SB();
            if (s == 2 || s == 8) BAR();
        }
        u32x2 hb[2][7];
        unsigned gsb = L3_GSC + 6144 + qd * 16; OPAQUE(gsb);
        f32x4 gsc[2][3];
#pragma unroll
        for (int k = 0; k < 3; ++k) gsc[0][k] = LDF4(gsb + 64 * k);
#pragma unroll
        for (int n = 0; n < 7; ++n) {
#pragma unroll
            for (int k = 0; k < 2; ++k) w2_ld(40 + n * 2 + k, lo16);
            if (n + 1 < 7) {
#pragma unroll
                for (int k = 0; k < 3; ++k) gsc[(n + 1) & 1][k] = LDF4(gsb + (n + 1) * 192 + 64 * k);
            }
            const f32x4 G = gsc[n & 1][0], Sv = gsc[n & 1][1], C = gsc[n & 1][2];
            const float bb[4] = {lo_f(b1v[n][0]), hi_f(b1v[n][0]), lo_f(b1v[n][1]), hi_f(b1v[n][1])};
#pragma unroll
            for (int tt = 0; tt < 2; ++tt) {
                float hv[4];
#pragma unroll
                for (int r = 0; r < 4; ++r) {
                    float z = fmaf(gm[tt], Sv[r], bb[r]); z = fmaf(be[tt], G[r], z); z = fmaf(de[tt], C[r], z); z = fmaf(al[tt], acc[tt][n][r], z);
                    const float u = z * fmaf(z * z, -0.044715f * 1.5957691216057308f * 1.4426950408889634f, -1.5957691216057308f * 1.4426950408889634f);
                    hv[r] = z * __builtin_amdgcn_rcpf(1.f + __builtin_amdgcn_exp2f(u));
                }
                hb[tt][n] = (u32x2){pack2(hv[0], hv[1]), pack2(hv[2], hv[3])};
            }
            SGB(0x20, 1); SGB(0x1, 40); SGB(0x20, 1); SGB(0x1, 60);
            SB();
#pragma unroll
            for (int tt = 0; tt < 2; ++tt) {
                const unsigned hbase = WS_HF + (T0 + tt) * 54 * 1024;
                if (n & 1) bstore16<SC1>((u32x4){hb[tt][n - 1][0], hb[tt][n - 1][1], hb[tt][n][0], hb[tt][n][1]}, rs, lo16, hbase + (3 * j3 + (n >> 1)) * 1024);
                if (n == 6 && j3 < 12) bstore8<SC1>(hb[tt][6], rs, lo16 + 8 * (j3 & 1), hbase + (48 + (j3 >> 1)) * 1024);
            }
            SB();
        }
        publish(rs, WS_F3 + (g3 * 16 + j3) * FS, tag);
    }

    {
        LANE_IDS();
        f32x4 gb[5]; u32x2 b2v[5];
#pragma unroll
        for (int f = 54; f < 70; ++f) w2_ld(f, lo16);
        SB();
        if (w == 0) {
            unsigned off[1] = {WS_F3 + ((P2 >> 1) * 16 + (l & 15)) * FS};
            poll_words<1>(ws, off, l < 16, tag);
        }
        BAR();
        constexpr int PD = 3;
        u32x4 tk[14][4];
        const unsigned tokb = __builtin_amdgcn_readfirstlane(WS_HF + (4 * P2 * 54 + s4) * 1024);
        auto issue = [&](int i) {
            const int s = step4(i);
#pragma unroll
            for (int a = 0; a < 4; ++a) tk[i][a] = bload16<SC1>(rs, lo16, tokb + (((a + w) & 3) * 54 + s) * 1024);
        };
#pragma unroll
        for (int i = 0; i < PD; ++i) issue(i);
        f32x4 acc[4][5];
#pragma unroll
        for (int a = 0; a < 4; ++a)
#pragma unroll
            for (int n = 0; n < 5; ++n) acc[a][n] = (f32x4){0, 0, 0, 0};
        SB();
#pragma unroll
        for (int i = 0; i < 14; ++i) {
            if (i < 13 || w < 2) {
                if (i + PD < 14) issue(i + PD);
                if (i == 11) {
#pragma unroll
                    for (int n = 0; n < 5; ++n) {
                        const int f = 80 * c2 + 16 * n;
                        gb[n] = asf4(bload16<SC1>(rs, qd * 16, WS_MODV + (3200 + f) * 4));
                        b2v[n] = *(const GAS u32x2*)(p.b2 + f + 4 * qd);
                    }
                }
#pragma unroll
                for (int a = 0; a < 4; ++a)
#pragma unroll
                    for (int n = 0; n < 5; ++n) acc[a][n] = mfma(w2f[i][n], tk[i][a], acc[a][n]);
#pragma unroll
                for (int a = 0; a < 4; ++a) { SGB(0x8, 2); if (i == 11) SGB(0x20, 3); else SGB(0x20, 1); SGB(0x8, 3); }
            }
            SB();
        }
#pragma unroll
        for (int a = 1; a < 4; ++a) {
            unsigned d = ((((a + w) & 3) * 3 + (3 - a)) * 5) * 1024 + lo16; OPAQUE(d);
#pragma unroll
            for (int n = 0; n < 5; ++n) STF4(d + n * 1024, acc[a][n]);
        }
        BAR();
        constexpr int L4_OUT = 61440;
        unsigned rb = w * 15360 + lo16; OPAQUE(rb);
        f32x4 part[5][3];
#pragma unroll
        for (int n = 0; n < 5; ++n)
#pragma unroll
            for (int u = 0; u < 3; ++u) part[n][u] = LDF4(rb + (u * 5 + n) * 1024);
        SB();
#pragma unroll
        for (int n = 0; n < 5; ++n) {
            const f32x4 v = (acc[0][n] + part[n][0]) + (part[n][1] + part[n][2]);
            const float x1[4] = {lo_f(x1b[n][0]), hi_f(x1b[n][0]), lo_f(x1b[n][1]), hi_f(x1b[n][1])};
            const float bv[4] = {lo_f(b2v[n][0]), hi_f(b2v[n][0]), lo_f(b2v[n][1]), hi_f(b2v[n][1])};
            float o[4];
#pragma unroll
            for (int r = 0; r < 4; ++r) o[r] = fmaf(gb[n][r], v[r] + bv[r], x1[r]);
            ST8(L4_OUT + (16 * w + t) * 176 + n * 32 + qd * 8, ((u32x2){pack2(o[0], o[1]), pack2(o[2], o[3])}));
        }
        asm volatile("s_waitcnt lgkmcnt(0)" ::: "memory");
        const rsrc_t ro = mk_rsrc(p.out, 2048 * 1280);
#pragma unroll
        for (int k = 0; k < 3; ++k) {
            const int ch = l + 64 * k;
            if (ch < 160) {
                const int row = ch / 10, cc = ch % 10;
                const u32x4 v = LD16(L4_OUT + (16 * w + row) * 176 + cc * 16);
                bstore16<0>(v, ro, row * 1280 + cc * 16, (16 * (4 * P2 + w)) * 1280 + c2 * 160);
            }
        }
    }
    asm volatile(".Ladaln_end:" ::: "memory");
}

extern "C" void kernel_launch(void* const* d_in, const int* in_sizes, int n_in, void* d_out, int out_size, void* d_ws, size_t ws_size, hipStream_t stream) {
    Params p;
    p.x = (const u16*)d_in[0]; p.wqkv = (const u16*)d_in[1]; p.wo = (const u16*)d_in[2]; p.w1 = (const u16*)d_in[4]; p.b1 = (const u16*)d_in[5];
    p.w2 = (const u16*)d_in[6]; p.b2 = (const u16*)d_in[7]; p.c = (const u16*)d_in[8]; p.wm = (const u16*)d_in[9];
    p.out = (u16*)d_out; p.ws = (char*)d_ws;
    (void)hipFuncSetAttribute((const void*)adaLN_block_kernel, hipFuncAttributeMaxDynamicSharedMemorySize, LDS_BYTES);
    adaLN_block_kernel<<<256, 256, LDS_BYTES, stream>>>(p);
}
```
